```python
import torch

def _softmax(x, axis=-1):
    return torch.softmax(x, dim=axis)
def _gelu(x):
    return torch.nn.functional.gelu(x, approximate="tanh")
def _silu(x):
    return torch.nn.functional.silu(x)
def _sigmoid(x):
    return torch.sigmoid(x)
def _layernorm(x, axis=-1, eps=1e-5):
    m = x.mean(dim=axis, keepdim=True)
    v = x.var(dim=axis, keepdim=True, unbiased=False)
    return (x - m) / torch.sqrt(v + eps)
def _scatter_add(tgt, idx, src, axis=0):
    return tgt.clone().index_add_(axis, idx, src)
def _segment_sum(src, seg, n):
    out = torch.zeros((n,) + src.shape[1:], dtype=src.dtype)
    return out.index_add_(0, seg, src)
def _pad(x, pw):
    flat = tuple(v for pair in reversed(pw) for v in pair)
    return torch.nn.functional.pad(x, flat)

kernel_name = 'gated_mix__linear_attn_block__compressed_kv_cla__prompt'

def setup_inputs():

    _w = (lambda a: (lambda t: t.to(torch.bfloat16) if t.is_floating_point() else t)(torch.as_tensor(a)))
    def _A_setup():
        B, S, D = 1, 2048, 512
        return {
            "x": _w((lambda *s: torch.randn(*s, dtype=torch.bfloat16) * 0.05)(B, S, D)),
            "wqkv": _w((lambda *s: torch.randn(*s, dtype=torch.bfloat16) * 0.8)(D, 3 * D)),
            "wo": _w((lambda *s: torch.randn(*s, dtype=torch.bfloat16) * 0.4)(D, D)),
        }
    _A_in = _A_setup()

    def _B_setup():
        B, H, Sq, Sk, D, M = 1, 32, 2048, 2048, 128, 512
        Dc = 4 * D
        return {
            "x": _w((lambda *s: torch.randn(*s, dtype=torch.bfloat16) * 0.05)(B, Sq, M)),
            "c_kv": _w((lambda *s: torch.randn(*s, dtype=torch.bfloat16) * 0.8)(B, Sk, Dc)),
            "w_q": _w((lambda *s: torch.randn(*s, dtype=torch.bfloat16) * 6.4)(M, H, D) * (M ** -0.5)),
            "w_uk": _w((lambda *s: torch.randn(*s, dtype=torch.bfloat16) * 6.4)(Dc, H, D) * (Dc ** -0.5)),
            "w_uv": _w((lambda *s: torch.randn(*s, dtype=torch.bfloat16) * 0.8)(Dc, H, D) * (Dc ** -0.5)),
            "w_o": _w((lambda *s: torch.randn(*s, dtype=torch.bfloat16) * 0.8)(H, D, M) * ((H * D) ** -0.5)),
        }
    _B_in = _B_setup()

    return {
        "A_x": _A_in["x"],
        "A_wqkv": _A_in["wqkv"],
        "A_wo": _A_in["wo"],
        "B_x": _B_in["x"],
        "B_c_kv": _B_in["c_kv"],
        "B_w_q": _B_in["w_q"],
        "B_w_uk": _B_in["w_uk"],
        "B_w_uv": _B_in["w_uv"],
        "B_w_o": _B_in["w_o"],
        "gate_w": _w((lambda *s: torch.randn(*s, dtype=torch.bfloat16) * 0.05)(512) * 16.0),
    }

def reference(A_x, A_wqkv, A_wo, B_x, B_c_kv, B_w_q, B_w_uk, B_w_uv, B_w_o, gate_w):
    def _A_ref(x, wqkv, wo):
        B, S, D = x.shape
        H = 32; Dh = D // H
        qkv = (lambda x, *d: x.permute(*d))((x @ wqkv).reshape(B, S, 3, H, Dh), 2, 0, 3, 1, 4)
        q, k, v = qkv[0], qkv[1], qkv[2]
        phi = lambda t: torch.where(t > 0, t + 1, torch.exp(t)) + 1e-6
        pq, pk = phi(q), phi(k)
        kv = torch.einsum("bhsd,bhse->bhde", pk, v)
        z = torch.einsum("bhsd->bhd", pk)
        num = torch.einsum("bhqd,bhde->bhqe", pq, kv)
        den = torch.einsum("bhqd,bhd->bhq", pq, z)[..., None] + 1e-6
        o = num / den
        return (lambda x, *d: x.permute(*d))(o, 0, 2, 1, 3).reshape(B, S, D) @ wo

    def _B_ref(x, c_kv, w_q, w_uk, w_uv, w_o):
        B, Sq, M = x.shape[0], x.shape[1], x.shape[2]
        Sk = c_kv.shape[1]
        H, D = w_q.shape[1], w_q.shape[2]
        q = torch.einsum("bqm,mhd->bhqd", x, w_q)
        k = torch.einsum("bkc,chd->bhkd", c_kv, w_uk)
        v = torch.einsum("bkc,chd->bhkd", c_kv, w_uv)
        scores = torch.einsum("bhqd,bhkd->bhqk", q, k) * (D ** -0.5)
        off = Sk - Sq
        qi = torch.arange(Sq)[:, None]
        kj = torch.arange(Sk)[None, :]
        cmask = kj <= (qi + off)
        scores = torch.where(cmask[None, None], scores, -1e9)
        p = _softmax(scores, axis=-1)
        ctx = torch.einsum("bhqk,bhkd->bhqd", p, v)
        return torch.einsum("bhqd,hdm->bqm", ctx, w_o)

    x = A_x
    g = _sigmoid((x * gate_w).sum(-1, keepdims=True))
    a = _A_ref(x, A_wqkv, A_wo)
    b = _B_ref(x, B_c_kv, B_w_q, B_w_uk, B_w_uv, B_w_o)
    return g * a + (1 - g) * b
```

```cpp
#include <hip/hip_runtime.h>
#include <hip/hip_bf16.h>
typedef unsigned short u16;
__device__ __forceinline__ float b2f(u16 v){ return __uint_as_float(((unsigned)v)<<16); }
__device__ __forceinline__ u16 f2b(float f){ unsigned u=__float_as_uint(f); u += 0x7fffu + ((u>>16)&1u); return (u16)(u>>16); }
__global__ void gemm_nn(const u16* A, const u16* B, u16* C, float* Cf, int M, int N, int K){
  __shared__ float As[32][33], Bs[32][33];
  int tx=threadIdx.x, ty=threadIdx.y; int row=blockIdx.y*32+ty, col=blockIdx.x*32+tx; float acc=0;
  for(int k0=0;k0<K;k0+=32){ As[ty][tx]=b2f(A[(size_t)row*K+k0+tx]); Bs[ty][tx]=b2f(B[(size_t)(k0+ty)*N+col]); __syncthreads();
    for(int k=0;k<32;++k) acc+=As[ty][k]*Bs[k][tx]; __syncthreads(); }
  if(C) C[(size_t)row*N+col]=f2b(acc); if(Cf) Cf[(size_t)row*N+col]=acc;
}
__global__ void attn(const u16* Q,const u16* K,const u16* V,u16* ctx){
  int q=blockIdx.x,h=blockIdx.y,t=threadIdx.x; __shared__ float qs[128]; __shared__ float p[2048]; __shared__ float red[128];
  qs[t]=b2f(Q[(size_t)q*4096+h*128+t]); __syncthreads();
  float m=-1e30f;
  for(int k=t;k<=q;k+=128){ float s=0; for(int d=0;d<128;++d) s+=qs[d]*b2f(K[(size_t)k*4096+h*128+d]); s*=0.08838834764831845f; p[k]=s; m=fmaxf(m,s);}
  red[t]=m; __syncthreads(); for(int o=64;o>0;o>>=1){ if(t<o) red[t]=fmaxf(red[t],red[t+o]); __syncthreads(); } m=red[0]; __syncthreads();
  float l=0; for(int k=t;k<=q;k+=128){ float e=__expf(p[k]-m); p[k]=e; l+=e; }
  red[t]=l; __syncthreads(); for(int o=64;o>0;o>>=1){ if(t<o) red[t]+=red[t+o]; __syncthreads(); } l=red[0];
  float acc=0; for(int k=0;k<=q;++k) acc+=p[k]*b2f(V[(size_t)k*4096+h*128+t]);
  ctx[(size_t)q*4096+h*128+t]=f2b(acc/l);
}
__device__ __forceinline__ float phi(float t){ return (t>0.f? t+1.f : __expf(t)) + 1e-6f; }
__global__ void astate(const u16* qkv, float* kv, float* z){
  int h=blockIdx.x,d=threadIdx.x>>4,e=threadIdx.x&15; float a=0,zz=0;
  for(int s=0;s<2048;++s){ float pk=phi(b2f(qkv[(size_t)s*1536+512+h*16+d])); a+=pk*b2f(qkv[(size_t)s*1536+1024+h*16+e]); zz+=pk; }
  kv[h*256+d*16+e]=a; if(e==0) z[h*16+d]=zz;
}
__global__ void aout(const u16* qkv,const float* kv,const float* z,u16* o){
  int s=blockIdx.x,c=threadIdx.x,h=c>>4,e=c&15; float num=0,den=0;
  for(int d=0;d<16;++d){ float pq=phi(b2f(qkv[(size_t)s*1536+h*16+d])); num+=pq*kv[h*256+d*16+e]; den+=pq*z[h*16+d]; }
  o[(size_t)s*512+c]=f2b(num/(den+1e-6f));
}
__global__ void mix(const u16* x,const u16* gw,const float* a,const float* b,u16* out){
  int s=blockIdx.x,c=threadIdx.x; __shared__ float red[512]; red[c]=b2f(x[(size_t)s*512+c])*b2f(gw[c]); __syncthreads();
  for(int o=256;o>0;o>>=1){ if(c<o) red[c]+=red[c+o]; __syncthreads(); }
  float g=1.f/(1.f+__expf(-red[0])); out[(size_t)s*512+c]=f2b(g*a[(size_t)s*512+c]+(1.f-g)*b[(size_t)s*512+c]);
}
extern "C" void kernel_launch(void* const* d_in, const int* in_sizes, int n_in, void* d_out, int out_size, void* d_ws, size_t ws_size, hipStream_t stream){
  const u16* x=(const u16*)d_in[0]; const u16* wqkv=(const u16*)d_in[1]; const u16* wo=(const u16*)d_in[2];
  const u16* ckv=(const u16*)d_in[4]; const u16* wq=(const u16*)d_in[5]; const u16* wuk=(const u16*)d_in[6]; const u16* wuv=(const u16*)d_in[7]; const u16* wO=(const u16*)d_in[8]; const u16* gw=(const u16*)d_in[9];
  char* ws=(char*)d_ws; size_t MB=1<<20;
  u16* Q=(u16*)(ws); u16* K=(u16*)(ws+16*MB); u16* V=(u16*)(ws+32*MB); u16* ctx=(u16*)(ws+48*MB); u16* qkv=(u16*)(ws+64*MB);
  float* kv=(float*)(ws+72*MB); float* z=(float*)(ws+73*MB); u16* o=(u16*)(ws+74*MB); float* a=(float*)(ws+80*MB); float* b=(float*)(ws+88*MB);
  dim3 blk(32,32);
  gemm_nn<<<dim3(128,64),blk,0,stream>>>(x,wq,Q,nullptr,2048,4096,512);
  gemm_nn<<<dim3(128,64),blk,0,stream>>>(ckv,wuk,K,nullptr,2048,4096,512);
  gemm_nn<<<dim3(128,64),blk,0,stream>>>(ckv,wuv,V,nullptr,2048,4096,512);
  gemm_nn<<<dim3(48,64),blk,0,stream>>>(x,wqkv,qkv,nullptr,2048,1536,512);
  attn<<<dim3(2048,32),128,0,stream>>>(Q,K,V,ctx);
  astate<<<32,256,0,stream>>>(qkv,kv,z);
  aout<<<2048,512,0,stream>>>(qkv,kv,z,o);
  gemm_nn<<<dim3(16,64),blk,0,stream>>>(o,wo,nullptr,a,2048,512,512);
  gemm_nn<<<dim3(16,64),blk,0,stream>>>(ctx,wO,nullptr,b,2048,512,4096);
  mix<<<2048,512,0,stream>>>(x,gw,a,b,(u16*)d_out);
}
```

```cpp
#include <hip/hip_runtime.h>

extern "C" __global__ __launch_bounds__(256, 1) __attribute__((aligned(4096))) void fused(const void* x, const void* wqkv, const void* wo, const void* ckv, const void* wq, const void* wuk, const void* wuv, const void* wO, const void* gate, void* out, void* ws) {
  __shared__ __attribute__((aligned(16))) char lds[163840];
  unsigned wv = __builtin_amdgcn_readfirstlane(threadIdx.x >> 6);
  unsigned lb = (unsigned)(size_t)lds;
  asm volatile(
"s_load_dwordx2 s[70:71], %0, 24\n"
"s_load_dwordx4 s[84:87], %0, 40\n"
"s_load_dwordx2 s[8:9], %0, 80\n"
"s_load_dwordx2 s[80:81], %0, 0\n"
"s_load_dwordx2 s[40:41], %0, 32\n"
"s_load_dwordx2 s[42:43], %0, 8\n"
"s_load_dwordx2 s[46:47], %0, 64\n"
"s_mov_b64 s[96:97], %0\n"
"s_mov_b32 s13, %1\n"
"s_mov_b32 s12, %2\n"
"s_and_b32 s50, %1, 7\n"
"s_lshr_b32 s51, %1, 3\n"
"s_and_b32 s11, s51, 7\n"
"s_lshr_b32 s51, s51, 3\n"
"s_lshl_b32 s10, s50, 2\n"
"s_add_u32 s10, s10, s51\n"
"v_mbcnt_lo_u32_b32 v250, -1, 0\n"
"v_mbcnt_hi_u32_b32 v250, -1, v250\n"
"v_lshrrev_b32 v251, 4, v250\n"
"v_and_b32 v252, 7, v250\n"
"v_xor_b32 v251, v251, v252\n"
"v_lshlrev_b32 v251, 4, v251\n"
"v_lshrrev_b32 v252, 3, v250\n"
"v_lshl_add_u32 v244, v252, 10, v251\n"
"v_xor_b32 v245, 64, v244\n"
"v_and_b32 v251, 3, v250\n"
"v_lshlrev_b32 v251, 4, v251\n"
"v_lshrrev_b32 v252, 5, v250\n"
"v_lshl_add_u32 v251, v252, 6, v251\n"
"v_bfe_u32 v252, v250, 2, 3\n"
"v_lshl_add_u32 v246, v252, 13, v251\n"
"v_xor_b32 v247, 32, v246\n"
"s_lshl_b32 s90, s12, 12\n"
"s_getpc_b64 s[58:59]\n"
"s_and_b32 s58, s58, 0xfffff000\n"
"v_lshrrev_b32 v251, 4, v250\n"
"v_lshlrev_b32 v251, 14, v251\n"
"v_and_b32 v252, 3, v250\n"
"v_lshl_add_u32 v251, v252, 7, v251\n"
"v_add_u32 v251, s90, v251\n"
"global_load_dword v249, v251, s[58:59] offset:4092\n"
"s_mul_i32 s51, s11, 196608\n"
"s_lshl_b32 s52, s12, 15\n"
"s_add_u32 s64, s51, s52\n"
"s_and_b32 s50, s12, 1\n"
"s_lshl_b32 s53, s50, 17\n"
"s_lshl_b32 s54, s10, 8\n"
"s_add_u32 s53, s53, s54\n"
"s_add_u32 s66, s10, s11\n"
"s_and_b32 s66, s66, 4\n"
"s_lshl_b32 s92, s66, 19\n"
"s_add_u32 s92, s92, s53\n"
"s_lshl_b32 s66, s66, 7\n"
"s_lshl_b32 s67, s12, 6\n"
"s_add_u32 s67, s67, s66\n"
"s_and_b32 s67, s67, 0x380\n"
"s_mov_b32 s93, 0x10000\n"
"s_mov_b32 s94, 128\n"
"s_mov_b32 s95, 0x40000\n"
"s_cmp_ge_u32 s12, 2\n"
"s_waitcnt lgkmcnt(0)\n"
"s_cselect_b64 s[84:85], s[86:87], s[84:85]\n"
"s_cselect_b64 s[44:45], s[42:43], s[40:41]\n"
"s_mov_b64 s[0:1], s[70:71]\n"
"s_and_b32 s85, s85, 0xffff\n"
"s_mov_b32 s86, 4194304\n"
"s_mov_b32 s87, 0x20000\n"
"s_and_b32 s1, s1, 0xffff\n"
"s_mov_b32 s2, 2097152\n"
"s_mov_b32 s3, 0x20000\n"
"s_add_u32 m0, s90, 65536\n"
"s_nop 0\n"
"buffer_load_dwordx4 v246, s[84:87], s92 offen lds\n"
"s_add_u32 m0, s90, 66560\n"
"s_add_u32 s88, s92, s94\n"
"buffer_load_dwordx4 v246, s[84:87], s88 offen lds\n"
"s_add_u32 m0, s90, 67584\n"
"s_add_u32 s89, s92, s93\n"
"buffer_load_dwordx4 v247, s[84:87], s89 offen lds\n"
"s_add_u32 m0, s90, 68608\n"
"s_add_u32 s88, s89, s94\n"
"buffer_load_dwordx4 v247, s[84:87], s88 offen lds\n"
"s_add_u32 s92, s92, s95\n"
"s_add_u32 m0, s90, 81920\n"
"s_nop 0\n"
"buffer_load_dwordx4 v246, s[84:87], s92 offen lds\n"
"s_add_u32 m0, s90, 82944\n"
"s_add_u32 s88, s92, s94\n"
"buffer_load_dwordx4 v246, s[84:87], s88 offen lds\n"
"s_add_u32 m0, s90, 83968\n"
"s_add_u32 s89, s92, s93\n"
"buffer_load_dwordx4 v247, s[84:87], s89 offen lds\n"
"s_add_u32 m0, s90, 84992\n"
"s_add_u32 s88, s89, s94\n"
"buffer_load_dwordx4 v247, s[84:87], s88 offen lds\n"
"s_add_u32 s92, s92, s95\n"
"s_and_b32 s92, s92, 0x3fffff\n"
"s_add_u32 s68, s64, s66\n"
"s_add_u32 s66, s66, 128\n"
"s_and_b32 s66, s66, 0x3ff\n"
"s_add_u32 m0, s90, 98304\n"
"s_add_u32 s88, s68, 0\n"
"buffer_load_dwordx4 v244, s[0:3], s88 offen lds\n"
"s_add_u32 m0, s90, 99328\n"
"s_add_u32 s88, s68, 8192\n"
"buffer_load_dwordx4 v245, s[0:3], s88 offen lds\n"
"s_add_u32 m0, s90, 100352\n"
"s_add_u32 s88, s68, 16384\n"
"buffer_load_dwordx4 v244, s[0:3], s88 offen lds\n"
"s_add_u32 m0, s90, 101376\n"
"s_add_u32 s88, s68, 24576\n"
"buffer_load_dwordx4 v245, s[0:3], s88 offen lds\n"
"s_add_u32 s62, s8, 56623104\n"
"s_addc_u32 s63, s9, 0\n"
"s_load_dwordx2 s[98:99], s[62:63], 0\n"
"v_lshlrev_b32 v248, 4, v250\n"
"v_and_b32 v251, 15, v250\n"
"v_lshrrev_b32 v252, 4, v250\n"
"v_lshrrev_b32 v254, 1, v251\n"
"v_xor_b32 v254, v254, v252\n"
"v_lshlrev_b32 v254, 4, v254\n"
"v_lshl_add_u32 v240, v251, 7, v254\n"
"v_xor_b32 v241, 64, v240\n"
"v_and_b32 v254, 1, v252\n"
"v_lshlrev_b32 v254, 5, v254\n"
"v_lshl_add_u32 v254, v252, 11, v254\n"
"v_bfe_u32 v242, v250, 2, 2\n"
"v_lshl_add_u32 v254, v242, 6, v254\n"
"v_and_b32 v242, 3, v250\n"
"v_lshl_add_u32 v242, v242, 3, v254\n"
"s_lshl_b32 s69, s12, 9\n"
"v_add_u32 v242, s69, v242\n"
"v_xor_b32 v243, 32, v242\n"
"v_mov_b32 v228, 897988541\n"
"v_mov_b32 v229, 897988541\n"
"v_mov_b32 v230, 897988541\n"
"v_mov_b32 v231, 897988541\n"
"s_lshr_b32 s58, s13, 3\n"
"s_lshl_b32 s58, s58, 2\n"
"s_add_u32 s58, s58, s12\n"
"v_bfe_u32 v251, v248, 4, 3\n"
"v_lshl_add_u32 v251, v251, 7, s58\n"
"v_lshlrev_b32 v251, 7, v251\n"
"s_getpc_b64 s[58:59]\n"
"Lfused_cta:\n"
"v_min_u32 v251, Lfused_fin-Lfused_cta, v251\n"
"global_load_dword v249, v251, s[58:59]\n"
"s_add_u32 m0, s90, 114688\n"
"s_nop 0\n"
"buffer_load_dwordx4 v246, s[84:87], s92 offen lds\n"
"s_add_u32 m0, s90, 115712\n"
"s_add_u32 s88, s92, s94\n"
"buffer_load_dwordx4 v246, s[84:87], s88 offen lds\n"
"s_add_u32 m0, s90, 116736\n"
"s_add_u32 s89, s92, s93\n"
"buffer_load_dwordx4 v247, s[84:87], s89 offen lds\n"
"s_add_u32 m0, s90, 117760\n"
"s_add_u32 s88, s89, s94\n"
"buffer_load_dwordx4 v247, s[84:87], s88 offen lds\n"
"s_add_u32 s92, s92, s95\n"
"s_add_u32 m0, s90, 131072\n"
"s_nop 0\n"
"buffer_load_dwordx4 v246, s[84:87], s92 offen lds\n"
"s_lshl_b32 s65, s50, 15\n"
"s_lshl_b32 s76, s11, 16\n"
"s_add_u32 s76, s76, s65\n"
"s_add_u32 s76, s76, 1572864\n"
"s_add_u32 s65, s65, s51\n"
"s_add_u32 s65, s65, 131072\n"
"s_lshl_b32 s55, s11, 17\n"
"s_add_u32 s49, s52, s55\n"
"s_sub_u32 s91, s52, s55\n"
"s_add_u32 s91, s91, 1966080\n"
"s_lshr_b32 s56, s90, 1\n"
"s_lshr_b32 s15, s51, 1\n"
"s_add_u32 s15, s15, s56\n"
"s_lshl_b32 s29, s11, 15\n"
"s_add_u32 s29, s29, s56\n"
"s_lshr_b32 s37, s12, 1\n"
"s_lshl_b32 s37, s37, 14\n"
"s_lshl_b32 s55, s50, 12\n"
"s_add_u32 s37, s37, s55\n"
"s_lshl_b32 s55, s10, 20\n"
"s_add_u32 s4, s8, s55\n"
"s_addc_u32 s5, s9, 0\n"
"s_and_b32 s5, s5, 0xffff\n"
"s_mov_b32 s6, 1048576\n"
"s_mov_b32 s7, 0x20000\n"
"s_and_b32 s81, s81, 0xffff\n"
"s_mov_b32 s82, 2097152\n"
"s_mov_b32 s83, 0x20000\n"
"s_add_u32 m0, s90, 132096\n"
"s_add_u32 s88, s92, s94\n"
"buffer_load_dwordx4 v246, s[84:87], s88 offen lds\n"
"s_add_u32 m0, s90, 133120\n"
"s_add_u32 s89, s92, s93\n"
"buffer_load_dwordx4 v247, s[84:87], s89 offen lds\n"
"s_add_u32 m0, s90, 134144\n"
"s_add_u32 s88, s89, s94\n"
"buffer_load_dwordx4 v247, s[84:87], s88 offen lds\n"
"s_add_u32 s92, s92, s95\n"
"s_and_b32 s92, s92, 0x3fffff\n"
"s_add_u32 s68, s64, s66\n"
"s_add_u32 s66, s66, 128\n"
"s_and_b32 s66, s66, 0x3ff\n"
"s_add_u32 m0, s90, 147456\n"
"s_add_u32 s88, s68, 0\n"
"buffer_load_dwordx4 v244, s[0:3], s88 offen lds\n"
"s_add_u32 m0, s90, 148480\n"
"s_add_u32 s88, s68, 8192\n"
"buffer_load_dwordx4 v245, s[0:3], s88 offen lds\n"
"s_and_b32 s45, s45, 0xffff\n"
"s_mul_i32 s55, s50, 49152\n"
"s_lshr_b32 s57, s54, 3\n"
"s_add_u32 s55, s55, s57\n"
"s_cmp_ge_u32 s12, 2\n"
"s_cselect_b32 s38, 1572864, s86\n"
"s_cselect_b32 s40, s55, s53\n"
"s_cselect_b32 s41, 24576, s93\n"
"s_cselect_b32 s42, 1572864, s94\n"
"s_cselect_b32 s43, 98304, s95\n"
"s_cselect_b64 vcc, -1, 0\n"
"v_and_b32 v251, 3, v250\n"
"v_lshrrev_b32 v252, 5, v250\n"
"v_lshl_add_u32 v251, v252, 2, v251\n"
"v_bfe_u32 v252, v250, 2, 3\n"
"v_mul_u32_u24 v252, 3072, v252\n"
"v_lshrrev_b32 v253, 1, v251\n"
"v_min_u32 v253, 2, v253\n"
"v_lshl_add_u32 v253, v253, 10, v252\n"
"v_and_b32 v254, 1, v251\n"
"v_lshl_add_u32 v253, v254, 4, v253\n"
"v_cndmask_b32 v236, v246, v253, vcc\n"
"v_xor_b32 v251, 2, v251\n"
"v_lshrrev_b32 v253, 1, v251\n"
"v_min_u32 v253, 2, v253\n"
"v_lshl_add_u32 v253, v253, 10, v252\n"
"v_and_b32 v254, 1, v251\n"
"v_lshl_add_u32 v253, v254, 4, v253\n"
"v_cndmask_b32 v237, v247, v253, vcc\n"
"s_add_u32 m0, s90, 149504\n"
"s_add_u32 s88, s68, 16384\n"
"buffer_load_dwordx4 v244, s[0:3], s88 offen lds\n"
"s_add_u32 m0, s90, 150528\n"
"s_add_u32 s88, s68, 24576\n"
"buffer_load_dwordx4 v245, s[0:3], s88 offen lds\n"
"s_add_u32 m0, s90, 0\n"
"s_nop 0\n"
"buffer_load_dwordx4 v246, s[84:87], s92 offen lds\n"
"s_add_u32 m0, s90, 1024\n"
"s_add_u32 s88, s92, s94\n"
"buffer_load_dwordx4 v246, s[84:87], s88 offen lds\n"
"s_add_u32 m0, s90, 2048\n"
"s_add_u32 s89, s92, s93\n"
"buffer_load_dwordx4 v247, s[84:87], s89 offen lds\n"
"v_lshrrev_b32 v251, 5, v250\n"
"v_bfe_u32 v252, v250, 2, 3\n"
"v_lshlrev_b32 v238, 13, v251\n"
"v_lshl_add_u32 v238, v252, 7, v238\n"
"v_mul_u32_u24 v251, 3072, v251\n"
"v_min_u32 v252, 2, v252\n"
"v_lshl_add_u32 v251, v252, 10, v251\n"
"v_cndmask_b32 v238, v238, v251, vcc\n"
"v_lshrrev_b32 v239, 2, v250\n"
"v_lshlrev_b32 v239, 7, v239\n"
"s_lshr_b32 s55, s13, 3\n"
"s_lshl_b32 s55, s55, 1\n"
"s_add_u32 s55, s55, s50\n"
"s_lshr_b32 s57, s41, 2\n"
"s_mul_i32 s24, s55, s57\n"
"s_and_b32 s55, s13, 7\n"
"s_cmp_ge_u32 s12, 2\n"
"s_cselect_b32 s57, 7, 10\n"
"s_lshl_b32 s55, s55, s57\n"
"s_add_u32 s24, s24, s55\n"
"s_lshl_b32 s26, s43, 2\n"
"s_lshl_b32 s25, s13, 13\n"
"s_add_u32 s25, s25, s56\n"
"s_lshr_b32 s27, s13, 3\n"
"s_lshl_b32 s27, s27, 2\n"
"s_add_u32 s27, s27, s12\n"
"s_lshl_b32 s27, s27, 14\n"
"v_lshlrev_b32 v235, 6, v250\n"
"s_mov_b64 s[20:21], s[44:45]\n"
"s_mov_b32 s22, s38\n"
"s_mov_b32 s23, 0x20000\n"
"s_add_u32 m0, s90, 3072\n"
"s_add_u32 s88, s89, s94\n"
"buffer_load_dwordx4 v247, s[84:87], s88 offen lds\n"
"s_add_u32 s92, s92, s95\n"
"s_add_u32 m0, s90, 16384\n"
"s_nop 0\n"
"buffer_load_dwordx4 v246, s[84:87], s92 offen lds\n"
"s_add_u32 m0, s90, 17408\n"
"s_add_u32 s88, s92, s94\n"
"buffer_load_dwordx4 v246, s[84:87], s88 offen lds\n"
"s_add_u32 m0, s90, 18432\n"
"s_add_u32 s89, s92, s93\n"
"buffer_load_dwordx4 v247, s[84:87], s89 offen lds\n"
"s_add_u32 m0, s90, 19456\n"
"s_add_u32 s88, s89, s94\n"
"buffer_load_dwordx4 v247, s[84:87], s88 offen lds\n"
"s_add_u32 s92, s92, s95\n"
"s_and_b32 s92, s92, 0x3fffff\n"
"s_waitcnt vmcnt(33)\n"
"s_mov_b64 exec, 0\n"
"s_cmp_eq_u32 s12, 0\n"
"s_cbranch_scc1 Lfused_hop0\n"
"s_cmp_eq_u32 s12, 1\n"
"s_cbranch_scc1 Lfused_hop1\n"
"s_cmp_eq_u32 s12, 2\n"
"s_cbranch_scc1 Lfused_hop2\n"
"s_branch Lfused_hop3\n"
"Lfused_hopend:\n"
"s_mov_b64 exec, -1\n"
"s_waitcnt vmcnt(21) lgkmcnt(0)\n"
"s_barrier\n"
"s_add_u32 s98, s98, 1\n"
"s_addc_u32 s99, s99, 0\n"
"v_add_u32 v240, 98304, v240\n"
"v_add_u32 v242, 65536, v242\n"
"v_add_u32 v243, 65536, v243\n"
"v_add_u32 v241, 98304, v241\n"
"ds_read_b64_tr_b16 a[0:1], v242 offset:0\n"
"ds_read_b64_tr_b16 a[2:3], v242 offset:256\n"
"ds_read_b128 v[128:131], v240 offset:0\n"
"ds_read_b64_tr_b16 a[4:5], v243 offset:0\n"
"ds_read_b64_tr_b16 a[6:7], v243 offset:256\n"
"ds_read_b128 v[132:135], v241 offset:0\n"
"ds_read_b64_tr_b16 a[8:9], v242 offset:8192\n"
"ds_read_b64_tr_b16 a[10:11], v242 offset:8448\n"
"ds_read_b128 v[136:139], v240 offset:2048\n"
"ds_read_b64_tr_b16 a[12:13], v243 offset:8192\n"
"ds_read_b128 v[140:143], v241 offset:2048\n"
"ds_read_b64_tr_b16 a[14:15], v243 offset:8448\n"
"ds_read_b64_tr_b16 a[16:17], v242 offset:16384\n"
"ds_read_b128 v[144:147], v240 offset:4096\n"
"ds_read_b64_tr_b16 a[18:19], v242 offset:16640\n"
"ds_read_b64_tr_b16 a[20:21], v243 offset:16384\n"
"ds_read_b128 v[148:151], v241 offset:4096\n"
"ds_read_b64_tr_b16 a[22:23], v243 offset:16640\n"
"ds_read_b64_tr_b16 a[24:25], v242 offset:24576\n"
"ds_read_b128 v[152:155], v240 offset:6144\n"
"ds_read_b64_tr_b16 a[26:27], v242 offset:24832\n"
"ds_read_b64_tr_b16 a[28:29], v243 offset:24576\n"
"ds_read_b128 v[156:159], v241 offset:6144\n"
"ds_read_b64_tr_b16 a[30:31], v243 offset:24832\n"
"s_waitcnt vmcnt(16) lgkmcnt(0)\n"
"v_mfma_f32_16x16x32_bf16 v[0:3], a[0:3], v[128:131], 0\n"
"s_barrier\n"
"v_mfma_f32_16x16x32_bf16 v[0:3], a[16:19], v[132:135], v[0:3]\n"
"ds_read_b128 v[160:163], v240 offset:8192\n"
"s_add_u32 s68, s64, s66\n"
"v_mfma_f32_16x16x32_bf16 v[4:7], a[4:7], v[128:131], 0\n"
"ds_read_b64_tr_b16 a[32:33], v242 offset:49152\n"
"s_add_u32 s66, s66, 128\n"
"v_mfma_f32_16x16x32_bf16 v[4:7], a[20:23], v[132:135], v[4:7]\n"
"ds_read_b128 v[164:167], v241 offset:8192\n"
"s_and_b32 s66, s66, 0x3ff\n"
"v_mfma_f32_16x16x32_bf16 v[16:19], v[128:131], a[8:11], 0\n"
"ds_read_b64_tr_b16 a[34:35], v242 offset:49408\n"
"s_add_u32 m0, s90, 32768\n"
"v_mfma_f32_16x16x32_bf16 v[16:19], v[132:135], a[24:27], v[16:19]\n"
"v_mfma_f32_16x16x32_bf16 v[24:27], v[128:131], a[12:15], 0\n"
"ds_read_b128 v[168:171], v240 offset:10240\n"
"s_add_u32 s88, s68, 0\n"
"v_mfma_f32_16x16x32_bf16 v[24:27], v[132:135], a[28:31], v[24:27]\n"
"buffer_load_dwordx4 v244, s[0:3], s88 offen lds\n"
"v_mfma_f32_16x16x32_bf16 v[8:11], a[0:3], v[136:139], 0\n"
"ds_read_b64_tr_b16 a[36:37], v243 offset:49152\n"
"s_add_u32 m0, s90, 33792\n"
"v_mfma_f32_16x16x32_bf16 v[8:11], a[16:19], v[140:143], v[8:11]\n"
"ds_read_b128 v[172:175], v241 offset:10240\n"
"v_mfma_f32_16x16x32_bf16 v[12:15], a[4:7], v[136:139], 0\n"
"ds_read_b64_tr_b16 a[38:39], v243 offset:49408\n"
"s_add_u32 s88, s68, 8192\n"
"v_mfma_f32_16x16x32_bf16 v[12:15], a[20:23], v[140:143], v[12:15]\n"
"buffer_load_dwordx4 v245, s[0:3], s88 offen lds\n"
"v_mfma_f32_16x16x32_bf16 v[20:23], v[136:139], a[8:11], 0\n"
"ds_read_b128 v[176:179], v240 offset:12288\n"
"s_add_u32 m0, s90, 34816\n"
"v_mfma_f32_16x16x32_bf16 v[20:23], v[140:143], a[24:27], v[20:23]\n"
"ds_read_b64_tr_b16 a[40:41], v242 offset:57344\n"
"s_add_u32 s88, s68, 16384\n"
"v_mfma_f32_16x16x32_bf16 v[28:31], v[136:139], a[12:15], 0\n"
"ds_read_b128 v[180:183], v241 offset:12288\n"
"v_mfma_f32_16x16x32_bf16 v[28:31], v[140:143], a[28:31], v[28:31]\n"
"buffer_load_dwordx4 v244, s[0:3], s88 offen lds\n"
"v_mfma_f32_16x16x32_bf16 v[32:35], a[0:3], v[144:147], 0\n"
"ds_read_b64_tr_b16 a[42:43], v242 offset:57600\n"
"s_add_u32 m0, s90, 35840\n"
"v_mfma_f32_16x16x32_bf16 v[32:35], a[16:19], v[148:151], v[32:35]\n"
"ds_read_b128 v[184:187], v240 offset:14336\n"
"s_add_u32 s88, s68, 24576\n"
"v_mfma_f32_16x16x32_bf16 v[36:39], a[4:7], v[144:147], 0\n"
"ds_read_b64_tr_b16 a[44:45], v243 offset:57344\n"
"v_mfma_f32_16x16x32_bf16 v[36:39], a[20:23], v[148:151], v[36:39]\n"
"buffer_load_dwordx4 v245, s[0:3], s88 offen lds\n"
"v_mfma_f32_16x16x32_bf16 v[48:51], v[144:147], a[8:11], 0\n"
"ds_read_b128 v[188:191], v241 offset:14336\n"
"s_add_u32 m0, s90, 49152\n"
"v_mfma_f32_16x16x32_bf16 v[48:51], v[148:151], a[24:27], v[48:51]\n"
"buffer_load_dwordx4 v246, s[84:87], s92 offen lds\n"
"v_mfma_f32_16x16x32_bf16 v[56:59], v[144:147], a[12:15], 0\n"
"ds_read_b64_tr_b16 a[46:47], v243 offset:57600\n"
"s_add_u32 m0, s90, 50176\n"
"v_mfma_f32_16x16x32_bf16 v[56:59], v[148:151], a[28:31], v[56:59]\n"
"v_mfma_f32_16x16x32_bf16 v[40:43], a[0:3], v[152:155], 0\n"
"s_add_u32 s88, s92, s94\n"
"v_mfma_f32_16x16x32_bf16 v[40:43], a[16:19], v[156:159], v[40:43]\n"
"buffer_load_dwordx4 v246, s[84:87], s88 offen lds\n"
"v_mfma_f32_16x16x32_bf16 v[44:47], a[4:7], v[152:155], 0\n"
"s_add_u32 m0, s90, 51200\n"
"v_mfma_f32_16x16x32_bf16 v[44:47], a[20:23], v[156:159], v[44:47]\n"
"v_mfma_f32_16x16x32_bf16 v[52:55], v[152:155], a[8:11], 0\n"
"s_add_u32 s89, s92, s93\n"
"v_mfma_f32_16x16x32_bf16 v[52:55], v[156:159], a[24:27], v[52:55]\n"
"buffer_load_dwordx4 v247, s[84:87], s89 offen lds\n"
"v_mfma_f32_16x16x32_bf16 v[60:63], v[152:155], a[12:15], 0\n"
"buffer_load_dword v249, v239, s[0:3], s25 offen\n"
"v_mfma_f32_16x16x32_bf16 v[60:63], v[156:159], a[28:31], v[60:63]\n"
"s_waitcnt vmcnt(16) lgkmcnt(0)\n"
"v_mfma_f32_16x16x32_bf16 v[64:67], a[0:3], v[160:163], 0\n"
"s_barrier\n"
"v_mfma_f32_16x16x32_bf16 v[64:67], a[16:19], v[164:167], v[64:67]\n"
"ds_read_b128 v[128:131], v240 offset:49152\n"
"s_add_u32 m0, s90, 52224\n"
"v_mfma_f32_16x16x32_bf16 v[68:71], a[4:7], v[160:163], 0\n"
"v_add_u32 v242, 32768, v242\n"
"s_add_u32 s88, s89, s94\n"
"v_mfma_f32_16x16x32_bf16 v[68:71], a[20:23], v[164:167], v[68:71]\n"
"buffer_load_dwordx4 v247, s[84:87], s88 offen lds\n"
"v_mfma_f32_16x16x32_bf16 v[80:83], v[160:163], a[8:11], 0\n"
"v_add_u32 v243, 32768, v243\n"
"s_add_u32 s92, s92, s95\n"
"v_mfma_f32_16x16x32_bf16 v[80:83], v[164:167], a[24:27], v[80:83]\n"
"ds_read_b128 v[132:135], v241 offset:49152\n"
"s_add_u32 m0, s90, 65536\n"
"v_mfma_f32_16x16x32_bf16 v[88:91], v[160:163], a[12:15], 0\n"
"buffer_load_dwordx4 v246, s[84:87], s92 offen lds\n"
"v_mfma_f32_16x16x32_bf16 v[88:91], v[164:167], a[28:31], v[88:91]\n"
"ds_read_b64_tr_b16 a[48:49], v242 offset:32768\n"
"s_add_u32 m0, s90, 66560\n"
"v_mfma_f32_16x16x32_bf16 v[72:75], a[0:3], v[168:171], 0\n"
"ds_read_b128 v[136:139], v240 offset:51200\n"
"s_add_u32 s88, s92, s94\n"
"v_mfma_f32_16x16x32_bf16 v[72:75], a[16:19], v[172:175], v[72:75]\n"
"buffer_load_dwordx4 v246, s[84:87], s88 offen lds\n"
"v_mfma_f32_16x16x32_bf16 v[76:79], a[4:7], v[168:171], 0\n"
"ds_read_b64_tr_b16 a[50:51], v242 offset:33024\n"
"s_add_u32 m0, s90, 67584\n"
"v_mfma_f32_16x16x32_bf16 v[76:79], a[20:23], v[172:175], v[76:79]\n"
"ds_read_b128 v[140:143], v241 offset:51200\n"
"s_add_u32 s89, s92, s93\n"
"v_mfma_f32_16x16x32_bf16 v[84:87], v[168:171], a[8:11], 0\n"
"buffer_load_dwordx4 v247, s[84:87], s89 offen lds\n"
"v_mfma_f32_16x16x32_bf16 v[84:87], v[172:175], a[24:27], v[84:87]\n"
"ds_read_b64_tr_b16 a[52:53], v243 offset:32768\n"
"s_add_u32 m0, s90, 68608\n"
"v_mfma_f32_16x16x32_bf16 v[92:95], v[168:171], a[12:15], 0\n"
"ds_read_b128 v[144:147], v240 offset:53248\n"
"s_add_u32 s88, s89, s94\n"
"v_mfma_f32_16x16x32_bf16 v[92:95], v[172:175], a[28:31], v[92:95]\n"
"buffer_load_dwordx4 v247, s[84:87], s88 offen lds\n"
"v_mfma_f32_16x16x32_bf16 v[96:99], a[0:3], v[176:179], 0\n"
"ds_read_b64_tr_b16 a[54:55], v243 offset:33024\n"
"s_add_u32 s92, s92, s95\n"
"v_mfma_f32_16x16x32_bf16 v[96:99], a[16:19], v[180:183], v[96:99]\n"
"ds_read_b64_tr_b16 a[56:57], v242 offset:40960\n"
"s_and_b32 s92, s92, 0x3fffff\n"
"v_mfma_f32_16x16x32_bf16 v[100:103], a[4:7], v[176:179], 0\n"
"ds_read_b128 v[148:151], v241 offset:53248\n"
"s_add_u32 s68, s64, s66\n"
"v_mfma_f32_16x16x32_bf16 v[100:103], a[20:23], v[180:183], v[100:103]\n"
"ds_read_b64_tr_b16 a[58:59], v242 offset:41216\n"
"s_add_u32 s66, s66, 128\n"
"v_mfma_f32_16x16x32_bf16 v[112:115], v[176:179], a[8:11], 0\n"
"ds_read_b128 v[152:155], v240 offset:55296\n"
"s_and_b32 s66, s66, 0x3ff\n"
"v_mfma_f32_16x16x32_bf16 v[112:115], v[180:183], a[24:27], v[112:115]\n"
"ds_read_b64_tr_b16 a[60:61], v243 offset:40960\n"
"s_add_u32 m0, s90, 81920\n"
"v_mfma_f32_16x16x32_bf16 v[120:123], v[176:179], a[12:15], 0\n"
"ds_read_b128 v[156:159], v241 offset:55296\n"
"s_add_u32 s88, s68, 0\n"
"v_mfma_f32_16x16x32_bf16 v[120:123], v[180:183], a[28:31], v[120:123]\n"
"buffer_load_dwordx4 v244, s[0:3], s88 offen lds\n"
"v_mfma_f32_16x16x32_bf16 v[104:107], a[0:3], v[184:187], 0\n"
"ds_read_b64_tr_b16 a[62:63], v243 offset:41216\n"
"s_add_u32 m0, s90, 82944\n"
"v_mfma_f32_16x16x32_bf16 v[104:107], a[16:19], v[188:191], v[104:107]\n"
"s_add_u32 s88, s68, 8192\n"
"v_mfma_f32_16x16x32_bf16 v[108:111], a[4:7], v[184:187], 0\n"
"buffer_load_dwordx4 v245, s[0:3], s88 offen lds\n"
"v_mfma_f32_16x16x32_bf16 v[108:111], a[20:23], v[188:191], v[108:111]\n"
"s_lshl_b32 s58, s13, 8\n"
"v_mfma_f32_16x16x32_bf16 v[116:119], v[184:187], a[8:11], 0\n"
"v_lshrrev_b32 v253, 2, v248\n"
"v_mfma_f32_16x16x32_bf16 v[116:119], v[188:191], a[24:27], v[116:119]\n"
"v_add_u32 v253, s58, v253\n"
"v_mfma_f32_16x16x32_bf16 v[124:127], v[184:187], a[12:15], 0\n"
"global_load_dword v249, v253, s[62:63] sc1\n"
"v_mfma_f32_16x16x32_bf16 v[124:127], v[188:191], a[28:31], v[124:127]\n"
"s_waitcnt vmcnt(20) lgkmcnt(0)\n"
"v_mfma_f32_16x16x32_bf16 v[0:3], a[32:35], v[128:131], v[0:3]\n"
"s_barrier\n"
"v_mfma_f32_16x16x32_bf16 v[0:3], a[48:51], v[132:135], v[0:3]\n"
"ds_read_b128 v[160:163], v240 offset:57344\n"
"s_add_u32 m0, s90, 83968\n"
"v_mfma_f32_16x16x32_bf16 v[4:7], a[36:39], v[128:131], v[4:7]\n"
"v_add_u32 v242, 4294868992, v242\n"
"s_add_u32 s88, s68, 16384\n"
"v_mfma_f32_16x16x32_bf16 v[4:7], a[52:55], v[132:135], v[4:7]\n"
"buffer_load_dwordx4 v244, s[0:3], s88 offen lds\n"
"v_mfma_f32_16x16x32_bf16 v[16:19], v[128:131], a[40:43], v[16:19]\n"
"v_add_u32 v243, 4294868992, v243\n"
"ds_read_b128 v[164:167], v241 offset:57344\n"
"v_mfma_f32_16x16x32_bf16 v[16:19], v[132:135], a[56:59], v[16:19]\n"
"ds_read_b64_tr_b16 a[64:65], v242 offset:0\n"
"s_add_u32 m0, s90, 84992\n"
"v_mfma_f32_16x16x32_bf16 v[24:27], v[128:131], a[44:47], v[24:27]\n"
"ds_read_b128 v[168:171], v240 offset:59392\n"
"s_add_u32 s88, s68, 24576\n"
"v_mfma_f32_16x16x32_bf16 v[24:27], v[132:135], a[60:63], v[24:27]\n"
"v_mfma_f32_16x16x32_bf16 v[8:11], a[32:35], v[136:139], v[8:11]\n"
"buffer_load_dwordx4 v245, s[0:3], s88 offen lds\n"
"v_mfma_f32_16x16x32_bf16 v[8:11], a[48:51], v[140:143], v[8:11]\n"
"ds_read_b64_tr_b16 a[66:67], v242 offset:256\n"
"s_add_u32 m0, s90, 98304\n"
"v_mfma_f32_16x16x32_bf16 v[12:15], a[36:39], v[136:139], v[12:15]\n"
"ds_read_b128 v[172:175], v241 offset:59392\n"
"ds_read_b64_tr_b16 a[68:69], v243 offset:0\n"
"v_mfma_f32_16x16x32_bf16 v[12:15], a[52:55], v[140:143], v[12:15]\n"
"buffer_load_dwordx4 v246, s[84:87], s92 offen lds\n"
"v_mfma_f32_16x16x32_bf16 v[20:23], v[136:139], a[40:43], v[20:23]\n"
"ds_read_b128 v[176:179], v240 offset:61440\n"
"s_add_u32 m0, s90, 99328\n"
"v_mfma_f32_16x16x32_bf16 v[20:23], v[140:143], a[56:59], v[20:23]\n"
"ds_read_b64_tr_b16 a[70:71], v243 offset:256\n"
"ds_read_b64_tr_b16 a[72:73], v242 offset:8192\n"
"v_mfma_f32_16x16x32_bf16 v[28:31], v[136:139], a[44:47], v[28:31]\n"
"s_add_u32 s88, s92, s94\n"
"v_mfma_f32_16x16x32_bf16 v[28:31], v[140:143], a[60:63], v[28:31]\n"
"buffer_load_dwordx4 v246, s[84:87], s88 offen lds\n"
"v_mfma_f32_16x16x32_bf16 v[32:35], a[32:35], v[144:147], v[32:35]\n"
"ds_read_b128 v[180:183], v241 offset:61440\n"
"ds_read_b64_tr_b16 a[74:75], v242 offset:8448\n"
"v_mfma_f32_16x16x32_bf16 v[32:35], a[48:51], v[148:151], v[32:35]\n"
"ds_read_b128 v[184:187], v240 offset:63488\n"
"s_add_u32 m0, s90, 100352\n"
"v_mfma_f32_16x16x32_bf16 v[36:39], a[36:39], v[144:147], v[36:39]\n"
"ds_read_b64_tr_b16 a[76:77], v243 offset:8192\n"
"s_add_u32 s89, s92, s93\n"
"v_mfma_f32_16x16x32_bf16 v[36:39], a[52:55], v[148:151], v[36:39]\n"
"ds_read_b128 v[188:191], v241 offset:63488\n"
"v_mfma_f32_16x16x32_bf16 v[48:51], v[144:147], a[40:43], v[48:51]\n"
"buffer_load_dwordx4 v247, s[84:87], s89 offen lds\n"
"v_mfma_f32_16x16x32_bf16 v[48:51], v[148:151], a[56:59], v[48:51]\n"
"ds_read_b64_tr_b16 a[78:79], v243 offset:8448\n"
"s_add_u32 m0, s90, 101376\n"
"v_mfma_f32_16x16x32_bf16 v[56:59], v[144:147], a[44:47], v[56:59]\n"
"v_mfma_f32_16x16x32_bf16 v[56:59], v[148:151], a[60:63], v[56:59]\n"
"s_add_u32 s88, s89, s94\n"
"v_mfma_f32_16x16x32_bf16 v[40:43], a[32:35], v[152:155], v[40:43]\n"
"buffer_load_dwordx4 v247, s[84:87], s88 offen lds\n"
"v_mfma_f32_16x16x32_bf16 v[40:43], a[48:51], v[156:159], v[40:43]\n"
"v_mfma_f32_16x16x32_bf16 v[44:47], a[36:39], v[152:155], v[44:47]\n"
"s_add_u32 s92, s92, s95\n"
"v_mfma_f32_16x16x32_bf16 v[44:47], a[52:55], v[156:159], v[44:47]\n"
"s_add_u32 m0, s90, 114688\n"
"v_mfma_f32_16x16x32_bf16 v[52:55], v[152:155], a[40:43], v[52:55]\n"
"v_mfma_f32_16x16x32_bf16 v[52:55], v[156:159], a[56:59], v[52:55]\n"
"buffer_load_dwordx4 v246, s[84:87], s92 offen lds\n"
"v_mfma_f32_16x16x32_bf16 v[60:63], v[152:155], a[44:47], v[60:63]\n"
"buffer_load_dword v249, v239, s[80:83], s25 offen\n"
"v_mfma_f32_16x16x32_bf16 v[60:63], v[156:159], a[60:63], v[60:63]\n"
"s_waitcnt vmcnt(20) lgkmcnt(0)\n"
"v_mfma_f32_16x16x32_bf16 v[64:67], a[32:35], v[160:163], v[64:67]\n"
"s_barrier\n"
"v_mfma_f32_16x16x32_bf16 v[64:67], a[48:51], v[164:167], v[64:67]\n"
"v_add_u32 v240, 4294901760, v240\n"
"s_add_u32 m0, s90, 115712\n"
"v_mfma_f32_16x16x32_bf16 v[68:71], a[36:39], v[160:163], v[68:71]\n"
"ds_read_b64_tr_b16 a[80:81], v242 offset:16384\n"
"s_add_u32 s88, s92, s94\n"
"v_mfma_f32_16x16x32_bf16 v[68:71], a[52:55], v[164:167], v[68:71]\n"
"buffer_load_dwordx4 v246, s[84:87], s88 offen lds\n"
"v_mfma_f32_16x16x32_bf16 v[80:83], v[160:163], a[40:43], v[80:83]\n"
"v_add_u32 v241, 4294901760, v241\n"
"s_add_u32 m0, s90, 116736\n"
"v_mfma_f32_16x16x32_bf16 v[80:83], v[164:167], a[56:59], v[80:83]\n"
"ds_read_b64_tr_b16 a[82:83], v242 offset:16640\n"
"s_add_u32 s89, s92, s93\n"
"v_mfma_f32_16x16x32_bf16 v[88:91], v[160:163], a[44:47], v[88:91]\n"
"buffer_load_dwordx4 v247, s[84:87], s89 offen lds\n"
"v_mfma_f32_16x16x32_bf16 v[88:91], v[164:167], a[60:63], v[88:91]\n"
"ds_read_b128 v[128:131], v240 offset:0\n"
"ds_read_b64_tr_b16 a[84:85], v243 offset:16384\n"
"v_mfma_f32_16x16x32_bf16 v[72:75], a[32:35], v[168:171], v[72:75]\n"
"ds_read_b128 v[132:135], v241 offset:0\n"
"s_add_u32 m0, s90, 117760\n"
"v_mfma_f32_16x16x32_bf16 v[72:75], a[48:51], v[172:175], v[72:75]\n"
"ds_read_b64_tr_b16 a[86:87], v243 offset:16640\n"
"s_add_u32 s88, s89, s94\n"
"v_mfma_f32_16x16x32_bf16 v[76:79], a[36:39], v[168:171], v[76:79]\n"
"buffer_load_dwordx4 v247, s[84:87], s88 offen lds\n"
"v_mfma_f32_16x16x32_bf16 v[76:79], a[52:55], v[172:175], v[76:79]\n"
"ds_read_b128 v[136:139], v240 offset:2048\n"
"s_add_u32 s92, s92, s95\n"
"v_mfma_f32_16x16x32_bf16 v[84:87], v[168:171], a[40:43], v[84:87]\n"
"ds_read_b128 v[140:143], v241 offset:2048\n"
"s_and_b32 s92, s92, 0x3fffff\n"
"v_mfma_f32_16x16x32_bf16 v[84:87], v[172:175], a[56:59], v[84:87]\n"
"ds_read_b64_tr_b16 a[88:89], v242 offset:24576\n"
"ds_read_b128 v[144:147], v240 offset:4096\n"
"v_mfma_f32_16x16x32_bf16 v[92:95], v[168:171], a[44:47], v[92:95]\n"
"Lfused_hop0:\n"
"s_cbranch_execz Lfused_hop4\n"
"s_add_u32 s68, s64, s66\n"
"s_lshl_b32 s50, s10, 2\n"
"v_mfma_f32_16x16x32_bf16 v[92:95], v[172:175], a[60:63], v[92:95]\n"
"ds_read_b64_tr_b16 a[90:91], v242 offset:24832\n"
"s_add_u32 s66, s66, 128\n"
"v_mfma_f32_16x16x32_bf16 v[96:99], a[32:35], v[176:179], v[96:99]\n"
"ds_read_b128 v[148:151], v241 offset:4096\n"
"s_and_b32 s66, s66, 0x3ff\n"
"v_mfma_f32_16x16x32_bf16 v[96:99], a[48:51], v[180:183], v[96:99]\n"
"ds_read_b64_tr_b16 a[92:93], v243 offset:24576\n"
"s_add_u32 m0, s90, 131072\n"
"v_mfma_f32_16x16x32_bf16 v[100:103], a[36:39], v[176:179], v[100:103]\n"
"ds_read_b128 v[152:155], v240 offset:6144\n"
"s_add_u32 s88, s68, 0\n"
"v_mfma_f32_16x16x32_bf16 v[100:103], a[52:55], v[180:183], v[100:103]\n"
"ds_read_b64_tr_b16 a[94:95], v243 offset:24832\n"
"s_add_u32 s50, s50, s12\n"
"v_mfma_f32_16x16x32_bf16 v[112:115], v[176:179], a[40:43], v[112:115]\n"
"buffer_load_dwordx4 v244, s[0:3], s88 offen lds\n"
"v_mfma_f32_16x16x32_bf16 v[112:115], v[180:183], a[56:59], v[112:115]\n"
"ds_read_b128 v[156:159], v241 offset:6144\n"
"s_add_u32 m0, s90, 132096\n"
"v_mfma_f32_16x16x32_bf16 v[120:123], v[176:179], a[44:47], v[120:123]\n"
"s_add_u32 s88, s68, 8192\n"
"s_lshl_b32 s51, s11, 7\n"
"v_mfma_f32_16x16x32_bf16 v[120:123], v[180:183], a[60:63], v[120:123]\n"
"buffer_load_dwordx4 v245, s[0:3], s88 offen lds\n"
"v_mfma_f32_16x16x32_bf16 v[104:107], a[32:35], v[184:187], v[104:107]\n"
"s_add_u32 m0, s90, 133120\n"
"s_add_u32 s52, s50, s51\n"
"v_mfma_f32_16x16x32_bf16 v[104:107], a[48:51], v[188:191], v[104:107]\n"
"s_sub_u32 s53, s50, s51\n"
"s_add_u32 s53, s53, 1920\n"
"v_mfma_f32_16x16x32_bf16 v[108:111], a[36:39], v[184:187], v[108:111]\n"
"s_add_u32 s88, s68, 16384\n"
"s_lshl_b32 s54, s52, 10\n"
"v_mfma_f32_16x16x32_bf16 v[108:111], a[52:55], v[188:191], v[108:111]\n"
"buffer_load_dwordx4 v244, s[0:3], s88 offen lds\n"
"v_mfma_f32_16x16x32_bf16 v[116:119], v[184:187], a[40:43], v[116:119]\n"
"s_add_u32 m0, s90, 134144\n"
"v_mfma_f32_16x16x32_bf16 v[116:119], v[188:191], a[56:59], v[116:119]\n"
"s_add_u32 s88, s68, 24576\n"
"v_mfma_f32_16x16x32_bf16 v[124:127], v[184:187], a[44:47], v[124:127]\n"
"buffer_load_dwordx4 v245, s[0:3], s88 offen lds\n"
"v_mfma_f32_16x16x32_bf16 v[124:127], v[188:191], a[60:63], v[124:127]\n"
"buffer_load_dwordx4 v[208:211], v248, s[80:83], s54 offen\n"
"s_waitcnt vmcnt(23) lgkmcnt(0)\n"
"v_mfma_f32_16x16x32_bf16 v[0:3], a[64:67], v[128:131], v[0:3]\n"
"s_barrier\n"
"v_mfma_f32_16x16x32_bf16 v[0:3], a[80:83], v[132:135], v[0:3]\n"
"ds_read_b128 v[160:163], v240 offset:8192\n"
"s_add_u32 m0, s90, 147456\n"
"v_mfma_f32_16x16x32_bf16 v[4:7], a[68:71], v[128:131], v[4:7]\n"
"buffer_load_dwordx4 v246, s[84:87], s92 offen lds\n"
"v_mfma_f32_16x16x32_bf16 v[4:7], a[84:87], v[132:135], v[4:7]\n"
"ds_read_b64_tr_b16 a[96:97], v242 offset:49152\n"
"s_add_u32 m0, s90, 148480\n"
"v_mfma_f32_16x16x32_bf16 v[16:19], v[128:131], a[72:75], v[16:19]\n"
"ds_read_b128 v[164:167], v241 offset:8192\n"
"ds_read_b64_tr_b16 a[98:99], v242 offset:49408\n"
"v_mfma_f32_16x16x32_bf16 v[16:19], v[132:135], a[88:91], v[16:19]\n"
"s_add_u32 s88, s92, s94\n"
"s_lshl_b32 s54, s53, 10\n"
"v_mfma_f32_16x16x32_bf16 v[24:27], v[128:131], a[76:79], v[24:27]\n"
"buffer_load_dwordx4 v246, s[84:87], s88 offen lds\n"
"v_mfma_f32_16x16x32_bf16 v[24:27], v[132:135], a[92:95], v[24:27]\n"
"ds_read_b128 v[168:171], v240 offset:10240\n"
"ds_read_b64_tr_b16 a[100:101], v243 offset:49152\n"
"v_mfma_f32_16x16x32_bf16 v[8:11], a[64:67], v[136:139], v[8:11]\n"
"ds_read_b128 v[172:175], v241 offset:10240\n"
"s_add_u32 m0, s90, 149504\n"
"v_mfma_f32_16x16x32_bf16 v[8:11], a[80:83], v[140:143], v[8:11]\n"
"s_add_u32 s89, s92, s93\n"
"v_mfma_f32_16x16x32_bf16 v[12:15], a[68:71], v[136:139], v[12:15]\n"
"buffer_load_dwordx4 v247, s[84:87], s89 offen lds\n"
"v_mfma_f32_16x16x32_bf16 v[12:15], a[84:87], v[140:143], v[12:15]\n"
"ds_read_b64_tr_b16 a[102:103], v243 offset:49408\n"
"ds_read_b128 v[176:179], v240 offset:12288\n"
"v_mfma_f32_16x16x32_bf16 v[20:23], v[136:139], a[72:75], v[20:23]\n"
"ds_read_b64_tr_b16 a[104:105], v242 offset:57344\n"
"s_add_u32 m0, s90, 150528\n"
"v_mfma_f32_16x16x32_bf16 v[20:23], v[140:143], a[88:91], v[20:23]\n"
"s_add_u32 s88, s89, s94\n"
"v_mfma_f32_16x16x32_bf16 v[28:31], v[136:139], a[76:79], v[28:31]\n"
"ds_read_b128 v[180:183], v241 offset:12288\n"
"v_mfma_f32_16x16x32_bf16 v[28:31], v[140:143], a[92:95], v[28:31]\n"
"buffer_load_dwordx4 v247, s[84:87], s88 offen lds\n"
"v_mfma_f32_16x16x32_bf16 v[32:35], a[64:67], v[144:147], v[32:35]\n"
"ds_read_b64_tr_b16 a[106:107], v242 offset:57600\n"
"s_add_u32 s92, s92, s95\n"
"v_mfma_f32_16x16x32_bf16 v[32:35], a[80:83], v[148:151], v[32:35]\n"
"ds_read_b128 v[184:187], v240 offset:14336\n"
"s_add_u32 m0, s90, 0\n"
"v_mfma_f32_16x16x32_bf16 v[36:39], a[68:71], v[144:147], v[36:39]\n"
"ds_read_b64_tr_b16 a[108:109], v243 offset:57344\n"
"v_mfma_f32_16x16x32_bf16 v[36:39], a[84:87], v[148:151], v[36:39]\n"
"buffer_load_dwordx4 v246, s[84:87], s92 offen lds\n"
"v_mfma_f32_16x16x32_bf16 v[48:51], v[144:147], a[72:75], v[48:51]\n"
"ds_read_b128 v[188:191], v241 offset:14336\n"
"s_add_u32 m0, s90, 1024\n"
"v_mfma_f32_16x16x32_bf16 v[48:51], v[148:151], a[88:91], v[48:51]\n"
"ds_read_b64_tr_b16 a[110:111], v243 offset:57600\n"
"v_mfma_f32_16x16x32_bf16 v[56:59], v[144:147], a[76:79], v[56:59]\n"
"s_add_u32 s88, s92, s94\n"
"v_mfma_f32_16x16x32_bf16 v[56:59], v[148:151], a[92:95], v[56:59]\n"
"buffer_load_dwordx4 v246, s[84:87], s88 offen lds\n"
"v_mfma_f32_16x16x32_bf16 v[40:43], a[64:67], v[152:155], v[40:43]\n"
"s_add_u32 m0, s90, 2048\n"
"v_mfma_f32_16x16x32_bf16 v[40:43], a[80:83], v[156:159], v[40:43]\n"
"buffer_load_dwordx4 v[212:215], v248, s[80:83], s54 offen\n"
"v_mfma_f32_16x16x32_bf16 v[44:47], a[68:71], v[152:155], v[44:47]\n"
"s_add_u32 s89, s92, s93\n"
"v_mfma_f32_16x16x32_bf16 v[44:47], a[84:87], v[156:159], v[44:47]\n"
"buffer_load_dwordx4 v247, s[84:87], s89 offen lds\n"
"v_mfma_f32_16x16x32_bf16 v[52:55], v[152:155], a[72:75], v[52:55]\n"
"global_load_dwordx4 v[216:219], v248, s[46:47]\n"
"v_mfma_f32_16x16x32_bf16 v[52:55], v[156:159], a[88:91], v[52:55]\n"
"buffer_load_dword v249, v238, s[20:23], s24 offen\n"
"v_mfma_f32_16x16x32_bf16 v[60:63], v[152:155], a[76:79], v[60:63]\n"
"s_add_u32 s24, s24, s26\n"
"v_mfma_f32_16x16x32_bf16 v[60:63], v[156:159], a[92:95], v[60:63]\n"
"s_waitcnt vmcnt(24) lgkmcnt(0)\n"
"v_mfma_f32_16x16x32_bf16 v[64:67], a[64:67], v[160:163], v[64:67]\n"
"s_barrier\n"
"v_mfma_f32_16x16x32_bf16 v[64:67], a[80:83], v[164:167], v[64:67]\n"
"ds_read_b128 v[128:131], v240 offset:49152\n"
"s_add_u32 m0, s90, 3072\n"
"v_mfma_f32_16x16x32_bf16 v[68:71], a[68:71], v[160:163], v[68:71]\n"
"v_add_u32 v242, 65536, v242\n"
"s_add_u32 s88, s89, s94\n"
"v_mfma_f32_16x16x32_bf16 v[68:71], a[84:87], v[164:167], v[68:71]\n"
"buffer_load_dwordx4 v247, s[84:87], s88 offen lds\n"
"v_mfma_f32_16x16x32_bf16 v[80:83], v[160:163], a[72:75], v[80:83]\n"
"v_add_u32 v243, 65536, v243\n"
"s_add_u32 s92, s92, s95\n"
"v_mfma_f32_16x16x32_bf16 v[80:83], v[164:167], a[88:91], v[80:83]\n"
"ds_read_b128 v[132:135], v241 offset:49152\n"
"s_and_b32 s92, s92, 0x3fffff\n"
"v_mfma_f32_16x16x32_bf16 v[88:91], v[160:163], a[76:79], v[88:91]\n"
"ds_read_b64_tr_b16 a[112:113], v242 offset:0\n"
"ds_read_b128 v[136:139], v240 offset:51200\n"
"v_mfma_f32_16x16x32_bf16 v[88:91], v[164:167], a[92:95], v[88:91]\n"
"s_add_u32 s68, s64, s66\n"
"v_mfma_f32_16x16x32_bf16 v[72:75], a[64:67], v[168:171], v[72:75]\n"
"ds_read_b64_tr_b16 a[114:115], v242 offset:256\n"
"s_add_u32 s66, s66, 128\n"
"v_mfma_f32_16x16x32_bf16 v[72:75], a[80:83], v[172:175], v[72:75]\n"
"ds_read_b128 v[140:143], v241 offset:51200\n"
"s_and_b32 s66, s66, 0x3ff\n"
"v_mfma_f32_16x16x32_bf16 v[76:79], a[68:71], v[168:171], v[76:79]\n"
"ds_read_b64_tr_b16 a[116:117], v243 offset:0\n"
"s_add_u32 m0, s90, 16384\n"
"v_mfma_f32_16x16x32_bf16 v[76:79], a[84:87], v[172:175], v[76:79]\n"
"ds_read_b128 v[144:147], v240 offset:53248\n"
"v_mfma_f32_16x16x32_bf16 v[84:87], v[168:171], a[72:75], v[84:87]\n"
"ds_read_b64_tr_b16 a[118:119], v243 offset:256\n"
"s_add_u32 s88, s68, 0\n"
"v_mfma_f32_16x16x32_bf16 v[84:87], v[172:175], a[88:91], v[84:87]\n"
"buffer_load_dwordx4 v244, s[0:3], s88 offen lds\n"
"v_mfma_f32_16x16x32_bf16 v[92:95], v[168:171], a[76:79], v[92:95]\n"
"ds_read_b64_tr_b16 a[120:121], v242 offset:8192\n"
"s_add_u32 m0, s90, 17408\n"
"v_mfma_f32_16x16x32_bf16 v[92:95], v[172:175], a[92:95], v[92:95]\n"
"ds_read_b128 v[148:151], v241 offset:53248\n"
"s_add_u32 s88, s68, 8192\n"
"v_mfma_f32_16x16x32_bf16 v[96:99], a[64:67], v[176:179], v[96:99]\n"
"ds_read_b64_tr_b16 a[122:123], v242 offset:8448\n"
"v_mfma_f32_16x16x32_bf16 v[96:99], a[80:83], v[180:183], v[96:99]\n"
"buffer_load_dwordx4 v245, s[0:3], s88 offen lds\n"
"v_mfma_f32_16x16x32_bf16 v[100:103], a[68:71], v[176:179], v[100:103]\n"
"ds_read_b128 v[152:155], v240 offset:55296\n"
"s_add_u32 m0, s90, 18432\n"
"v_mfma_f32_16x16x32_bf16 v[100:103], a[84:87], v[180:183], v[100:103]\n"
"ds_read_b64_tr_b16 a[124:125], v243 offset:8192\n"
"s_add_u32 s88, s68, 16384\n"
"v_mfma_f32_16x16x32_bf16 v[112:115], v[176:179], a[72:75], v[112:115]\n"
"buffer_load_dwordx4 v244, s[0:3], s88 offen lds\n"
"v_mfma_f32_16x16x32_bf16 v[112:115], v[180:183], a[88:91], v[112:115]\n"
"ds_read_b128 v[156:159], v241 offset:55296\n"
"ds_read_b64_tr_b16 a[126:127], v243 offset:8448\n"
"v_mfma_f32_16x16x32_bf16 v[120:123], v[176:179], a[76:79], v[120:123]\n"
"s_add_u32 m0, s90, 19456\n"
"v_mfma_f32_16x16x32_bf16 v[120:123], v[180:183], a[92:95], v[120:123]\n"
"s_add_u32 s88, s68, 24576\n"
"v_mfma_f32_16x16x32_bf16 v[104:107], a[64:67], v[184:187], v[104:107]\n"
"buffer_load_dwordx4 v245, s[0:3], s88 offen lds\n"
"v_mfma_f32_16x16x32_bf16 v[104:107], a[80:83], v[188:191], v[104:107]\n"
"s_add_u32 m0, s90, 32768\n"
"v_mfma_f32_16x16x32_bf16 v[108:111], a[68:71], v[184:187], v[108:111]\n"
"v_mfma_f32_16x16x32_bf16 v[108:111], a[84:87], v[188:191], v[108:111]\n"
"buffer_load_dwordx4 v246, s[84:87], s92 offen lds\n"
"v_mfma_f32_16x16x32_bf16 v[116:119], v[184:187], a[72:75], v[116:119]\n"
"s_add_u32 m0, s90, 33792\n"
"v_mfma_f32_16x16x32_bf16 v[116:119], v[188:191], a[88:91], v[116:119]\n"
"s_add_u32 s88, s92, s94\n"
"v_mfma_f32_16x16x32_bf16 v[124:127], v[184:187], a[76:79], v[124:127]\n"
"buffer_load_dwordx4 v246, s[84:87], s88 offen lds\n"
"v_mfma_f32_16x16x32_bf16 v[124:127], v[188:191], a[92:95], v[124:127]\n"
"s_waitcnt vmcnt(27) lgkmcnt(0)\n"
"v_mfma_f32_16x16x32_bf16 v[0:3], a[96:99], v[128:131], v[0:3]\n"
"s_barrier\n"
"v_mfma_f32_16x16x32_bf16 v[0:3], a[112:115], v[132:135], v[0:3]\n"
"ds_read_b128 v[160:163], v240 offset:57344\n"
"s_add_u32 m0, s90, 34816\n"
"v_mfma_f32_16x16x32_bf16 v[4:7], a[100:103], v[128:131], v[4:7]\n"
"ds_read_b64_tr_b16 a[128:129], v242 offset:32768\n"
"s_add_u32 s89, s92, s93\n"
"v_mfma_f32_16x16x32_bf16 v[4:7], a[116:119], v[132:135], v[4:7]\n"
"buffer_load_dwordx4 v247, s[84:87], s89 offen lds\n"
"v_mfma_f32_16x16x32_bf16 v[16:19], v[128:131], a[104:107], v[16:19]\n"
"ds_read_b128 v[164:167], v241 offset:57344\n"
"s_add_u32 m0, s90, 35840\n"
"v_mfma_f32_16x16x32_bf16 v[16:19], v[132:135], a[120:123], v[16:19]\n"
"ds_read_b64_tr_b16 a[130:131], v242 offset:33024\n"
"s_add_u32 s88, s89, s94\n"
"v_mfma_f32_16x16x32_bf16 v[24:27], v[128:131], a[108:111], v[24:27]\n"
"buffer_load_dwordx4 v247, s[84:87], s88 offen lds\n"
"v_mfma_f32_16x16x32_bf16 v[24:27], v[132:135], a[124:127], v[24:27]\n"
"ds_read_b128 v[168:171], v240 offset:59392\n"
"s_add_u32 s92, s92, s95\n"
"v_mfma_f32_16x16x32_bf16 v[8:11], a[96:99], v[136:139], v[8:11]\n"
"ds_read_b64_tr_b16 a[132:133], v243 offset:32768\n"
"s_add_u32 m0, s90, 49152\n"
"v_mfma_f32_16x16x32_bf16 v[8:11], a[112:115], v[140:143], v[8:11]\n"
"buffer_load_dwordx4 v246, s[84:87], s92 offen lds\n"
"v_mfma_f32_16x16x32_bf16 v[12:15], a[100:103], v[136:139], v[12:15]\n"
"ds_read_b128 v[172:175], v241 offset:59392\n"
"s_add_u32 m0, s90, 50176\n"
"v_mfma_f32_16x16x32_bf16 v[12:15], a[116:119], v[140:143], v[12:15]\n"
"ds_read_b64_tr_b16 a[134:135], v243 offset:33024\n"
"ds_read_b128 v[176:179], v240 offset:61440\n"
"v_mfma_f32_16x16x32_bf16 v[20:23], v[136:139], a[104:107], v[20:23]\n"
"ds_read_b64_tr_b16 a[136:137], v242 offset:40960\n"
"s_add_u32 s88, s92, s94\n"
"v_mfma_f32_16x16x32_bf16 v[20:23], v[140:143], a[120:123], v[20:23]\n"
"buffer_load_dwordx4 v246, s[84:87], s88 offen lds\n"
"v_mfma_f32_16x16x32_bf16 v[28:31], v[136:139], a[108:111], v[28:31]\n"
"ds_read_b128 v[180:183], v241 offset:61440\n"
"s_add_u32 m0, s90, 51200\n"
"v_mfma_f32_16x16x32_bf16 v[28:31], v[140:143], a[124:127], v[28:31]\n"
"ds_read_b64_tr_b16 a[138:139], v242 offset:41216\n"
"s_add_u32 s89, s92, s93\n"
"v_mfma_f32_16x16x32_bf16 v[32:35], a[96:99], v[144:147], v[32:35]\n"
"buffer_load_dwordx4 v247, s[84:87], s89 offen lds\n"
"v_mfma_f32_16x16x32_bf16 v[32:35], a[112:115], v[148:151], v[32:35]\n"
"ds_read_b128 v[184:187], v240 offset:63488\n"
"s_add_u32 m0, s90, 52224\n"
"v_mfma_f32_16x16x32_bf16 v[36:39], a[100:103], v[144:147], v[36:39]\n"
"ds_read_b64_tr_b16 a[140:141], v243 offset:40960\n"
"s_add_u32 s88, s89, s94\n"
"v_mfma_f32_16x16x32_bf16 v[36:39], a[116:119], v[148:151], v[36:39]\n"
"buffer_load_dwordx4 v247, s[84:87], s88 offen lds\n"
"v_mfma_f32_16x16x32_bf16 v[48:51], v[144:147], a[104:107], v[48:51]\n"
"ds_read_b128 v[188:191], v241 offset:63488\n"
"s_add_u32 s92, s92, s95\n"
"v_mfma_f32_16x16x32_bf16 v[48:51], v[148:151], a[120:123], v[48:51]\n"
"ds_read_b64_tr_b16 a[142:143], v243 offset:41216\n"
"v_mfma_f32_16x16x32_bf16 v[56:59], v[144:147], a[108:111], v[56:59]\n"
"s_and_b32 s92, s92, 0x3fffff\n"
"v_mfma_f32_16x16x32_bf16 v[56:59], v[148:151], a[124:127], v[56:59]\n"
"s_add_u32 s68, s64, s66\n"
"v_mfma_f32_16x16x32_bf16 v[40:43], a[96:99], v[152:155], v[40:43]\n"
"s_add_u32 s66, s66, 128\n"
"v_mfma_f32_16x16x32_bf16 v[40:43], a[112:115], v[156:159], v[40:43]\n"
"s_and_b32 s66, s66, 0x3ff\n"
"v_mfma_f32_16x16x32_bf16 v[44:47], a[100:103], v[152:155], v[44:47]\n"
"s_add_u32 m0, s90, 65536\n"
"v_mfma_f32_16x16x32_bf16 v[44:47], a[116:119], v[156:159], v[44:47]\n"
"s_add_u32 s88, s68, 0\n"
"v_mfma_f32_16x16x32_bf16 v[52:55], v[152:155], a[104:107], v[52:55]\n"
"buffer_load_dwordx4 v244, s[0:3], s88 offen lds\n"
"v_mfma_f32_16x16x32_bf16 v[52:55], v[156:159], a[120:123], v[52:55]\n"
"buffer_load_dword v249, v238, s[20:23], s24 offen\n"
"v_mfma_f32_16x16x32_bf16 v[60:63], v[152:155], a[108:111], v[60:63]\n"
"s_add_u32 s24, s24, s26\n"
"v_mfma_f32_16x16x32_bf16 v[60:63], v[156:159], a[124:127], v[60:63]\n"
"s_waitcnt vmcnt(26) lgkmcnt(0)\n"
"v_mfma_f32_16x16x32_bf16 v[64:67], a[96:99], v[160:163], v[64:67]\n"
"s_barrier\n"
"v_mfma_f32_16x16x32_bf16 v[64:67], a[112:115], v[164:167], v[64:67]\n"
"v_add_u32 v240, 65536, v240\n"
"s_add_u32 m0, s90, 66560\n"
"v_mfma_f32_16x16x32_bf16 v[68:71], a[100:103], v[160:163], v[68:71]\n"
"ds_read_b64_tr_b16 a[144:145], v242 offset:49152\n"
"s_add_u32 s88, s68, 8192\n"
"v_mfma_f32_16x16x32_bf16 v[68:71], a[116:119], v[164:167], v[68:71]\n"
"buffer_load_dwordx4 v245, s[0:3], s88 offen lds\n"
"v_mfma_f32_16x16x32_bf16 v[80:83], v[160:163], a[104:107], v[80:83]\n"
"v_add_u32 v241, 65536, v241\n"
"ds_read_b64_tr_b16 a[146:147], v242 offset:49408\n"
"v_mfma_f32_16x16x32_bf16 v[80:83], v[164:167], a[120:123], v[80:83]\n"
"ds_read_b128 v[128:131], v240 offset:32768\n"
"s_add_u32 m0, s90, 67584\n"
"v_mfma_f32_16x16x32_bf16 v[88:91], v[160:163], a[108:111], v[88:91]\n"
"ds_read_b64_tr_b16 a[148:149], v243 offset:49152\n"
"s_add_u32 s88, s68, 16384\n"
"v_mfma_f32_16x16x32_bf16 v[88:91], v[164:167], a[124:127], v[88:91]\n"
"v_mfma_f32_16x16x32_bf16 v[72:75], a[96:99], v[168:171], v[72:75]\n"
"buffer_load_dwordx4 v244, s[0:3], s88 offen lds\n"
"v_mfma_f32_16x16x32_bf16 v[72:75], a[112:115], v[172:175], v[72:75]\n"
"ds_read_b128 v[132:135], v241 offset:32768\n"
"s_add_u32 m0, s90, 68608\n"
"v_mfma_f32_16x16x32_bf16 v[76:79], a[100:103], v[168:171], v[76:79]\n"
"ds_read_b64_tr_b16 a[150:151], v243 offset:49408\n"
"ds_read_b128 v[136:139], v240 offset:34816\n"
"v_mfma_f32_16x16x32_bf16 v[76:79], a[116:119], v[172:175], v[76:79]\n"
"ds_read_b128 v[140:143], v241 offset:34816\n"
"s_add_u32 s88, s68, 24576\n"
"v_mfma_f32_16x16x32_bf16 v[84:87], v[168:171], a[104:107], v[84:87]\n"
"buffer_load_dwordx4 v245, s[0:3], s88 offen lds\n"
"v_mfma_f32_16x16x32_bf16 v[84:87], v[172:175], a[120:123], v[84:87]\n"
"ds_read_b64_tr_b16 a[152:153], v242 offset:57344\n"
"ds_read_b128 v[144:147], v240 offset:36864\n"
"v_mfma_f32_16x16x32_bf16 v[92:95], v[168:171], a[108:111], v[92:95]\n"
"s_add_u32 m0, s90, 81920\n"
"v_mfma_f32_16x16x32_bf16 v[92:95], v[172:175], a[124:127], v[92:95]\n"
"buffer_load_dwordx4 v246, s[84:87], s92 offen lds\n"
"v_mfma_f32_16x16x32_bf16 v[96:99], a[96:99], v[176:179], v[96:99]\n"
"ds_read_b64_tr_b16 a[154:155], v242 offset:57600\n"
"ds_read_b128 v[148:151], v241 offset:36864\n"
"v_mfma_f32_16x16x32_bf16 v[96:99], a[112:115], v[180:183], v[96:99]\n"
"ds_read_b64_tr_b16 a[156:157], v243 offset:57344\n"
"s_add_u32 m0, s90, 82944\n"
"v_mfma_f32_16x16x32_bf16 v[100:103], a[100:103], v[176:179], v[100:103]\n"
"ds_read_b128 v[152:155], v240 offset:38912\n"
"s_add_u32 s88, s92, s94\n"
"v_mfma_f32_16x16x32_bf16 v[100:103], a[116:119], v[180:183], v[100:103]\n"
"ds_read_b64_tr_b16 a[158:159], v243 offset:57600\n"
"v_mfma_f32_16x16x32_bf16 v[112:115], v[176:179], a[104:107], v[112:115]\n"
"buffer_load_dwordx4 v246, s[84:87], s88 offen lds\n"
"v_mfma_f32_16x16x32_bf16 v[112:115], v[180:183], a[120:123], v[112:115]\n"
"ds_read_b128 v[156:159], v241 offset:38912\n"
"s_add_u32 m0, s90, 83968\n"
"v_mfma_f32_16x16x32_bf16 v[120:123], v[176:179], a[108:111], v[120:123]\n"
"v_mfma_f32_16x16x32_bf16 v[120:123], v[180:183], a[124:127], v[120:123]\n"
"s_add_u32 s89, s92, s93\n"
"v_mfma_f32_16x16x32_bf16 v[104:107], a[96:99], v[184:187], v[104:107]\n"
"buffer_load_dwordx4 v247, s[84:87], s89 offen lds\n"
"v_mfma_f32_16x16x32_bf16 v[104:107], a[112:115], v[188:191], v[104:107]\n"
"v_mfma_f32_16x16x32_bf16 v[108:111], a[100:103], v[184:187], v[108:111]\n"
"s_add_u32 m0, s90, 84992\n"
"v_mfma_f32_16x16x32_bf16 v[108:111], a[116:119], v[188:191], v[108:111]\n"
"s_add_u32 s88, s89, s94\n"
"v_mfma_f32_16x16x32_bf16 v[116:119], v[184:187], a[104:107], v[116:119]\n"
"v_mfma_f32_16x16x32_bf16 v[116:119], v[188:191], a[120:123], v[116:119]\n"
"buffer_load_dwordx4 v247, s[84:87], s88 offen lds\n"
"v_mfma_f32_16x16x32_bf16 v[124:127], v[184:187], a[108:111], v[124:127]\n"
"s_add_u32 s92, s92, s95\n"
"v_mfma_f32_16x16x32_bf16 v[124:127], v[188:191], a[124:127], v[124:127]\n"
"s_waitcnt vmcnt(28) lgkmcnt(0)\n"
"v_mfma_f32_16x16x32_bf16 v[0:3], a[128:131], v[128:131], v[0:3]\n"
"s_barrier\n"
"v_mfma_f32_16x16x32_bf16 v[0:3], a[144:147], v[132:135], v[0:3]\n"
"ds_read_b128 v[160:163], v240 offset:40960\n"
"s_add_u32 m0, s90, 98304\n"
"v_mfma_f32_16x16x32_bf16 v[4:7], a[132:135], v[128:131], v[4:7]\n"
"buffer_load_dwordx4 v246, s[84:87], s92 offen lds\n"
"v_mfma_f32_16x16x32_bf16 v[4:7], a[148:151], v[132:135], v[4:7]\n"
"v_add_u32 v242, 32768, v242\n"
"s_add_u32 m0, s90, 99328\n"
"v_mfma_f32_16x16x32_bf16 v[16:19], v[128:131], a[136:139], v[16:19]\n"
"v_add_u32 v243, 32768, v243\n"
"s_add_u32 s88, s92, s94\n"
"v_mfma_f32_16x16x32_bf16 v[16:19], v[132:135], a[152:155], v[16:19]\n"
"buffer_load_dwordx4 v246, s[84:87], s88 offen lds\n"
"v_mfma_f32_16x16x32_bf16 v[24:27], v[128:131], a[140:143], v[24:27]\n"
"ds_read_b128 v[164:167], v241 offset:40960\n"
"s_add_u32 m0, s90, 100352\n"
"v_mfma_f32_16x16x32_bf16 v[24:27], v[132:135], a[156:159], v[24:27]\n"
"ds_read_b64_tr_b16 a[160:161], v242 offset:49152\n"
"s_add_u32 s89, s92, s93\n"
"v_mfma_f32_16x16x32_bf16 v[8:11], a[128:131], v[136:139], v[8:11]\n"
"ds_read_b128 v[168:171], v240 offset:43008\n"
"ds_read_b64_tr_b16 a[162:163], v242 offset:49408\n"
"v_mfma_f32_16x16x32_bf16 v[8:11], a[144:147], v[140:143], v[8:11]\n"
"buffer_load_dwordx4 v247, s[84:87], s89 offen lds\n"
"v_mfma_f32_16x16x32_bf16 v[12:15], a[132:135], v[136:139], v[12:15]\n"
"ds_read_b128 v[172:175], v241 offset:43008\n"
"s_add_u32 m0, s90, 101376\n"
"v_mfma_f32_16x16x32_bf16 v[12:15], a[148:151], v[140:143], v[12:15]\n"
"ds_read_b64_tr_b16 a[164:165], v243 offset:49152\n"
"s_add_u32 s88, s89, s94\n"
"v_mfma_f32_16x16x32_bf16 v[20:23], v[136:139], a[136:139], v[20:23]\n"
"buffer_load_dwordx4 v247, s[84:87], s88 offen lds\n"
"v_mfma_f32_16x16x32_bf16 v[20:23], v[140:143], a[152:155], v[20:23]\n"
"ds_read_b128 v[176:179], v240 offset:45056\n"
"s_add_u32 s92, s92, s95\n"
"v_mfma_f32_16x16x32_bf16 v[28:31], v[136:139], a[140:143], v[28:31]\n"
"ds_read_b64_tr_b16 a[166:167], v243 offset:49408\n"
"s_and_b32 s92, s92, 0x3fffff\n"
"v_mfma_f32_16x16x32_bf16 v[28:31], v[140:143], a[156:159], v[28:31]\n"
"ds_read_b64_tr_b16 a[168:169], v242 offset:57344\n"
"s_add_u32 s68, s64, s66\n"
"v_mfma_f32_16x16x32_bf16 v[32:35], a[128:131], v[144:147], v[32:35]\n"
"ds_read_b128 v[180:183], v241 offset:45056\n"
"ds_read_b64_tr_b16 a[170:171], v242 offset:57600\n"
"v_mfma_f32_16x16x32_bf16 v[32:35], a[144:147], v[148:151], v[32:35]\n"
"ds_read_b128 v[184:187], v240 offset:47104\n"
"s_add_u32 s66, s66, 128\n"
"v_mfma_f32_16x16x32_bf16 v[36:39], a[132:135], v[144:147], v[36:39]\n"
"ds_read_b64_tr_b16 a[172:173], v243 offset:57344\n"
"s_and_b32 s66, s66, 0x3ff\n"
"v_mfma_f32_16x16x32_bf16 v[36:39], a[148:151], v[148:151], v[36:39]\n"
"ds_read_b128 v[188:191], v241 offset:47104\n"
"s_add_u32 m0, s90, 114688\n"
"v_mfma_f32_16x16x32_bf16 v[48:51], v[144:147], a[136:139], v[48:51]\n"
"ds_read_b64_tr_b16 a[174:175], v243 offset:57600\n"
"s_add_u32 s88, s68, 0\n"
"v_mfma_f32_16x16x32_bf16 v[48:51], v[148:151], a[152:155], v[48:51]\n"
"buffer_load_dwordx4 v244, s[0:3], s88 offen lds\n"
"v_mfma_f32_16x16x32_bf16 v[56:59], v[144:147], a[140:143], v[56:59]\n"
"s_add_u32 m0, s90, 115712\n"
"v_mfma_f32_16x16x32_bf16 v[56:59], v[148:151], a[156:159], v[56:59]\n"
"s_add_u32 s88, s68, 8192\n"
"v_mfma_f32_16x16x32_bf16 v[40:43], a[128:131], v[152:155], v[40:43]\n"
"v_mfma_f32_16x16x32_bf16 v[40:43], a[144:147], v[156:159], v[40:43]\n"
"buffer_load_dwordx4 v245, s[0:3], s88 offen lds\n"
"v_mfma_f32_16x16x32_bf16 v[44:47], a[132:135], v[152:155], v[44:47]\n"
"s_add_u32 m0, s90, 116736\n"
"v_mfma_f32_16x16x32_bf16 v[44:47], a[148:151], v[156:159], v[44:47]\n"
"s_add_u32 s88, s68, 16384\n"
"v_mfma_f32_16x16x32_bf16 v[52:55], v[152:155], a[136:139], v[52:55]\n"
"buffer_load_dwordx4 v244, s[0:3], s88 offen lds\n"
"v_mfma_f32_16x16x32_bf16 v[52:55], v[156:159], a[152:155], v[52:55]\n"
"buffer_load_dword v249, v238, s[20:23], s24 offen\n"
"v_mfma_f32_16x16x32_bf16 v[60:63], v[152:155], a[140:143], v[60:63]\n"
"s_add_u32 s24, s24, s26\n"
"v_mfma_f32_16x16x32_bf16 v[60:63], v[156:159], a[156:159], v[60:63]\n"
"s_waitcnt vmcnt(25) lgkmcnt(0)\n"
"v_mfma_f32_16x16x32_bf16 v[64:67], a[128:131], v[160:163], v[64:67]\n"
"s_barrier\n"
"v_mfma_f32_16x16x32_bf16 v[64:67], a[144:147], v[164:167], v[64:67]\n"
"v_add_u32 v240, 4294885376, v240\n"
"s_add_u32 m0, s90, 117760\n"
"v_mfma_f32_16x16x32_bf16 v[68:71], a[132:135], v[160:163], v[68:71]\n"
"v_add_u32 v242, 4294868992, v242\n"
"s_add_u32 s88, s68, 24576\n"
"v_mfma_f32_16x16x32_bf16 v[68:71], a[148:151], v[164:167], v[68:71]\n"
"buffer_load_dwordx4 v245, s[0:3], s88 offen lds\n"
"v_mfma_f32_16x16x32_bf16 v[80:83], v[160:163], a[136:139], v[80:83]\n"
"v_add_u32 v241, 4294885376, v241\n"
"s_add_u32 s68, s65, s67\n"
"v_mfma_f32_16x16x32_bf16 v[80:83], v[164:167], a[152:155], v[80:83]\n"
"v_add_u32 v243, 4294868992, v243\n"
"s_add_u32 s67, s67, 256\n"
"v_mfma_f32_16x16x32_bf16 v[88:91], v[160:163], a[140:143], v[88:91]\n"
"ds_read_b128 v[128:131], v240 offset:0\n"
"s_and_b32 s67, s67, 0x3ff\n"
"v_mfma_f32_16x16x32_bf16 v[88:91], v[164:167], a[156:159], v[88:91]\n"
"ds_read_b64_tr_b16 a[176:177], v242 offset:0\n"
"s_add_u32 m0, s90, 131072\n"
"v_mfma_f32_16x16x32_bf16 v[72:75], a[128:131], v[168:171], v[72:75]\n"
"ds_read_b128 v[132:135], v241 offset:0\n"
"ds_read_b64_tr_b16 a[178:179], v242 offset:256\n"
"v_mfma_f32_16x16x32_bf16 v[72:75], a[144:147], v[172:175], v[72:75]\n"
"ds_read_b128 v[136:139], v240 offset:2048\n"
"s_add_u32 s88, s68, 0\n"
"v_mfma_f32_16x16x32_bf16 v[76:79], a[132:135], v[168:171], v[76:79]\n"
"buffer_load_dwordx4 v244, s[0:3], s88 offen lds\n"
"v_mfma_f32_16x16x32_bf16 v[76:79], a[148:151], v[172:175], v[76:79]\n"
"ds_read_b64_tr_b16 a[180:181], v243 offset:0\n"
"s_add_u32 m0, s90, 132096\n"
"v_mfma_f32_16x16x32_bf16 v[84:87], v[168:171], a[136:139], v[84:87]\n"
"ds_read_b128 v[140:143], v241 offset:2048\n"
"s_add_u32 s88, s68, 8192\n"
"v_mfma_f32_16x16x32_bf16 v[84:87], v[172:175], a[152:155], v[84:87]\n"
"buffer_load_dwordx4 v245, s[0:3], s88 offen lds\n"
"v_mfma_f32_16x16x32_bf16 v[92:95], v[168:171], a[140:143], v[92:95]\n"
"ds_read_b64_tr_b16 a[182:183], v243 offset:256\n"
"s_add_u32 m0, s90, 133120\n"
"v_mfma_f32_16x16x32_bf16 v[92:95], v[172:175], a[156:159], v[92:95]\n"
"ds_read_b128 v[144:147], v240 offset:4096\n"
"s_add_u32 s88, s68, 16384\n"
"v_mfma_f32_16x16x32_bf16 v[96:99], a[128:131], v[176:179], v[96:99]\n"
"ds_read_b64_tr_b16 a[184:185], v242 offset:8192\n"
"ds_read_b128 v[148:151], v241 offset:4096\n"
"v_mfma_f32_16x16x32_bf16 v[96:99], a[144:147], v[180:183], v[96:99]\n"
"buffer_load_dwordx4 v244, s[0:3], s88 offen lds\n"
"v_mfma_f32_16x16x32_bf16 v[100:103], a[132:135], v[176:179], v[100:103]\n"
"ds_read_b64_tr_b16 a[186:187], v242 offset:8448\n"
"s_add_u32 m0, s90, 134144\n"
"v_mfma_f32_16x16x32_bf16 v[100:103], a[148:151], v[180:183], v[100:103]\n"
"ds_read_b128 v[152:155], v240 offset:6144\n"
"s_add_u32 s88, s68, 24576\n"
"v_mfma_f32_16x16x32_bf16 v[112:115], v[176:179], a[136:139], v[112:115]\n"
"buffer_load_dwordx4 v245, s[0:3], s88 offen lds\n"
"v_mfma_f32_16x16x32_bf16 v[112:115], v[180:183], a[152:155], v[112:115]\n"
"ds_read_b64_tr_b16 a[188:189], v243 offset:8192\n"
"s_add_u32 s68, s65, s67\n"
"v_mfma_f32_16x16x32_bf16 v[120:123], v[176:179], a[140:143], v[120:123]\n"
"ds_read_b128 v[156:159], v241 offset:6144\n"
"s_add_u32 s67, s67, 256\n"
"v_mfma_f32_16x16x32_bf16 v[120:123], v[180:183], a[156:159], v[120:123]\n"
"ds_read_b64_tr_b16 a[190:191], v243 offset:8448\n"
"s_and_b32 s67, s67, 0x3ff\n"
"v_mfma_f32_16x16x32_bf16 v[104:107], a[128:131], v[184:187], v[104:107]\n"
"v_lshlrev_b32 v222, 16, v208\n"
"v_lshlrev_b32 v223, 16, v216\n"
"v_mfma_f32_16x16x32_bf16 v[104:107], a[144:147], v[188:191], v[104:107]\n"
"s_add_u32 m0, s90, 147456\n"
"v_mul_f32 v220, v222, v223\n"
"v_mfma_f32_16x16x32_bf16 v[108:111], a[132:135], v[184:187], v[108:111]\n"
"s_add_u32 s88, s68, 0\n"
"v_and_b32 v222, 0xffff0000, v208\n"
"v_mfma_f32_16x16x32_bf16 v[108:111], a[148:151], v[188:191], v[108:111]\n"
"buffer_load_dwordx4 v244, s[0:3], s88 offen lds\n"
"v_mfma_f32_16x16x32_bf16 v[116:119], v[184:187], a[136:139], v[116:119]\n"
"s_add_u32 m0, s90, 148480\n"
"v_and_b32 v223, 0xffff0000, v216\n"
"v_mfma_f32_16x16x32_bf16 v[116:119], v[188:191], a[152:155], v[116:119]\n"
"s_add_u32 s88, s68, 8192\n"
"v_fmac_f32 v220, v222, v223\n"
"v_mfma_f32_16x16x32_bf16 v[124:127], v[184:187], a[140:143], v[124:127]\n"
"buffer_load_dwordx4 v245, s[0:3], s88 offen lds\n"
"v_mfma_f32_16x16x32_bf16 v[124:127], v[188:191], a[156:159], v[124:127]\n"
"v_lshlrev_b32 v222, 16, v209\n"
"v_lshlrev_b32 v223, 16, v217\n"
"s_waitcnt vmcnt(28) lgkmcnt(0)\n"
"v_mfma_f32_16x16x32_bf16 v[0:3], a[160:163], v[128:131], v[0:3]\n"
"s_barrier\n"
"v_mfma_f32_16x16x32_bf16 v[0:3], a[176:179], v[132:135], v[0:3]\n"
"ds_read_b128 v[160:163], v240 offset:8192\n"
"s_add_u32 m0, s90, 149504\n"
"v_mfma_f32_16x16x32_bf16 v[4:7], a[164:167], v[128:131], v[4:7]\n"
"ds_read_b64_tr_b16 a[192:193], v242 offset:32768\n"
"s_add_u32 s88, s68, 16384\n"
"v_mfma_f32_16x16x32_bf16 v[4:7], a[180:183], v[132:135], v[4:7]\n"
"buffer_load_dwordx4 v244, s[0:3], s88 offen lds\n"
"v_mfma_f32_16x16x32_bf16 v[16:19], v[128:131], a[168:171], v[16:19]\n"
"ds_read_b128 v[164:167], v241 offset:8192\n"
"ds_read_b64_tr_b16 a[194:195], v242 offset:33024\n"
"v_mfma_f32_16x16x32_bf16 v[16:19], v[132:135], a[184:187], v[16:19]\n"
"s_add_u32 m0, s90, 150528\n"
"v_fmac_f32 v220, v222, v223\n"
"v_mfma_f32_16x16x32_bf16 v[24:27], v[128:131], a[172:175], v[24:27]\n"
"ds_read_b128 v[168:171], v240 offset:10240\n"
"s_add_u32 s88, s68, 24576\n"
"v_mfma_f32_16x16x32_bf16 v[24:27], v[132:135], a[188:191], v[24:27]\n"
"buffer_load_dwordx4 v245, s[0:3], s88 offen lds\n"
"v_mfma_f32_16x16x32_bf16 v[8:11], a[160:163], v[136:139], v[8:11]\n"
"ds_read_b64_tr_b16 a[196:197], v243 offset:32768\n"
"ds_read_b128 v[172:175], v241 offset:10240\n"
"v_mfma_f32_16x16x32_bf16 v[8:11], a[176:179], v[140:143], v[8:11]\n"
"s_add_u32 s68, s65, s67\n"
"v_and_b32 v222, 0xffff0000, v209\n"
"v_mfma_f32_16x16x32_bf16 v[12:15], a[164:167], v[136:139], v[12:15]\n"
"Lfused_hop1:\n"
"s_cbranch_execz Lfused_hop5\n"
"ds_read_b64_tr_b16 a[198:199], v243 offset:33024\n"
"s_add_u32 s67, s67, 256\n"
"v_mfma_f32_16x16x32_bf16 v[12:15], a[180:183], v[140:143], v[12:15]\n"
"ds_read_b128 v[176:179], v240 offset:12288\n"
"s_and_b32 s67, s67, 0x3ff\n"
"v_mfma_f32_16x16x32_bf16 v[20:23], v[136:139], a[168:171], v[20:23]\n"
"ds_read_b64_tr_b16 a[200:201], v242 offset:40960\n"
"v_and_b32 v223, 0xffff0000, v217\n"
"v_mfma_f32_16x16x32_bf16 v[20:23], v[140:143], a[184:187], v[20:23]\n"
"s_add_u32 m0, s90, 0\n"
"v_fmac_f32 v220, v222, v223\n"
"v_mfma_f32_16x16x32_bf16 v[28:31], v[136:139], a[172:175], v[28:31]\n"
"ds_read_b128 v[180:183], v241 offset:12288\n"
"s_add_u32 s88, s68, 0\n"
"v_mfma_f32_16x16x32_bf16 v[28:31], v[140:143], a[188:191], v[28:31]\n"
"buffer_load_dwordx4 v244, s[0:3], s88 offen lds\n"
"v_mfma_f32_16x16x32_bf16 v[32:35], a[160:163], v[144:147], v[32:35]\n"
"ds_read_b64_tr_b16 a[202:203], v242 offset:41216\n"
"ds_read_b128 v[184:187], v240 offset:14336\n"
"v_mfma_f32_16x16x32_bf16 v[32:35], a[176:179], v[148:151], v[32:35]\n"
"s_add_u32 m0, s90, 1024\n"
"v_lshlrev_b32 v222, 16, v210\n"
"v_mfma_f32_16x16x32_bf16 v[36:39], a[164:167], v[144:147], v[36:39]\n"
"ds_read_b64_tr_b16 a[204:205], v243 offset:40960\n"
"s_add_u32 s88, s68, 8192\n"
"v_mfma_f32_16x16x32_bf16 v[36:39], a[180:183], v[148:151], v[36:39]\n"
"buffer_load_dwordx4 v245, s[0:3], s88 offen lds\n"
"v_mfma_f32_16x16x32_bf16 v[48:51], v[144:147], a[168:171], v[48:51]\n"
"ds_read_b128 v[188:191], v241 offset:14336\n"
"ds_read_b64_tr_b16 a[206:207], v243 offset:41216\n"
"v_mfma_f32_16x16x32_bf16 v[48:51], v[148:151], a[184:187], v[48:51]\n"
"s_add_u32 m0, s90, 2048\n"
"v_lshlrev_b32 v223, 16, v218\n"
"v_mfma_f32_16x16x32_bf16 v[56:59], v[144:147], a[172:175], v[56:59]\n"
"s_add_u32 s88, s68, 16384\n"
"v_fmac_f32 v220, v222, v223\n"
"v_mfma_f32_16x16x32_bf16 v[56:59], v[148:151], a[188:191], v[56:59]\n"
"buffer_load_dwordx4 v244, s[0:3], s88 offen lds\n"
"v_mfma_f32_16x16x32_bf16 v[40:43], a[160:163], v[152:155], v[40:43]\n"
"v_and_b32 v222, 0xffff0000, v210\n"
"v_and_b32 v223, 0xffff0000, v218\n"
"v_mfma_f32_16x16x32_bf16 v[40:43], a[176:179], v[156:159], v[40:43]\n"
"s_add_u32 m0, s90, 3072\n"
"v_fmac_f32 v220, v222, v223\n"
"v_mfma_f32_16x16x32_bf16 v[44:47], a[164:167], v[152:155], v[44:47]\n"
"s_add_u32 s88, s68, 24576\n"
"v_lshlrev_b32 v222, 16, v211\n"
"v_mfma_f32_16x16x32_bf16 v[44:47], a[180:183], v[156:159], v[44:47]\n"
"buffer_load_dwordx4 v245, s[0:3], s88 offen lds\n"
"v_mfma_f32_16x16x32_bf16 v[52:55], v[152:155], a[168:171], v[52:55]\n"
"v_lshlrev_b32 v223, 16, v219\n"
"v_fmac_f32 v220, v222, v223\n"
"v_mfma_f32_16x16x32_bf16 v[52:55], v[156:159], a[184:187], v[52:55]\n"
"buffer_load_dword v249, v238, s[20:23], s24 offen\n"
"v_mfma_f32_16x16x32_bf16 v[60:63], v[152:155], a[172:175], v[60:63]\n"
"s_add_u32 s24, s24, s26\n"
"v_and_b32 v222, 0xffff0000, v211\n"
"v_mfma_f32_16x16x32_bf16 v[60:63], v[156:159], a[188:191], v[60:63]\n"
"v_and_b32 v223, 0xffff0000, v219\n"
"v_fmac_f32 v220, v222, v223\n"
"s_waitcnt vmcnt(26) lgkmcnt(0)\n"
"v_mfma_f32_16x16x32_bf16 v[64:67], a[160:163], v[160:163], v[64:67]\n"
"s_barrier\n"
"v_mfma_f32_16x16x32_bf16 v[64:67], a[176:179], v[164:167], v[64:67]\n"
"ds_read_b128 v[128:131], v240 offset:49152\n"
"s_add_u32 s68, s65, s67\n"
"v_mfma_f32_16x16x32_bf16 v[68:71], a[164:167], v[160:163], v[68:71]\n"
"ds_read_b64_tr_b16 a[208:209], v242 offset:49152\n"
"s_add_u32 s67, s67, 256\n"
"v_mfma_f32_16x16x32_bf16 v[68:71], a[180:183], v[164:167], v[68:71]\n"
"ds_read_b128 v[132:135], v241 offset:49152\n"
"s_and_b32 s67, s67, 0x3ff\n"
"v_mfma_f32_16x16x32_bf16 v[80:83], v[160:163], a[168:171], v[80:83]\n"
"ds_read_b64_tr_b16 a[210:211], v242 offset:49408\n"
"s_add_u32 m0, s90, 16384\n"
"v_mfma_f32_16x16x32_bf16 v[80:83], v[164:167], a[184:187], v[80:83]\n"
"s_add_u32 s88, s68, 0\n"
"v_lshlrev_b32 v222, 16, v212\n"
"v_mfma_f32_16x16x32_bf16 v[88:91], v[160:163], a[172:175], v[88:91]\n"
"buffer_load_dwordx4 v244, s[0:3], s88 offen lds\n"
"v_mfma_f32_16x16x32_bf16 v[88:91], v[164:167], a[188:191], v[88:91]\n"
"ds_read_b128 v[136:139], v240 offset:51200\n"
"s_add_u32 m0, s90, 17408\n"
"v_mfma_f32_16x16x32_bf16 v[72:75], a[160:163], v[168:171], v[72:75]\n"
"ds_read_b64_tr_b16 a[212:213], v243 offset:49152\n"
"ds_read_b128 v[140:143], v241 offset:51200\n"
"v_mfma_f32_16x16x32_bf16 v[72:75], a[176:179], v[172:175], v[72:75]\n"
"s_add_u32 s88, s68, 8192\n"
"v_lshlrev_b32 v223, 16, v216\n"
"v_mfma_f32_16x16x32_bf16 v[76:79], a[164:167], v[168:171], v[76:79]\n"
"buffer_load_dwordx4 v245, s[0:3], s88 offen lds\n"
"v_mfma_f32_16x16x32_bf16 v[76:79], a[180:183], v[172:175], v[76:79]\n"
"ds_read_b64_tr_b16 a[214:215], v243 offset:49408\n"
"s_add_u32 m0, s90, 18432\n"
"v_mfma_f32_16x16x32_bf16 v[84:87], v[168:171], a[168:171], v[84:87]\n"
"ds_read_b128 v[144:147], v240 offset:53248\n"
"s_add_u32 s88, s68, 16384\n"
"v_mfma_f32_16x16x32_bf16 v[84:87], v[172:175], a[184:187], v[84:87]\n"
"buffer_load_dwordx4 v244, s[0:3], s88 offen lds\n"
"v_mfma_f32_16x16x32_bf16 v[92:95], v[168:171], a[172:175], v[92:95]\n"
"ds_read_b64_tr_b16 a[216:217], v242 offset:57344\n"
"s_add_u32 m0, s90, 19456\n"
"v_mfma_f32_16x16x32_bf16 v[92:95], v[172:175], a[188:191], v[92:95]\n"
"ds_read_b128 v[148:151], v241 offset:53248\n"
"s_add_u32 s88, s68, 24576\n"
"v_mfma_f32_16x16x32_bf16 v[96:99], a[160:163], v[176:179], v[96:99]\n"
"ds_read_b64_tr_b16 a[218:219], v242 offset:57600\n"
"ds_read_b128 v[152:155], v240 offset:55296\n"
"v_mfma_f32_16x16x32_bf16 v[96:99], a[176:179], v[180:183], v[96:99]\n"
"buffer_load_dwordx4 v245, s[0:3], s88 offen lds\n"
"v_mfma_f32_16x16x32_bf16 v[100:103], a[164:167], v[176:179], v[100:103]\n"
"ds_read_b64_tr_b16 a[220:221], v243 offset:57344\n"
"s_add_u32 s68, s76, s67\n"
"v_mfma_f32_16x16x32_bf16 v[100:103], a[180:183], v[180:183], v[100:103]\n"
"ds_read_b128 v[156:159], v241 offset:55296\n"
"s_add_u32 s67, s67, 256\n"
"v_mfma_f32_16x16x32_bf16 v[112:115], v[176:179], a[168:171], v[112:115]\n"
"ds_read_b64_tr_b16 a[222:223], v243 offset:57600\n"
"s_and_b32 s67, s67, 0x3ff\n"
"v_mfma_f32_16x16x32_bf16 v[112:115], v[180:183], a[184:187], v[112:115]\n"
"s_add_u32 m0, s90, 32768\n"
"v_mul_f32 v221, v222, v223\n"
"v_mfma_f32_16x16x32_bf16 v[120:123], v[176:179], a[172:175], v[120:123]\n"
"s_add_u32 s88, s68, 0\n"
"v_and_b32 v222, 0xffff0000, v212\n"
"v_mfma_f32_16x16x32_bf16 v[120:123], v[180:183], a[188:191], v[120:123]\n"
"buffer_load_dwordx4 v244, s[0:3], s88 offen lds\n"
"v_mfma_f32_16x16x32_bf16 v[104:107], a[160:163], v[184:187], v[104:107]\n"
"v_and_b32 v223, 0xffff0000, v216\n"
"v_fmac_f32 v221, v222, v223\n"
"v_mfma_f32_16x16x32_bf16 v[104:107], a[176:179], v[188:191], v[104:107]\n"
"s_add_u32 m0, s90, 33792\n"
"v_lshlrev_b32 v222, 16, v213\n"
"v_mfma_f32_16x16x32_bf16 v[108:111], a[164:167], v[184:187], v[108:111]\n"
"s_add_u32 s88, s68, 8192\n"
"v_lshlrev_b32 v223, 16, v217\n"
"v_mfma_f32_16x16x32_bf16 v[108:111], a[180:183], v[188:191], v[108:111]\n"
"buffer_load_dwordx4 v245, s[0:3], s88 offen lds\n"
"v_mfma_f32_16x16x32_bf16 v[116:119], v[184:187], a[168:171], v[116:119]\n"
"s_add_u32 m0, s90, 34816\n"
"v_fmac_f32 v221, v222, v223\n"
"v_mfma_f32_16x16x32_bf16 v[116:119], v[188:191], a[184:187], v[116:119]\n"
"s_add_u32 s88, s68, 16384\n"
"v_and_b32 v222, 0xffff0000, v213\n"
"v_mfma_f32_16x16x32_bf16 v[124:127], v[184:187], a[172:175], v[124:127]\n"
"buffer_load_dwordx4 v244, s[0:3], s88 offen lds\n"
"v_mfma_f32_16x16x32_bf16 v[124:127], v[188:191], a[188:191], v[124:127]\n"
"v_and_b32 v223, 0xffff0000, v217\n"
"v_fmac_f32 v221, v222, v223\n"
"s_waitcnt vmcnt(29) lgkmcnt(0)\n"
"v_mfma_f32_16x16x32_bf16 v[0:3], a[192:195], v[128:131], v[0:3]\n"
"s_barrier\n"
"v_mfma_f32_16x16x32_bf16 v[0:3], a[208:211], v[132:135], v[0:3]\n"
"ds_read_b128 v[160:163], v240 offset:57344\n"
"s_add_u32 m0, s90, 35840\n"
"v_mfma_f32_16x16x32_bf16 v[4:7], a[196:199], v[128:131], v[4:7]\n"
"v_add_u32 v242, 81920, v242\n"
"s_add_u32 s88, s68, 24576\n"
"v_mfma_f32_16x16x32_bf16 v[4:7], a[212:215], v[132:135], v[4:7]\n"
"v_add_u32 v243, 81920, v243\n"
"v_lshlrev_b32 v222, 16, v214\n"
"v_mfma_f32_16x16x32_bf16 v[16:19], v[128:131], a[200:203], v[16:19]\n"
"buffer_load_dwordx4 v245, s[0:3], s88 offen lds\n"
"v_mfma_f32_16x16x32_bf16 v[16:19], v[132:135], a[216:219], v[16:19]\n"
"ds_read_b128 v[164:167], v241 offset:57344\n"
"ds_read_b64_tr_b16 a[224:225], v242 offset:0\n"
"v_mfma_f32_16x16x32_bf16 v[24:27], v[128:131], a[204:207], v[24:27]\n"
"ds_read_b128 v[168:171], v240 offset:59392\n"
"s_add_u32 s68, s76, s67\n"
"v_mfma_f32_16x16x32_bf16 v[24:27], v[132:135], a[220:223], v[24:27]\n"
"s_add_u32 s67, s67, 256\n"
"v_lshlrev_b32 v223, 16, v218\n"
"v_mfma_f32_16x16x32_bf16 v[8:11], a[192:195], v[136:139], v[8:11]\n"
"ds_read_b64_tr_b16 a[226:227], v242 offset:256\n"
"v_fmac_f32 v221, v222, v223\n"
"v_mfma_f32_16x16x32_bf16 v[8:11], a[208:211], v[140:143], v[8:11]\n"
"ds_read_b128 v[172:175], v241 offset:59392\n"
"s_and_b32 s67, s67, 0x3ff\n"
"v_mfma_f32_16x16x32_bf16 v[12:15], a[196:199], v[136:139], v[12:15]\n"
"ds_read_b64_tr_b16 a[228:229], v243 offset:0\n"
"v_and_b32 v222, 0xffff0000, v214\n"
"v_mfma_f32_16x16x32_bf16 v[12:15], a[212:215], v[140:143], v[12:15]\n"
"ds_read_b128 v[176:179], v240 offset:61440\n"
"s_add_u32 m0, s90, 49152\n"
"v_mfma_f32_16x16x32_bf16 v[20:23], v[136:139], a[200:203], v[20:23]\n"
"ds_read_b64_tr_b16 a[230:231], v243 offset:256\n"
"v_and_b32 v223, 0xffff0000, v218\n"
"v_mfma_f32_16x16x32_bf16 v[20:23], v[140:143], a[216:219], v[20:23]\n"
"ds_read_b64_tr_b16 a[232:233], v242 offset:8192\n"
"s_add_u32 s88, s68, 0\n"
"v_mfma_f32_16x16x32_bf16 v[28:31], v[136:139], a[204:207], v[28:31]\n"
"buffer_load_dwordx4 v244, s[0:3], s88 offen lds\n"
"v_mfma_f32_16x16x32_bf16 v[28:31], v[140:143], a[220:223], v[28:31]\n"
"ds_read_b128 v[180:183], v241 offset:61440\n"
"v_fmac_f32 v221, v222, v223\n"
"v_mfma_f32_16x16x32_bf16 v[32:35], a[192:195], v[144:147], v[32:35]\n"
"ds_read_b64_tr_b16 a[234:235], v242 offset:8448\n"
"s_add_u32 m0, s90, 50176\n"
"v_mfma_f32_16x16x32_bf16 v[32:35], a[208:211], v[148:151], v[32:35]\n"
"ds_read_b128 v[184:187], v240 offset:63488\n"
"v_lshlrev_b32 v222, 16, v215\n"
"v_mfma_f32_16x16x32_bf16 v[36:39], a[196:199], v[144:147], v[36:39]\n"
"ds_read_b64_tr_b16 a[236:237], v243 offset:8192\n"
"s_add_u32 s88, s68, 8192\n"
"v_mfma_f32_16x16x32_bf16 v[36:39], a[212:215], v[148:151], v[36:39]\n"
"buffer_load_dwordx4 v245, s[0:3], s88 offen lds\n"
"v_mfma_f32_16x16x32_bf16 v[48:51], v[144:147], a[200:203], v[48:51]\n"
"ds_read_b128 v[188:191], v241 offset:63488\n"
"ds_read_b64_tr_b16 a[238:239], v243 offset:8448\n"
"v_mfma_f32_16x16x32_bf16 v[48:51], v[148:151], a[216:219], v[48:51]\n"
"s_add_u32 m0, s90, 51200\n"
"v_lshlrev_b32 v223, 16, v219\n"
"v_mfma_f32_16x16x32_bf16 v[56:59], v[144:147], a[204:207], v[56:59]\n"
"v_fmac_f32 v221, v222, v223\n"
"v_and_b32 v222, 0xffff0000, v215\n"
"v_mfma_f32_16x16x32_bf16 v[56:59], v[148:151], a[220:223], v[56:59]\n"
"s_add_u32 s88, s68, 16384\n"
"v_and_b32 v223, 0xffff0000, v219\n"
"v_mfma_f32_16x16x32_bf16 v[40:43], a[192:195], v[152:155], v[40:43]\n"
"v_fmac_f32 v221, v222, v223\n"
"v_add_f32_dpp v220, v220, v220 row_ror:8 row_mask:0xf bank_mask:0xf\n"
"v_mfma_f32_16x16x32_bf16 v[40:43], a[208:211], v[156:159], v[40:43]\n"
"buffer_load_dwordx4 v244, s[0:3], s88 offen lds\n"
"v_mfma_f32_16x16x32_bf16 v[44:47], a[196:199], v[152:155], v[44:47]\n"
"s_add_u32 m0, s90, 52224\n"
"v_add_f32_dpp v221, v221, v221 row_ror:8 row_mask:0xf bank_mask:0xf\n"
"v_mfma_f32_16x16x32_bf16 v[44:47], a[212:215], v[156:159], v[44:47]\n"
"v_add_f32_dpp v220, v220, v220 row_ror:4 row_mask:0xf bank_mask:0xf\n"
"v_add_f32_dpp v221, v221, v221 row_ror:4 row_mask:0xf bank_mask:0xf\n"
"v_mfma_f32_16x16x32_bf16 v[52:55], v[152:155], a[200:203], v[52:55]\n"
"s_add_u32 s88, s68, 24576\n"
"v_add_f32_dpp v220, v220, v220 row_ror:2 row_mask:0xf bank_mask:0xf\n"
"v_mfma_f32_16x16x32_bf16 v[52:55], v[156:159], a[216:219], v[52:55]\n"
"v_add_f32_dpp v221, v221, v221 row_ror:2 row_mask:0xf bank_mask:0xf\n"
"v_add_f32_dpp v220, v220, v220 row_ror:1 row_mask:0xf bank_mask:0xf\n"
"v_mfma_f32_16x16x32_bf16 v[60:63], v[152:155], a[204:207], v[60:63]\n"
"buffer_load_dwordx4 v245, s[0:3], s88 offen lds\n"
"v_mfma_f32_16x16x32_bf16 v[60:63], v[156:159], a[220:223], v[60:63]\n"
"v_add_f32_dpp v221, v221, v221 row_ror:1 row_mask:0xf bank_mask:0xf\n"
"v_mov_b32 v222, v220\n"
"s_waitcnt vmcnt(25) lgkmcnt(0)\n"
"v_mfma_f32_16x16x32_bf16 v[64:67], a[192:195], v[160:163], v[64:67]\n"
"s_barrier\n"
"v_mfma_f32_16x16x32_bf16 v[64:67], a[208:211], v[164:167], v[64:67]\n"
"s_add_u32 s68, s76, s67\n"
"s_add_u32 s67, s67, 256\n"
"v_mfma_f32_16x16x32_bf16 v[68:71], a[196:199], v[160:163], v[68:71]\n"
"v_add_u32 v240, 81920, v240\n"
"s_and_b32 s67, s67, 0x3ff\n"
"v_mfma_f32_16x16x32_bf16 v[68:71], a[212:215], v[164:167], v[68:71]\n"
"ds_read_b64_tr_b16 a[240:241], v242 offset:16384\n"
"s_add_u32 m0, s90, 65536\n"
"v_mfma_f32_16x16x32_bf16 v[80:83], v[160:163], a[200:203], v[80:83]\n"
"v_add_u32 v241, 81920, v241\n"
"s_add_u32 s88, s68, 0\n"
"v_mfma_f32_16x16x32_bf16 v[80:83], v[164:167], a[216:219], v[80:83]\n"
"buffer_load_dwordx4 v244, s[0:3], s88 offen lds\n"
"v_mfma_f32_16x16x32_bf16 v[88:91], v[160:163], a[204:207], v[88:91]\n"
"ds_read_b64_tr_b16 a[242:243], v242 offset:16640\n"
"s_add_u32 m0, s90, 66560\n"
"v_mfma_f32_16x16x32_bf16 v[88:91], v[164:167], a[220:223], v[88:91]\n"
"ds_read_b128 v[128:131], v240 offset:16384\n"
"s_add_u32 s88, s68, 8192\n"
"v_mfma_f32_16x16x32_bf16 v[72:75], a[192:195], v[168:171], v[72:75]\n"
"buffer_load_dwordx4 v245, s[0:3], s88 offen lds\n"
"v_mfma_f32_16x16x32_bf16 v[72:75], a[208:211], v[172:175], v[72:75]\n"
"s_add_u32 m0, s90, 67584\n"
"s_add_u32 s88, s68, 16384\n"
"v_mfma_f32_16x16x32_bf16 v[76:79], a[196:199], v[168:171], v[76:79]\n"
"buffer_load_dwordx4 v244, s[0:3], s88 offen lds\n"
"v_mfma_f32_16x16x32_bf16 v[76:79], a[212:215], v[172:175], v[76:79]\n"
"ds_read_b64_tr_b16 a[244:245], v243 offset:16384\n"
"s_add_u32 m0, s90, 68608\n"
"v_mfma_f32_16x16x32_bf16 v[84:87], v[168:171], a[200:203], v[84:87]\n"
"ds_read_b128 v[132:135], v241 offset:16384\n"
"s_add_u32 s88, s68, 24576\n"
"v_mfma_f32_16x16x32_bf16 v[84:87], v[172:175], a[216:219], v[84:87]\n"
"buffer_load_dwordx4 v245, s[0:3], s88 offen lds\n"
"v_mfma_f32_16x16x32_bf16 v[92:95], v[168:171], a[204:207], v[92:95]\n"
"s_add_u32 s68, s76, s67\n"
"s_add_u32 s67, s67, 256\n"
"v_mfma_f32_16x16x32_bf16 v[92:95], v[172:175], a[220:223], v[92:95]\n"
"ds_read_b64_tr_b16 a[246:247], v243 offset:16640\n"
"s_and_b32 s67, s67, 0x3ff\n"
"v_mfma_f32_16x16x32_bf16 v[96:99], a[192:195], v[176:179], v[96:99]\n"
"ds_read_b128 v[136:139], v240 offset:18432\n"
"s_add_u32 m0, s90, 81920\n"
"v_mfma_f32_16x16x32_bf16 v[96:99], a[208:211], v[180:183], v[96:99]\n"
"ds_read_b128 v[140:143], v241 offset:18432\n"
"s_add_u32 s88, s68, 0\n"
"v_mfma_f32_16x16x32_bf16 v[100:103], a[196:199], v[176:179], v[100:103]\n"
"buffer_load_dwordx4 v244, s[0:3], s88 offen lds\n"
"v_mfma_f32_16x16x32_bf16 v[100:103], a[212:215], v[180:183], v[100:103]\n"
"s_add_u32 m0, s90, 82944\n"
"s_add_u32 s88, s68, 8192\n"
"v_mfma_f32_16x16x32_bf16 v[112:115], v[176:179], a[200:203], v[112:115]\n"
"buffer_load_dwordx4 v245, s[0:3], s88 offen lds\n"
"v_mfma_f32_16x16x32_bf16 v[112:115], v[180:183], a[216:219], v[112:115]\n"
"ds_read_b64_tr_b16 a[248:249], v242 offset:24576\n"
"s_add_u32 m0, s90, 83968\n"
"v_mfma_f32_16x16x32_bf16 v[120:123], v[176:179], a[204:207], v[120:123]\n"
"ds_read_b128 v[144:147], v240 offset:20480\n"
"s_add_u32 s88, s68, 16384\n"
"v_mfma_f32_16x16x32_bf16 v[120:123], v[180:183], a[220:223], v[120:123]\n"
"buffer_load_dwordx4 v244, s[0:3], s88 offen lds\n"
"v_mfma_f32_16x16x32_bf16 v[104:107], a[192:195], v[184:187], v[104:107]\n"
"ds_read_b64_tr_b16 a[250:251], v242 offset:24832\n"
"s_lshr_b32 s58, s13, 3\n"
"v_mfma_f32_16x16x32_bf16 v[104:107], a[208:211], v[188:191], v[104:107]\n"
"s_lshl_b32 s58, s58, 2\n"
"s_add_u32 s58, s58, s12\n"
"v_mfma_f32_16x16x32_bf16 v[108:111], a[196:199], v[184:187], v[108:111]\n"
"ds_read_b128 v[148:151], v241 offset:20480\n"
"v_bfe_u32 v253, v248, 4, 1\n"
"v_mfma_f32_16x16x32_bf16 v[108:111], a[212:215], v[188:191], v[108:111]\n"
"ds_read_b64_tr_b16 a[252:253], v243 offset:24576\n"
"v_lshl_add_u32 v253, v253, 7, s58\n"
"v_mfma_f32_16x16x32_bf16 v[116:119], v[184:187], a[200:203], v[116:119]\n"
"ds_read_b128 v[152:155], v240 offset:22528\n"
"v_lshlrev_b32 v253, 7, v253\n"
"v_mfma_f32_16x16x32_bf16 v[116:119], v[188:191], a[216:219], v[116:119]\n"
"s_getpc_b64 s[58:59]\n"
"Lfused_ct13:\n"
"v_mfma_f32_16x16x32_bf16 v[124:127], v[184:187], a[204:207], v[124:127]\n"
"ds_read_b64_tr_b16 a[254:255], v243 offset:24832\n"
"v_min_u32 v253, Lfused_fin-Lfused_ct13, v253\n"
"v_mfma_f32_16x16x32_bf16 v[124:127], v[188:191], a[220:223], v[124:127]\n"
"ds_read_b128 v[156:159], v241 offset:22528\n"
"global_load_dword v249, v253, s[58:59]\n"
"s_waitcnt vmcnt(33) lgkmcnt(0)\n"
"v_mfma_f32_16x16x32_bf16 v[0:3], a[224:227], v[128:131], v[0:3]\n"
"s_barrier\n"
"v_mfma_f32_16x16x32_bf16 v[0:3], a[240:243], v[132:135], v[0:3]\n"
"ds_read_b128 v[160:163], v240 offset:24576\n"
"s_add_u32 m0, s90, 84992\n"
"v_mfma_f32_16x16x32_bf16 v[4:7], a[228:231], v[128:131], v[4:7]\n"
"s_add_u32 s88, s68, 24576\n"
"v_mov_b32 v223, v221\n"
"v_mfma_f32_16x16x32_bf16 v[4:7], a[244:247], v[132:135], v[4:7]\n"
"buffer_load_dwordx4 v245, s[0:3], s88 offen lds\n"
"v_mfma_f32_16x16x32_bf16 v[16:19], v[128:131], a[232:235], v[16:19]\n"
"ds_read_b128 v[164:167], v241 offset:24576\n"
"v_permlane16_swap_b32 v220, v222\n"
"v_mfma_f32_16x16x32_bf16 v[16:19], v[132:135], a[248:251], v[16:19]\n"
"s_mov_b64 s[84:85], s[44:45]\n"
"v_permlane16_swap_b32 v221, v223\n"
"v_mfma_f32_16x16x32_bf16 v[24:27], v[128:131], a[236:239], v[24:27]\n"
"ds_read_b128 v[168:171], v240 offset:26624\n"
"s_mov_b32 s86, s38\n"
"v_mfma_f32_16x16x32_bf16 v[24:27], v[132:135], a[252:255], v[24:27]\n"
"v_add_f32 v220, v220, v222\n"
"v_add_f32 v221, v221, v223\n"
"v_mfma_f32_16x16x32_bf16 v[8:11], a[224:227], v[136:139], v[8:11]\n"
"ds_read_b128 v[172:175], v241 offset:26624\n"
"s_mov_b64 s[92:93], s[40:41]\n"
"v_mfma_f32_16x16x32_bf16 v[8:11], a[240:243], v[140:143], v[8:11]\n"
"s_mov_b64 s[94:95], s[42:43]\n"
"v_mov_b32 v222, v220\n"
"v_mfma_f32_16x16x32_bf16 v[12:15], a[228:231], v[136:139], v[12:15]\n"
"v_mov_b32 v246, v236\n"
"v_mov_b32 v223, v221\n"
"v_mfma_f32_16x16x32_bf16 v[12:15], a[244:247], v[140:143], v[12:15]\n"
"ds_read_b128 v[176:179], v240 offset:28672\n"
"v_permlane32_swap_b32 v220, v222\n"
"v_mfma_f32_16x16x32_bf16 v[20:23], v[136:139], a[232:235], v[20:23]\n"
"v_mov_b32 v247, v237\n"
"v_permlane32_swap_b32 v221, v223\n"
"v_mfma_f32_16x16x32_bf16 v[20:23], v[140:143], a[248:251], v[20:23]\n"
"s_mov_b64 s[0:1], s[80:81]\n"
"v_add_f32 v220, v220, v222\n"
"v_mfma_f32_16x16x32_bf16 v[28:31], v[136:139], a[236:239], v[28:31]\n"
"ds_read_b128 v[180:183], v241 offset:28672\n"
"v_add_f32 v221, v221, v223\n"
"v_mfma_f32_16x16x32_bf16 v[28:31], v[140:143], a[252:255], v[28:31]\n"
"s_add_u32 m0, s90, 98304\n"
"v_mul_f32 v220, 0xbfb8aa3b, v220\n"
"v_mfma_f32_16x16x32_bf16 v[32:35], a[224:227], v[144:147], v[32:35]\n"
"ds_read_b128 v[184:187], v240 offset:30720\n"
"s_add_u32 s88, s91, 0\n"
"v_mfma_f32_16x16x32_bf16 v[32:35], a[240:243], v[148:151], v[32:35]\n"
"buffer_load_dwordx4 v244, s[0:3], s88 offen lds\n"
"v_mfma_f32_16x16x32_bf16 v[36:39], a[228:231], v[144:147], v[36:39]\n"
"v_mul_f32 v221, 0xbfb8aa3b, v221\n"
"v_mfma_f32_16x16x32_bf16 v[36:39], a[244:247], v[148:151], v[36:39]\n"
"ds_read_b128 v[188:191], v241 offset:30720\n"
"s_add_u32 m0, s90, 99328\n"
"v_mfma_f32_16x16x32_bf16 v[48:51], v[144:147], a[232:235], v[48:51]\n"
"s_add_u32 s88, s91, 8192\n"
"v_mfma_f32_16x16x32_bf16 v[48:51], v[148:151], a[248:251], v[48:51]\n"
"v_exp_f32 v220, v220\n"
"v_mfma_f32_16x16x32_bf16 v[56:59], v[144:147], a[236:239], v[56:59]\n"
"buffer_load_dwordx4 v245, s[0:3], s88 offen lds\n"
"v_mfma_f32_16x16x32_bf16 v[56:59], v[148:151], a[252:255], v[56:59]\n"
"s_add_u32 m0, s90, 100352\n"
"v_mfma_f32_16x16x32_bf16 v[40:43], a[224:227], v[152:155], v[40:43]\n"
"s_add_u32 s88, s91, 16384\n"
"v_mfma_f32_16x16x32_bf16 v[40:43], a[240:243], v[156:159], v[40:43]\n"
"v_exp_f32 v221, v221\n"
"v_mfma_f32_16x16x32_bf16 v[44:47], a[228:231], v[152:155], v[44:47]\n"
"buffer_load_dwordx4 v244, s[0:3], s88 offen lds\n"
"v_mfma_f32_16x16x32_bf16 v[44:47], a[244:247], v[156:159], v[44:47]\n"
"s_add_u32 m0, s90, 101376\n"
"v_add_f32 v220, 1.0, v220\n"
"v_mfma_f32_16x16x32_bf16 v[52:55], v[152:155], a[232:235], v[52:55]\n"
"v_add_f32 v221, 1.0, v221\n"
"v_mfma_f32_16x16x32_bf16 v[52:55], v[156:159], a[248:251], v[52:55]\n"
"s_add_u32 s88, s91, 24576\n"
"v_mfma_f32_16x16x32_bf16 v[60:63], v[152:155], a[236:239], v[60:63]\n"
"buffer_load_dwordx4 v245, s[0:3], s88 offen lds\n"
"v_mfma_f32_16x16x32_bf16 v[60:63], v[156:159], a[252:255], v[60:63]\n"
"v_rcp_f32 v220, v220\n"
"s_waitcnt vmcnt(34) lgkmcnt(0)\n"
"v_mfma_f32_16x16x32_bf16 v[64:67], a[224:227], v[160:163], v[64:67]\n"
"s_barrier\n"
"v_mfma_f32_16x16x32_bf16 v[64:67], a[240:243], v[164:167], v[64:67]\n"
"ds_read_b128 v[192:195], v240 offset:32768\n"
"s_add_u32 m0, s90, 114688\n"
"v_mfma_f32_16x16x32_bf16 v[68:71], a[228:231], v[160:163], v[68:71]\n"
"v_rcp_f32 v221, v221\n"
"v_mfma_f32_16x16x32_bf16 v[68:71], a[244:247], v[164:167], v[68:71]\n"
"s_add_u32 s88, s49, 0\n"
"v_mfma_f32_16x16x32_bf16 v[80:83], v[160:163], a[232:235], v[80:83]\n"
"s_add_u32 s56, s8, 55574528\n"
"s_addc_u32 s57, s9, 0\n"
"v_mfma_f32_16x16x32_bf16 v[80:83], v[164:167], a[248:251], v[80:83]\n"
"s_lshl_b32 s54, s52, 2\n"
"v_mov_b32 v222, s54\n"
"v_mfma_f32_16x16x32_bf16 v[88:91], v[160:163], a[236:239], v[88:91]\n"
"buffer_load_dwordx4 v244, s[0:3], s88 offen lds\n"
"v_mfma_f32_16x16x32_bf16 v[88:91], v[164:167], a[252:255], v[88:91]\n"
"ds_read_b128 v[196:199], v241 offset:32768\n"
"s_lshl_b32 s54, s53, 2\n"
"v_mfma_f32_16x16x32_bf16 v[72:75], a[224:227], v[168:171], v[72:75]\n"
"s_add_u32 m0, s90, 115712\n"
"v_mov_b32 v223, s54\n"
"v_mfma_f32_16x16x32_bf16 v[72:75], a[240:243], v[172:175], v[72:75]\n"
"s_mov_b64 exec, 1\n"
"global_store_dword v222, v220, s[56:57] sc1\n"
"global_store_dword v223, v221, s[56:57] sc1\n"
"s_mov_b64 exec, -1\n"
"v_mfma_f32_16x16x32_bf16 v[76:79], a[228:231], v[168:171], v[76:79]\n"
"v_mfma_f32_16x16x32_bf16 v[76:79], a[244:247], v[172:175], v[76:79]\n"
"ds_read_b128 v[200:203], v240 offset:40960\n"
"s_add_u32 s88, s49, 8192\n"
"v_mfma_f32_16x16x32_bf16 v[84:87], v[168:171], a[232:235], v[84:87]\n"
"v_mfma_f32_16x16x32_bf16 v[84:87], v[172:175], a[248:251], v[84:87]\n"
"buffer_load_dwordx4 v245, s[0:3], s88 offen lds\n"
"v_mfma_f32_16x16x32_bf16 v[92:95], v[168:171], a[236:239], v[92:95]\n"
"v_mfma_f32_16x16x32_bf16 v[92:95], v[172:175], a[252:255], v[92:95]\n"
"v_mfma_f32_16x16x32_bf16 v[96:99], a[224:227], v[176:179], v[96:99]\n"
"ds_read_b128 v[204:207], v241 offset:40960\n"
"s_add_u32 m0, s90, 116736\n"
"v_mfma_f32_16x16x32_bf16 v[96:99], a[240:243], v[180:183], v[96:99]\n"
"v_mfma_f32_16x16x32_bf16 v[100:103], a[228:231], v[176:179], v[100:103]\n"
"v_mfma_f32_16x16x32_bf16 v[100:103], a[244:247], v[180:183], v[100:103]\n"
"s_add_u32 s88, s49, 16384\n"
"v_mfma_f32_16x16x32_bf16 v[112:115], v[176:179], a[232:235], v[112:115]\n"
"v_mfma_f32_16x16x32_bf16 v[112:115], v[180:183], a[248:251], v[112:115]\n"
"buffer_load_dwordx4 v244, s[0:3], s88 offen lds\n"
"v_mfma_f32_16x16x32_bf16 v[120:123], v[176:179], a[236:239], v[120:123]\n"
"v_mfma_f32_16x16x32_bf16 v[120:123], v[180:183], a[252:255], v[120:123]\n"
"v_mfma_f32_16x16x32_bf16 v[104:107], a[224:227], v[184:187], v[104:107]\n"
"s_add_u32 m0, s90, 117760\n"
"v_mfma_f32_16x16x32_bf16 v[104:107], a[240:243], v[188:191], v[104:107]\n"
"v_mfma_f32_16x16x32_bf16 v[108:111], a[228:231], v[184:187], v[108:111]\n"
"s_add_u32 s88, s49, 24576\n"
"v_mfma_f32_16x16x32_bf16 v[108:111], a[244:247], v[188:191], v[108:111]\n"
"v_mfma_f32_16x16x32_bf16 v[116:119], v[184:187], a[232:235], v[116:119]\n"
"v_mfma_f32_16x16x32_bf16 v[116:119], v[188:191], a[248:251], v[116:119]\n"
"buffer_load_dwordx4 v245, s[0:3], s88 offen lds\n"
"v_mfma_f32_16x16x32_bf16 v[124:127], v[184:187], a[236:239], v[124:127]\n"
"v_mfma_f32_16x16x32_bf16 v[124:127], v[188:191], a[252:255], v[124:127]\n"
"s_waitcnt lgkmcnt(0)\n"
"v_mfma_f32_16x16x32_bf16 v[128:131], a[0:3], v[192:195], 0\n"
"v_mfma_f32_16x16x32_bf16 v[128:131], a[16:19], v[196:199], v[128:131]\n"
"ds_read_b128 v[208:211], v240 offset:34816\n"
"v_subrev_u32 v242, s69, v242\n"
"v_mfma_f32_16x16x32_bf16 v[128:131], a[32:35], v[200:203], v[128:131]\n"
"v_subrev_u32 v243, s69, v243\n"
"v_mfma_f32_16x16x32_bf16 v[128:131], a[48:51], v[204:207], v[128:131]\n"
"ds_read_b128 v[212:215], v241 offset:34816\n"
"v_mfma_f32_16x16x32_bf16 v[132:135], a[4:7], v[192:195], 0\n"
"v_mfma_f32_16x16x32_bf16 v[132:135], a[20:23], v[196:199], v[132:135]\n"
"v_mfma_f32_16x16x32_bf16 v[132:135], a[36:39], v[200:203], v[132:135]\n"
"ds_read_b128 v[216:219], v240 offset:43008\n"
"v_mfma_f32_16x16x32_bf16 v[132:135], a[52:55], v[204:207], v[132:135]\n"
"v_mfma_f32_16x16x32_bf16 v[144:147], v[192:195], a[8:11], 0\n"
"ds_read_b128 v[220:223], v241 offset:43008\n"
"v_mfma_f32_16x16x32_bf16 v[144:147], v[196:199], a[24:27], v[144:147]\n"
"v_mfma_f32_16x16x32_bf16 v[144:147], v[200:203], a[40:43], v[144:147]\n"
"v_mfma_f32_16x16x32_bf16 v[144:147], v[204:207], a[56:59], v[144:147]\n"
"v_mfma_f32_16x16x32_bf16 v[152:155], v[192:195], a[12:15], 0\n"
"v_mfma_f32_16x16x32_bf16 v[152:155], v[196:199], a[28:31], v[152:155]\n"
"v_mfma_f32_16x16x32_bf16 v[152:155], v[200:203], a[44:47], v[152:155]\n"
"v_mfma_f32_16x16x32_bf16 v[152:155], v[204:207], a[60:63], v[152:155]\n"
"s_waitcnt lgkmcnt(0)\n"
"v_mfma_f32_16x16x32_bf16 v[136:139], a[0:3], v[208:211], 0\n"
"v_mfma_f32_16x16x32_bf16 v[136:139], a[16:19], v[212:215], v[136:139]\n"
"ds_read_b128 v[192:195], v240 offset:36864\n"
"s_add_u32 s36, s15, 32768\n"
"v_mfma_f32_16x16x32_bf16 v[136:139], a[32:35], v[216:219], v[136:139]\n"
"v_mul_f32 v64, 1040356334, v64\n"
"v_mfma_f32_16x16x32_bf16 v[136:139], a[48:51], v[220:223], v[136:139]\n"
"ds_read_b128 v[196:199], v241 offset:36864\n"
"v_mul_f32 v65, 1040356334, v65\n"
"v_mfma_f32_16x16x32_bf16 v[140:143], a[4:7], v[208:211], 0\n"
"v_mul_f32 v66, 1040356334, v66\n"
"v_mfma_f32_16x16x32_bf16 v[140:143], a[20:23], v[212:215], v[140:143]\n"
"v_mul_f32 v67, 1040356334, v67\n"
"v_mfma_f32_16x16x32_bf16 v[140:143], a[36:39], v[216:219], v[140:143]\n"
"ds_read_b128 v[200:203], v240 offset:45056\n"
"v_mul_f32 v68, 1040356334, v68\n"
"v_mfma_f32_16x16x32_bf16 v[140:143], a[52:55], v[220:223], v[140:143]\n"
"v_mul_f32 v69, 1040356334, v69\n"
"v_mfma_f32_16x16x32_bf16 v[148:151], v[208:211], a[8:11], 0\n"
"ds_read_b128 v[204:207], v241 offset:45056\n"
"v_mul_f32 v70, 1040356334, v70\n"
"v_mfma_f32_16x16x32_bf16 v[148:151], v[212:215], a[24:27], v[148:151]\n"
"v_mul_f32 v71, 1040356334, v71\n"
"v_mfma_f32_16x16x32_bf16 v[148:151], v[216:219], a[40:43], v[148:151]\n"
"v_cvt_pk_bf16_f32 v64, v64, v65\n"
"v_mfma_f32_16x16x32_bf16 v[148:151], v[220:223], a[56:59], v[148:151]\n"
"v_cvt_pk_bf16_f32 v65, v66, v67\n"
"v_mfma_f32_16x16x32_bf16 v[156:159], v[208:211], a[12:15], 0\n"
"v_cvt_pk_bf16_f32 v66, v68, v69\n"
"v_mfma_f32_16x16x32_bf16 v[156:159], v[212:215], a[28:31], v[156:159]\n"
"v_cvt_pk_bf16_f32 v67, v70, v71\n"
"v_mfma_f32_16x16x32_bf16 v[156:159], v[216:219], a[44:47], v[156:159]\n"
"buffer_store_dwordx4 v[64:67], v248, s[4:7], s36 offen offset:0 sc1\n"
"v_mfma_f32_16x16x32_bf16 v[156:159], v[220:223], a[60:63], v[156:159]\n"
"s_waitcnt lgkmcnt(0)\n"
"v_mfma_f32_16x16x32_bf16 v[160:163], a[0:3], v[192:195], 0\n"
"v_mfma_f32_16x16x32_bf16 v[160:163], a[16:19], v[196:199], v[160:163]\n"
"ds_read_b128 v[208:211], v240 offset:38912\n"
"v_mul_f32 v72, 1040356334, v72\n"
"v_mfma_f32_16x16x32_bf16 v[160:163], a[32:35], v[200:203], v[160:163]\n"
"v_mul_f32 v73, 1040356334, v73\n"
"v_mfma_f32_16x16x32_bf16 v[160:163], a[48:51], v[204:207], v[160:163]\n"
"ds_read_b128 v[212:215], v241 offset:38912\n"
"v_mul_f32 v74, 1040356334, v74\n"
"v_mfma_f32_16x16x32_bf16 v[164:167], a[4:7], v[192:195], 0\n"
"v_mul_f32 v75, 1040356334, v75\n"
"v_mfma_f32_16x16x32_bf16 v[164:167], a[20:23], v[196:199], v[164:167]\n"
"v_mul_f32 v76, 1040356334, v76\n"
"v_mfma_f32_16x16x32_bf16 v[164:167], a[36:39], v[200:203], v[164:167]\n"
"ds_read_b128 v[216:219], v240 offset:47104\n"
"v_mul_f32 v77, 1040356334, v77\n"
"v_mfma_f32_16x16x32_bf16 v[164:167], a[52:55], v[204:207], v[164:167]\n"
"v_mul_f32 v78, 1040356334, v78\n"
"v_mfma_f32_16x16x32_bf16 v[176:179], v[192:195], a[8:11], 0\n"
"ds_read_b128 v[220:223], v241 offset:47104\n"
"v_mfma_f32_16x16x32_bf16 v[176:179], v[196:199], a[24:27], v[176:179]\n"
"v_mul_f32 v79, 1040356334, v79\n"
"v_mfma_f32_16x16x32_bf16 v[176:179], v[200:203], a[40:43], v[176:179]\n"
"v_cvt_pk_bf16_f32 v72, v72, v73\n"
"v_mfma_f32_16x16x32_bf16 v[176:179], v[204:207], a[56:59], v[176:179]\n"
"v_cvt_pk_bf16_f32 v73, v74, v75\n"
"v_mfma_f32_16x16x32_bf16 v[184:187], v[192:195], a[12:15], 0\n"
"Lfused_hop2:\n"
"s_cbranch_execz Lfused_hop6\n"
"v_cvt_pk_bf16_f32 v74, v76, v77\n"
"v_mfma_f32_16x16x32_bf16 v[184:187], v[196:199], a[28:31], v[184:187]\n"
"v_cvt_pk_bf16_f32 v75, v78, v79\n"
"v_mfma_f32_16x16x32_bf16 v[184:187], v[200:203], a[44:47], v[184:187]\n"
"buffer_store_dwordx4 v[72:75], v248, s[4:7], s36 offen offset:1024 sc1\n"
"v_mfma_f32_16x16x32_bf16 v[184:187], v[204:207], a[60:63], v[184:187]\n"
"s_waitcnt vmcnt(38) lgkmcnt(0)\n"
"v_mfma_f32_16x16x32_bf16 v[168:171], a[0:3], v[208:211], 0\n"
"s_barrier\n"
"v_mfma_f32_16x16x32_bf16 v[168:171], a[16:19], v[212:215], v[168:171]\n"
"ds_read_b128 v[192:195], v240 offset:49152\n"
"s_add_u32 m0, s90, 131072\n"
"v_mfma_f32_16x16x32_bf16 v[168:171], a[32:35], v[216:219], v[168:171]\n"
"buffer_load_dwordx4 v246, s[84:87], s92 offen lds\n"
"v_mfma_f32_16x16x32_bf16 v[168:171], a[48:51], v[220:223], v[168:171]\n"
"ds_read_b128 v[196:199], v241 offset:49152\n"
"v_mfma_f32_16x16x32_bf16 v[172:175], a[4:7], v[208:211], 0\n"
"s_add_u32 s36, s15, 40960\n"
"v_mfma_f32_16x16x32_bf16 v[172:175], a[20:23], v[212:215], v[172:175]\n"
"v_mfma_f32_16x16x32_bf16 v[172:175], a[36:39], v[216:219], v[172:175]\n"
"ds_read_b128 v[200:203], v240 offset:57344\n"
"v_cvt_pk_bf16_f32 v80, v80, v81\n"
"v_mfma_f32_16x16x32_bf16 v[172:175], a[52:55], v[220:223], v[172:175]\n"
"v_mfma_f32_16x16x32_bf16 v[180:183], v[208:211], a[8:11], 0\n"
"ds_read_b128 v[204:207], v241 offset:57344\n"
"v_cvt_pk_bf16_f32 v81, v82, v83\n"
"v_mfma_f32_16x16x32_bf16 v[180:183], v[212:215], a[24:27], v[180:183]\n"
"v_mfma_f32_16x16x32_bf16 v[180:183], v[216:219], a[40:43], v[180:183]\n"
"v_cvt_pk_bf16_f32 v82, v84, v85\n"
"v_mfma_f32_16x16x32_bf16 v[180:183], v[220:223], a[56:59], v[180:183]\n"
"v_mfma_f32_16x16x32_bf16 v[188:191], v[208:211], a[12:15], 0\n"
"v_cvt_pk_bf16_f32 v83, v86, v87\n"
"v_mfma_f32_16x16x32_bf16 v[188:191], v[212:215], a[28:31], v[188:191]\n"
"v_mfma_f32_16x16x32_bf16 v[188:191], v[216:219], a[44:47], v[188:191]\n"
"buffer_store_dwordx4 v[80:83], v248, s[4:7], s36 offen offset:0 sc1\n"
"v_mfma_f32_16x16x32_bf16 v[188:191], v[220:223], a[60:63], v[188:191]\n"
"s_waitcnt lgkmcnt(0)\n"
"v_mfma_f32_16x16x32_bf16 v[128:131], a[64:67], v[192:195], v[128:131]\n"
"v_mfma_f32_16x16x32_bf16 v[128:131], a[80:83], v[196:199], v[128:131]\n"
"ds_read_b128 v[208:211], v240 offset:51200\n"
"s_add_u32 m0, s90, 132096\n"
"v_mfma_f32_16x16x32_bf16 v[128:131], a[96:99], v[200:203], v[128:131]\n"
"s_add_u32 s88, s92, s94\n"
"v_mfma_f32_16x16x32_bf16 v[128:131], a[112:115], v[204:207], v[128:131]\n"
"ds_read_b128 v[212:215], v241 offset:51200\n"
"v_mfma_f32_16x16x32_bf16 v[132:135], a[68:71], v[192:195], v[132:135]\n"
"buffer_load_dwordx4 v246, s[84:87], s88 offen lds\n"
"v_mfma_f32_16x16x32_bf16 v[132:135], a[84:87], v[196:199], v[132:135]\n"
"v_mfma_f32_16x16x32_bf16 v[132:135], a[100:103], v[200:203], v[132:135]\n"
"ds_read_b128 v[216:219], v240 offset:59392\n"
"v_cvt_pk_bf16_f32 v88, v88, v89\n"
"v_mfma_f32_16x16x32_bf16 v[132:135], a[116:119], v[204:207], v[132:135]\n"
"v_mfma_f32_16x16x32_bf16 v[144:147], v[192:195], a[72:75], v[144:147]\n"
"ds_read_b128 v[220:223], v241 offset:59392\n"
"v_cvt_pk_bf16_f32 v89, v90, v91\n"
"v_mfma_f32_16x16x32_bf16 v[144:147], v[196:199], a[88:91], v[144:147]\n"
"v_mfma_f32_16x16x32_bf16 v[144:147], v[200:203], a[104:107], v[144:147]\n"
"v_cvt_pk_bf16_f32 v90, v92, v93\n"
"v_mfma_f32_16x16x32_bf16 v[144:147], v[204:207], a[120:123], v[144:147]\n"
"v_mfma_f32_16x16x32_bf16 v[152:155], v[192:195], a[76:79], v[152:155]\n"
"v_cvt_pk_bf16_f32 v91, v94, v95\n"
"v_mfma_f32_16x16x32_bf16 v[152:155], v[196:199], a[92:95], v[152:155]\n"
"v_mfma_f32_16x16x32_bf16 v[152:155], v[200:203], a[108:111], v[152:155]\n"
"buffer_store_dwordx4 v[88:91], v248, s[4:7], s36 offen offset:1024 sc1\n"
"v_mfma_f32_16x16x32_bf16 v[152:155], v[204:207], a[124:127], v[152:155]\n"
"s_waitcnt lgkmcnt(0)\n"
"v_mfma_f32_16x16x32_bf16 v[136:139], a[64:67], v[208:211], v[136:139]\n"
"v_mfma_f32_16x16x32_bf16 v[136:139], a[80:83], v[212:215], v[136:139]\n"
"ds_read_b128 v[192:195], v240 offset:53248\n"
"s_add_u32 m0, s90, 133120\n"
"v_mfma_f32_16x16x32_bf16 v[136:139], a[96:99], v[216:219], v[136:139]\n"
"v_mfma_f32_16x16x32_bf16 v[136:139], a[112:115], v[220:223], v[136:139]\n"
"ds_read_b128 v[196:199], v241 offset:53248\n"
"s_add_u32 s89, s92, s93\n"
"v_mfma_f32_16x16x32_bf16 v[140:143], a[68:71], v[208:211], v[140:143]\n"
"v_mfma_f32_16x16x32_bf16 v[140:143], a[84:87], v[212:215], v[140:143]\n"
"buffer_load_dwordx4 v247, s[84:87], s89 offen lds\n"
"v_mfma_f32_16x16x32_bf16 v[140:143], a[100:103], v[216:219], v[140:143]\n"
"ds_read_b128 v[200:203], v240 offset:61440\n"
"v_mfma_f32_16x16x32_bf16 v[140:143], a[116:119], v[220:223], v[140:143]\n"
"s_add_u32 m0, s90, 134144\n"
"v_mfma_f32_16x16x32_bf16 v[148:151], v[208:211], a[72:75], v[148:151]\n"
"ds_read_b128 v[204:207], v241 offset:61440\n"
"v_mfma_f32_16x16x32_bf16 v[148:151], v[212:215], a[88:91], v[148:151]\n"
"s_add_u32 s88, s89, s94\n"
"v_mfma_f32_16x16x32_bf16 v[148:151], v[216:219], a[104:107], v[148:151]\n"
"v_mfma_f32_16x16x32_bf16 v[148:151], v[220:223], a[120:123], v[148:151]\n"
"buffer_load_dwordx4 v247, s[84:87], s88 offen lds\n"
"v_mfma_f32_16x16x32_bf16 v[156:159], v[208:211], a[76:79], v[156:159]\n"
"v_mfma_f32_16x16x32_bf16 v[156:159], v[212:215], a[92:95], v[156:159]\n"
"s_add_u32 s92, s92, s95\n"
"v_mfma_f32_16x16x32_bf16 v[156:159], v[216:219], a[108:111], v[156:159]\n"
"v_mfma_f32_16x16x32_bf16 v[156:159], v[220:223], a[124:127], v[156:159]\n"
"s_waitcnt lgkmcnt(0)\n"
"v_mfma_f32_16x16x32_bf16 v[160:163], a[64:67], v[192:195], v[160:163]\n"
"v_mfma_f32_16x16x32_bf16 v[160:163], a[80:83], v[196:199], v[160:163]\n"
"ds_read_b128 v[208:211], v240 offset:55296\n"
"s_add_u32 s36, s15, 49152\n"
"v_mfma_f32_16x16x32_bf16 v[160:163], a[96:99], v[200:203], v[160:163]\n"
"v_mul_f32 v96, 1040356334, v96\n"
"v_mfma_f32_16x16x32_bf16 v[160:163], a[112:115], v[204:207], v[160:163]\n"
"ds_read_b128 v[212:215], v241 offset:55296\n"
"v_mul_f32 v97, 1040356334, v97\n"
"v_mfma_f32_16x16x32_bf16 v[164:167], a[68:71], v[192:195], v[164:167]\n"
"v_mul_f32 v98, 1040356334, v98\n"
"v_mfma_f32_16x16x32_bf16 v[164:167], a[84:87], v[196:199], v[164:167]\n"
"v_mul_f32 v99, 1040356334, v99\n"
"v_mfma_f32_16x16x32_bf16 v[164:167], a[100:103], v[200:203], v[164:167]\n"
"ds_read_b128 v[216:219], v240 offset:63488\n"
"v_mul_f32 v100, 1040356334, v100\n"
"v_mfma_f32_16x16x32_bf16 v[164:167], a[116:119], v[204:207], v[164:167]\n"
"v_mul_f32 v101, 1040356334, v101\n"
"v_mfma_f32_16x16x32_bf16 v[176:179], v[192:195], a[72:75], v[176:179]\n"
"ds_read_b128 v[220:223], v241 offset:63488\n"
"v_mul_f32 v102, 1040356334, v102\n"
"v_mfma_f32_16x16x32_bf16 v[176:179], v[196:199], a[88:91], v[176:179]\n"
"v_mul_f32 v103, 1040356334, v103\n"
"v_mfma_f32_16x16x32_bf16 v[176:179], v[200:203], a[104:107], v[176:179]\n"
"v_cvt_pk_bf16_f32 v96, v96, v97\n"
"v_mfma_f32_16x16x32_bf16 v[176:179], v[204:207], a[120:123], v[176:179]\n"
"v_cvt_pk_bf16_f32 v97, v98, v99\n"
"v_mfma_f32_16x16x32_bf16 v[184:187], v[192:195], a[76:79], v[184:187]\n"
"v_cvt_pk_bf16_f32 v98, v100, v101\n"
"v_mfma_f32_16x16x32_bf16 v[184:187], v[196:199], a[92:95], v[184:187]\n"
"v_cvt_pk_bf16_f32 v99, v102, v103\n"
"v_mfma_f32_16x16x32_bf16 v[184:187], v[200:203], a[108:111], v[184:187]\n"
"buffer_store_dwordx4 v[96:99], v248, s[4:7], s36 offen offset:0 sc1\n"
"v_mfma_f32_16x16x32_bf16 v[184:187], v[204:207], a[124:127], v[184:187]\n"
"s_waitcnt vmcnt(41) lgkmcnt(0)\n"
"v_mfma_f32_16x16x32_bf16 v[168:171], a[64:67], v[208:211], v[168:171]\n"
"s_barrier\n"
"v_mfma_f32_16x16x32_bf16 v[168:171], a[80:83], v[212:215], v[168:171]\n"
"v_add_u32 v240, 4294868992, v240\n"
"s_add_u32 m0, s90, 147456\n"
"v_mfma_f32_16x16x32_bf16 v[168:171], a[96:99], v[216:219], v[168:171]\n"
"buffer_load_dwordx4 v246, s[84:87], s92 offen lds\n"
"v_mfma_f32_16x16x32_bf16 v[168:171], a[112:115], v[220:223], v[168:171]\n"
"v_add_u32 v241, 4294868992, v241\n"
"v_mul_f32 v104, 1040356334, v104\n"
"v_mfma_f32_16x16x32_bf16 v[172:175], a[68:71], v[208:211], v[172:175]\n"
"ds_read_b128 v[192:195], v240 offset:0\n"
"v_mul_f32 v105, 1040356334, v105\n"
"v_mfma_f32_16x16x32_bf16 v[172:175], a[84:87], v[212:215], v[172:175]\n"
"v_mul_f32 v106, 1040356334, v106\n"
"s_mov_b64 s[16:17], s[4:5]\n"
"v_mfma_f32_16x16x32_bf16 v[172:175], a[100:103], v[216:219], v[172:175]\n"
"ds_read_b128 v[196:199], v241 offset:0\n"
"v_mul_f32 v107, 1040356334, v107\n"
"v_mfma_f32_16x16x32_bf16 v[172:175], a[116:119], v[220:223], v[172:175]\n"
"ds_read_b128 v[200:203], v240 offset:8192\n"
"v_mul_f32 v108, 1040356334, v108\n"
"v_mfma_f32_16x16x32_bf16 v[180:183], v[208:211], a[72:75], v[180:183]\n"
"v_mul_f32 v109, 1040356334, v109\n"
"s_mov_b32 s18, 557056\n"
"v_mfma_f32_16x16x32_bf16 v[180:183], v[212:215], a[88:91], v[180:183]\n"
"ds_read_b128 v[204:207], v241 offset:8192\n"
"v_mul_f32 v110, 1040356334, v110\n"
"v_mfma_f32_16x16x32_bf16 v[180:183], v[216:219], a[104:107], v[180:183]\n"
"v_mul_f32 v111, 1040356334, v111\n"
"s_mov_b32 s19, 0x20000\n"
"v_mfma_f32_16x16x32_bf16 v[180:183], v[220:223], a[120:123], v[180:183]\n"
"v_cvt_pk_bf16_f32 v104, v104, v105\n"
"s_lshl_b32 s54, s11, 2\n"
"v_mfma_f32_16x16x32_bf16 v[188:191], v[208:211], a[76:79], v[188:191]\n"
"v_cvt_pk_bf16_f32 v105, v106, v107\n"
"s_add_u32 s39, s54, s12\n"
"v_mfma_f32_16x16x32_bf16 v[188:191], v[212:215], a[92:95], v[188:191]\n"
"v_cvt_pk_bf16_f32 v106, v108, v109\n"
"s_and_b32 s50, s90, 0x1000\n"
"v_mfma_f32_16x16x32_bf16 v[188:191], v[216:219], a[108:111], v[188:191]\n"
"v_cvt_pk_bf16_f32 v107, v110, v111\n"
"s_lshr_b32 s51, s12, 1\n"
"v_mfma_f32_16x16x32_bf16 v[188:191], v[220:223], a[124:127], v[188:191]\n"
"buffer_store_dwordx4 v[104:107], v248, s[4:7], s36 offen offset:1024 sc1\n"
"s_waitcnt lgkmcnt(0)\n"
"v_mfma_f32_16x16x32_bf16 v[128:131], a[128:131], v[192:195], v[128:131]\n"
"v_mfma_f32_16x16x32_bf16 v[128:131], a[144:147], v[196:199], v[128:131]\n"
"ds_read_b128 v[208:211], v240 offset:2048\n"
"s_add_u32 m0, s90, 148480\n"
"v_mfma_f32_16x16x32_bf16 v[128:131], a[160:163], v[200:203], v[128:131]\n"
"s_add_u32 s88, s92, s94\n"
"s_lshl_b32 s52, s51, 15\n"
"v_mfma_f32_16x16x32_bf16 v[128:131], a[176:179], v[204:207], v[128:131]\n"
"ds_read_b128 v[212:215], v241 offset:2048\n"
"s_add_u32 s50, s50, s52\n"
"v_mfma_f32_16x16x32_bf16 v[132:135], a[132:135], v[192:195], v[132:135]\n"
"buffer_load_dwordx4 v246, s[84:87], s88 offen lds\n"
"v_mfma_f32_16x16x32_bf16 v[132:135], a[148:151], v[196:199], v[132:135]\n"
"s_mov_b32 s44, s50\n"
"s_sub_u32 s52, 4, s51\n"
"v_mfma_f32_16x16x32_bf16 v[132:135], a[164:167], v[200:203], v[132:135]\n"
"ds_read_b128 v[216:219], v240 offset:10240\n"
"s_add_u32 s36, s15, 57344\n"
"v_mfma_f32_16x16x32_bf16 v[132:135], a[180:183], v[204:207], v[132:135]\n"
"v_cvt_pk_bf16_f32 v112, v112, v113\n"
"s_lshl_b32 s52, s52, 14\n"
"v_mfma_f32_16x16x32_bf16 v[144:147], v[192:195], a[136:139], v[144:147]\n"
"ds_read_b128 v[220:223], v241 offset:10240\n"
"s_add_u32 s28, s90, s52\n"
"v_mfma_f32_16x16x32_bf16 v[144:147], v[196:199], a[152:155], v[144:147]\n"
"v_cvt_pk_bf16_f32 v113, v114, v115\n"
"v_mfma_f32_16x16x32_bf16 v[144:147], v[200:203], a[168:171], v[144:147]\n"
"s_cmp_eq_u32 s51, 1\n"
"s_cselect_b32 s30, 24576, 0\n"
"s_cselect_b32 s31, 0, 8192\n"
"v_mfma_f32_16x16x32_bf16 v[144:147], v[204:207], a[184:187], v[144:147]\n"
"v_cvt_pk_bf16_f32 v114, v116, v117\n"
"s_add_u32 s30, s30, s50\n"
"v_mfma_f32_16x16x32_bf16 v[152:155], v[192:195], a[140:143], v[152:155]\n"
"v_cvt_pk_bf16_f32 v115, v118, v119\n"
"s_add_u32 s31, s31, s50\n"
"v_mfma_f32_16x16x32_bf16 v[152:155], v[196:199], a[156:159], v[152:155]\n"
"s_sub_u32 s52, 30, s54\n"
"s_lshl_b32 s52, s52, 14\n"
"v_mfma_f32_16x16x32_bf16 v[152:155], v[200:203], a[172:175], v[152:155]\n"
"buffer_store_dwordx4 v[112:115], v248, s[4:7], s36 offen offset:0 sc1\n"
"v_mfma_f32_16x16x32_bf16 v[152:155], v[204:207], a[188:191], v[152:155]\n"
"s_add_u32 s20, s16, s52\n"
"s_addc_u32 s21, s17, 0\n"
"s_waitcnt lgkmcnt(0)\n"
"v_mfma_f32_16x16x32_bf16 v[136:139], a[128:131], v[208:211], v[136:139]\n"
"v_mfma_f32_16x16x32_bf16 v[136:139], a[144:147], v[212:215], v[136:139]\n"
"ds_read_b128 v[192:195], v240 offset:4096\n"
"s_add_u32 m0, s90, 149504\n"
"v_mfma_f32_16x16x32_bf16 v[136:139], a[160:163], v[216:219], v[136:139]\n"
"s_add_u32 s89, s92, s93\n"
"v_mfma_f32_16x16x32_bf16 v[136:139], a[176:179], v[220:223], v[136:139]\n"
"ds_read_b128 v[196:199], v241 offset:4096\n"
"v_mfma_f32_16x16x32_bf16 v[140:143], a[132:135], v[208:211], v[140:143]\n"
"buffer_load_dwordx4 v247, s[84:87], s89 offen lds\n"
"v_mfma_f32_16x16x32_bf16 v[140:143], a[148:151], v[212:215], v[140:143]\n"
"s_or_b32 s52, s11, s51\n"
"s_cselect_b32 s22, 0, s18\n"
"v_mfma_f32_16x16x32_bf16 v[140:143], a[164:167], v[216:219], v[140:143]\n"
"ds_read_b128 v[200:203], v240 offset:12288\n"
"v_cvt_pk_bf16_f32 v120, v120, v121\n"
"v_mfma_f32_16x16x32_bf16 v[140:143], a[180:183], v[220:223], v[140:143]\n"
"s_mov_b32 s23, 0x20000\n"
"s_mov_b32 s14, 0\n"
"v_mfma_f32_16x16x32_bf16 v[148:151], v[208:211], a[136:139], v[148:151]\n"
"ds_read_b128 v[204:207], v241 offset:12288\n"
"v_cvt_pk_bf16_f32 v121, v122, v123\n"
"v_mfma_f32_16x16x32_bf16 v[148:151], v[212:215], a[152:155], v[148:151]\n"
"s_add_u32 s24, s8, 33554432\n"
"s_addc_u32 s25, s9, 0\n"
"v_mfma_f32_16x16x32_bf16 v[148:151], v[216:219], a[168:171], v[148:151]\n"
"v_cvt_pk_bf16_f32 v122, v124, v125\n"
"s_and_b32 s25, s25, 0xffff\n"
"v_mfma_f32_16x16x32_bf16 v[148:151], v[220:223], a[184:187], v[148:151]\n"
"s_mov_b32 s26, 18874368\n"
"s_mov_b32 s27, 0x20000\n"
"v_mfma_f32_16x16x32_bf16 v[156:159], v[208:211], a[140:143], v[156:159]\n"
"v_cvt_pk_bf16_f32 v123, v126, v127\n"
"s_mul_i32 s48, s39, 294912\n"
"v_mfma_f32_16x16x32_bf16 v[156:159], v[212:215], a[156:159], v[156:159]\n"
"s_lshl_b32 s50, s10, 12\n"
"s_add_u32 s48, s48, s50\n"
"v_mfma_f32_16x16x32_bf16 v[156:159], v[216:219], a[172:175], v[156:159]\n"
"buffer_store_dwordx4 v[120:123], v248, s[4:7], s36 offen offset:1024 sc1\n"
"v_mfma_f32_16x16x32_bf16 v[156:159], v[220:223], a[188:191], v[156:159]\n"
"s_sub_u32 s51, 60, s54\n"
"s_add_u32 s51, s51, s12\n"
"s_waitcnt lgkmcnt(0)\n"
"v_mfma_f32_16x16x32_bf16 v[160:163], a[128:131], v[192:195], v[160:163]\n"
"v_mfma_f32_16x16x32_bf16 v[160:163], a[144:147], v[196:199], v[160:163]\n"
"ds_read_b128 v[208:211], v240 offset:6144\n"
"s_add_u32 m0, s90, 150528\n"
"v_mfma_f32_16x16x32_bf16 v[160:163], a[160:163], v[200:203], v[160:163]\n"
"s_mul_i32 s47, s51, 294912\n"
"s_add_u32 s47, s47, s50\n"
"v_mfma_f32_16x16x32_bf16 v[160:163], a[176:179], v[204:207], v[160:163]\n"
"ds_read_b128 v[212:215], v241 offset:6144\n"
"s_add_u32 s52, s12, 30\n"
"v_mfma_f32_16x16x32_bf16 v[164:167], a[132:135], v[192:195], v[164:167]\n"
"s_add_u32 s88, s89, s94\n"
"v_mfma_f32_16x16x32_bf16 v[164:167], a[148:151], v[196:199], v[164:167]\n"
"s_and_b32 s53, s12, 1\n"
"s_cselect_b32 s38, s52, s51\n"
"s_cselect_b32 s41, 0x10000, 0\n"
"v_mfma_f32_16x16x32_bf16 v[164:167], a[164:167], v[200:203], v[164:167]\n"
"ds_read_b128 v[216:219], v240 offset:14336\n"
"s_or_b32 s41, s41, 0x4000\n"
"v_mfma_f32_16x16x32_bf16 v[164:167], a[180:183], v[204:207], v[164:167]\n"
"s_xor_b32 s40, s38, 1\n"
"s_mov_b32 s43, 1317011456\n"
"v_mfma_f32_16x16x32_bf16 v[176:179], v[192:195], a[136:139], v[176:179]\n"
"buffer_load_dwordx4 v247, s[84:87], s88 offen lds\n"
"v_mfma_f32_16x16x32_bf16 v[176:179], v[196:199], a[152:155], v[176:179]\n"
"ds_read_b128 v[220:223], v241 offset:14336\n"
"s_load_dwordx2 s[80:81], s[96:97], 72\n"
"v_mfma_f32_16x16x32_bf16 v[176:179], v[200:203], a[168:171], v[176:179]\n"
"v_mfma_f32_16x16x32_bf16 v[176:179], v[204:207], a[184:187], v[176:179]\n"
"v_mfma_f32_16x16x32_bf16 v[184:187], v[192:195], a[140:143], v[184:187]\n"
"s_add_u32 s92, s92, s95\n"
"v_mfma_f32_16x16x32_bf16 v[184:187], v[196:199], a[156:159], v[184:187]\n"
"v_mfma_f32_16x16x32_bf16 v[184:187], v[200:203], a[172:175], v[184:187]\n"
"v_mfma_f32_16x16x32_bf16 v[184:187], v[204:207], a[188:191], v[184:187]\n"
"s_waitcnt vmcnt(43) lgkmcnt(0)\n"
"v_mfma_f32_16x16x32_bf16 v[168:171], a[128:131], v[208:211], v[168:171]\n"
"s_barrier\n"
"v_mfma_f32_16x16x32_bf16 v[168:171], a[144:147], v[212:215], v[168:171]\n"
"s_add_u32 m0, s90, 0\n"
"s_add_u32 s88, s91, 128\n"
"v_mfma_f32_16x16x32_bf16 v[168:171], a[160:163], v[216:219], v[168:171]\n"
"buffer_load_dwordx4 v244, s[0:3], s88 offen lds\n"
"v_mfma_f32_16x16x32_bf16 v[168:171], a[176:179], v[220:223], v[168:171]\n"
"ds_read_b128 v[192:195], v240 offset:16384\n"
"s_add_u32 s36, s15, 0\n"
"v_mfma_f32_16x16x32_bf16 v[172:175], a[132:135], v[208:211], v[172:175]\n"
"ds_read_b128 v[196:199], v241 offset:16384\n"
"v_mul_f32 v0, 1040356334, v0\n"
"v_mfma_f32_16x16x32_bf16 v[172:175], a[148:151], v[212:215], v[172:175]\n"
"v_mul_f32 v1, 1040356334, v1\n"
"s_and_b32 s81, s81, 0xffff\n"
"v_mfma_f32_16x16x32_bf16 v[172:175], a[164:167], v[216:219], v[172:175]\n"
"ds_read_b128 v[200:203], v240 offset:24576\n"
"v_mul_f32 v2, 1040356334, v2\n"
"v_mfma_f32_16x16x32_bf16 v[172:175], a[180:183], v[220:223], v[172:175]\n"
"v_mul_f32 v3, 1040356334, v3\n"
"s_mov_b32 s82, 2097152\n"
"v_mfma_f32_16x16x32_bf16 v[180:183], v[208:211], a[136:139], v[180:183]\n"
"v_mul_f32 v4, 1040356334, v4\n"
"v_mul_f32 v5, 1040356334, v5\n"
"v_mfma_f32_16x16x32_bf16 v[180:183], v[212:215], a[152:155], v[180:183]\n"
"ds_read_b128 v[204:207], v241 offset:24576\n"
"v_mul_f32 v6, 1040356334, v6\n"
"v_mfma_f32_16x16x32_bf16 v[180:183], v[216:219], a[168:171], v[180:183]\n"
"v_mul_f32 v7, 1040356334, v7\n"
"s_mov_b32 s83, 0x20000\n"
"v_mfma_f32_16x16x32_bf16 v[180:183], v[220:223], a[184:187], v[180:183]\n"
"v_cvt_pk_bf16_f32 v0, v0, v1\n"
"s_bfe_u32 s78, s13, 0x20001\n"
"v_mfma_f32_16x16x32_bf16 v[188:191], v[208:211], a[140:143], v[188:191]\n"
"v_cvt_pk_bf16_f32 v1, v2, v3\n"
"s_bfe_u32 s77, s13, 0x20003\n"
"v_mfma_f32_16x16x32_bf16 v[188:191], v[212:215], a[156:159], v[188:191]\n"
"v_cvt_pk_bf16_f32 v2, v4, v5\n"
"s_lshl_b32 s77, s77, 1\n"
"v_mfma_f32_16x16x32_bf16 v[188:191], v[216:219], a[172:175], v[188:191]\n"
"v_cvt_pk_bf16_f32 v3, v6, v7\n"
"s_and_b32 s50, s12, 1\n"
"v_mfma_f32_16x16x32_bf16 v[188:191], v[220:223], a[188:191], v[188:191]\n"
"buffer_store_dwordx4 v[0:3], v248, s[4:7], s36 offen offset:0 sc1\n"
"s_waitcnt lgkmcnt(0)\n"
"v_mfma_f32_16x16x32_bf16 v[128:131], a[192:195], v[192:195], v[128:131]\n"
"v_mfma_f32_16x16x32_bf16 v[128:131], a[208:211], v[196:199], v[128:131]\n"
"s_add_u32 m0, s90, 1024\n"
"s_add_u32 s88, s91, 8320\n"
"v_mfma_f32_16x16x32_bf16 v[128:131], a[224:227], v[200:203], v[128:131]\n"
"buffer_load_dwordx4 v245, s[0:3], s88 offen lds\n"
"v_mfma_f32_16x16x32_bf16 v[128:131], a[240:243], v[204:207], v[128:131]\n"
"ds_read_b128 v[208:211], v240 offset:18432\n"
"v_mul_f32 v8, 1040356334, v8\n"
"v_mfma_f32_16x16x32_bf16 v[132:135], a[196:199], v[192:195], v[132:135]\n"
"ds_read_b128 v[212:215], v241 offset:18432\n"
"v_mul_f32 v9, 1040356334, v9\n"
"v_mfma_f32_16x16x32_bf16 v[132:135], a[212:215], v[196:199], v[132:135]\n"
"v_mul_f32 v10, 1040356334, v10\n"
"s_add_u32 s77, s77, s50\n"
"v_mfma_f32_16x16x32_bf16 v[132:135], a[228:231], v[200:203], v[132:135]\n"
"ds_read_b128 v[216:219], v240 offset:26624\n"
"v_mul_f32 v11, 1040356334, v11\n"
"v_mfma_f32_16x16x32_bf16 v[132:135], a[244:247], v[204:207], v[132:135]\n"
"v_mul_f32 v12, 1040356334, v12\n"
"s_lshr_b32 s76, s13, 5\n"
"v_mfma_f32_16x16x32_bf16 v[144:147], v[192:195], a[200:203], v[144:147]\n"
"ds_read_b128 v[220:223], v241 offset:26624\n"
"v_mul_f32 v13, 1040356334, v13\n"
"v_mfma_f32_16x16x32_bf16 v[144:147], v[196:199], a[216:219], v[144:147]\n"
"v_mul_f32 v14, 1040356334, v14\n"
"s_lshl_b32 s76, s76, 1\n"
"v_mfma_f32_16x16x32_bf16 v[144:147], v[200:203], a[232:235], v[144:147]\n"
"v_mul_f32 v15, 1040356334, v15\n"
"s_and_b32 s50, s13, 1\n"
"v_mfma_f32_16x16x32_bf16 v[144:147], v[204:207], a[248:251], v[144:147]\n"
"v_cvt_pk_bf16_f32 v8, v8, v9\n"
"s_add_u32 s76, s76, s50\n"
"v_mfma_f32_16x16x32_bf16 v[152:155], v[192:195], a[204:207], v[152:155]\n"
"v_cvt_pk_bf16_f32 v9, v10, v11\n"
"s_mul_i32 s79, s77, 589824\n"
"v_mfma_f32_16x16x32_bf16 v[152:155], v[196:199], a[220:223], v[152:155]\n"
"v_cvt_pk_bf16_f32 v10, v12, v13\n"
"s_lshl_b32 s50, s78, 14\n"
"v_mfma_f32_16x16x32_bf16 v[152:155], v[200:203], a[236:239], v[152:155]\n"
"v_cvt_pk_bf16_f32 v11, v14, v15\n"
"s_add_u32 s46, s79, s50\n"
"v_mfma_f32_16x16x32_bf16 v[152:155], v[204:207], a[252:255], v[152:155]\n"
"buffer_store_dwordx4 v[8:11], v248, s[4:7], s36 offen offset:1024 sc1\n"
"s_waitcnt lgkmcnt(0)\n"
"v_mfma_f32_16x16x32_bf16 v[136:139], a[192:195], v[208:211], v[136:139]\n"
"v_mfma_f32_16x16x32_bf16 v[136:139], a[208:211], v[212:215], v[136:139]\n"
"ds_read_b128 v[192:195], v240 offset:20480\n"
"s_add_u32 m0, s90, 2048\n"
"v_mfma_f32_16x16x32_bf16 v[136:139], a[224:227], v[216:219], v[136:139]\n"
"s_add_u32 s88, s91, 16512\n"
"s_add_u32 s46, s46, 524288\n"
"v_mfma_f32_16x16x32_bf16 v[136:139], a[240:243], v[220:223], v[136:139]\n"
"ds_read_b128 v[196:199], v241 offset:20480\n"
"s_lshl_b32 s50, s78, 17\n"
"v_mfma_f32_16x16x32_bf16 v[140:143], a[196:199], v[208:211], v[140:143]\n"
"buffer_load_dwordx4 v244, s[0:3], s88 offen lds\n"
"v_mfma_f32_16x16x32_bf16 v[140:143], a[212:215], v[212:215], v[140:143]\n"
"s_add_u32 s79, s79, s50\n"
"s_lshr_b32 s50, s12, 1\n"
"v_mfma_f32_16x16x32_bf16 v[140:143], a[228:231], v[216:219], v[140:143]\n"
"ds_read_b128 v[200:203], v240 offset:28672\n"
"s_add_u32 s36, s15, 8192\n"
"v_mfma_f32_16x16x32_bf16 v[140:143], a[244:247], v[220:223], v[140:143]\n"
"v_cvt_pk_bf16_f32 v16, v16, v17\n"
"s_lshl_b32 s50, s50, 12\n"
"v_mfma_f32_16x16x32_bf16 v[148:151], v[208:211], a[200:203], v[148:151]\n"
"ds_read_b128 v[204:207], v241 offset:28672\n"
"s_add_u32 s45, s79, s50\n"
"v_mfma_f32_16x16x32_bf16 v[148:151], v[212:215], a[216:219], v[148:151]\n"
"v_cvt_pk_bf16_f32 v17, v18, v19\n"
"s_add_u32 s46, s46, s50\n"
"v_mfma_f32_16x16x32_bf16 v[148:151], v[216:219], a[232:235], v[148:151]\n"
"v_bfe_u32 v232, v248, 6, 3\n"
"v_lshlrev_b32 v232, 5, v232\n"
"v_mfma_f32_16x16x32_bf16 v[148:151], v[220:223], a[248:251], v[148:151]\n"
"v_cvt_pk_bf16_f32 v18, v20, v21\n"
"v_bfe_u32 v233, v248, 4, 2\n"
"v_mfma_f32_16x16x32_bf16 v[156:159], v[208:211], a[204:207], v[156:159]\n"
"v_cvt_pk_bf16_f32 v19, v22, v23\n"
"s_and_b32 s50, s13, 0xc7\n"
"v_mfma_f32_16x16x32_bf16 v[156:159], v[212:215], a[220:223], v[156:159]\n"
"s_lshl_b32 s50, s50, 2\n"
"v_add3_u32 v232, v232, v233, s50\n"
"v_mfma_f32_16x16x32_bf16 v[156:159], v[216:219], a[236:239], v[156:159]\n"
"buffer_store_dwordx4 v[16:19], v248, s[4:7], s36 offen offset:0 sc1\n"
"v_mfma_f32_16x16x32_bf16 v[156:159], v[220:223], a[252:255], v[156:159]\n"
"v_bfe_u32 v233, v248, 4, 5\n"
"s_lshl_b32 s50, s78, 8\n"
"s_waitcnt lgkmcnt(0)\n"
"v_mfma_f32_16x16x32_bf16 v[160:163], a[192:195], v[192:195], v[160:163]\n"
"v_mfma_f32_16x16x32_bf16 v[160:163], a[208:211], v[196:199], v[160:163]\n"
"s_add_u32 m0, s90, 3072\n"
"s_add_u32 s88, s91, 24704\n"
"v_mfma_f32_16x16x32_bf16 v[160:163], a[224:227], v[200:203], v[160:163]\n"
"buffer_load_dwordx4 v245, s[0:3], s88 offen lds\n"
"v_mfma_f32_16x16x32_bf16 v[160:163], a[240:243], v[204:207], v[160:163]\n"
"ds_read_b128 v[208:211], v240 offset:22528\n"
"v_cvt_pk_bf16_f32 v24, v24, v25\n"
"v_mfma_f32_16x16x32_bf16 v[164:167], a[196:199], v[192:195], v[164:167]\n"
"ds_read_b128 v[212:215], v241 offset:22528\n"
"v_cvt_pk_bf16_f32 v25, v26, v27\n"
"v_mfma_f32_16x16x32_bf16 v[164:167], a[212:215], v[196:199], v[164:167]\n"
"v_cvt_pk_bf16_f32 v26, v28, v29\n"
"s_add_u32 s50, s50, s77\n"
"v_mfma_f32_16x16x32_bf16 v[164:167], a[228:231], v[200:203], v[164:167]\n"
"ds_read_b128 v[216:219], v240 offset:30720\n"
"v_cvt_pk_bf16_f32 v27, v30, v31\n"
"v_mfma_f32_16x16x32_bf16 v[164:167], a[244:247], v[204:207], v[164:167]\n"
"buffer_store_dwordx4 v[24:27], v248, s[4:7], s36 offen offset:1024 sc1\n"
"v_mfma_f32_16x16x32_bf16 v[176:179], v[192:195], a[200:203], v[176:179]\n"
"s_lshr_b32 s58, s13, 3\n"
"s_lshl_b32 s58, s58, 2\n"
"v_mfma_f32_16x16x32_bf16 v[176:179], v[196:199], a[216:219], v[176:179]\n"
"ds_read_b128 v[220:223], v241 offset:30720\n"
"s_add_u32 s58, s58, s12\n"
"v_mfma_f32_16x16x32_bf16 v[176:179], v[200:203], a[232:235], v[176:179]\n"
"v_bfe_u32 v253, v248, 4, 1\n"
"v_lshl_add_u32 v233, v233, 3, s50\n"
"v_mfma_f32_16x16x32_bf16 v[176:179], v[204:207], a[248:251], v[176:179]\n"
"v_lshl_add_u32 v253, v253, 7, s58\n"
"v_cmp_gt_u32 vcc, 0x200, v248\n"
"v_mfma_f32_16x16x32_bf16 v[184:187], v[192:195], a[204:207], v[184:187]\n"
"v_lshlrev_b32 v253, 7, v253\n"
"v_cndmask_b32 v232, v233, v232, vcc\n"
"v_mfma_f32_16x16x32_bf16 v[184:187], v[196:199], a[220:223], v[184:187]\n"
"s_getpc_b64 s[58:59]\n"
"Lfused_ct30:\n"
"v_mfma_f32_16x16x32_bf16 v[184:187], v[200:203], a[236:239], v[184:187]\n"
"v_min_u32 v253, Lfused_fin-Lfused_ct30, v253\n"
"v_lshlrev_b32 v232, 3, v232\n"
"v_mfma_f32_16x16x32_bf16 v[184:187], v[204:207], a[252:255], v[184:187]\n"
"global_load_dword v249, v253, s[58:59]\n"
"s_waitcnt vmcnt(48) lgkmcnt(0)\n"
"v_mfma_f32_16x16x32_bf16 v[168:171], a[192:195], v[208:211], v[168:171]\n"
"s_barrier\n"
"v_mfma_f32_16x16x32_bf16 v[168:171], a[208:211], v[212:215], v[168:171]\n"
"ds_read_b128 v[192:195], v240 offset:32768\n"
"s_add_u32 m0, s90, 16384\n"
"v_mfma_f32_16x16x32_bf16 v[168:171], a[224:227], v[216:219], v[168:171]\n"
"s_add_u32 s88, s49, 128\n"
"v_add_u32 v251, 8192, v232\n"
"v_mfma_f32_16x16x32_bf16 v[168:171], a[240:243], v[220:223], v[168:171]\n"
"ds_read_b128 v[196:199], v241 offset:32768\n"
"v_mov_b32 v250, v248\n"
"v_mfma_f32_16x16x32_bf16 v[172:175], a[196:199], v[208:211], v[172:175]\n"
"buffer_load_dwordx4 v244, s[0:3], s88 offen lds\n"
"v_mfma_f32_16x16x32_bf16 v[172:175], a[212:215], v[212:215], v[172:175]\n"
"v_bfe_u32 v234, v248, 4, 4\n"
"v_lshrrev_b32 v235, 8, v248\n"
"v_mfma_f32_16x16x32_bf16 v[172:175], a[228:231], v[216:219], v[172:175]\n"
"ds_read_b128 v[200:203], v240 offset:40960\n"
"s_add_u32 m0, s90, 17408\n"
"v_mfma_f32_16x16x32_bf16 v[172:175], a[244:247], v[220:223], v[172:175]\n"
"s_add_u32 s88, s49, 8320\n"
"v_lshlrev_b32 v235, 2, v235\n"
"v_mfma_f32_16x16x32_bf16 v[180:183], v[208:211], a[200:203], v[180:183]\n"
"ds_read_b128 v[204:207], v241 offset:40960\n"
"v_sub_u32 v235, v234, v235\n"
"v_mfma_f32_16x16x32_bf16 v[180:183], v[212:215], a[216:219], v[180:183]\n"
"buffer_load_dwordx4 v245, s[0:3], s88 offen lds\n"
"v_mfma_f32_16x16x32_bf16 v[180:183], v[216:219], a[232:235], v[180:183]\n"
"v_cmp_gt_i32 s[64:65], 0, v235\n"
"v_cmp_gt_i32 s[66:67], 1, v235\n"
"v_mfma_f32_16x16x32_bf16 v[180:183], v[220:223], a[248:251], v[180:183]\n"
"s_add_u32 m0, s90, 18432\n"
"v_cmp_gt_i32 s[68:69], 2, v235\n"
"v_mfma_f32_16x16x32_bf16 v[188:191], v[208:211], a[204:207], v[188:191]\n"
"s_add_u32 s88, s49, 16512\n"
"v_cmp_gt_i32 s[70:71], 3, v235\n"
"v_mfma_f32_16x16x32_bf16 v[188:191], v[212:215], a[220:223], v[188:191]\n"
"s_lshr_b32 s50, s13, 1\n"
"s_and_b32 s51, s13, 1\n"
"v_mfma_f32_16x16x32_bf16 v[188:191], v[216:219], a[236:239], v[188:191]\n"
"buffer_load_dwordx4 v244, s[0:3], s88 offen lds\n"
"v_mfma_f32_16x16x32_bf16 v[188:191], v[220:223], a[252:255], v[188:191]\n"
"s_lshl_b32 s51, s51, 2\n"
"s_add_u32 s51, s51, s12\n"
"s_waitcnt lgkmcnt(0)\n"
"v_mfma_f32_16x16x32_bf16 v[64:67], a[0:3], v[192:195], 0\n"
"v_mfma_f32_16x16x32_bf16 v[64:67], a[16:19], v[196:199], v[64:67]\n"
"ds_read_b128 v[208:211], v240 offset:34816\n"
"s_lshr_b32 s52, s50, 5\n"
"v_mfma_f32_16x16x32_bf16 v[64:67], a[32:35], v[200:203], v[64:67]\n"
"s_lshl_b32 s52, s52, 2\n"
"s_and_b32 s53, s50, 31\n"
"v_mfma_f32_16x16x32_bf16 v[64:67], a[48:51], v[204:207], v[64:67]\n"
"ds_read_b128 v[212:215], v241 offset:34816\n"
"s_xor_b32 s53, s53, 24\n"
"v_mfma_f32_16x16x32_bf16 v[68:71], a[4:7], v[192:195], 0\n"
"s_add_u32 m0, s90, 19456\n"
"s_add_u32 s88, s49, 24704\n"
"v_mfma_f32_16x16x32_bf16 v[68:71], a[20:23], v[196:199], v[68:71]\n"
"buffer_load_dwordx4 v245, s[0:3], s88 offen lds\n"
"v_mfma_f32_16x16x32_bf16 v[68:71], a[36:39], v[200:203], v[68:71]\n"
"s_add_u32 s36, s15, 16384\n"
"v_mul_f32 v32, 1040356334, v32\n"
"v_mfma_f32_16x16x32_bf16 v[68:71], a[52:55], v[204:207], v[68:71]\n"
"ds_read_b128 v[216:219], v240 offset:43008\n"
"v_mul_f32 v33, 1040356334, v33\n"
"v_mfma_f32_16x16x32_bf16 v[80:83], v[192:195], a[8:11], 0\n"
"v_mul_f32 v34, 1040356334, v34\n"
"v_mul_f32 v35, 1040356334, v35\n"
"v_mfma_f32_16x16x32_bf16 v[80:83], v[196:199], a[24:27], v[80:83]\n"
"ds_read_b128 v[220:223], v241 offset:43008\n"
"v_mul_f32 v36, 1040356334, v36\n"
"v_mfma_f32_16x16x32_bf16 v[80:83], v[200:203], a[40:43], v[80:83]\n"
"v_mul_f32 v37, 1040356334, v37\n"
"s_add_u32 s52, s52, s53\n"
"v_mfma_f32_16x16x32_bf16 v[80:83], v[204:207], a[56:59], v[80:83]\n"
"v_mul_f32 v38, 1040356334, v38\n"
"v_mul_f32 v39, 1040356334, v39\n"
"v_mfma_f32_16x16x32_bf16 v[88:91], v[192:195], a[12:15], 0\n"
"v_cvt_pk_bf16_f32 v32, v32, v33\n"
"s_lshl_b32 s56, s51, 7\n"
"v_mfma_f32_16x16x32_bf16 v[88:91], v[196:199], a[28:31], v[88:91]\n"
"Lfused_hop3:\n"
"s_cbranch_execz Lfused_hop7\n"
"v_cvt_pk_bf16_f32 v33, v34, v35\n"
"v_cvt_pk_bf16_f32 v34, v36, v37\n"
"v_mfma_f32_16x16x32_bf16 v[88:91], v[200:203], a[44:47], v[88:91]\n"
"v_cvt_pk_bf16_f32 v35, v38, v39\n"
"s_lshl_b32 s54, s50, 15\n"
"v_mfma_f32_16x16x32_bf16 v[88:91], v[204:207], a[60:63], v[88:91]\n"
"buffer_store_dwordx4 v[32:35], v248, s[4:7], s36 offen offset:0 sc1\n"
"s_waitcnt lgkmcnt(0)\n"
"v_mfma_f32_16x16x32_bf16 v[72:75], a[0:3], v[208:211], 0\n"
"v_mfma_f32_16x16x32_bf16 v[72:75], a[16:19], v[212:215], v[72:75]\n"
"ds_read_b128 v[192:195], v240 offset:36864\n"
"v_mul_f32 v40, 1040356334, v40\n"
"v_mfma_f32_16x16x32_bf16 v[72:75], a[32:35], v[216:219], v[72:75]\n"
"v_mul_f32 v41, 1040356334, v41\n"
"s_add_u32 s54, s54, s56\n"
"v_mfma_f32_16x16x32_bf16 v[72:75], a[48:51], v[220:223], v[72:75]\n"
"ds_read_b128 v[196:199], v241 offset:36864\n"
"v_mul_f32 v42, 1040356334, v42\n"
"v_mfma_f32_16x16x32_bf16 v[76:79], a[4:7], v[208:211], 0\n"
"v_mul_f32 v43, 1040356334, v43\n"
"s_lshl_b32 s55, s52, 15\n"
"v_mfma_f32_16x16x32_bf16 v[76:79], a[20:23], v[212:215], v[76:79]\n"
"v_mul_f32 v44, 1040356334, v44\n"
"s_add_u32 s55, s55, s56\n"
"v_mfma_f32_16x16x32_bf16 v[76:79], a[36:39], v[216:219], v[76:79]\n"
"ds_read_b128 v[200:203], v240 offset:45056\n"
"v_mul_f32 v45, 1040356334, v45\n"
"v_mfma_f32_16x16x32_bf16 v[76:79], a[52:55], v[220:223], v[76:79]\n"
"v_mul_f32 v46, 1040356334, v46\n"
"s_mul_i32 s56, s51, 589824\n"
"v_mfma_f32_16x16x32_bf16 v[84:87], v[208:211], a[8:11], 0\n"
"ds_read_b128 v[204:207], v241 offset:45056\n"
"s_lshl_b32 s34, s50, 12\n"
"v_mfma_f32_16x16x32_bf16 v[84:87], v[212:215], a[24:27], v[84:87]\n"
"v_mul_f32 v47, 1040356334, v47\n"
"s_add_u32 s34, s34, s56\n"
"v_mfma_f32_16x16x32_bf16 v[84:87], v[216:219], a[40:43], v[84:87]\n"
"v_cvt_pk_bf16_f32 v40, v40, v41\n"
"s_add_u32 s52, s52, 128\n"
"v_mfma_f32_16x16x32_bf16 v[84:87], v[220:223], a[56:59], v[84:87]\n"
"v_cvt_pk_bf16_f32 v41, v42, v43\n"
"s_lshl_b32 s35, s52, 12\n"
"v_mfma_f32_16x16x32_bf16 v[92:95], v[208:211], a[12:15], 0\n"
"v_cvt_pk_bf16_f32 v42, v44, v45\n"
"s_add_u32 s35, s35, s56\n"
"v_mfma_f32_16x16x32_bf16 v[92:95], v[212:215], a[28:31], v[92:95]\n"
"v_cvt_pk_bf16_f32 v43, v46, v47\n"
"v_mfma_f32_16x16x32_bf16 v[92:95], v[216:219], a[44:47], v[92:95]\n"
"buffer_store_dwordx4 v[40:43], v248, s[4:7], s36 offen offset:1024 sc1\n"
"v_mfma_f32_16x16x32_bf16 v[92:95], v[220:223], a[60:63], v[92:95]\n"
"s_cmp_lt_u32 s53, 4\n"
"s_cselect_b32 s56, 524288, 0\n"
"s_waitcnt lgkmcnt(0)\n"
"v_mfma_f32_16x16x32_bf16 v[96:99], a[0:3], v[192:195], 0\n"
"v_mfma_f32_16x16x32_bf16 v[96:99], a[16:19], v[196:199], v[96:99]\n"
"ds_read_b128 v[208:211], v240 offset:38912\n"
"s_add_u32 s36, s15, 24576\n"
"v_mfma_f32_16x16x32_bf16 v[96:99], a[32:35], v[200:203], v[96:99]\n"
"s_add_u32 s72, s8, 57671680\n"
"s_addc_u32 s73, s9, 0\n"
"v_mfma_f32_16x16x32_bf16 v[96:99], a[48:51], v[204:207], v[96:99]\n"
"ds_read_b128 v[212:215], v241 offset:38912\n"
"v_cvt_pk_bf16_f32 v48, v48, v49\n"
"v_mfma_f32_16x16x32_bf16 v[100:103], a[4:7], v[192:195], 0\n"
"s_and_b32 s73, s73, 0xffff\n"
"s_mov_b32 s74, 4718592\n"
"v_mfma_f32_16x16x32_bf16 v[100:103], a[20:23], v[196:199], v[100:103]\n"
"s_mov_b32 s75, 0x20000\n"
"s_load_dwordx2 s[50:51], s[96:97], 56\n"
"v_mfma_f32_16x16x32_bf16 v[100:103], a[36:39], v[200:203], v[100:103]\n"
"ds_read_b128 v[216:219], v240 offset:47104\n"
"v_cvt_pk_bf16_f32 v49, v50, v51\n"
"v_mfma_f32_16x16x32_bf16 v[100:103], a[52:55], v[204:207], v[100:103]\n"
"s_load_dwordx2 s[52:53], s[96:97], 16\n"
"v_mfma_f32_16x16x32_bf16 v[112:115], v[192:195], a[8:11], 0\n"
"ds_read_b128 v[220:223], v241 offset:47104\n"
"v_cvt_pk_bf16_f32 v50, v52, v53\n"
"v_mfma_f32_16x16x32_bf16 v[112:115], v[196:199], a[24:27], v[112:115]\n"
"v_mfma_f32_16x16x32_bf16 v[112:115], v[200:203], a[40:43], v[112:115]\n"
"v_mfma_f32_16x16x32_bf16 v[112:115], v[204:207], a[56:59], v[112:115]\n"
"v_cvt_pk_bf16_f32 v51, v54, v55\n"
"v_mfma_f32_16x16x32_bf16 v[120:123], v[192:195], a[12:15], 0\n"
"v_mfma_f32_16x16x32_bf16 v[120:123], v[196:199], a[28:31], v[120:123]\n"
"buffer_store_dwordx4 v[48:51], v248, s[4:7], s36 offen offset:0 sc1\n"
"v_mfma_f32_16x16x32_bf16 v[120:123], v[200:203], a[44:47], v[120:123]\n"
"v_mfma_f32_16x16x32_bf16 v[120:123], v[204:207], a[60:63], v[120:123]\n"
"s_waitcnt vmcnt(51) lgkmcnt(0)\n"
"v_mfma_f32_16x16x32_bf16 v[104:107], a[0:3], v[208:211], 0\n"
"s_barrier\n"
"v_mfma_f32_16x16x32_bf16 v[104:107], a[16:19], v[212:215], v[104:107]\n"
"ds_read_b128 v[192:195], v240 offset:49152\n"
"s_add_u32 m0, s90, 32768\n"
"v_mfma_f32_16x16x32_bf16 v[104:107], a[32:35], v[216:219], v[104:107]\n"
"s_and_b32 s51, s51, 0xffff\n"
"s_and_b32 s53, s53, 0xffff\n"
"v_mfma_f32_16x16x32_bf16 v[104:107], a[48:51], v[220:223], v[104:107]\n"
"buffer_load_dwordx4 v246, s[84:87], s92 offen lds\n"
"v_mfma_f32_16x16x32_bf16 v[108:111], a[4:7], v[208:211], 0\n"
"ds_read_b128 v[196:199], v241 offset:49152\n"
"v_mfma_f32_16x16x32_bf16 v[108:111], a[20:23], v[212:215], v[108:111]\n"
"v_cvt_pk_bf16_f32 v56, v56, v57\n"
"v_mfma_f32_16x16x32_bf16 v[108:111], a[36:39], v[216:219], v[108:111]\n"
"ds_read_b128 v[200:203], v240 offset:57344\n"
"v_mfma_f32_16x16x32_bf16 v[108:111], a[52:55], v[220:223], v[108:111]\n"
"v_cvt_pk_bf16_f32 v57, v58, v59\n"
"v_mfma_f32_16x16x32_bf16 v[116:119], v[208:211], a[8:11], 0\n"
"ds_read_b128 v[204:207], v241 offset:57344\n"
"v_mfma_f32_16x16x32_bf16 v[116:119], v[212:215], a[24:27], v[116:119]\n"
"v_cvt_pk_bf16_f32 v58, v60, v61\n"
"v_mfma_f32_16x16x32_bf16 v[116:119], v[216:219], a[40:43], v[116:119]\n"
"v_mfma_f32_16x16x32_bf16 v[116:119], v[220:223], a[56:59], v[116:119]\n"
"v_cvt_pk_bf16_f32 v59, v62, v63\n"
"v_mfma_f32_16x16x32_bf16 v[124:127], v[208:211], a[12:15], 0\n"
"v_mfma_f32_16x16x32_bf16 v[124:127], v[212:215], a[28:31], v[124:127]\n"
"buffer_store_dwordx4 v[56:59], v248, s[4:7], s36 offen offset:1024 sc1\n"
"v_mfma_f32_16x16x32_bf16 v[124:127], v[216:219], a[44:47], v[124:127]\n"
"v_mfma_f32_16x16x32_bf16 v[124:127], v[220:223], a[60:63], v[124:127]\n"
"s_waitcnt lgkmcnt(0)\n"
"v_mfma_f32_16x16x32_bf16 v[64:67], a[64:67], v[192:195], v[64:67]\n"
"v_mfma_f32_16x16x32_bf16 v[64:67], a[80:83], v[196:199], v[64:67]\n"
"ds_read_b128 v[208:211], v240 offset:51200\n"
"s_add_u32 m0, s90, 33792\n"
"v_mfma_f32_16x16x32_bf16 v[64:67], a[96:99], v[200:203], v[64:67]\n"
"s_add_u32 s88, s92, s94\n"
"v_mfma_f32_16x16x32_bf16 v[64:67], a[112:115], v[204:207], v[64:67]\n"
"ds_read_b128 v[212:215], v241 offset:51200\n"
"v_mfma_f32_16x16x32_bf16 v[68:71], a[68:71], v[192:195], v[68:71]\n"
"buffer_load_dwordx4 v246, s[84:87], s88 offen lds\n"
"v_mfma_f32_16x16x32_bf16 v[68:71], a[84:87], v[196:199], v[68:71]\n"
"s_add_u32 m0, s90, 34816\n"
"v_mfma_f32_16x16x32_bf16 v[68:71], a[100:103], v[200:203], v[68:71]\n"
"ds_read_b128 v[216:219], v240 offset:59392\n"
"v_mfma_f32_16x16x32_bf16 v[68:71], a[116:119], v[204:207], v[68:71]\n"
"s_add_u32 s89, s92, s93\n"
"v_mfma_f32_16x16x32_bf16 v[80:83], v[192:195], a[72:75], v[80:83]\n"
"buffer_load_dwordx4 v247, s[84:87], s89 offen lds\n"
"v_mfma_f32_16x16x32_bf16 v[80:83], v[196:199], a[88:91], v[80:83]\n"
"ds_read_b128 v[220:223], v241 offset:59392\n"
"v_mfma_f32_16x16x32_bf16 v[80:83], v[200:203], a[104:107], v[80:83]\n"
"s_add_u32 m0, s90, 35840\n"
"v_mfma_f32_16x16x32_bf16 v[80:83], v[204:207], a[120:123], v[80:83]\n"
"s_add_u32 s88, s89, s94\n"
"v_mfma_f32_16x16x32_bf16 v[88:91], v[192:195], a[76:79], v[88:91]\n"
"v_mfma_f32_16x16x32_bf16 v[88:91], v[196:199], a[92:95], v[88:91]\n"
"buffer_load_dwordx4 v247, s[84:87], s88 offen lds\n"
"v_mfma_f32_16x16x32_bf16 v[88:91], v[200:203], a[108:111], v[88:91]\n"
"s_add_u32 s92, s92, s95\n"
"v_mfma_f32_16x16x32_bf16 v[88:91], v[204:207], a[124:127], v[88:91]\n"
"s_waitcnt lgkmcnt(0)\n"
"v_mfma_f32_16x16x32_bf16 v[72:75], a[64:67], v[208:211], v[72:75]\n"
"v_mfma_f32_16x16x32_bf16 v[72:75], a[80:83], v[212:215], v[72:75]\n"
"ds_read_b128 v[192:195], v240 offset:53248\n"
"s_add_u32 s36, s15, 65536\n"
"v_mfma_f32_16x16x32_bf16 v[72:75], a[96:99], v[216:219], v[72:75]\n"
"v_mul_f32 v128, 1040356334, v128\n"
"v_mfma_f32_16x16x32_bf16 v[72:75], a[112:115], v[220:223], v[72:75]\n"
"ds_read_b128 v[196:199], v241 offset:53248\n"
"v_mul_f32 v129, 1040356334, v129\n"
"v_mfma_f32_16x16x32_bf16 v[76:79], a[68:71], v[208:211], v[76:79]\n"
"v_mul_f32 v130, 1040356334, v130\n"
"v_mfma_f32_16x16x32_bf16 v[76:79], a[84:87], v[212:215], v[76:79]\n"
"v_mul_f32 v131, 1040356334, v131\n"
"v_mfma_f32_16x16x32_bf16 v[76:79], a[100:103], v[216:219], v[76:79]\n"
"ds_read_b128 v[200:203], v240 offset:61440\n"
"v_mul_f32 v132, 1040356334, v132\n"
"v_mfma_f32_16x16x32_bf16 v[76:79], a[116:119], v[220:223], v[76:79]\n"
"v_mul_f32 v133, 1040356334, v133\n"
"v_mfma_f32_16x16x32_bf16 v[84:87], v[208:211], a[72:75], v[84:87]\n"
"ds_read_b128 v[204:207], v241 offset:61440\n"
"v_mul_f32 v134, 1040356334, v134\n"
"v_mfma_f32_16x16x32_bf16 v[84:87], v[212:215], a[88:91], v[84:87]\n"
"v_mul_f32 v135, 1040356334, v135\n"
"v_mfma_f32_16x16x32_bf16 v[84:87], v[216:219], a[104:107], v[84:87]\n"
"v_cvt_pk_bf16_f32 v128, v128, v129\n"
"v_mfma_f32_16x16x32_bf16 v[84:87], v[220:223], a[120:123], v[84:87]\n"
"v_cvt_pk_bf16_f32 v129, v130, v131\n"
"v_mfma_f32_16x16x32_bf16 v[92:95], v[208:211], a[76:79], v[92:95]\n"
"v_cvt_pk_bf16_f32 v130, v132, v133\n"
"v_mfma_f32_16x16x32_bf16 v[92:95], v[212:215], a[92:95], v[92:95]\n"
"v_cvt_pk_bf16_f32 v131, v134, v135\n"
"v_mfma_f32_16x16x32_bf16 v[92:95], v[216:219], a[108:111], v[92:95]\n"
"buffer_store_dwordx4 v[128:131], v248, s[4:7], s36 offen offset:0 sc1\n"
"v_mfma_f32_16x16x32_bf16 v[92:95], v[220:223], a[124:127], v[92:95]\n"
"s_waitcnt lgkmcnt(0)\n"
"v_mfma_f32_16x16x32_bf16 v[96:99], a[64:67], v[192:195], v[96:99]\n"
"v_mfma_f32_16x16x32_bf16 v[96:99], a[80:83], v[196:199], v[96:99]\n"
"ds_read_b128 v[208:211], v240 offset:55296\n"
"v_mul_f32 v136, 1040356334, v136\n"
"v_mfma_f32_16x16x32_bf16 v[96:99], a[96:99], v[200:203], v[96:99]\n"
"v_mul_f32 v137, 1040356334, v137\n"
"v_mfma_f32_16x16x32_bf16 v[96:99], a[112:115], v[204:207], v[96:99]\n"
"ds_read_b128 v[212:215], v241 offset:55296\n"
"v_mul_f32 v138, 1040356334, v138\n"
"v_mfma_f32_16x16x32_bf16 v[100:103], a[68:71], v[192:195], v[100:103]\n"
"v_mul_f32 v139, 1040356334, v139\n"
"v_mfma_f32_16x16x32_bf16 v[100:103], a[84:87], v[196:199], v[100:103]\n"
"v_mul_f32 v140, 1040356334, v140\n"
"v_mfma_f32_16x16x32_bf16 v[100:103], a[100:103], v[200:203], v[100:103]\n"
"ds_read_b128 v[216:219], v240 offset:63488\n"
"v_mul_f32 v141, 1040356334, v141\n"
"v_mfma_f32_16x16x32_bf16 v[100:103], a[116:119], v[204:207], v[100:103]\n"
"v_mul_f32 v142, 1040356334, v142\n"
"v_mfma_f32_16x16x32_bf16 v[112:115], v[192:195], a[72:75], v[112:115]\n"
"ds_read_b128 v[220:223], v241 offset:63488\n"
"v_mfma_f32_16x16x32_bf16 v[112:115], v[196:199], a[88:91], v[112:115]\n"
"v_mul_f32 v143, 1040356334, v143\n"
"v_mfma_f32_16x16x32_bf16 v[112:115], v[200:203], a[104:107], v[112:115]\n"
"v_cvt_pk_bf16_f32 v136, v136, v137\n"
"v_mfma_f32_16x16x32_bf16 v[112:115], v[204:207], a[120:123], v[112:115]\n"
"v_cvt_pk_bf16_f32 v137, v138, v139\n"
"v_mfma_f32_16x16x32_bf16 v[120:123], v[192:195], a[76:79], v[120:123]\n"
"v_cvt_pk_bf16_f32 v138, v140, v141\n"
"v_mfma_f32_16x16x32_bf16 v[120:123], v[196:199], a[92:95], v[120:123]\n"
"v_cvt_pk_bf16_f32 v139, v142, v143\n"
"v_mfma_f32_16x16x32_bf16 v[120:123], v[200:203], a[108:111], v[120:123]\n"
"buffer_store_dwordx4 v[136:139], v248, s[4:7], s36 offen offset:1024 sc1\n"
"v_mfma_f32_16x16x32_bf16 v[120:123], v[204:207], a[124:127], v[120:123]\n"
"s_waitcnt vmcnt(54) lgkmcnt(0)\n"
"v_mfma_f32_16x16x32_bf16 v[104:107], a[64:67], v[208:211], v[104:107]\n"
"s_barrier\n"
"v_mfma_f32_16x16x32_bf16 v[104:107], a[80:83], v[212:215], v[104:107]\n"
"v_add_u32 v240, 65536, v240\n"
"s_add_u32 m0, s90, 49152\n"
"v_mfma_f32_16x16x32_bf16 v[104:107], a[96:99], v[216:219], v[104:107]\n"
"buffer_load_dwordx4 v246, s[84:87], s92 offen lds\n"
"v_mfma_f32_16x16x32_bf16 v[104:107], a[112:115], v[220:223], v[104:107]\n"
"v_add_u32 v241, 65536, v241\n"
"v_mfma_f32_16x16x32_bf16 v[108:111], a[68:71], v[208:211], v[108:111]\n"
"ds_read_b128 v[192:195], v240 offset:0\n"
"s_add_u32 s36, s15, 73728\n"
"v_mfma_f32_16x16x32_bf16 v[108:111], a[84:87], v[212:215], v[108:111]\n"
"v_mfma_f32_16x16x32_bf16 v[108:111], a[100:103], v[216:219], v[108:111]\n"
"ds_read_b128 v[196:199], v241 offset:0\n"
"v_cvt_pk_bf16_f32 v144, v144, v145\n"
"v_mfma_f32_16x16x32_bf16 v[108:111], a[116:119], v[220:223], v[108:111]\n"
"ds_read_b128 v[200:203], v240 offset:8192\n"
"v_mfma_f32_16x16x32_bf16 v[116:119], v[208:211], a[72:75], v[116:119]\n"
"v_cvt_pk_bf16_f32 v145, v146, v147\n"
"v_mfma_f32_16x16x32_bf16 v[116:119], v[212:215], a[88:91], v[116:119]\n"
"ds_read_b128 v[204:207], v241 offset:8192\n"
"v_mfma_f32_16x16x32_bf16 v[116:119], v[216:219], a[104:107], v[116:119]\n"
"v_cvt_pk_bf16_f32 v146, v148, v149\n"
"v_mfma_f32_16x16x32_bf16 v[116:119], v[220:223], a[120:123], v[116:119]\n"
"v_mfma_f32_16x16x32_bf16 v[124:127], v[208:211], a[76:79], v[124:127]\n"
"v_cvt_pk_bf16_f32 v147, v150, v151\n"
"v_mfma_f32_16x16x32_bf16 v[124:127], v[212:215], a[92:95], v[124:127]\n"
"v_mfma_f32_16x16x32_bf16 v[124:127], v[216:219], a[108:111], v[124:127]\n"
"buffer_store_dwordx4 v[144:147], v248, s[4:7], s36 offen offset:0 sc1\n"
"v_mfma_f32_16x16x32_bf16 v[124:127], v[220:223], a[124:127], v[124:127]\n"
"s_waitcnt lgkmcnt(0)\n"
"v_mfma_f32_16x16x32_bf16 v[64:67], a[128:131], v[192:195], v[64:67]\n"
"v_mfma_f32_16x16x32_bf16 v[64:67], a[144:147], v[196:199], v[64:67]\n"
"ds_read_b128 v[208:211], v240 offset:2048\n"
"s_add_u32 m0, s90, 50176\n"
"v_mfma_f32_16x16x32_bf16 v[64:67], a[160:163], v[200:203], v[64:67]\n"
"s_add_u32 s88, s92, s94\n"
"v_mfma_f32_16x16x32_bf16 v[64:67], a[176:179], v[204:207], v[64:67]\n"
"ds_read_b128 v[212:215], v241 offset:2048\n"
"v_mfma_f32_16x16x32_bf16 v[68:71], a[132:135], v[192:195], v[68:71]\n"
"buffer_load_dwordx4 v246, s[84:87], s88 offen lds\n"
"v_mfma_f32_16x16x32_bf16 v[68:71], a[148:151], v[196:199], v[68:71]\n"
"v_mfma_f32_16x16x32_bf16 v[68:71], a[164:167], v[200:203], v[68:71]\n"
"ds_read_b128 v[216:219], v240 offset:10240\n"
"v_cvt_pk_bf16_f32 v152, v152, v153\n"
"v_mfma_f32_16x16x32_bf16 v[68:71], a[180:183], v[204:207], v[68:71]\n"
"v_mfma_f32_16x16x32_bf16 v[80:83], v[192:195], a[136:139], v[80:83]\n"
"ds_read_b128 v[220:223], v241 offset:10240\n"
"v_cvt_pk_bf16_f32 v153, v154, v155\n"
"v_mfma_f32_16x16x32_bf16 v[80:83], v[196:199], a[152:155], v[80:83]\n"
"v_mfma_f32_16x16x32_bf16 v[80:83], v[200:203], a[168:171], v[80:83]\n"
"v_cvt_pk_bf16_f32 v154, v156, v157\n"
"v_mfma_f32_16x16x32_bf16 v[80:83], v[204:207], a[184:187], v[80:83]\n"
"v_mfma_f32_16x16x32_bf16 v[88:91], v[192:195], a[140:143], v[88:91]\n"
"v_cvt_pk_bf16_f32 v155, v158, v159\n"
"v_mfma_f32_16x16x32_bf16 v[88:91], v[196:199], a[156:159], v[88:91]\n"
"v_mfma_f32_16x16x32_bf16 v[88:91], v[200:203], a[172:175], v[88:91]\n"
"buffer_store_dwordx4 v[152:155], v248, s[4:7], s36 offen offset:1024 sc1\n"
"v_mfma_f32_16x16x32_bf16 v[88:91], v[204:207], a[188:191], v[88:91]\n"
"s_waitcnt lgkmcnt(0)\n"
"v_mfma_f32_16x16x32_bf16 v[72:75], a[128:131], v[208:211], v[72:75]\n"
"v_mfma_f32_16x16x32_bf16 v[72:75], a[144:147], v[212:215], v[72:75]\n"
"ds_read_b128 v[192:195], v240 offset:4096\n"
"s_add_u32 m0, s90, 51200\n"
"v_mfma_f32_16x16x32_bf16 v[72:75], a[160:163], v[216:219], v[72:75]\n"
"v_mfma_f32_16x16x32_bf16 v[72:75], a[176:179], v[220:223], v[72:75]\n"
"ds_read_b128 v[196:199], v241 offset:4096\n"
"s_add_u32 s89, s92, s93\n"
"v_mfma_f32_16x16x32_bf16 v[76:79], a[132:135], v[208:211], v[76:79]\n"
"v_mfma_f32_16x16x32_bf16 v[76:79], a[148:151], v[212:215], v[76:79]\n"
"buffer_load_dwordx4 v247, s[84:87], s89 offen lds\n"
"v_mfma_f32_16x16x32_bf16 v[76:79], a[164:167], v[216:219], v[76:79]\n"
"ds_read_b128 v[200:203], v240 offset:12288\n"
"v_mfma_f32_16x16x32_bf16 v[76:79], a[180:183], v[220:223], v[76:79]\n"
"s_add_u32 m0, s90, 52224\n"
"v_mfma_f32_16x16x32_bf16 v[84:87], v[208:211], a[136:139], v[84:87]\n"
"ds_read_b128 v[204:207], v241 offset:12288\n"
"v_mfma_f32_16x16x32_bf16 v[84:87], v[212:215], a[152:155], v[84:87]\n"
"s_add_u32 s88, s89, s94\n"
"v_mfma_f32_16x16x32_bf16 v[84:87], v[216:219], a[168:171], v[84:87]\n"
"v_mfma_f32_16x16x32_bf16 v[84:87], v[220:223], a[184:187], v[84:87]\n"
"buffer_load_dwordx4 v247, s[84:87], s88 offen lds\n"
"v_mfma_f32_16x16x32_bf16 v[92:95], v[208:211], a[140:143], v[92:95]\n"
"v_mfma_f32_16x16x32_bf16 v[92:95], v[212:215], a[156:159], v[92:95]\n"
"s_add_u32 s92, s92, s95\n"
"v_mfma_f32_16x16x32_bf16 v[92:95], v[216:219], a[172:175], v[92:95]\n"
"v_mfma_f32_16x16x32_bf16 v[92:95], v[220:223], a[188:191], v[92:95]\n"
"s_waitcnt lgkmcnt(0)\n"
"v_mfma_f32_16x16x32_bf16 v[96:99], a[128:131], v[192:195], v[96:99]\n"
"v_mfma_f32_16x16x32_bf16 v[96:99], a[144:147], v[196:199], v[96:99]\n"
"ds_read_b128 v[208:211], v240 offset:6144\n"
"s_add_u32 s36, s15, 81920\n"
"v_mfma_f32_16x16x32_bf16 v[96:99], a[160:163], v[200:203], v[96:99]\n"
"v_mul_f32 v160, 1040356334, v160\n"
"v_mfma_f32_16x16x32_bf16 v[96:99], a[176:179], v[204:207], v[96:99]\n"
"ds_read_b128 v[212:215], v241 offset:6144\n"
"v_mul_f32 v161, 1040356334, v161\n"
"v_mfma_f32_16x16x32_bf16 v[100:103], a[132:135], v[192:195], v[100:103]\n"
"v_mul_f32 v162, 1040356334, v162\n"
"v_mfma_f32_16x16x32_bf16 v[100:103], a[148:151], v[196:199], v[100:103]\n"
"v_mul_f32 v163, 1040356334, v163\n"
"v_mfma_f32_16x16x32_bf16 v[100:103], a[164:167], v[200:203], v[100:103]\n"
"ds_read_b128 v[216:219], v240 offset:14336\n"
"v_mul_f32 v164, 1040356334, v164\n"
"v_mfma_f32_16x16x32_bf16 v[100:103], a[180:183], v[204:207], v[100:103]\n"
"v_mul_f32 v165, 1040356334, v165\n"
"v_mfma_f32_16x16x32_bf16 v[112:115], v[192:195], a[136:139], v[112:115]\n"
"ds_read_b128 v[220:223], v241 offset:14336\n"
"v_mul_f32 v166, 1040356334, v166\n"
"v_mfma_f32_16x16x32_bf16 v[112:115], v[196:199], a[152:155], v[112:115]\n"
"v_mul_f32 v167, 1040356334, v167\n"
"v_mfma_f32_16x16x32_bf16 v[112:115], v[200:203], a[168:171], v[112:115]\n"
"v_cvt_pk_bf16_f32 v160, v160, v161\n"
"v_mfma_f32_16x16x32_bf16 v[112:115], v[204:207], a[184:187], v[112:115]\n"
"v_cvt_pk_bf16_f32 v161, v162, v163\n"
"v_mfma_f32_16x16x32_bf16 v[120:123], v[192:195], a[140:143], v[120:123]\n"
"v_cvt_pk_bf16_f32 v162, v164, v165\n"
"v_mfma_f32_16x16x32_bf16 v[120:123], v[196:199], a[156:159], v[120:123]\n"
"v_cvt_pk_bf16_f32 v163, v166, v167\n"
"v_mfma_f32_16x16x32_bf16 v[120:123], v[200:203], a[172:175], v[120:123]\n"
"buffer_store_dwordx4 v[160:163], v248, s[4:7], s36 offen offset:0 sc1\n"
"v_mfma_f32_16x16x32_bf16 v[120:123], v[204:207], a[188:191], v[120:123]\n"
"s_waitcnt vmcnt(30) lgkmcnt(0)\n"
"v_mfma_f32_16x16x32_bf16 v[104:107], a[128:131], v[208:211], v[104:107]\n"
"s_barrier\n"
"v_mfma_f32_16x16x32_bf16 v[104:107], a[144:147], v[212:215], v[104:107]\n"
"s_add_u32 m0, s90, 65536\n"
"s_add_u32 s88, s91, 256\n"
"v_mfma_f32_16x16x32_bf16 v[104:107], a[160:163], v[216:219], v[104:107]\n"
"buffer_load_dwordx4 v244, s[0:3], s88 offen lds\n"
"v_mfma_f32_16x16x32_bf16 v[104:107], a[176:179], v[220:223], v[104:107]\n"
"ds_read_b128 v[192:195], v240 offset:16384\n"
"v_mul_f32 v168, 1040356334, v168\n"
"v_mfma_f32_16x16x32_bf16 v[108:111], a[132:135], v[208:211], v[108:111]\n"
"ds_read_b128 v[196:199], v241 offset:16384\n"
"v_mul_f32 v169, 1040356334, v169\n"
"v_mfma_f32_16x16x32_bf16 v[108:111], a[148:151], v[212:215], v[108:111]\n"
"v_mul_f32 v170, 1040356334, v170\n"
"v_mfma_f32_16x16x32_bf16 v[108:111], a[164:167], v[216:219], v[108:111]\n"
"ds_read_b128 v[200:203], v240 offset:24576\n"
"v_mul_f32 v171, 1040356334, v171\n"
"v_mfma_f32_16x16x32_bf16 v[108:111], a[180:183], v[220:223], v[108:111]\n"
"v_mul_f32 v172, 1040356334, v172\n"
"v_mfma_f32_16x16x32_bf16 v[116:119], v[208:211], a[136:139], v[116:119]\n"
"ds_read_b128 v[204:207], v241 offset:24576\n"
"v_mul_f32 v173, 1040356334, v173\n"
"v_mfma_f32_16x16x32_bf16 v[116:119], v[212:215], a[152:155], v[116:119]\n"
"v_mul_f32 v174, 1040356334, v174\n"
"v_mfma_f32_16x16x32_bf16 v[116:119], v[216:219], a[168:171], v[116:119]\n"
"v_mul_f32 v175, 1040356334, v175\n"
"v_mfma_f32_16x16x32_bf16 v[116:119], v[220:223], a[184:187], v[116:119]\n"
"v_cvt_pk_bf16_f32 v168, v168, v169\n"
"v_mfma_f32_16x16x32_bf16 v[124:127], v[208:211], a[140:143], v[124:127]\n"
"v_cvt_pk_bf16_f32 v169, v170, v171\n"
"v_mfma_f32_16x16x32_bf16 v[124:127], v[212:215], a[156:159], v[124:127]\n"
"v_cvt_pk_bf16_f32 v170, v172, v173\n"
"v_mfma_f32_16x16x32_bf16 v[124:127], v[216:219], a[172:175], v[124:127]\n"
"v_cvt_pk_bf16_f32 v171, v174, v175\n"
"v_mfma_f32_16x16x32_bf16 v[124:127], v[220:223], a[188:191], v[124:127]\n"
"buffer_store_dwordx4 v[168:171], v248, s[4:7], s36 offen offset:1024 sc1\n"
"s_waitcnt lgkmcnt(0)\n"
"v_mfma_f32_16x16x32_bf16 v[64:67], a[192:195], v[192:195], v[64:67]\n"
"v_mfma_f32_16x16x32_bf16 v[64:67], a[208:211], v[196:199], v[64:67]\n"
"ds_read_b128 v[208:211], v240 offset:18432\n"
"s_add_u32 m0, s90, 66560\n"
"v_mfma_f32_16x16x32_bf16 v[64:67], a[224:227], v[200:203], v[64:67]\n"
"ds_read_b128 v[212:215], v241 offset:18432\n"
"s_add_u32 s88, s91, 8448\n"
"v_mfma_f32_16x16x32_bf16 v[64:67], a[240:243], v[204:207], v[64:67]\n"
"ds_read_b128 v[216:219], v240 offset:26624\n"
"ds_read_b128 v[220:223], v241 offset:26624\n"
"v_mfma_f32_16x16x32_bf16 v[68:71], a[196:199], v[192:195], v[68:71]\n"
"buffer_load_dwordx4 v245, s[0:3], s88 offen lds\n"
"v_mfma_f32_16x16x32_bf16 v[68:71], a[212:215], v[196:199], v[68:71]\n"
"ds_read_b64_tr_b16 a[112:113], v242 offset:57344\n"
"ds_read_b64_tr_b16 a[114:115], v242 offset:57600\n"
"v_mfma_f32_16x16x32_bf16 v[68:71], a[228:231], v[200:203], v[68:71]\n"
"ds_read_b64_tr_b16 a[120:121], v243 offset:57344\n"
"s_add_u32 s36, s15, 90112\n"
"v_mfma_f32_16x16x32_bf16 v[68:71], a[244:247], v[204:207], v[68:71]\n"
"ds_read_b64_tr_b16 a[122:123], v243 offset:57600\n"
"v_cvt_pk_bf16_f32 v176, v176, v177\n"
"v_mfma_f32_16x16x32_bf16 v[80:83], v[192:195], a[200:203], v[80:83]\n"
"ds_read_b64_tr_b16 a[128:129], v242 offset:57856\n"
"v_mfma_f32_16x16x32_bf16 v[80:83], v[196:199], a[216:219], v[80:83]\n"
"ds_read_b64_tr_b16 a[130:131], v242 offset:58112\n"
"v_cvt_pk_bf16_f32 v177, v178, v179\n"
"v_mfma_f32_16x16x32_bf16 v[80:83], v[200:203], a[232:235], v[80:83]\n"
"ds_read_b64_tr_b16 a[136:137], v242 offset:49152\n"
"v_mfma_f32_16x16x32_bf16 v[80:83], v[204:207], a[248:251], v[80:83]\n"
"v_cvt_pk_bf16_f32 v178, v180, v181\n"
"v_mfma_f32_16x16x32_bf16 v[88:91], v[192:195], a[204:207], v[88:91]\n"
"v_cvt_pk_bf16_f32 v179, v182, v183\n"
"v_mfma_f32_16x16x32_bf16 v[88:91], v[196:199], a[220:223], v[88:91]\n"
"v_mfma_f32_16x16x32_bf16 v[88:91], v[200:203], a[236:239], v[88:91]\n"
"buffer_store_dwordx4 v[176:179], v248, s[4:7], s36 offen offset:0 sc1\n"
"v_mfma_f32_16x16x32_bf16 v[88:91], v[204:207], a[252:255], v[88:91]\n"
"s_waitcnt lgkmcnt(0)\n"
"v_mfma_f32_16x16x32_bf16 v[72:75], a[192:195], v[208:211], v[72:75]\n"
"v_mfma_f32_16x16x32_bf16 v[72:75], a[208:211], v[212:215], v[72:75]\n"
"s_add_u32 m0, s90, 67584\n"
"s_add_u32 s88, s91, 16640\n"
"v_mfma_f32_16x16x32_bf16 v[72:75], a[224:227], v[216:219], v[72:75]\n"
"buffer_load_dwordx4 v244, s[0:3], s88 offen lds\n"
"v_mfma_f32_16x16x32_bf16 v[72:75], a[240:243], v[220:223], v[72:75]\n"
"ds_read_b128 v[192:195], v240 offset:20480\n"
"v_cvt_pk_bf16_f32 v184, v184, v185\n"
"v_mfma_f32_16x16x32_bf16 v[76:79], a[196:199], v[208:211], v[76:79]\n"
"ds_read_b128 v[196:199], v241 offset:20480\n"
"v_cvt_pk_bf16_f32 v185, v186, v187\n"
"v_mfma_f32_16x16x32_bf16 v[76:79], a[212:215], v[212:215], v[76:79]\n"
"ds_read_b128 v[200:203], v240 offset:28672\n"
"v_cvt_pk_bf16_f32 v186, v188, v189\n"
"v_mfma_f32_16x16x32_bf16 v[76:79], a[228:231], v[216:219], v[76:79]\n"
"ds_read_b128 v[204:207], v241 offset:28672\n"
"v_cvt_pk_bf16_f32 v187, v190, v191\n"
"v_mfma_f32_16x16x32_bf16 v[76:79], a[244:247], v[220:223], v[76:79]\n"
"buffer_store_dwordx4 v[184:187], v248, s[4:7], s36 offen offset:1024 sc1\n"
"v_mfma_f32_16x16x32_bf16 v[84:87], v[208:211], a[200:203], v[84:87]\n"
"s_lshr_b32 s58, s13, 3\n"
"s_lshl_b32 s58, s58, 2\n"
"v_mfma_f32_16x16x32_bf16 v[84:87], v[212:215], a[216:219], v[84:87]\n"
"ds_read_b64_tr_b16 a[138:139], v242 offset:49408\n"
"s_add_u32 s58, s58, s12\n"
"v_mfma_f32_16x16x32_bf16 v[84:87], v[216:219], a[232:235], v[84:87]\n"
"ds_read_b64_tr_b16 a[144:145], v243 offset:49152\n"
"v_bfe_u32 v253, v248, 4, 1\n"
"v_mfma_f32_16x16x32_bf16 v[84:87], v[220:223], a[248:251], v[84:87]\n"
"ds_read_b64_tr_b16 a[146:147], v243 offset:49408\n"
"v_lshl_add_u32 v253, v253, 7, s58\n"
"v_mfma_f32_16x16x32_bf16 v[92:95], v[208:211], a[204:207], v[92:95]\n"
"v_add_u32 v242, 16384, v242\n"
"v_lshlrev_b32 v253, 7, v253\n"
"v_mfma_f32_16x16x32_bf16 v[92:95], v[212:215], a[220:223], v[92:95]\n"
"s_getpc_b64 s[58:59]\n"
"Lfused_ct45:\n"
"v_mfma_f32_16x16x32_bf16 v[92:95], v[216:219], a[236:239], v[92:95]\n"
"v_add_u32 v243, 16384, v243\n"
"v_min_u32 v253, Lfused_fin-Lfused_ct45, v253\n"
"v_mfma_f32_16x16x32_bf16 v[92:95], v[220:223], a[252:255], v[92:95]\n"
"ds_read_b64_tr_b16 a[116:117], v242 offset:57344\n"
"ds_read_b64_tr_b16 a[118:119], v242 offset:57600\n"
"global_load_dword v249, v253, s[58:59]\n"
"s_waitcnt lgkmcnt(0)\n"
"v_mfma_f32_16x16x32_bf16 v[96:99], a[192:195], v[192:195], v[96:99]\n"
"v_mfma_f32_16x16x32_bf16 v[96:99], a[208:211], v[196:199], v[96:99]\n"
"ds_read_b128 v[208:211], v240 offset:22528\n"
"s_add_u32 m0, s90, 68608\n"
"v_mfma_f32_16x16x32_bf16 v[96:99], a[224:227], v[200:203], v[96:99]\n"
"ds_read_b128 v[212:215], v241 offset:22528\n"
"ds_read_b128 v[216:219], v240 offset:30720\n"
"v_mfma_f32_16x16x32_bf16 v[96:99], a[240:243], v[204:207], v[96:99]\n"
"ds_read_b128 v[220:223], v241 offset:30720\n"
"v_mfma_f32_16x16x32_bf16 v[100:103], a[196:199], v[192:195], v[100:103]\n"
"ds_read_b64_tr_b16 a[124:125], v243 offset:57344\n"
"v_mfma_f32_16x16x32_bf16 v[100:103], a[212:215], v[196:199], v[100:103]\n"
"ds_read_b64_tr_b16 a[126:127], v243 offset:57600\n"
"v_mfma_f32_16x16x32_bf16 v[100:103], a[228:231], v[200:203], v[100:103]\n"
"ds_read_b64_tr_b16 a[132:133], v242 offset:57856\n"
"s_add_u32 s88, s91, 24832\n"
"v_mfma_f32_16x16x32_bf16 v[100:103], a[244:247], v[204:207], v[100:103]\n"
"ds_read_b64_tr_b16 a[134:135], v242 offset:58112\n"
"ds_read_b64_tr_b16 a[140:141], v242 offset:49152\n"
"v_mfma_f32_16x16x32_bf16 v[112:115], v[192:195], a[200:203], v[112:115]\n"
"ds_read_b64_tr_b16 a[142:143], v242 offset:49408\n"
"v_mfma_f32_16x16x32_bf16 v[112:115], v[196:199], a[216:219], v[112:115]\n"
"ds_read_b64_tr_b16 a[148:149], v243 offset:49152\n"
"v_mfma_f32_16x16x32_bf16 v[112:115], v[200:203], a[232:235], v[112:115]\n"
"ds_read_b64_tr_b16 a[150:151], v243 offset:49408\n"
"v_mfma_f32_16x16x32_bf16 v[112:115], v[204:207], a[248:251], v[112:115]\n"
"buffer_load_dwordx4 v245, s[0:3], s88 offen lds\n"
"v_mfma_f32_16x16x32_bf16 v[120:123], v[192:195], a[204:207], v[120:123]\n"
"v_mfma_f32_16x16x32_bf16 v[120:123], v[196:199], a[220:223], v[120:123]\n"
"v_mfma_f32_16x16x32_bf16 v[120:123], v[200:203], a[236:239], v[120:123]\n"
"v_mfma_f32_16x16x32_bf16 v[120:123], v[204:207], a[252:255], v[120:123]\n"
"s_waitcnt vmcnt(38) lgkmcnt(0)\n"
"v_mfma_f32_16x16x32_bf16 v[104:107], a[192:195], v[208:211], v[104:107]\n"
"s_barrier\n"
"v_mfma_f32_16x16x32_bf16 v[104:107], a[208:211], v[212:215], v[104:107]\n"
"v_add_u32 v240, s90, v240\n"
"s_add_u32 m0, s90, 81920\n"
"v_mfma_f32_16x16x32_bf16 v[104:107], a[224:227], v[216:219], v[104:107]\n"
"v_add_u32 v241, s90, v241\n"
"s_add_u32 s88, s49, 256\n"
"v_mfma_f32_16x16x32_bf16 v[104:107], a[240:243], v[220:223], v[104:107]\n"
"ds_read_b128 a[0:3], v240 offset:32768\n"
"v_mfma_f32_16x16x32_bf16 v[108:111], a[196:199], v[208:211], v[108:111]\n"
"buffer_load_dwordx4 v244, s[0:3], s88 offen lds\n"
"v_mfma_f32_16x16x32_bf16 v[108:111], a[212:215], v[212:215], v[108:111]\n"
"ds_read_b128 a[4:7], v241 offset:32768\n"
"ds_read_b128 a[8:11], v240 offset:34816\n"
"v_mfma_f32_16x16x32_bf16 v[108:111], a[228:231], v[216:219], v[108:111]\n"
"ds_read_b128 a[12:15], v241 offset:34816\n"
"s_add_u32 m0, s90, 82944\n"
"v_mfma_f32_16x16x32_bf16 v[108:111], a[244:247], v[220:223], v[108:111]\n"
"ds_read_b128 a[16:19], v240 offset:49152\n"
"s_add_u32 s88, s49, 8448\n"
"v_mfma_f32_16x16x32_bf16 v[116:119], v[208:211], a[200:203], v[116:119]\n"
"ds_read_b128 a[20:23], v241 offset:49152\n"
"v_mfma_f32_16x16x32_bf16 v[116:119], v[212:215], a[216:219], v[116:119]\n"
"buffer_load_dwordx4 v245, s[0:3], s88 offen lds\n"
"v_mfma_f32_16x16x32_bf16 v[116:119], v[216:219], a[232:235], v[116:119]\n"
"ds_read_b128 a[24:27], v240 offset:51200\n"
"ds_read_b128 a[28:31], v241 offset:51200\n"
"v_mfma_f32_16x16x32_bf16 v[116:119], v[220:223], a[248:251], v[116:119]\n"
"s_add_u32 m0, s90, 83968\n"
"v_mfma_f32_16x16x32_bf16 v[124:127], v[208:211], a[204:207], v[124:127]\n"
"s_add_u32 s88, s49, 16640\n"
"v_mfma_f32_16x16x32_bf16 v[124:127], v[212:215], a[220:223], v[124:127]\n"
"v_mfma_f32_16x16x32_bf16 v[124:127], v[216:219], a[236:239], v[124:127]\n"
"buffer_load_dwordx4 v244, s[0:3], s88 offen lds\n"
"v_mfma_f32_16x16x32_bf16 v[124:127], v[220:223], a[252:255], v[124:127]\n"
"s_waitcnt vmcnt(41) lgkmcnt(0)\n"
"v_mfma_f32_16x16x32_bf16 v[192:195], a[112:115], a[0:3], 0\n"
"s_barrier\n"
"v_mfma_f32_16x16x32_bf16 v[192:195], a[116:119], a[4:7], v[192:195]\n"
"ds_read_b64_tr_b16 a[64:65], v242 offset:33280\n"
"v_mfma_f32_16x16x32_bf16 v[196:199], a[112:115], a[8:11], 0\n"
"Lfused_hop4:\n"
"s_cbranch_execz Lfused_hop8\n"
"ds_read_b64_tr_b16 a[66:67], v242 offset:33536\n"
"v_mfma_f32_16x16x32_bf16 v[196:199], a[116:119], a[12:15], v[196:199]\n"
"ds_read_b64_tr_b16 a[72:73], v243 offset:33280\n"
"v_mfma_f32_16x16x32_bf16 v[200:203], a[112:115], a[16:19], 0\n"
"s_add_u32 m0, s90, 84992\n"
"s_add_u32 s88, s49, 24832\n"
"v_mfma_f32_16x16x32_bf16 v[200:203], a[116:119], a[20:23], v[200:203]\n"
"buffer_load_dwordx4 v245, s[0:3], s88 offen lds\n"
"v_mfma_f32_16x16x32_bf16 v[204:207], a[112:115], a[24:27], 0\n"
"ds_read_b64_tr_b16 a[74:75], v243 offset:33536\n"
"s_add_u32 s36, s29, 786432\n"
"v_mfma_f32_16x16x32_bf16 v[204:207], a[116:119], a[28:31], v[204:207]\n"
"ds_read_b64_tr_b16 a[80:81], v242 offset:33792\n"
"v_mul_f32 v64, 1040356334, v64\n"
"v_mfma_f32_16x16x32_bf16 v[208:211], a[0:3], a[120:123], v[228:231]\n"
"ds_read_b64_tr_b16 a[82:83], v242 offset:34048\n"
"v_mul_f32 v65, 1040356334, v65\n"
"v_mfma_f32_16x16x32_bf16 v[208:211], a[4:7], a[124:127], v[208:211]\n"
"ds_read_b64_tr_b16 a[88:89], v243 offset:33792\n"
"v_mul_f32 v66, 1040356334, v66\n"
"v_mfma_f32_16x16x32_bf16 v[212:215], a[8:11], a[120:123], v[228:231]\n"
"ds_read_b64_tr_b16 a[90:91], v243 offset:34048\n"
"v_mul_f32 v67, 1040356334, v67\n"
"v_mfma_f32_16x16x32_bf16 v[212:215], a[12:15], a[124:127], v[212:215]\n"
"ds_read_b64_tr_b16 a[96:97], v242 offset:34304\n"
"v_mul_f32 v68, 1040356334, v68\n"
"v_mfma_f32_16x16x32_bf16 v[216:219], a[16:19], a[120:123], v[228:231]\n"
"ds_read_b64_tr_b16 a[98:99], v242 offset:34560\n"
"v_mul_f32 v69, 1040356334, v69\n"
"v_mfma_f32_16x16x32_bf16 v[216:219], a[20:23], a[124:127], v[216:219]\n"
"ds_read_b64_tr_b16 a[104:105], v243 offset:34304\n"
"v_mul_f32 v70, 1040356334, v70\n"
"v_mfma_f32_16x16x32_bf16 v[220:223], a[24:27], a[120:123], v[228:231]\n"
"ds_read_b64_tr_b16 a[106:107], v243 offset:34560\n"
"v_mul_f32 v71, 1040356334, v71\n"
"v_mfma_f32_16x16x32_bf16 v[220:223], a[28:31], a[124:127], v[220:223]\n"
"ds_read_b64_tr_b16 a[68:69], v242 offset:49664\n"
"v_cvt_pk_bf16_f32 v64, v64, v65\n"
"v_mfma_f32_16x16x32_bf16 v[224:227], a[0:3], a[128:131], 0\n"
"ds_read_b64_tr_b16 a[70:71], v242 offset:49920\n"
"v_cvt_pk_bf16_f32 v65, v66, v67\n"
"v_mfma_f32_16x16x32_bf16 v[224:227], a[4:7], a[132:135], v[224:227]\n"
"ds_read_b64_tr_b16 a[76:77], v243 offset:49664\n"
"v_cvt_pk_bf16_f32 v66, v68, v69\n"
"v_mfma_f32_16x16x32_bf16 v[228:231], a[8:11], a[128:131], 0\n"
"ds_read_b64_tr_b16 a[78:79], v243 offset:49920\n"
"v_cvt_pk_bf16_f32 v67, v70, v71\n"
"v_mfma_f32_16x16x32_bf16 v[228:231], a[12:15], a[132:135], v[228:231]\n"
"buffer_store_dwordx4 v[64:67], v248, s[4:7], s36 offen offset:0 sc1\n"
"v_mfma_f32_16x16x32_bf16 v[232:235], a[16:19], a[128:131], 0\n"
"ds_read_b64_tr_b16 a[84:85], v242 offset:50176\n"
"s_add_u32 m0, s90, 98304\n"
"v_mfma_f32_16x16x32_bf16 v[232:235], a[20:23], a[132:135], v[232:235]\n"
"buffer_load_dwordx4 v246, s[84:87], s92 offen lds\n"
"v_mfma_f32_16x16x32_bf16 v[236:239], a[24:27], a[128:131], 0\n"
"ds_read_b64_tr_b16 a[86:87], v242 offset:50432\n"
"s_add_u32 m0, s90, 99328\n"
"v_mfma_f32_16x16x32_bf16 v[236:239], a[28:31], a[132:135], v[236:239]\n"
"ds_read_b64_tr_b16 a[92:93], v243 offset:50176\n"
"s_add_u32 s88, s92, s94\n"
"v_mfma_f32_16x16x32_bf16 v[0:3], a[136:139], a[0:3], 0\n"
"buffer_load_dwordx4 v246, s[84:87], s88 offen lds\n"
"v_mfma_f32_16x16x32_bf16 v[0:3], a[140:143], a[4:7], v[0:3]\n"
"ds_read_b64_tr_b16 a[94:95], v243 offset:50432\n"
"s_add_u32 m0, s90, 100352\n"
"v_mfma_f32_16x16x32_bf16 v[32:35], a[136:139], a[8:11], 0\n"
"ds_read_b64_tr_b16 a[100:101], v242 offset:50688\n"
"s_add_u32 s89, s92, s93\n"
"v_mfma_f32_16x16x32_bf16 v[32:35], a[140:143], a[12:15], v[32:35]\n"
"buffer_load_dwordx4 v247, s[84:87], s89 offen lds\n"
"v_mfma_f32_16x16x32_bf16 v[128:131], a[136:139], a[16:19], 0\n"
"ds_read_b64_tr_b16 a[102:103], v242 offset:50944\n"
"s_add_u32 m0, s90, 101376\n"
"v_mfma_f32_16x16x32_bf16 v[128:131], a[140:143], a[20:23], v[128:131]\n"
"ds_read_b64_tr_b16 a[108:109], v243 offset:50688\n"
"s_add_u32 s88, s89, s94\n"
"v_mfma_f32_16x16x32_bf16 v[160:163], a[136:139], a[24:27], 0\n"
"buffer_load_dwordx4 v247, s[84:87], s88 offen lds\n"
"v_mfma_f32_16x16x32_bf16 v[160:163], a[140:143], a[28:31], v[160:163]\n"
"ds_read_b64_tr_b16 a[110:111], v243 offset:50944\n"
"s_add_u32 s92, s92, s95\n"
"v_mfma_f32_16x16x32_bf16 v[4:7], a[144:147], a[0:3], 0\n"
"s_add_u32 m0, s90, 114688\n"
"v_mfma_f32_16x16x32_bf16 v[4:7], a[148:151], a[4:7], v[4:7]\n"
"buffer_load_dwordx4 v246, s[84:87], s92 offen lds\n"
"v_mfma_f32_16x16x32_bf16 v[36:39], a[144:147], a[8:11], 0\n"
"s_add_u32 m0, s90, 115712\n"
"v_mfma_f32_16x16x32_bf16 v[36:39], a[148:151], a[12:15], v[36:39]\n"
"s_add_u32 s88, s92, s94\n"
"v_mfma_f32_16x16x32_bf16 v[132:135], a[144:147], a[16:19], 0\n"
"buffer_load_dwordx4 v246, s[84:87], s88 offen lds\n"
"v_mfma_f32_16x16x32_bf16 v[132:135], a[148:151], a[20:23], v[132:135]\n"
"s_add_u32 m0, s90, 116736\n"
"v_mfma_f32_16x16x32_bf16 v[164:167], a[144:147], a[24:27], 0\n"
"s_add_u32 s89, s92, s93\n"
"v_mfma_f32_16x16x32_bf16 v[164:167], a[148:151], a[28:31], v[164:167]\n"
"buffer_load_dwordx4 v247, s[84:87], s89 offen lds\n"
"s_waitcnt vmcnt(21) lgkmcnt(0)\n"
"v_mfma_f32_16x16x32_bf16 v[8:11], a[64:67], a[0:3], 0\n"
"s_barrier\n"
"v_mfma_f32_16x16x32_bf16 v[8:11], a[68:71], a[4:7], v[8:11]\n"
"v_add_u32 v240, 4294901760, v240\n"
"s_add_u32 m0, s90, 117760\n"
"v_mfma_f32_16x16x32_bf16 v[40:43], a[64:67], a[8:11], 0\n"
"v_add_u32 v241, 4294901760, v241\n"
"s_add_u32 s88, s89, s94\n"
"v_mfma_f32_16x16x32_bf16 v[40:43], a[68:71], a[12:15], v[40:43]\n"
"buffer_load_dwordx4 v247, s[84:87], s88 offen lds\n"
"v_mfma_f32_16x16x32_bf16 v[136:139], a[64:67], a[16:19], 0\n"
"ds_read_b128 a[32:35], v240 offset:0\n"
"s_add_u32 s92, s92, s95\n"
"v_mfma_f32_16x16x32_bf16 v[136:139], a[68:71], a[20:23], v[136:139]\n"
"ds_read_b128 a[36:39], v241 offset:0\n"
"v_mul_f32 v72, 1040356334, v72\n"
"v_mfma_f32_16x16x32_bf16 v[168:171], a[64:67], a[24:27], 0\n"
"ds_read_b128 a[40:43], v240 offset:2048\n"
"ds_read_b128 a[44:47], v241 offset:2048\n"
"v_mfma_f32_16x16x32_bf16 v[168:171], a[68:71], a[28:31], v[168:171]\n"
"ds_read_b128 a[48:51], v240 offset:16384\n"
"v_mul_f32 v73, 1040356334, v73\n"
"v_mfma_f32_16x16x32_bf16 v[12:15], a[72:75], a[0:3], 0\n"
"ds_read_b128 a[52:55], v241 offset:16384\n"
"v_mul_f32 v74, 1040356334, v74\n"
"v_mfma_f32_16x16x32_bf16 v[12:15], a[76:79], a[4:7], v[12:15]\n"
"ds_read_b128 a[56:59], v240 offset:18432\n"
"v_mul_f32 v75, 1040356334, v75\n"
"v_mfma_f32_16x16x32_bf16 v[44:47], a[72:75], a[8:11], 0\n"
"ds_read_b128 a[60:63], v241 offset:18432\n"
"v_mul_f32 v76, 1040356334, v76\n"
"v_mfma_f32_16x16x32_bf16 v[44:47], a[76:79], a[12:15], v[44:47]\n"
"v_add_u32 v242, 4294901760, v242\n"
"v_mfma_f32_16x16x32_bf16 v[140:143], a[72:75], a[16:19], 0\n"
"v_add_u32 v243, 4294901760, v243\n"
"v_mul_f32 v77, 1040356334, v77\n"
"v_mfma_f32_16x16x32_bf16 v[140:143], a[76:79], a[20:23], v[140:143]\n"
"ds_read_b64_tr_b16 a[112:113], v242 offset:8192\n"
"v_mul_f32 v78, 1040356334, v78\n"
"v_mfma_f32_16x16x32_bf16 v[172:175], a[72:75], a[24:27], 0\n"
"ds_read_b64_tr_b16 a[114:115], v242 offset:8448\n"
"v_mul_f32 v79, 1040356334, v79\n"
"v_mfma_f32_16x16x32_bf16 v[172:175], a[76:79], a[28:31], v[172:175]\n"
"ds_read_b64_tr_b16 a[120:121], v243 offset:8192\n"
"v_cvt_pk_bf16_f32 v72, v72, v73\n"
"v_mfma_f32_16x16x32_bf16 v[16:19], a[80:83], a[0:3], 0\n"
"ds_read_b64_tr_b16 a[122:123], v243 offset:8448\n"
"v_mfma_f32_16x16x32_bf16 v[16:19], a[84:87], a[4:7], v[16:19]\n"
"ds_read_b64_tr_b16 a[128:129], v242 offset:8704\n"
"v_cvt_pk_bf16_f32 v73, v74, v75\n"
"v_mfma_f32_16x16x32_bf16 v[48:51], a[80:83], a[8:11], 0\n"
"ds_read_b64_tr_b16 a[130:131], v242 offset:8960\n"
"v_cvt_pk_bf16_f32 v74, v76, v77\n"
"v_mfma_f32_16x16x32_bf16 v[48:51], a[84:87], a[12:15], v[48:51]\n"
"ds_read_b64_tr_b16 a[136:137], v242 offset:0\n"
"v_cvt_pk_bf16_f32 v75, v78, v79\n"
"v_mfma_f32_16x16x32_bf16 v[144:147], a[80:83], a[16:19], 0\n"
"buffer_store_dwordx4 v[72:75], v248, s[4:7], s36 offen offset:1024 sc1\n"
"v_mfma_f32_16x16x32_bf16 v[144:147], a[84:87], a[20:23], v[144:147]\n"
"ds_read_b64_tr_b16 a[138:139], v242 offset:256\n"
"ds_read_b64_tr_b16 a[144:145], v243 offset:0\n"
"v_mfma_f32_16x16x32_bf16 v[176:179], a[80:83], a[24:27], 0\n"
"ds_read_b64_tr_b16 a[146:147], v243 offset:256\n"
"s_add_u32 m0, s90, 131072\n"
"v_mfma_f32_16x16x32_bf16 v[176:179], a[84:87], a[28:31], v[176:179]\n"
"ds_read_b64_tr_b16 a[116:117], v242 offset:24576\n"
"s_add_u32 s88, s91, 384\n"
"v_mfma_f32_16x16x32_bf16 v[20:23], a[88:91], a[0:3], 0\n"
"buffer_load_dwordx4 v244, s[0:3], s88 offen lds\n"
"v_mfma_f32_16x16x32_bf16 v[20:23], a[92:95], a[4:7], v[20:23]\n"
"ds_read_b64_tr_b16 a[118:119], v242 offset:24832\n"
"s_add_u32 m0, s90, 132096\n"
"v_mfma_f32_16x16x32_bf16 v[52:55], a[88:91], a[8:11], 0\n"
"ds_read_b64_tr_b16 a[124:125], v243 offset:24576\n"
"s_add_u32 s88, s91, 8576\n"
"v_mfma_f32_16x16x32_bf16 v[52:55], a[92:95], a[12:15], v[52:55]\n"
"ds_read_b64_tr_b16 a[126:127], v243 offset:24832\n"
"ds_read_b64_tr_b16 a[132:133], v242 offset:25088\n"
"v_mfma_f32_16x16x32_bf16 v[148:151], a[88:91], a[16:19], 0\n"
"buffer_load_dwordx4 v245, s[0:3], s88 offen lds\n"
"v_mfma_f32_16x16x32_bf16 v[148:151], a[92:95], a[20:23], v[148:151]\n"
"ds_read_b64_tr_b16 a[134:135], v242 offset:25344\n"
"s_add_u32 m0, s90, 133120\n"
"v_mfma_f32_16x16x32_bf16 v[180:183], a[88:91], a[24:27], 0\n"
"ds_read_b64_tr_b16 a[140:141], v242 offset:16384\n"
"s_add_u32 s88, s91, 16768\n"
"v_mfma_f32_16x16x32_bf16 v[180:183], a[92:95], a[28:31], v[180:183]\n"
"buffer_load_dwordx4 v244, s[0:3], s88 offen lds\n"
"v_mfma_f32_16x16x32_bf16 v[24:27], a[96:99], a[0:3], 0\n"
"ds_read_b64_tr_b16 a[142:143], v242 offset:16640\n"
"ds_read_b64_tr_b16 a[148:149], v243 offset:16384\n"
"v_mfma_f32_16x16x32_bf16 v[24:27], a[100:103], a[4:7], v[24:27]\n"
"ds_read_b64_tr_b16 a[150:151], v243 offset:16640\n"
"s_add_u32 m0, s90, 134144\n"
"v_mfma_f32_16x16x32_bf16 v[56:59], a[96:99], a[8:11], 0\n"
"s_add_u32 s88, s91, 24960\n"
"v_mfma_f32_16x16x32_bf16 v[56:59], a[100:103], a[12:15], v[56:59]\n"
"buffer_load_dwordx4 v245, s[0:3], s88 offen lds\n"
"v_mfma_f32_16x16x32_bf16 v[152:155], a[96:99], a[16:19], 0\n"
"s_add_u32 m0, s90, 147456\n"
"v_mfma_f32_16x16x32_bf16 v[152:155], a[100:103], a[20:23], v[152:155]\n"
"v_mfma_f32_16x16x32_bf16 v[184:187], a[96:99], a[24:27], 0\n"
"s_add_u32 s88, s49, 384\n"
"v_mfma_f32_16x16x32_bf16 v[184:187], a[100:103], a[28:31], v[184:187]\n"
"buffer_load_dwordx4 v244, s[0:3], s88 offen lds\n"
"v_mfma_f32_16x16x32_bf16 v[28:31], a[104:107], a[0:3], 0\n"
"s_add_u32 m0, s90, 148480\n"
"v_mfma_f32_16x16x32_bf16 v[28:31], a[108:111], a[4:7], v[28:31]\n"
"s_add_u32 s88, s49, 8576\n"
"v_mfma_f32_16x16x32_bf16 v[60:63], a[104:107], a[8:11], 0\n"
"v_mfma_f32_16x16x32_bf16 v[60:63], a[108:111], a[12:15], v[60:63]\n"
"buffer_load_dwordx4 v245, s[0:3], s88 offen lds\n"
"v_mfma_f32_16x16x32_bf16 v[156:159], a[104:107], a[16:19], 0\n"
"s_add_u32 m0, s90, 149504\n"
"v_mfma_f32_16x16x32_bf16 v[156:159], a[108:111], a[20:23], v[156:159]\n"
"s_add_u32 s88, s49, 16768\n"
"v_mfma_f32_16x16x32_bf16 v[188:191], a[104:107], a[24:27], 0\n"
"buffer_load_dwordx4 v244, s[0:3], s88 offen lds\n"
"v_mfma_f32_16x16x32_bf16 v[188:191], a[108:111], a[28:31], v[188:191]\n"
"s_waitcnt vmcnt(30) lgkmcnt(0)\n"
"v_mfma_f32_16x16x32_bf16 v[192:195], a[112:115], a[32:35], v[192:195]\n"
"s_barrier\n"
"v_mfma_f32_16x16x32_bf16 v[192:195], a[116:119], a[36:39], v[192:195]\n"
"ds_read_b64_tr_b16 a[64:65], v242 offset:512\n"
"s_add_u32 m0, s90, 150528\n"
"v_mfma_f32_16x16x32_bf16 v[196:199], a[112:115], a[40:43], v[196:199]\n"
"ds_read_b64_tr_b16 a[66:67], v242 offset:768\n"
"s_add_u32 s88, s49, 24960\n"
"v_mfma_f32_16x16x32_bf16 v[196:199], a[116:119], a[44:47], v[196:199]\n"
"ds_read_b64_tr_b16 a[72:73], v243 offset:512\n"
"v_mfma_f32_16x16x32_bf16 v[200:203], a[112:115], a[48:51], v[200:203]\n"
"buffer_load_dwordx4 v245, s[0:3], s88 offen lds\n"
"v_mfma_f32_16x16x32_bf16 v[200:203], a[116:119], a[52:55], v[200:203]\n"
"ds_read_b64_tr_b16 a[74:75], v243 offset:768\n"
"ds_read_b64_tr_b16 a[80:81], v242 offset:1024\n"
"v_mfma_f32_16x16x32_bf16 v[204:207], a[112:115], a[56:59], v[204:207]\n"
"ds_read_b64_tr_b16 a[82:83], v242 offset:1280\n"
"s_add_u32 m0, s90, 0\n"
"v_mfma_f32_16x16x32_bf16 v[204:207], a[116:119], a[60:63], v[204:207]\n"
"buffer_load_dwordx4 v246, s[84:87], s92 offen lds\n"
"v_mfma_f32_16x16x32_bf16 v[208:211], a[32:35], a[120:123], v[208:211]\n"
"ds_read_b64_tr_b16 a[88:89], v243 offset:1024\n"
"ds_read_b64_tr_b16 a[90:91], v243 offset:1280\n"
"v_mfma_f32_16x16x32_bf16 v[208:211], a[36:39], a[124:127], v[208:211]\n"
"s_add_u32 m0, s90, 1024\n"
"v_mfma_f32_16x16x32_bf16 v[212:215], a[40:43], a[120:123], v[212:215]\n"
"ds_read_b64_tr_b16 a[96:97], v242 offset:1536\n"
"v_mfma_f32_16x16x32_bf16 v[212:215], a[44:47], a[124:127], v[212:215]\n"
"ds_read_b64_tr_b16 a[98:99], v242 offset:1792\n"
"s_add_u32 s88, s92, s94\n"
"v_mfma_f32_16x16x32_bf16 v[216:219], a[48:51], a[120:123], v[216:219]\n"
"buffer_load_dwordx4 v246, s[84:87], s88 offen lds\n"
"v_mfma_f32_16x16x32_bf16 v[216:219], a[52:55], a[124:127], v[216:219]\n"
"ds_read_b64_tr_b16 a[104:105], v243 offset:1536\n"
"ds_read_b64_tr_b16 a[106:107], v243 offset:1792\n"
"v_mfma_f32_16x16x32_bf16 v[220:223], a[56:59], a[120:123], v[220:223]\n"
"ds_read_b64_tr_b16 a[68:69], v242 offset:16896\n"
"s_add_u32 m0, s90, 2048\n"
"v_mfma_f32_16x16x32_bf16 v[220:223], a[60:63], a[124:127], v[220:223]\n"
"ds_read_b64_tr_b16 a[70:71], v242 offset:17152\n"
"v_mfma_f32_16x16x32_bf16 v[224:227], a[32:35], a[128:131], v[224:227]\n"
"ds_read_b64_tr_b16 a[76:77], v243 offset:16896\n"
"s_add_u32 s89, s92, s93\n"
"v_mfma_f32_16x16x32_bf16 v[224:227], a[36:39], a[132:135], v[224:227]\n"
"buffer_load_dwordx4 v247, s[84:87], s89 offen lds\n"
"v_mfma_f32_16x16x32_bf16 v[228:231], a[40:43], a[128:131], v[228:231]\n"
"ds_read_b64_tr_b16 a[78:79], v243 offset:17152\n"
"v_mfma_f32_16x16x32_bf16 v[228:231], a[44:47], a[132:135], v[228:231]\n"
"ds_read_b64_tr_b16 a[84:85], v242 offset:17408\n"
"s_add_u32 m0, s90, 3072\n"
"v_mfma_f32_16x16x32_bf16 v[232:235], a[48:51], a[128:131], v[232:235]\n"
"ds_read_b64_tr_b16 a[86:87], v242 offset:17664\n"
"v_mfma_f32_16x16x32_bf16 v[232:235], a[52:55], a[132:135], v[232:235]\n"
"ds_read_b64_tr_b16 a[92:93], v243 offset:17408\n"
"s_add_u32 s88, s89, s94\n"
"v_mfma_f32_16x16x32_bf16 v[236:239], a[56:59], a[128:131], v[236:239]\n"
"ds_read_b64_tr_b16 a[94:95], v243 offset:17664\n"
"v_mfma_f32_16x16x32_bf16 v[236:239], a[60:63], a[132:135], v[236:239]\n"
"buffer_load_dwordx4 v247, s[84:87], s88 offen lds\n"
"v_mfma_f32_16x16x32_bf16 v[0:3], a[136:139], a[32:35], v[0:3]\n"
"ds_read_b64_tr_b16 a[100:101], v242 offset:17920\n"
"s_add_u32 s92, s92, s95\n"
"v_mfma_f32_16x16x32_bf16 v[0:3], a[140:143], a[36:39], v[0:3]\n"
"ds_read_b64_tr_b16 a[102:103], v242 offset:18176\n"
"ds_read_b64_tr_b16 a[108:109], v243 offset:17920\n"
"v_mfma_f32_16x16x32_bf16 v[32:35], a[136:139], a[40:43], v[32:35]\n"
"ds_read_b64_tr_b16 a[110:111], v243 offset:18176\n"
"s_add_u32 m0, s90, 16384\n"
"v_mfma_f32_16x16x32_bf16 v[32:35], a[140:143], a[44:47], v[32:35]\n"
"v_mfma_f32_16x16x32_bf16 v[128:131], a[136:139], a[48:51], v[128:131]\n"
"buffer_load_dwordx4 v246, s[84:87], s92 offen lds\n"
"v_mfma_f32_16x16x32_bf16 v[128:131], a[140:143], a[52:55], v[128:131]\n"
"s_add_u32 m0, s90, 17408\n"
"v_mfma_f32_16x16x32_bf16 v[160:163], a[136:139], a[56:59], v[160:163]\n"
"v_mfma_f32_16x16x32_bf16 v[160:163], a[140:143], a[60:63], v[160:163]\n"
"s_add_u32 s88, s92, s94\n"
"v_mfma_f32_16x16x32_bf16 v[4:7], a[144:147], a[32:35], v[4:7]\n"
"v_mfma_f32_16x16x32_bf16 v[4:7], a[148:151], a[36:39], v[4:7]\n"
"buffer_load_dwordx4 v246, s[84:87], s88 offen lds\n"
"v_mfma_f32_16x16x32_bf16 v[36:39], a[144:147], a[40:43], v[36:39]\n"
"s_add_u32 m0, s90, 18432\n"
"v_mfma_f32_16x16x32_bf16 v[36:39], a[148:151], a[44:47], v[36:39]\n"
"v_mfma_f32_16x16x32_bf16 v[132:135], a[144:147], a[48:51], v[132:135]\n"
"s_add_u32 s89, s92, s93\n"
"v_mfma_f32_16x16x32_bf16 v[132:135], a[148:151], a[52:55], v[132:135]\n"
"v_mfma_f32_16x16x32_bf16 v[164:167], a[144:147], a[56:59], v[164:167]\n"
"buffer_load_dwordx4 v247, s[84:87], s89 offen lds\n"
"v_mfma_f32_16x16x32_bf16 v[164:167], a[148:151], a[60:63], v[164:167]\n"
"s_waitcnt vmcnt(16) lgkmcnt(0)\n"
"v_mfma_f32_16x16x32_bf16 v[8:11], a[64:67], a[32:35], v[8:11]\n"
"s_barrier\n"
"v_mfma_f32_16x16x32_bf16 v[8:11], a[68:71], a[36:39], v[8:11]\n"
"s_cmp_eq_u32 s12, 0\n"
"s_cbranch_scc0 Lfused_pub1\n"
"s_lshl_b32 s58, s10, 7\n"
"s_lshl_b32 s59, s11, 3\n"
"s_add_u32 s58, s58, s59\n"
"s_add_u32 s58, s58, 4096\n"
"v_mov_b32 v252, s98\n"
"v_mov_b32 v253, s99\n"
"v_mov_b32 v254, s58\n"
"s_mov_b64 exec, 1\n"
"global_store_dwordx2 v254, v[252:253], s[62:63] sc1\n"
"s_mov_b64 exec, -1\n"
"Lfused_pub1:\n"
"v_mfma_f32_16x16x32_bf16 v[40:43], a[64:67], a[40:43], v[40:43]\n"
"v_add_u32 v240, 65536, v240\n"
"s_add_u32 m0, s90, 19456\n"
"v_mfma_f32_16x16x32_bf16 v[40:43], a[68:71], a[44:47], v[40:43]\n"
"v_add_u32 v241, 65536, v241\n"
"s_add_u32 s88, s89, s94\n"
"v_mfma_f32_16x16x32_bf16 v[136:139], a[64:67], a[48:51], v[136:139]\n"
"ds_read_b128 a[0:3], v240 offset:0\n"
"ds_read_b128 a[4:7], v241 offset:0\n"
"v_mfma_f32_16x16x32_bf16 v[136:139], a[68:71], a[52:55], v[136:139]\n"
"buffer_load_dwordx4 v247, s[84:87], s88 offen lds\n"
"v_mfma_f32_16x16x32_bf16 v[168:171], a[64:67], a[56:59], v[168:171]\n"
"ds_read_b128 a[8:11], v240 offset:2048\n"
"s_add_u32 s92, s92, s95\n"
"v_mfma_f32_16x16x32_bf16 v[168:171], a[68:71], a[60:63], v[168:171]\n"
"ds_read_b128 a[12:15], v241 offset:2048\n"
"ds_read_b128 a[16:19], v240 offset:16384\n"
"v_mfma_f32_16x16x32_bf16 v[12:15], a[72:75], a[32:35], v[12:15]\n"
"ds_read_b128 a[20:23], v241 offset:16384\n"
"s_add_u32 s36, s29, 794624\n"
"v_mfma_f32_16x16x32_bf16 v[12:15], a[76:79], a[36:39], v[12:15]\n"
"ds_read_b128 a[24:27], v240 offset:18432\n"
"v_cvt_pk_bf16_f32 v80, v80, v81\n"
"v_mfma_f32_16x16x32_bf16 v[44:47], a[72:75], a[40:43], v[44:47]\n"
"ds_read_b128 a[28:31], v241 offset:18432\n"
"v_mfma_f32_16x16x32_bf16 v[44:47], a[76:79], a[44:47], v[44:47]\n"
"v_add_u32 v242, 65536, v242\n"
"v_cvt_pk_bf16_f32 v81, v82, v83\n"
"v_mfma_f32_16x16x32_bf16 v[140:143], a[72:75], a[48:51], v[140:143]\n"
"v_add_u32 v243, 65536, v243\n"
"v_cvt_pk_bf16_f32 v82, v84, v85\n"
"v_mfma_f32_16x16x32_bf16 v[140:143], a[76:79], a[52:55], v[140:143]\n"
"ds_read_b64_tr_b16 a[112:113], v242 offset:8192\n"
"v_mfma_f32_16x16x32_bf16 v[172:175], a[72:75], a[56:59], v[172:175]\n"
"ds_read_b64_tr_b16 a[114:115], v242 offset:8448\n"
"v_cvt_pk_bf16_f32 v83, v86, v87\n"
"v_mfma_f32_16x16x32_bf16 v[172:175], a[76:79], a[60:63], v[172:175]\n"
"buffer_store_dwordx4 v[80:83], v248, s[4:7], s36 offen offset:0 sc1\n"
"v_mfma_f32_16x16x32_bf16 v[16:19], a[80:83], a[32:35], v[16:19]\n"
"ds_read_b64_tr_b16 a[120:121], v243 offset:8192\n"
"ds_read_b64_tr_b16 a[122:123], v243 offset:8448\n"
"v_mfma_f32_16x16x32_bf16 v[16:19], a[84:87], a[36:39], v[16:19]\n"
"ds_read_b64_tr_b16 a[128:129], v242 offset:8704\n"
"s_add_u32 m0, s90, 32768\n"
"v_mfma_f32_16x16x32_bf16 v[48:51], a[80:83], a[40:43], v[48:51]\n"
"ds_read_b64_tr_b16 a[130:131], v242 offset:8960\n"
"s_add_u32 s88, s91, 512\n"
"v_mfma_f32_16x16x32_bf16 v[48:51], a[84:87], a[44:47], v[48:51]\n"
"ds_read_b64_tr_b16 a[136:137], v242 offset:0\n"
"v_mfma_f32_16x16x32_bf16 v[144:147], a[80:83], a[48:51], v[144:147]\n"
"buffer_load_dwordx4 v244, s[0:3], s88 offen lds\n"
"v_mfma_f32_16x16x32_bf16 v[144:147], a[84:87], a[52:55], v[144:147]\n"
"ds_read_b64_tr_b16 a[138:139], v242 offset:256\n"
"s_add_u32 m0, s90, 33792\n"
"v_mfma_f32_16x16x32_bf16 v[176:179], a[80:83], a[56:59], v[176:179]\n"
"ds_read_b64_tr_b16 a[144:145], v243 offset:0\n"
"ds_read_b64_tr_b16 a[146:147], v243 offset:256\n"
"v_mfma_f32_16x16x32_bf16 v[176:179], a[84:87], a[60:63], v[176:179]\n"
"ds_read_b64_tr_b16 a[116:117], v242 offset:24576\n"
"s_add_u32 s88, s91, 8704\n"
"v_mfma_f32_16x16x32_bf16 v[20:23], a[88:91], a[32:35], v[20:23]\n"
"buffer_load_dwordx4 v245, s[0:3], s88 offen lds\n"
"v_mfma_f32_16x16x32_bf16 v[20:23], a[92:95], a[36:39], v[20:23]\n"
"ds_read_b64_tr_b16 a[118:119], v242 offset:24832\n"
"s_add_u32 m0, s90, 34816\n"
"v_mfma_f32_16x16x32_bf16 v[52:55], a[88:91], a[40:43], v[52:55]\n"
"ds_read_b64_tr_b16 a[124:125], v243 offset:24576\n"
"ds_read_b64_tr_b16 a[126:127], v243 offset:24832\n"
"v_mfma_f32_16x16x32_bf16 v[52:55], a[92:95], a[44:47], v[52:55]\n"
"ds_read_b64_tr_b16 a[132:133], v242 offset:25088\n"
"s_add_u32 s88, s91, 16896\n"
"v_mfma_f32_16x16x32_bf16 v[148:151], a[88:91], a[48:51], v[148:151]\n"
"buffer_load_dwordx4 v244, s[0:3], s88 offen lds\n"
"v_mfma_f32_16x16x32_bf16 v[148:151], a[92:95], a[52:55], v[148:151]\n"
"ds_read_b64_tr_b16 a[134:135], v242 offset:25344\n"
"ds_read_b64_tr_b16 a[140:141], v242 offset:16384\n"
"v_mfma_f32_16x16x32_bf16 v[180:183], a[88:91], a[56:59], v[180:183]\n"
"ds_read_b64_tr_b16 a[142:143], v242 offset:16640\n"
"s_add_u32 m0, s90, 35840\n"
"v_mfma_f32_16x16x32_bf16 v[180:183], a[92:95], a[60:63], v[180:183]\n"
"ds_read_b64_tr_b16 a[148:149], v243 offset:16384\n"
"s_add_u32 s88, s91, 25088\n"
"v_mfma_f32_16x16x32_bf16 v[24:27], a[96:99], a[32:35], v[24:27]\n"
"ds_read_b64_tr_b16 a[150:151], v243 offset:16640\n"
"v_mfma_f32_16x16x32_bf16 v[24:27], a[100:103], a[36:39], v[24:27]\n"
"buffer_load_dwordx4 v245, s[0:3], s88 offen lds\n"
"v_mfma_f32_16x16x32_bf16 v[56:59], a[96:99], a[40:43], v[56:59]\n"
"s_add_u32 m0, s90, 49152\n"
"v_mfma_f32_16x16x32_bf16 v[56:59], a[100:103], a[44:47], v[56:59]\n"
"v_mfma_f32_16x16x32_bf16 v[152:155], a[96:99], a[48:51], v[152:155]\n"
"s_add_u32 s88, s49, 512\n"
"v_mfma_f32_16x16x32_bf16 v[152:155], a[100:103], a[52:55], v[152:155]\n"
"buffer_load_dwordx4 v244, s[0:3], s88 offen lds\n"
"v_mfma_f32_16x16x32_bf16 v[184:187], a[96:99], a[56:59], v[184:187]\n"
"v_mfma_f32_16x16x32_bf16 v[184:187], a[100:103], a[60:63], v[184:187]\n"
"s_add_u32 m0, s90, 50176\n"
"v_mfma_f32_16x16x32_bf16 v[28:31], a[104:107], a[32:35], v[28:31]\n"
"s_add_u32 s88, s49, 8704\n"
"v_mfma_f32_16x16x32_bf16 v[28:31], a[108:111], a[36:39], v[28:31]\n"
"v_mfma_f32_16x16x32_bf16 v[60:63], a[104:107], a[40:43], v[60:63]\n"
"buffer_load_dwordx4 v245, s[0:3], s88 offen lds\n"
"v_mfma_f32_16x16x32_bf16 v[60:63], a[108:111], a[44:47], v[60:63]\n"
"s_add_u32 m0, s90, 51200\n"
"v_mfma_f32_16x16x32_bf16 v[156:159], a[104:107], a[48:51], v[156:159]\n"
"v_mfma_f32_16x16x32_bf16 v[156:159], a[108:111], a[52:55], v[156:159]\n"
"s_add_u32 s88, s49, 16896\n"
"v_mfma_f32_16x16x32_bf16 v[188:191], a[104:107], a[56:59], v[188:191]\n"
"buffer_load_dwordx4 v244, s[0:3], s88 offen lds\n"
"v_mfma_f32_16x16x32_bf16 v[188:191], a[108:111], a[60:63], v[188:191]\n"
"s_waitcnt vmcnt(25) lgkmcnt(0)\n"
"v_mfma_f32_16x16x32_bf16 v[192:195], a[112:115], a[0:3], v[192:195]\n"
"s_barrier\n"
"v_mfma_f32_16x16x32_bf16 v[192:195], a[116:119], a[4:7], v[192:195]\n"
"ds_read_b64_tr_b16 a[64:65], v242 offset:512\n"
"s_add_u32 m0, s90, 52224\n"
"v_mfma_f32_16x16x32_bf16 v[196:199], a[112:115], a[8:11], v[196:199]\n"
"ds_read_b64_tr_b16 a[66:67], v242 offset:768\n"
"s_add_u32 s88, s49, 25088\n"
"v_mfma_f32_16x16x32_bf16 v[196:199], a[116:119], a[12:15], v[196:199]\n"
"buffer_load_dwordx4 v245, s[0:3], s88 offen lds\n"
"v_mfma_f32_16x16x32_bf16 v[200:203], a[112:115], a[16:19], v[200:203]\n"
"ds_read_b64_tr_b16 a[72:73], v243 offset:512\n"
"ds_read_b64_tr_b16 a[74:75], v243 offset:768\n"
"v_mfma_f32_16x16x32_bf16 v[200:203], a[116:119], a[20:23], v[200:203]\n"
"ds_read_b64_tr_b16 a[80:81], v242 offset:1024\n"
"v_cvt_pk_bf16_f32 v88, v88, v89\n"
"v_mfma_f32_16x16x32_bf16 v[204:207], a[112:115], a[24:27], v[204:207]\n"
"ds_read_b64_tr_b16 a[82:83], v242 offset:1280\n"
"v_cvt_pk_bf16_f32 v89, v90, v91\n"
"v_mfma_f32_16x16x32_bf16 v[204:207], a[116:119], a[28:31], v[204:207]\n"
"ds_read_b64_tr_b16 a[88:89], v243 offset:1024\n"
"v_cvt_pk_bf16_f32 v90, v92, v93\n"
"v_mfma_f32_16x16x32_bf16 v[208:211], a[0:3], a[120:123], v[208:211]\n"
"ds_read_b64_tr_b16 a[90:91], v243 offset:1280\n"
"v_mfma_f32_16x16x32_bf16 v[208:211], a[4:7], a[124:127], v[208:211]\n"
"v_cvt_pk_bf16_f32 v91, v94, v95\n"
"v_mfma_f32_16x16x32_bf16 v[212:215], a[8:11], a[120:123], v[212:215]\n"
"buffer_store_dwordx4 v[88:91], v248, s[4:7], s36 offen offset:1024 sc1\n"
"v_mfma_f32_16x16x32_bf16 v[212:215], a[12:15], a[124:127], v[212:215]\n"
"ds_read_b64_tr_b16 a[96:97], v242 offset:1536\n"
"ds_read_b64_tr_b16 a[98:99], v242 offset:1792\n"
"v_mfma_f32_16x16x32_bf16 v[216:219], a[16:19], a[120:123], v[216:219]\n"
"ds_read_b64_tr_b16 a[104:105], v243 offset:1536\n"
"s_add_u32 m0, s90, 65536\n"
"v_mfma_f32_16x16x32_bf16 v[216:219], a[20:23], a[124:127], v[216:219]\n"
"buffer_load_dwordx4 v246, s[84:87], s92 offen lds\n"
"v_mfma_f32_16x16x32_bf16 v[220:223], a[24:27], a[120:123], v[220:223]\n"
"ds_read_b64_tr_b16 a[106:107], v243 offset:1792\n"
"s_add_u32 m0, s90, 66560\n"
"v_mfma_f32_16x16x32_bf16 v[220:223], a[28:31], a[124:127], v[220:223]\n"
"ds_read_b64_tr_b16 a[68:69], v242 offset:16896\n"
"ds_read_b64_tr_b16 a[70:71], v242 offset:17152\n"
"v_mfma_f32_16x16x32_bf16 v[224:227], a[0:3], a[128:131], v[224:227]\n"
"ds_read_b64_tr_b16 a[76:77], v243 offset:16896\n"
"s_add_u32 s88, s92, s94\n"
"v_mfma_f32_16x16x32_bf16 v[224:227], a[4:7], a[132:135], v[224:227]\n"
"buffer_load_dwordx4 v246, s[84:87], s88 offen lds\n"
"v_mfma_f32_16x16x32_bf16 v[228:231], a[8:11], a[128:131], v[228:231]\n"
"ds_read_b64_tr_b16 a[78:79], v243 offset:17152\n"
"v_mfma_f32_16x16x32_bf16 v[228:231], a[12:15], a[132:135], v[228:231]\n"
"ds_read_b64_tr_b16 a[84:85], v242 offset:17408\n"
"s_add_u32 m0, s90, 67584\n"
"v_mfma_f32_16x16x32_bf16 v[232:235], a[16:19], a[128:131], v[232:235]\n"
"ds_read_b64_tr_b16 a[86:87], v242 offset:17664\n"
"s_add_u32 s89, s92, s93\n"
"v_mfma_f32_16x16x32_bf16 v[232:235], a[20:23], a[132:135], v[232:235]\n"
"buffer_load_dwordx4 v247, s[84:87], s89 offen lds\n"
"v_mfma_f32_16x16x32_bf16 v[236:239], a[24:27], a[128:131], v[236:239]\n"
"ds_read_b64_tr_b16 a[92:93], v243 offset:17408\n"
"ds_read_b64_tr_b16 a[94:95], v243 offset:17664\n"
"v_mfma_f32_16x16x32_bf16 v[236:239], a[28:31], a[132:135], v[236:239]\n"
"ds_read_b64_tr_b16 a[100:101], v242 offset:17920\n"
"s_add_u32 m0, s90, 68608\n"
"v_mfma_f32_16x16x32_bf16 v[0:3], a[136:139], a[0:3], v[0:3]\n"
"ds_read_b64_tr_b16 a[102:103], v242 offset:18176\n"
"s_add_u32 s88, s89, s94\n"
"v_mfma_f32_16x16x32_bf16 v[0:3], a[140:143], a[4:7], v[0:3]\n"
"ds_read_b64_tr_b16 a[108:109], v243 offset:17920\n"
"v_mfma_f32_16x16x32_bf16 v[32:35], a[136:139], a[8:11], v[32:35]\n"
"buffer_load_dwordx4 v247, s[84:87], s88 offen lds\n"
"v_mfma_f32_16x16x32_bf16 v[32:35], a[140:143], a[12:15], v[32:35]\n"
"ds_read_b64_tr_b16 a[110:111], v243 offset:18176\n"
"s_add_u32 s92, s92, s95\n"
"v_mfma_f32_16x16x32_bf16 v[128:131], a[136:139], a[16:19], v[128:131]\n"
"s_add_u32 m0, s90, 81920\n"
"v_mfma_f32_16x16x32_bf16 v[128:131], a[140:143], a[20:23], v[128:131]\n"
"v_mfma_f32_16x16x32_bf16 v[160:163], a[136:139], a[24:27], v[160:163]\n"
"buffer_load_dwordx4 v246, s[84:87], s92 offen lds\n"
"v_mfma_f32_16x16x32_bf16 v[160:163], a[140:143], a[28:31], v[160:163]\n"
"s_add_u32 m0, s90, 82944\n"
"v_mfma_f32_16x16x32_bf16 v[4:7], a[144:147], a[0:3], v[4:7]\n"
"v_mfma_f32_16x16x32_bf16 v[4:7], a[148:151], a[4:7], v[4:7]\n"
"s_add_u32 s88, s92, s94\n"
"v_mfma_f32_16x16x32_bf16 v[36:39], a[144:147], a[8:11], v[36:39]\n"
"buffer_load_dwordx4 v246, s[84:87], s88 offen lds\n"
"v_mfma_f32_16x16x32_bf16 v[36:39], a[148:151], a[12:15], v[36:39]\n"
"s_add_u32 m0, s90, 83968\n"
"v_mfma_f32_16x16x32_bf16 v[132:135], a[144:147], a[16:19], v[132:135]\n"
"v_mfma_f32_16x16x32_bf16 v[132:135], a[148:151], a[20:23], v[132:135]\n"
"s_add_u32 s89, s92, s93\n"
"v_mfma_f32_16x16x32_bf16 v[164:167], a[144:147], a[24:27], v[164:167]\n"
"buffer_load_dwordx4 v247, s[84:87], s89 offen lds\n"
"v_mfma_f32_16x16x32_bf16 v[164:167], a[148:151], a[28:31], v[164:167]\n"
"s_waitcnt vmcnt(17) lgkmcnt(0)\n"
"v_mfma_f32_16x16x32_bf16 v[8:11], a[64:67], a[0:3], v[8:11]\n"
"s_barrier\n"
"v_mfma_f32_16x16x32_bf16 v[8:11], a[68:71], a[4:7], v[8:11]\n"
"v_add_u32 v240, 32768, v240\n"
"s_add_u32 m0, s90, 84992\n"
"v_mfma_f32_16x16x32_bf16 v[40:43], a[64:67], a[8:11], v[40:43]\n"
"Lfused_hop5:\n"
"s_cbranch_execz Lfused_hop9\n"
"v_add_u32 v241, 32768, v241\n"
"s_add_u32 s88, s89, s94\n"
"v_mfma_f32_16x16x32_bf16 v[40:43], a[68:71], a[12:15], v[40:43]\n"
"ds_read_b128 a[32:35], v240 offset:32768\n"
"v_lshrrev_b32 v93, 7, v248\n"
"v_mfma_f32_16x16x32_bf16 v[136:139], a[64:67], a[16:19], v[136:139]\n"
"buffer_load_dwordx4 v247, s[84:87], s88 offen lds\n"
"v_mfma_f32_16x16x32_bf16 v[136:139], a[68:71], a[20:23], v[136:139]\n"
"ds_read_b128 a[36:39], v241 offset:32768\n"
"ds_read_b128 a[40:43], v240 offset:34816\n"
"v_mfma_f32_16x16x32_bf16 v[168:171], a[64:67], a[24:27], v[168:171]\n"
"ds_read_b128 a[44:47], v241 offset:34816\n"
"s_add_u32 s92, s92, s95\n"
"v_mfma_f32_16x16x32_bf16 v[168:171], a[68:71], a[28:31], v[168:171]\n"
"ds_read_b128 a[48:51], v240 offset:49152\n"
"v_and_b32 v94, 6, v93\n"
"v_mfma_f32_16x16x32_bf16 v[12:15], a[72:75], a[0:3], v[12:15]\n"
"ds_read_b128 a[52:55], v241 offset:49152\n"
"s_add_u32 m0, s90, 98304\n"
"v_mfma_f32_16x16x32_bf16 v[12:15], a[76:79], a[4:7], v[12:15]\n"
"ds_read_b128 a[56:59], v240 offset:51200\n"
"v_bfe_u32 v95, v248, 4, 3\n"
"v_mfma_f32_16x16x32_bf16 v[44:47], a[72:75], a[8:11], v[44:47]\n"
"ds_read_b128 a[60:63], v241 offset:51200\n"
"s_add_u32 s88, s91, 640\n"
"v_mfma_f32_16x16x32_bf16 v[44:47], a[76:79], a[12:15], v[44:47]\n"
"v_add_u32 v242, 4294868992, v242\n"
"v_xor_b32 v94, v94, v95\n"
"v_mfma_f32_16x16x32_bf16 v[140:143], a[72:75], a[16:19], v[140:143]\n"
"buffer_load_dwordx4 v244, s[0:3], s88 offen lds\n"
"v_mfma_f32_16x16x32_bf16 v[140:143], a[76:79], a[20:23], v[140:143]\n"
"v_add_u32 v243, 4294868992, v243\n"
"ds_read_b64_tr_b16 a[112:113], v242 offset:8192\n"
"v_mfma_f32_16x16x32_bf16 v[172:175], a[72:75], a[24:27], v[172:175]\n"
"ds_read_b64_tr_b16 a[114:115], v242 offset:8448\n"
"s_add_u32 m0, s90, 99328\n"
"v_mfma_f32_16x16x32_bf16 v[172:175], a[76:79], a[28:31], v[172:175]\n"
"ds_read_b64_tr_b16 a[120:121], v243 offset:8192\n"
"v_lshlrev_b32 v94, 4, v94\n"
"v_mfma_f32_16x16x32_bf16 v[16:19], a[80:83], a[0:3], v[16:19]\n"
"ds_read_b64_tr_b16 a[122:123], v243 offset:8448\n"
"s_add_u32 s88, s91, 8832\n"
"v_mfma_f32_16x16x32_bf16 v[16:19], a[84:87], a[4:7], v[16:19]\n"
"buffer_load_dwordx4 v245, s[0:3], s88 offen lds\n"
"v_mfma_f32_16x16x32_bf16 v[48:51], a[80:83], a[8:11], v[48:51]\n"
"ds_read_b64_tr_b16 a[128:129], v242 offset:8704\n"
"ds_read_b64_tr_b16 a[130:131], v242 offset:8960\n"
"v_mfma_f32_16x16x32_bf16 v[48:51], a[84:87], a[12:15], v[48:51]\n"
"ds_read_b64_tr_b16 a[136:137], v242 offset:0\n"
"s_add_u32 m0, s90, 100352\n"
"v_mfma_f32_16x16x32_bf16 v[144:147], a[80:83], a[16:19], v[144:147]\n"
"ds_read_b64_tr_b16 a[138:139], v242 offset:256\n"
"v_lshl_add_u32 v92, v93, 10, v94\n"
"v_mfma_f32_16x16x32_bf16 v[144:147], a[84:87], a[20:23], v[144:147]\n"
"ds_read_b64_tr_b16 a[144:145], v243 offset:0\n"
"s_add_u32 s88, s91, 17024\n"
"v_mfma_f32_16x16x32_bf16 v[176:179], a[80:83], a[24:27], v[176:179]\n"
"ds_read_b64_tr_b16 a[146:147], v243 offset:256\n"
"v_lshrrev_b32 v93, 6, v248\n"
"v_mfma_f32_16x16x32_bf16 v[176:179], a[84:87], a[28:31], v[176:179]\n"
"buffer_load_dwordx4 v244, s[0:3], s88 offen lds\n"
"v_mfma_f32_16x16x32_bf16 v[20:23], a[88:91], a[0:3], v[20:23]\n"
"ds_read_b64_tr_b16 a[116:117], v242 offset:24576\n"
"ds_read_b64_tr_b16 a[118:119], v242 offset:24832\n"
"v_mfma_f32_16x16x32_bf16 v[20:23], a[92:95], a[4:7], v[20:23]\n"
"ds_read_b64_tr_b16 a[124:125], v243 offset:24576\n"
"s_add_u32 m0, s90, 101376\n"
"v_mfma_f32_16x16x32_bf16 v[52:55], a[88:91], a[8:11], v[52:55]\n"
"ds_read_b64_tr_b16 a[126:127], v243 offset:24832\n"
"v_and_b32 v94, 6, v93\n"
"v_mfma_f32_16x16x32_bf16 v[52:55], a[92:95], a[12:15], v[52:55]\n"
"ds_read_b64_tr_b16 a[132:133], v242 offset:25088\n"
"s_add_u32 s88, s91, 25216\n"
"v_mfma_f32_16x16x32_bf16 v[148:151], a[88:91], a[16:19], v[148:151]\n"
"ds_read_b64_tr_b16 a[134:135], v242 offset:25344\n"
"v_bfe_u32 v95, v248, 4, 2\n"
"v_mfma_f32_16x16x32_bf16 v[148:151], a[92:95], a[20:23], v[148:151]\n"
"buffer_load_dwordx4 v245, s[0:3], s88 offen lds\n"
"v_mfma_f32_16x16x32_bf16 v[180:183], a[88:91], a[24:27], v[180:183]\n"
"ds_read_b64_tr_b16 a[140:141], v242 offset:16384\n"
"ds_read_b64_tr_b16 a[142:143], v242 offset:16640\n"
"v_mfma_f32_16x16x32_bf16 v[180:183], a[92:95], a[28:31], v[180:183]\n"
"ds_read_b64_tr_b16 a[148:149], v243 offset:16384\n"
"s_add_u32 m0, s90, 114688\n"
"v_mfma_f32_16x16x32_bf16 v[24:27], a[96:99], a[0:3], v[24:27]\n"
"ds_read_b64_tr_b16 a[150:151], v243 offset:16640\n"
"s_add_u32 s88, s49, 640\n"
"v_mfma_f32_16x16x32_bf16 v[24:27], a[100:103], a[4:7], v[24:27]\n"
"v_xor_b32 v94, v94, v95\n"
"v_lshlrev_b32 v94, 4, v94\n"
"v_mfma_f32_16x16x32_bf16 v[56:59], a[96:99], a[8:11], v[56:59]\n"
"buffer_load_dwordx4 v244, s[0:3], s88 offen lds\n"
"v_mfma_f32_16x16x32_bf16 v[56:59], a[100:103], a[12:15], v[56:59]\n"
"v_lshl_add_u32 v93, v93, 7, v94\n"
"s_add_u32 s57, s90, 131072\n"
"v_mfma_f32_16x16x32_bf16 v[152:155], a[96:99], a[16:19], v[152:155]\n"
"s_add_u32 m0, s90, 115712\n"
"v_add_u32 v88, s57, v93\n"
"v_mfma_f32_16x16x32_bf16 v[152:155], a[100:103], a[20:23], v[152:155]\n"
"v_xor_b32 v89, 64, v88\n"
"v_mfma_f32_16x16x32_bf16 v[184:187], a[96:99], a[24:27], v[184:187]\n"
"s_add_u32 s88, s49, 8832\n"
"v_mfma_f32_16x16x32_bf16 v[184:187], a[100:103], a[28:31], v[184:187]\n"
"v_mfma_f32_16x16x32_bf16 v[28:31], a[104:107], a[0:3], v[28:31]\n"
"buffer_load_dwordx4 v245, s[0:3], s88 offen lds\n"
"v_mfma_f32_16x16x32_bf16 v[28:31], a[108:111], a[4:7], v[28:31]\n"
"v_mfma_f32_16x16x32_bf16 v[60:63], a[104:107], a[8:11], v[60:63]\n"
"s_add_u32 m0, s90, 116736\n"
"v_mfma_f32_16x16x32_bf16 v[60:63], a[108:111], a[12:15], v[60:63]\n"
"v_mfma_f32_16x16x32_bf16 v[156:159], a[104:107], a[16:19], v[156:159]\n"
"s_add_u32 s88, s49, 17024\n"
"v_mfma_f32_16x16x32_bf16 v[156:159], a[108:111], a[20:23], v[156:159]\n"
"v_mfma_f32_16x16x32_bf16 v[188:191], a[104:107], a[24:27], v[188:191]\n"
"buffer_load_dwordx4 v244, s[0:3], s88 offen lds\n"
"v_mfma_f32_16x16x32_bf16 v[188:191], a[108:111], a[28:31], v[188:191]\n"
"s_waitcnt vmcnt(25) lgkmcnt(0)\n"
"v_mfma_f32_16x16x32_bf16 v[192:195], a[112:115], a[32:35], v[192:195]\n"
"s_barrier\n"
"v_mfma_f32_16x16x32_bf16 v[192:195], a[116:119], a[36:39], v[192:195]\n"
"ds_read_b64_tr_b16 a[64:65], v242 offset:512\n"
"s_add_u32 m0, s90, 117760\n"
"v_mfma_f32_16x16x32_bf16 v[196:199], a[112:115], a[40:43], v[196:199]\n"
"ds_read_b64_tr_b16 a[66:67], v242 offset:768\n"
"s_add_u32 s88, s49, 25216\n"
"v_mfma_f32_16x16x32_bf16 v[196:199], a[116:119], a[44:47], v[196:199]\n"
"buffer_load_dwordx4 v245, s[0:3], s88 offen lds\n"
"v_mfma_f32_16x16x32_bf16 v[200:203], a[112:115], a[48:51], v[200:203]\n"
"ds_read_b64_tr_b16 a[72:73], v243 offset:512\n"
"s_add_u32 s36, s29, 802816\n"
"v_mfma_f32_16x16x32_bf16 v[200:203], a[116:119], a[52:55], v[200:203]\n"
"ds_read_b64_tr_b16 a[74:75], v243 offset:768\n"
"v_mul_f32 v96, 1040356334, v96\n"
"v_mfma_f32_16x16x32_bf16 v[204:207], a[112:115], a[56:59], v[204:207]\n"
"ds_read_b64_tr_b16 a[80:81], v242 offset:1024\n"
"v_mul_f32 v97, 1040356334, v97\n"
"v_mfma_f32_16x16x32_bf16 v[204:207], a[116:119], a[60:63], v[204:207]\n"
"ds_read_b64_tr_b16 a[82:83], v242 offset:1280\n"
"v_mul_f32 v98, 1040356334, v98\n"
"v_mfma_f32_16x16x32_bf16 v[208:211], a[32:35], a[120:123], v[208:211]\n"
"ds_read_b64_tr_b16 a[88:89], v243 offset:1024\n"
"v_mul_f32 v99, 1040356334, v99\n"
"v_mfma_f32_16x16x32_bf16 v[208:211], a[36:39], a[124:127], v[208:211]\n"
"ds_read_b64_tr_b16 a[90:91], v243 offset:1280\n"
"v_mul_f32 v100, 1040356334, v100\n"
"v_mfma_f32_16x16x32_bf16 v[212:215], a[40:43], a[120:123], v[212:215]\n"
"ds_read_b64_tr_b16 a[96:97], v242 offset:1536\n"
"v_mul_f32 v101, 1040356334, v101\n"
"v_mfma_f32_16x16x32_bf16 v[212:215], a[44:47], a[124:127], v[212:215]\n"
"ds_read_b64_tr_b16 a[98:99], v242 offset:1792\n"
"v_mul_f32 v102, 1040356334, v102\n"
"v_mfma_f32_16x16x32_bf16 v[216:219], a[48:51], a[120:123], v[216:219]\n"
"ds_read_b64_tr_b16 a[104:105], v243 offset:1536\n"
"v_mul_f32 v103, 1040356334, v103\n"
"v_mfma_f32_16x16x32_bf16 v[216:219], a[52:55], a[124:127], v[216:219]\n"
"ds_read_b64_tr_b16 a[106:107], v243 offset:1792\n"
"v_cvt_pk_bf16_f32 v96, v96, v97\n"
"v_mfma_f32_16x16x32_bf16 v[220:223], a[56:59], a[120:123], v[220:223]\n"
"ds_read_b64_tr_b16 a[68:69], v242 offset:16896\n"
"v_cvt_pk_bf16_f32 v97, v98, v99\n"
"v_mfma_f32_16x16x32_bf16 v[220:223], a[60:63], a[124:127], v[220:223]\n"
"ds_read_b64_tr_b16 a[70:71], v242 offset:17152\n"
"v_cvt_pk_bf16_f32 v98, v100, v101\n"
"v_mfma_f32_16x16x32_bf16 v[224:227], a[32:35], a[128:131], v[224:227]\n"
"ds_read_b64_tr_b16 a[76:77], v243 offset:16896\n"
"v_cvt_pk_bf16_f32 v99, v102, v103\n"
"v_mfma_f32_16x16x32_bf16 v[224:227], a[36:39], a[132:135], v[224:227]\n"
"buffer_store_dwordx4 v[96:99], v248, s[4:7], s36 offen offset:0 sc1\n"
"v_mfma_f32_16x16x32_bf16 v[228:231], a[40:43], a[128:131], v[228:231]\n"
"ds_read_b64_tr_b16 a[78:79], v243 offset:17152\n"
"s_add_u32 m0, s90, 131072\n"
"v_mfma_f32_16x16x32_bf16 v[228:231], a[44:47], a[132:135], v[228:231]\n"
"buffer_load_dwordx4 v246, s[84:87], s92 offen lds\n"
"v_mfma_f32_16x16x32_bf16 v[232:235], a[48:51], a[128:131], v[232:235]\n"
"ds_read_b64_tr_b16 a[84:85], v242 offset:17408\n"
"ds_read_b64_tr_b16 a[86:87], v242 offset:17664\n"
"v_mfma_f32_16x16x32_bf16 v[232:235], a[52:55], a[132:135], v[232:235]\n"
"ds_read_b64_tr_b16 a[92:93], v243 offset:17408\n"
"s_add_u32 m0, s90, 132096\n"
"v_mfma_f32_16x16x32_bf16 v[236:239], a[56:59], a[128:131], v[236:239]\n"
"ds_read_b64_tr_b16 a[94:95], v243 offset:17664\n"
"s_add_u32 s88, s92, s94\n"
"v_mfma_f32_16x16x32_bf16 v[236:239], a[60:63], a[132:135], v[236:239]\n"
"buffer_load_dwordx4 v246, s[84:87], s88 offen lds\n"
"v_mfma_f32_16x16x32_bf16 v[0:3], a[136:139], a[32:35], v[0:3]\n"
"ds_read_b64_tr_b16 a[100:101], v242 offset:17920\n"
"s_add_u32 m0, s90, 133120\n"
"v_mfma_f32_16x16x32_bf16 v[0:3], a[140:143], a[36:39], v[0:3]\n"
"ds_read_b64_tr_b16 a[102:103], v242 offset:18176\n"
"s_add_u32 s89, s92, s93\n"
"v_mfma_f32_16x16x32_bf16 v[32:35], a[136:139], a[40:43], v[32:35]\n"
"buffer_load_dwordx4 v247, s[84:87], s89 offen lds\n"
"v_mfma_f32_16x16x32_bf16 v[32:35], a[140:143], a[44:47], v[32:35]\n"
"ds_read_b64_tr_b16 a[108:109], v243 offset:17920\n"
"s_add_u32 m0, s90, 134144\n"
"v_mfma_f32_16x16x32_bf16 v[128:131], a[136:139], a[48:51], v[128:131]\n"
"ds_read_b64_tr_b16 a[110:111], v243 offset:18176\n"
"s_add_u32 s88, s89, s94\n"
"v_mfma_f32_16x16x32_bf16 v[128:131], a[140:143], a[52:55], v[128:131]\n"
"buffer_load_dwordx4 v247, s[84:87], s88 offen lds\n"
"v_mfma_f32_16x16x32_bf16 v[160:163], a[136:139], a[56:59], v[160:163]\n"
"s_add_u32 s92, s92, s95\n"
"v_bfe_u32 v64, v248, 4, 2\n"
"v_mfma_f32_16x16x32_bf16 v[160:163], a[140:143], a[60:63], v[160:163]\n"
"s_add_u32 m0, s90, 147456\n"
"v_lshrrev_b32 v65, v64, 5\n"
"v_mfma_f32_16x16x32_bf16 v[4:7], a[144:147], a[32:35], v[4:7]\n"
"buffer_load_dwordx4 v246, s[84:87], s92 offen lds\n"
"v_mfma_f32_16x16x32_bf16 v[4:7], a[148:151], a[36:39], v[4:7]\n"
"s_add_u32 m0, s90, 148480\n"
"v_bfe_i32 v65, v65, 0, 1\n"
"v_mfma_f32_16x16x32_bf16 v[36:39], a[144:147], a[40:43], v[36:39]\n"
"s_add_u32 s88, s92, s94\n"
"v_and_b32 v65, 0x3f803f80, v65\n"
"v_mfma_f32_16x16x32_bf16 v[36:39], a[148:151], a[44:47], v[36:39]\n"
"buffer_load_dwordx4 v246, s[84:87], s88 offen lds\n"
"v_mfma_f32_16x16x32_bf16 v[132:135], a[144:147], a[48:51], v[132:135]\n"
"s_add_u32 m0, s90, 149504\n"
"v_accvgpr_write_b32 a232, v65\n"
"v_mfma_f32_16x16x32_bf16 v[132:135], a[148:151], a[52:55], v[132:135]\n"
"s_add_u32 s89, s92, s93\n"
"v_accvgpr_write_b32 a233, v65\n"
"v_mfma_f32_16x16x32_bf16 v[164:167], a[144:147], a[56:59], v[164:167]\n"
"buffer_load_dwordx4 v247, s[84:87], s89 offen lds\n"
"v_mfma_f32_16x16x32_bf16 v[164:167], a[148:151], a[60:63], v[164:167]\n"
"v_accvgpr_write_b32 a234, v65\n"
"v_accvgpr_write_b32 a235, v65\n"
"s_waitcnt vmcnt(16) lgkmcnt(0)\n"
"v_mfma_f32_16x16x32_bf16 v[8:11], a[64:67], a[32:35], v[8:11]\n"
"s_barrier\n"
"v_mfma_f32_16x16x32_bf16 v[8:11], a[68:71], a[36:39], v[8:11]\n"
"v_add_u32 v240, 4294901760, v240\n"
"v_bfe_u32 v254, v248, 4, 3\n"
"v_mfma_f32_16x16x32_bf16 v[40:43], a[64:67], a[40:43], v[40:43]\n"
"v_add_u32 v241, 4294901760, v241\n"
"s_lshl_b32 s59, s10, 7\n"
"v_mfma_f32_16x16x32_bf16 v[40:43], a[68:71], a[44:47], v[40:43]\n"
"ds_read_b128 a[0:3], v240 offset:0\n"
"s_add_u32 s59, s59, 4096\n"
"v_mfma_f32_16x16x32_bf16 v[136:139], a[64:67], a[48:51], v[136:139]\n"
"ds_read_b128 a[4:7], v241 offset:0\n"
"v_lshl_add_u32 v254, v254, 3, s59\n"
"v_mfma_f32_16x16x32_bf16 v[136:139], a[68:71], a[52:55], v[136:139]\n"
"global_load_dwordx2 v[252:253], v254, s[62:63] sc1\n"
"v_mfma_f32_16x16x32_bf16 v[168:171], a[64:67], a[56:59], v[168:171]\n"
"ds_read_b128 a[8:11], v240 offset:2048\n"
"s_add_u32 m0, s90, 150528\n"
"v_mfma_f32_16x16x32_bf16 v[168:171], a[68:71], a[60:63], v[168:171]\n"
"ds_read_b128 a[12:15], v241 offset:2048\n"
"s_add_u32 s88, s89, s94\n"
"v_mfma_f32_16x16x32_bf16 v[12:15], a[72:75], a[32:35], v[12:15]\n"
"buffer_load_dwordx4 v247, s[84:87], s88 offen lds\n"
"v_mfma_f32_16x16x32_bf16 v[12:15], a[76:79], a[36:39], v[12:15]\n"
"ds_read_b128 a[16:19], v240 offset:16384\n"
"s_add_u32 s92, s92, s95\n"
"v_mfma_f32_16x16x32_bf16 v[44:47], a[72:75], a[40:43], v[44:47]\n"
"ds_read_b128 a[20:23], v241 offset:16384\n"
"v_mul_f32 v104, 1040356334, v104\n"
"v_mfma_f32_16x16x32_bf16 v[44:47], a[76:79], a[44:47], v[44:47]\n"
"ds_read_b128 a[24:27], v240 offset:18432\n"
"v_mul_f32 v105, 1040356334, v105\n"
"v_mfma_f32_16x16x32_bf16 v[140:143], a[72:75], a[48:51], v[140:143]\n"
"ds_read_b128 a[28:31], v241 offset:18432\n"
"v_add_u32 v242, 65536, v242\n"
"v_mfma_f32_16x16x32_bf16 v[140:143], a[76:79], a[52:55], v[140:143]\n"
"v_add_u32 v243, 65536, v243\n"
"v_mul_f32 v106, 1040356334, v106\n"
"v_mfma_f32_16x16x32_bf16 v[172:175], a[72:75], a[56:59], v[172:175]\n"
"ds_read_b64_tr_b16 a[112:113], v242 offset:8192\n"
"v_mul_f32 v107, 1040356334, v107\n"
"v_mfma_f32_16x16x32_bf16 v[172:175], a[76:79], a[60:63], v[172:175]\n"
"ds_read_b64_tr_b16 a[114:115], v242 offset:8448\n"
"v_mul_f32 v108, 1040356334, v108\n"
"v_mfma_f32_16x16x32_bf16 v[16:19], a[80:83], a[32:35], v[16:19]\n"
"ds_read_b64_tr_b16 a[120:121], v243 offset:8192\n"
"v_mul_f32 v109, 1040356334, v109\n"
"v_mfma_f32_16x16x32_bf16 v[16:19], a[84:87], a[36:39], v[16:19]\n"
"ds_read_b64_tr_b16 a[122:123], v243 offset:8448\n"
"v_mul_f32 v110, 1040356334, v110\n"
"v_mfma_f32_16x16x32_bf16 v[48:51], a[80:83], a[40:43], v[48:51]\n"
"ds_read_b64_tr_b16 a[128:129], v242 offset:8704\n"
"v_mul_f32 v111, 1040356334, v111\n"
"v_mfma_f32_16x16x32_bf16 v[48:51], a[84:87], a[44:47], v[48:51]\n"
"ds_read_b64_tr_b16 a[130:131], v242 offset:8960\n"
"v_cvt_pk_bf16_f32 v104, v104, v105\n"
"v_mfma_f32_16x16x32_bf16 v[144:147], a[80:83], a[48:51], v[144:147]\n"
"ds_read_b64_tr_b16 a[136:137], v242 offset:0\n"
"v_cvt_pk_bf16_f32 v105, v106, v107\n"
"v_mfma_f32_16x16x32_bf16 v[144:147], a[84:87], a[52:55], v[144:147]\n"
"ds_read_b64_tr_b16 a[138:139], v242 offset:256\n"
"v_cvt_pk_bf16_f32 v106, v108, v109\n"
"v_mfma_f32_16x16x32_bf16 v[176:179], a[80:83], a[56:59], v[176:179]\n"
"ds_read_b64_tr_b16 a[144:145], v243 offset:0\n"
"v_cvt_pk_bf16_f32 v107, v110, v111\n"
"v_mfma_f32_16x16x32_bf16 v[176:179], a[84:87], a[60:63], v[176:179]\n"
"buffer_store_dwordx4 v[104:107], v248, s[4:7], s36 offen offset:1024 sc1\n"
"v_mfma_f32_16x16x32_bf16 v[20:23], a[88:91], a[32:35], v[20:23]\n"
"ds_read_b64_tr_b16 a[146:147], v243 offset:256\n"
"ds_read_b64_tr_b16 a[116:117], v242 offset:24576\n"
"v_mfma_f32_16x16x32_bf16 v[20:23], a[92:95], a[36:39], v[20:23]\n"
"ds_read_b64_tr_b16 a[118:119], v242 offset:24832\n"
"s_add_u32 m0, s90, 0\n"
"v_mfma_f32_16x16x32_bf16 v[52:55], a[88:91], a[40:43], v[52:55]\n"
"ds_read_b64_tr_b16 a[124:125], v243 offset:24576\n"
"s_add_u32 s88, s91, 768\n"
"v_mfma_f32_16x16x32_bf16 v[52:55], a[92:95], a[44:47], v[52:55]\n"
"buffer_load_dwordx4 v244, s[0:3], s88 offen lds\n"
"v_mfma_f32_16x16x32_bf16 v[148:151], a[88:91], a[48:51], v[148:151]\n"
"ds_read_b64_tr_b16 a[126:127], v243 offset:24832\n"
"s_add_u32 m0, s90, 1024\n"
"v_mfma_f32_16x16x32_bf16 v[148:151], a[92:95], a[52:55], v[148:151]\n"
"ds_read_b64_tr_b16 a[132:133], v242 offset:25088\n"
"s_add_u32 s88, s91, 8960\n"
"v_mfma_f32_16x16x32_bf16 v[180:183], a[88:91], a[56:59], v[180:183]\n"
"buffer_load_dwordx4 v245, s[0:3], s88 offen lds\n"
"v_mfma_f32_16x16x32_bf16 v[180:183], a[92:95], a[60:63], v[180:183]\n"
"ds_read_b64_tr_b16 a[134:135], v242 offset:25344\n"
"s_add_u32 m0, s90, 2048\n"
"v_mfma_f32_16x16x32_bf16 v[24:27], a[96:99], a[32:35], v[24:27]\n"
"ds_read_b64_tr_b16 a[140:141], v242 offset:16384\n"
"s_add_u32 s88, s91, 17152\n"
"v_mfma_f32_16x16x32_bf16 v[24:27], a[100:103], a[36:39], v[24:27]\n"
"buffer_load_dwordx4 v244, s[0:3], s88 offen lds\n"
"v_mfma_f32_16x16x32_bf16 v[56:59], a[96:99], a[40:43], v[56:59]\n"
"ds_read_b64_tr_b16 a[142:143], v242 offset:16640\n"
"s_add_u32 m0, s90, 3072\n"
"v_mfma_f32_16x16x32_bf16 v[56:59], a[100:103], a[44:47], v[56:59]\n"
"ds_read_b64_tr_b16 a[148:149], v243 offset:16384\n"
"s_add_u32 s88, s91, 25344\n"
"v_mfma_f32_16x16x32_bf16 v[152:155], a[96:99], a[48:51], v[152:155]\n"
"ds_read_b64_tr_b16 a[150:151], v243 offset:16640\n"
"v_lshrrev_b32 v65, v64, 6\n"
"v_mfma_f32_16x16x32_bf16 v[152:155], a[100:103], a[52:55], v[152:155]\n"
"buffer_load_dwordx4 v245, s[0:3], s88 offen lds\n"
"v_mfma_f32_16x16x32_bf16 v[184:187], a[96:99], a[56:59], v[184:187]\n"
"s_add_u32 m0, s90, 16384\n"
"v_bfe_i32 v65, v65, 0, 1\n"
"v_mfma_f32_16x16x32_bf16 v[184:187], a[100:103], a[60:63], v[184:187]\n"
"s_add_u32 s88, s49, 768\n"
"v_and_b32 v65, 0x3f803f80, v65\n"
"v_mfma_f32_16x16x32_bf16 v[28:31], a[104:107], a[32:35], v[28:31]\n"
"buffer_load_dwordx4 v244, s[0:3], s88 offen lds\n"
"v_mfma_f32_16x16x32_bf16 v[28:31], a[108:111], a[36:39], v[28:31]\n"
"s_add_u32 m0, s90, 17408\n"
"v_accvgpr_write_b32 a236, v65\n"
"v_mfma_f32_16x16x32_bf16 v[60:63], a[104:107], a[40:43], v[60:63]\n"
"s_add_u32 s88, s49, 8960\n"
"v_accvgpr_write_b32 a237, v65\n"
"v_mfma_f32_16x16x32_bf16 v[60:63], a[108:111], a[44:47], v[60:63]\n"
"buffer_load_dwordx4 v245, s[0:3], s88 offen lds\n"
"v_mfma_f32_16x16x32_bf16 v[156:159], a[104:107], a[48:51], v[156:159]\n"
"s_add_u32 m0, s90, 18432\n"
"v_accvgpr_write_b32 a238, v65\n"
"v_mfma_f32_16x16x32_bf16 v[156:159], a[108:111], a[52:55], v[156:159]\n"
"s_add_u32 s88, s49, 17152\n"
"v_accvgpr_write_b32 a239, v65\n"
"v_mfma_f32_16x16x32_bf16 v[188:191], a[104:107], a[56:59], v[188:191]\n"
"buffer_load_dwordx4 v244, s[0:3], s88 offen lds\n"
"v_mfma_f32_16x16x32_bf16 v[188:191], a[108:111], a[60:63], v[188:191]\n"
"s_lshl_b32 s57, s12, 13\n"
"s_add_u32 s57, s57, 0x10000\n"
"s_waitcnt vmcnt(26) lgkmcnt(0)\n"
"v_mfma_f32_16x16x32_bf16 v[192:195], a[112:115], a[0:3], v[192:195]\n"
"s_barrier\n"
"v_mfma_f32_16x16x32_bf16 v[192:195], a[116:119], a[4:7], v[192:195]\n"
"ds_read_b64_tr_b16 a[64:65], v242 offset:512\n"
"s_add_u32 m0, s90, 19456\n"
"v_mfma_f32_16x16x32_bf16 v[196:199], a[112:115], a[8:11], v[196:199]\n"
"ds_read_b64_tr_b16 a[66:67], v242 offset:768\n"
"s_add_u32 s88, s49, 25344\n"
"v_mfma_f32_16x16x32_bf16 v[196:199], a[116:119], a[12:15], v[196:199]\n"
"ds_read_b64_tr_b16 a[72:73], v243 offset:512\n"
"v_add_u32 v66, s57, v248\n"
"v_mfma_f32_16x16x32_bf16 v[200:203], a[112:115], a[16:19], v[200:203]\n"
"buffer_load_dwordx4 v245, s[0:3], s88 offen lds\n"
"v_mfma_f32_16x16x32_bf16 v[200:203], a[116:119], a[20:23], v[200:203]\n"
"ds_read_b64_tr_b16 a[74:75], v243 offset:768\n"
"ds_read_b64_tr_b16 a[80:81], v242 offset:1024\n"
"v_mfma_f32_16x16x32_bf16 v[204:207], a[112:115], a[24:27], v[204:207]\n"
"ds_read_b64_tr_b16 a[82:83], v242 offset:1280\n"
"s_add_u32 m0, s90, 32768\n"
"v_mfma_f32_16x16x32_bf16 v[204:207], a[116:119], a[28:31], v[204:207]\n"
"buffer_load_dwordx4 v246, s[84:87], s92 offen lds\n"
"v_mfma_f32_16x16x32_bf16 v[208:211], a[0:3], a[120:123], v[208:211]\n"
"ds_read_b64_tr_b16 a[88:89], v243 offset:1024\n"
"ds_read_b64_tr_b16 a[90:91], v243 offset:1280\n"
"v_mfma_f32_16x16x32_bf16 v[208:211], a[4:7], a[124:127], v[208:211]\n"
"s_add_u32 m0, s90, 33792\n"
"s_mov_b32 s42, 1069066811\n"
"v_mfma_f32_16x16x32_bf16 v[212:215], a[8:11], a[120:123], v[212:215]\n"
"ds_read_b64_tr_b16 a[96:97], v242 offset:1536\n"
"v_mov_b32 v69, 0xb5c1a2a0\n"
"v_mfma_f32_16x16x32_bf16 v[212:215], a[12:15], a[124:127], v[212:215]\n"
"ds_read_b64_tr_b16 a[98:99], v242 offset:1792\n"
"s_add_u32 s88, s92, s94\n"
"v_mfma_f32_16x16x32_bf16 v[216:219], a[16:19], a[120:123], v[216:219]\n"
"buffer_load_dwordx4 v246, s[84:87], s88 offen lds\n"
"v_mfma_f32_16x16x32_bf16 v[216:219], a[20:23], a[124:127], v[216:219]\n"
"ds_read_b64_tr_b16 a[104:105], v243 offset:1536\n"
"ds_read_b64_tr_b16 a[106:107], v243 offset:1792\n"
"v_mfma_f32_16x16x32_bf16 v[220:223], a[24:27], a[120:123], v[220:223]\n"
"ds_read_b64_tr_b16 a[68:69], v242 offset:16896\n"
"s_add_u32 m0, s90, 34816\n"
"v_mfma_f32_16x16x32_bf16 v[220:223], a[28:31], a[124:127], v[220:223]\n"
"ds_read_b64_tr_b16 a[70:71], v242 offset:17152\n"
"v_mov_b32 v72, 0x3f803f80\n"
"v_mfma_f32_16x16x32_bf16 v[224:227], a[0:3], a[128:131], v[224:227]\n"
"ds_read_b64_tr_b16 a[76:77], v243 offset:16896\n"
"s_add_u32 s89, s92, s93\n"
"v_mfma_f32_16x16x32_bf16 v[224:227], a[4:7], a[132:135], v[224:227]\n"
"buffer_load_dwordx4 v247, s[84:87], s89 offen lds\n"
"v_mfma_f32_16x16x32_bf16 v[228:231], a[8:11], a[128:131], v[228:231]\n"
"ds_read_b64_tr_b16 a[78:79], v243 offset:17152\n"
"v_mov_b32 v73, 0x3f803f80\n"
"v_mfma_f32_16x16x32_bf16 v[228:231], a[12:15], a[132:135], v[228:231]\n"
"ds_read_b64_tr_b16 a[84:85], v242 offset:17408\n"
"s_add_u32 m0, s90, 35840\n"
"v_mfma_f32_16x16x32_bf16 v[232:235], a[16:19], a[128:131], v[232:235]\n"
"ds_read_b64_tr_b16 a[86:87], v242 offset:17664\n"
"v_mov_b32 v74, 0x3f803f80\n"
"v_mfma_f32_16x16x32_bf16 v[232:235], a[20:23], a[132:135], v[232:235]\n"
"ds_read_b64_tr_b16 a[92:93], v243 offset:17408\n"
"s_add_u32 s88, s89, s94\n"
"v_mfma_f32_16x16x32_bf16 v[236:239], a[24:27], a[128:131], v[236:239]\n"
"ds_read_b64_tr_b16 a[94:95], v243 offset:17664\n"
"v_mov_b32 v75, 0x3f803f80\n"
"v_mfma_f32_16x16x32_bf16 v[236:239], a[28:31], a[132:135], v[236:239]\n"
"buffer_load_dwordx4 v247, s[84:87], s88 offen lds\n"
"v_mfma_f32_16x16x32_bf16 v[0:3], a[136:139], a[0:3], v[0:3]\n"
"ds_read_b64_tr_b16 a[100:101], v242 offset:17920\n"
"s_add_u32 s92, s92, s95\n"
"v_mfma_f32_16x16x32_bf16 v[0:3], a[140:143], a[4:7], v[0:3]\n"
"ds_read_b64_tr_b16 a[102:103], v242 offset:18176\n"
"ds_read_b64_tr_b16 a[108:109], v243 offset:17920\n"
"v_mfma_f32_16x16x32_bf16 v[32:35], a[136:139], a[8:11], v[32:35]\n"
"ds_read_b64_tr_b16 a[110:111], v243 offset:18176\n"
"s_add_u32 m0, s90, 49152\n"
"v_mfma_f32_16x16x32_bf16 v[32:35], a[140:143], a[12:15], v[32:35]\n"
"v_mfma_f32_16x16x32_bf16 v[128:131], a[136:139], a[16:19], v[128:131]\n"
"buffer_load_dwordx4 v246, s[84:87], s92 offen lds\n"
"v_mfma_f32_16x16x32_bf16 v[128:131], a[140:143], a[20:23], v[128:131]\n"
"s_add_u32 m0, s90, 50176\n"
"v_mfma_f32_16x16x32_bf16 v[160:163], a[136:139], a[24:27], v[160:163]\n"
"v_mfma_f32_16x16x32_bf16 v[160:163], a[140:143], a[28:31], v[160:163]\n"
"s_add_u32 s88, s92, s94\n"
"v_mfma_f32_16x16x32_bf16 v[4:7], a[144:147], a[0:3], v[4:7]\n"
"v_mfma_f32_16x16x32_bf16 v[4:7], a[148:151], a[4:7], v[4:7]\n"
"buffer_load_dwordx4 v246, s[84:87], s88 offen lds\n"
"v_mfma_f32_16x16x32_bf16 v[36:39], a[144:147], a[8:11], v[36:39]\n"
"s_add_u32 m0, s90, 51200\n"
"v_mfma_f32_16x16x32_bf16 v[36:39], a[148:151], a[12:15], v[36:39]\n"
"v_mfma_f32_16x16x32_bf16 v[132:135], a[144:147], a[16:19], v[132:135]\n"
"s_add_u32 s89, s92, s93\n"
"v_mfma_f32_16x16x32_bf16 v[132:135], a[148:151], a[20:23], v[132:135]\n"
"v_mfma_f32_16x16x32_bf16 v[164:167], a[144:147], a[24:27], v[164:167]\n"
"buffer_load_dwordx4 v247, s[84:87], s89 offen lds\n"
"v_mfma_f32_16x16x32_bf16 v[164:167], a[148:151], a[28:31], v[164:167]\n"
"s_waitcnt vmcnt(16) lgkmcnt(0)\n"
"v_mfma_f32_16x16x32_bf16 v[8:11], a[64:67], a[0:3], v[8:11]\n"
"s_barrier\n"
"v_mfma_f32_16x16x32_bf16 v[8:11], a[68:71], a[4:7], v[8:11]\n"
"v_add_u32 v240, 65536, v240\n"
"s_add_u32 m0, s90, 52224\n"
"v_mfma_f32_16x16x32_bf16 v[40:43], a[64:67], a[8:11], v[40:43]\n"
"v_add_u32 v241, 65536, v241\n"
"s_add_u32 s88, s89, s94\n"
"v_mfma_f32_16x16x32_bf16 v[40:43], a[68:71], a[12:15], v[40:43]\n"
"ds_read_b128 a[32:35], v240 offset:0\n"
"v_mfma_f32_16x16x32_bf16 v[136:139], a[64:67], a[16:19], v[136:139]\n"
"buffer_load_dwordx4 v247, s[84:87], s88 offen lds\n"
"v_mfma_f32_16x16x32_bf16 v[136:139], a[68:71], a[20:23], v[136:139]\n"
"ds_read_b128 a[36:39], v241 offset:0\n"
"s_add_u32 s92, s92, s95\n"
"v_mfma_f32_16x16x32_bf16 v[168:171], a[64:67], a[24:27], v[168:171]\n"
"ds_read_b128 a[40:43], v240 offset:2048\n"
"ds_read_b128 a[44:47], v241 offset:2048\n"
"v_mfma_f32_16x16x32_bf16 v[168:171], a[68:71], a[28:31], v[168:171]\n"
"ds_read_b128 a[48:51], v240 offset:16384\n"
"s_add_u32 s36, s29, 811008\n"
"v_mfma_f32_16x16x32_bf16 v[12:15], a[72:75], a[0:3], v[12:15]\n"
"ds_read_b128 a[52:55], v241 offset:16384\n"
"v_cvt_pk_bf16_f32 v112, v112, v113\n"
"v_mfma_f32_16x16x32_bf16 v[12:15], a[76:79], a[4:7], v[12:15]\n"
"ds_read_b128 a[56:59], v240 offset:18432\n"
"v_mfma_f32_16x16x32_bf16 v[44:47], a[72:75], a[8:11], v[44:47]\n"
"ds_read_b128 a[60:63], v241 offset:18432\n"
"v_cvt_pk_bf16_f32 v113, v114, v115\n"
"v_mfma_f32_16x16x32_bf16 v[44:47], a[76:79], a[12:15], v[44:47]\n"
"v_add_u32 v242, 32768, v242\n"
"v_cvt_pk_bf16_f32 v114, v116, v117\n"
"v_mfma_f32_16x16x32_bf16 v[140:143], a[72:75], a[16:19], v[140:143]\n"
"v_add_u32 v243, 32768, v243\n"
"v_mfma_f32_16x16x32_bf16 v[140:143], a[76:79], a[20:23], v[140:143]\n"
"ds_read_b64_tr_b16 a[112:113], v242 offset:40960\n"
"v_cvt_pk_bf16_f32 v115, v118, v119\n"
"v_mfma_f32_16x16x32_bf16 v[172:175], a[72:75], a[24:27], v[172:175]\n"
"buffer_store_dwordx4 v[112:115], v248, s[4:7], s36 offen offset:0 sc1\n"
"v_mfma_f32_16x16x32_bf16 v[172:175], a[76:79], a[28:31], v[172:175]\n"
"ds_read_b64_tr_b16 a[114:115], v242 offset:41216\n"
"ds_read_b64_tr_b16 a[120:121], v243 offset:40960\n"
"v_mfma_f32_16x16x32_bf16 v[16:19], a[80:83], a[0:3], v[16:19]\n"
"ds_read_b64_tr_b16 a[122:123], v243 offset:41216\n"
"s_add_u32 m0, s90, 65536\n"
"v_mfma_f32_16x16x32_bf16 v[16:19], a[84:87], a[4:7], v[16:19]\n"
"ds_read_b64_tr_b16 a[128:129], v242 offset:41472\n"
"s_add_u32 s88, s91, 896\n"
"v_mfma_f32_16x16x32_bf16 v[48:51], a[80:83], a[8:11], v[48:51]\n"
"ds_read_b64_tr_b16 a[130:131], v242 offset:41728\n"
"v_mfma_f32_16x16x32_bf16 v[48:51], a[84:87], a[12:15], v[48:51]\n"
"buffer_load_dwordx4 v244, s[0:3], s88 offen lds\n"
"v_mfma_f32_16x16x32_bf16 v[144:147], a[80:83], a[16:19], v[144:147]\n"
"ds_read_b64_tr_b16 a[136:137], v242 offset:32768\n"
"s_add_u32 m0, s90, 66560\n"
"v_mfma_f32_16x16x32_bf16 v[144:147], a[84:87], a[20:23], v[144:147]\n"
"ds_read_b64_tr_b16 a[138:139], v242 offset:33024\n"
"ds_read_b64_tr_b16 a[144:145], v243 offset:32768\n"
"v_mfma_f32_16x16x32_bf16 v[176:179], a[80:83], a[24:27], v[176:179]\n"
"ds_read_b64_tr_b16 a[146:147], v243 offset:33024\n"
"s_add_u32 s88, s91, 9088\n"
"v_mfma_f32_16x16x32_bf16 v[176:179], a[84:87], a[28:31], v[176:179]\n"
"buffer_load_dwordx4 v245, s[0:3], s88 offen lds\n"
"v_mfma_f32_16x16x32_bf16 v[20:23], a[88:91], a[0:3], v[20:23]\n"
"ds_read_b64_tr_b16 a[116:117], v242 offset:57344\n"
"ds_read_b64_tr_b16 a[118:119], v242 offset:57600\n"
"v_mfma_f32_16x16x32_bf16 v[20:23], a[92:95], a[4:7], v[20:23]\n"
"ds_read_b64_tr_b16 a[124:125], v243 offset:57344\n"
"s_add_u32 m0, s90, 67584\n"
"v_mfma_f32_16x16x32_bf16 v[52:55], a[88:91], a[8:11], v[52:55]\n"
"ds_read_b64_tr_b16 a[126:127], v243 offset:57600\n"
"s_add_u32 s88, s91, 17280\n"
"v_mfma_f32_16x16x32_bf16 v[52:55], a[92:95], a[12:15], v[52:55]\n"
"ds_read_b64_tr_b16 a[132:133], v242 offset:57856\n"
"v_mfma_f32_16x16x32_bf16 v[148:151], a[88:91], a[16:19], v[148:151]\n"
"Lfused_hop6:\n"
"s_cbranch_execz Lfused_hop10\n"
"buffer_load_dwordx4 v244, s[0:3], s88 offen lds\n"
"v_mfma_f32_16x16x32_bf16 v[148:151], a[92:95], a[20:23], v[148:151]\n"
"ds_read_b64_tr_b16 a[134:135], v242 offset:58112\n"
"s_add_u32 m0, s90, 68608\n"
"v_mfma_f32_16x16x32_bf16 v[180:183], a[88:91], a[24:27], v[180:183]\n"
"ds_read_b64_tr_b16 a[140:141], v242 offset:49152\n"
"ds_read_b64_tr_b16 a[142:143], v242 offset:49408\n"
"v_mfma_f32_16x16x32_bf16 v[180:183], a[92:95], a[28:31], v[180:183]\n"
"ds_read_b64_tr_b16 a[148:149], v243 offset:49152\n"
"s_add_u32 s88, s91, 25472\n"
"v_mfma_f32_16x16x32_bf16 v[24:27], a[96:99], a[0:3], v[24:27]\n"
"buffer_load_dwordx4 v245, s[0:3], s88 offen lds\n"
"v_mfma_f32_16x16x32_bf16 v[24:27], a[100:103], a[4:7], v[24:27]\n"
"ds_read_b64_tr_b16 a[150:151], v243 offset:49408\n"
"v_mfma_f32_16x16x32_bf16 v[56:59], a[96:99], a[8:11], v[56:59]\n"
"s_add_u32 m0, s90, 81920\n"
"v_mfma_f32_16x16x32_bf16 v[56:59], a[100:103], a[12:15], v[56:59]\n"
"s_add_u32 s88, s49, 896\n"
"v_mfma_f32_16x16x32_bf16 v[152:155], a[96:99], a[16:19], v[152:155]\n"
"v_mfma_f32_16x16x32_bf16 v[152:155], a[100:103], a[20:23], v[152:155]\n"
"buffer_load_dwordx4 v244, s[0:3], s88 offen lds\n"
"v_mfma_f32_16x16x32_bf16 v[184:187], a[96:99], a[24:27], v[184:187]\n"
"s_add_u32 m0, s90, 82944\n"
"v_mfma_f32_16x16x32_bf16 v[184:187], a[100:103], a[28:31], v[184:187]\n"
"v_mfma_f32_16x16x32_bf16 v[28:31], a[104:107], a[0:3], v[28:31]\n"
"s_add_u32 s88, s49, 9088\n"
"v_mfma_f32_16x16x32_bf16 v[28:31], a[108:111], a[4:7], v[28:31]\n"
"buffer_load_dwordx4 v245, s[0:3], s88 offen lds\n"
"v_mfma_f32_16x16x32_bf16 v[60:63], a[104:107], a[8:11], v[60:63]\n"
"v_mfma_f32_16x16x32_bf16 v[60:63], a[108:111], a[12:15], v[60:63]\n"
"s_add_u32 m0, s90, 83968\n"
"v_mfma_f32_16x16x32_bf16 v[156:159], a[104:107], a[16:19], v[156:159]\n"
"s_add_u32 s88, s49, 17280\n"
"v_mfma_f32_16x16x32_bf16 v[156:159], a[108:111], a[20:23], v[156:159]\n"
"v_mfma_f32_16x16x32_bf16 v[188:191], a[104:107], a[24:27], v[188:191]\n"
"buffer_load_dwordx4 v244, s[0:3], s88 offen lds\n"
"v_mfma_f32_16x16x32_bf16 v[188:191], a[108:111], a[28:31], v[188:191]\n"
"s_waitcnt vmcnt(25) lgkmcnt(0)\n"
"v_mfma_f32_16x16x32_bf16 v[192:195], a[112:115], a[32:35], v[192:195]\n"
"s_barrier\n"
"v_mfma_f32_16x16x32_bf16 v[192:195], a[116:119], a[36:39], v[192:195]\n"
"ds_read_b64_tr_b16 a[64:65], v242 offset:33280\n"
"s_add_u32 m0, s90, 84992\n"
"v_mfma_f32_16x16x32_bf16 v[196:199], a[112:115], a[40:43], v[196:199]\n"
"ds_read_b64_tr_b16 a[66:67], v242 offset:33536\n"
"s_add_u32 s88, s49, 25472\n"
"v_mfma_f32_16x16x32_bf16 v[196:199], a[116:119], a[44:47], v[196:199]\n"
"buffer_load_dwordx4 v245, s[0:3], s88 offen lds\n"
"v_mfma_f32_16x16x32_bf16 v[200:203], a[112:115], a[48:51], v[200:203]\n"
"ds_read_b64_tr_b16 a[72:73], v243 offset:33280\n"
"ds_read_b64_tr_b16 a[74:75], v243 offset:33536\n"
"v_mfma_f32_16x16x32_bf16 v[200:203], a[116:119], a[52:55], v[200:203]\n"
"ds_read_b64_tr_b16 a[80:81], v242 offset:33792\n"
"v_cvt_pk_bf16_f32 v120, v120, v121\n"
"v_mfma_f32_16x16x32_bf16 v[204:207], a[112:115], a[56:59], v[204:207]\n"
"ds_read_b64_tr_b16 a[82:83], v242 offset:34048\n"
"v_cvt_pk_bf16_f32 v121, v122, v123\n"
"v_mfma_f32_16x16x32_bf16 v[204:207], a[116:119], a[60:63], v[204:207]\n"
"ds_read_b64_tr_b16 a[88:89], v243 offset:33792\n"
"v_cvt_pk_bf16_f32 v122, v124, v125\n"
"v_mfma_f32_16x16x32_bf16 v[208:211], a[32:35], a[120:123], v[208:211]\n"
"ds_read_b64_tr_b16 a[90:91], v243 offset:34048\n"
"v_mfma_f32_16x16x32_bf16 v[208:211], a[36:39], a[124:127], v[208:211]\n"
"v_cvt_pk_bf16_f32 v123, v126, v127\n"
"v_mfma_f32_16x16x32_bf16 v[212:215], a[40:43], a[120:123], v[212:215]\n"
"buffer_store_dwordx4 v[120:123], v248, s[4:7], s36 offen offset:1024 sc1\n"
"v_mfma_f32_16x16x32_bf16 v[212:215], a[44:47], a[124:127], v[212:215]\n"
"ds_read_b64_tr_b16 a[96:97], v242 offset:34304\n"
"ds_read_b64_tr_b16 a[98:99], v242 offset:34560\n"
"v_mfma_f32_16x16x32_bf16 v[216:219], a[48:51], a[120:123], v[216:219]\n"
"ds_read_b64_tr_b16 a[104:105], v243 offset:34304\n"
"s_add_u32 m0, s90, 98304\n"
"v_mfma_f32_16x16x32_bf16 v[216:219], a[52:55], a[124:127], v[216:219]\n"
"buffer_load_dwordx4 v246, s[84:87], s92 offen lds\n"
"v_mfma_f32_16x16x32_bf16 v[220:223], a[56:59], a[120:123], v[220:223]\n"
"ds_read_b64_tr_b16 a[106:107], v243 offset:34560\n"
"s_add_u32 m0, s90, 99328\n"
"v_mfma_f32_16x16x32_bf16 v[220:223], a[60:63], a[124:127], v[220:223]\n"
"ds_read_b64_tr_b16 a[68:69], v242 offset:49664\n"
"ds_read_b64_tr_b16 a[70:71], v242 offset:49920\n"
"v_mfma_f32_16x16x32_bf16 v[224:227], a[32:35], a[128:131], v[224:227]\n"
"ds_read_b64_tr_b16 a[76:77], v243 offset:49664\n"
"s_add_u32 s88, s92, s94\n"
"v_mfma_f32_16x16x32_bf16 v[224:227], a[36:39], a[132:135], v[224:227]\n"
"buffer_load_dwordx4 v246, s[84:87], s88 offen lds\n"
"v_mfma_f32_16x16x32_bf16 v[228:231], a[40:43], a[128:131], v[228:231]\n"
"ds_read_b64_tr_b16 a[78:79], v243 offset:49920\n"
"v_mfma_f32_16x16x32_bf16 v[228:231], a[44:47], a[132:135], v[228:231]\n"
"ds_read_b64_tr_b16 a[84:85], v242 offset:50176\n"
"s_add_u32 m0, s90, 100352\n"
"v_mfma_f32_16x16x32_bf16 v[232:235], a[48:51], a[128:131], v[232:235]\n"
"ds_read_b64_tr_b16 a[86:87], v242 offset:50432\n"
"s_add_u32 s89, s92, s93\n"
"v_mfma_f32_16x16x32_bf16 v[232:235], a[52:55], a[132:135], v[232:235]\n"
"buffer_load_dwordx4 v247, s[84:87], s89 offen lds\n"
"v_mfma_f32_16x16x32_bf16 v[236:239], a[56:59], a[128:131], v[236:239]\n"
"ds_read_b64_tr_b16 a[92:93], v243 offset:50176\n"
"ds_read_b64_tr_b16 a[94:95], v243 offset:50432\n"
"v_mfma_f32_16x16x32_bf16 v[236:239], a[60:63], a[132:135], v[236:239]\n"
"ds_read_b64_tr_b16 a[100:101], v242 offset:50688\n"
"s_add_u32 m0, s90, 101376\n"
"v_mfma_f32_16x16x32_bf16 v[0:3], a[136:139], a[32:35], v[0:3]\n"
"ds_read_b64_tr_b16 a[102:103], v242 offset:50944\n"
"s_add_u32 s88, s89, s94\n"
"v_mfma_f32_16x16x32_bf16 v[0:3], a[140:143], a[36:39], v[0:3]\n"
"ds_read_b64_tr_b16 a[108:109], v243 offset:50688\n"
"v_mfma_f32_16x16x32_bf16 v[32:35], a[136:139], a[40:43], v[32:35]\n"
"buffer_load_dwordx4 v247, s[84:87], s88 offen lds\n"
"v_mfma_f32_16x16x32_bf16 v[32:35], a[140:143], a[44:47], v[32:35]\n"
"ds_read_b64_tr_b16 a[110:111], v243 offset:50944\n"
"s_add_u32 s92, s92, s95\n"
"v_mfma_f32_16x16x32_bf16 v[128:131], a[136:139], a[48:51], v[128:131]\n"
"s_add_u32 m0, s90, 114688\n"
"v_mfma_f32_16x16x32_bf16 v[128:131], a[140:143], a[52:55], v[128:131]\n"
"v_mfma_f32_16x16x32_bf16 v[160:163], a[136:139], a[56:59], v[160:163]\n"
"buffer_load_dwordx4 v246, s[84:87], s92 offen lds\n"
"v_mfma_f32_16x16x32_bf16 v[160:163], a[140:143], a[60:63], v[160:163]\n"
"s_add_u32 m0, s90, 115712\n"
"v_mfma_f32_16x16x32_bf16 v[4:7], a[144:147], a[32:35], v[4:7]\n"
"v_mfma_f32_16x16x32_bf16 v[4:7], a[148:151], a[36:39], v[4:7]\n"
"s_add_u32 s88, s92, s94\n"
"v_mfma_f32_16x16x32_bf16 v[36:39], a[144:147], a[40:43], v[36:39]\n"
"buffer_load_dwordx4 v246, s[84:87], s88 offen lds\n"
"v_mfma_f32_16x16x32_bf16 v[36:39], a[148:151], a[44:47], v[36:39]\n"
"s_add_u32 m0, s90, 116736\n"
"v_mfma_f32_16x16x32_bf16 v[132:135], a[144:147], a[48:51], v[132:135]\n"
"v_mfma_f32_16x16x32_bf16 v[132:135], a[148:151], a[52:55], v[132:135]\n"
"s_add_u32 s89, s92, s93\n"
"v_mfma_f32_16x16x32_bf16 v[164:167], a[144:147], a[56:59], v[164:167]\n"
"buffer_load_dwordx4 v247, s[84:87], s89 offen lds\n"
"v_mfma_f32_16x16x32_bf16 v[164:167], a[148:151], a[60:63], v[164:167]\n"
"s_waitcnt vmcnt(17) lgkmcnt(0)\n"
"v_mfma_f32_16x16x32_bf16 v[8:11], a[64:67], a[32:35], v[8:11]\n"
"s_barrier\n"
"v_mfma_f32_16x16x32_bf16 v[8:11], a[68:71], a[36:39], v[8:11]\n"
"v_add_u32 v240, 4294868992, v240\n"
"s_add_u32 m0, s90, 117760\n"
"v_mfma_f32_16x16x32_bf16 v[40:43], a[64:67], a[40:43], v[40:43]\n"
"v_add_u32 v241, 4294868992, v241\n"
"s_add_u32 s88, s89, s94\n"
"v_mfma_f32_16x16x32_bf16 v[40:43], a[68:71], a[44:47], v[40:43]\n"
"ds_read_b128 a[0:3], v240 offset:0\n"
"v_mov_b64 v[96:97], 0\n"
"v_mfma_f32_16x16x32_bf16 v[136:139], a[64:67], a[48:51], v[136:139]\n"
"buffer_load_dwordx4 v247, s[84:87], s88 offen lds\n"
"v_mfma_f32_16x16x32_bf16 v[136:139], a[68:71], a[52:55], v[136:139]\n"
"ds_read_b128 a[4:7], v241 offset:0\n"
"s_add_u32 s92, s92, s95\n"
"v_mfma_f32_16x16x32_bf16 v[168:171], a[64:67], a[56:59], v[168:171]\n"
"ds_read_b128 a[8:11], v240 offset:2048\n"
"ds_read_b128 a[12:15], v241 offset:2048\n"
"v_mfma_f32_16x16x32_bf16 v[168:171], a[68:71], a[60:63], v[168:171]\n"
"ds_read_b128 a[16:19], v240 offset:16384\n"
"s_mov_b64 s[0:1], s[50:51]\n"
"v_mfma_f32_16x16x32_bf16 v[12:15], a[72:75], a[32:35], v[12:15]\n"
"ds_read_b128 a[20:23], v241 offset:16384\n"
"v_mov_b64 v[98:99], 0\n"
"v_mfma_f32_16x16x32_bf16 v[12:15], a[76:79], a[36:39], v[12:15]\n"
"ds_read_b128 a[24:27], v240 offset:18432\n"
"s_mov_b32 s2, 4194304\n"
"v_mfma_f32_16x16x32_bf16 v[44:47], a[72:75], a[40:43], v[44:47]\n"
"ds_read_b128 a[28:31], v241 offset:18432\n"
"s_add_u32 m0, s90, 131072\n"
"v_mfma_f32_16x16x32_bf16 v[44:47], a[76:79], a[44:47], v[44:47]\n"
"v_add_u32 v242, 4294901760, v242\n"
"v_mov_b64 v[100:101], 0\n"
"v_mfma_f32_16x16x32_bf16 v[140:143], a[72:75], a[48:51], v[140:143]\n"
"v_add_u32 v243, 4294901760, v243\n"
"s_add_u32 s88, s54, 0\n"
"v_mfma_f32_16x16x32_bf16 v[140:143], a[76:79], a[52:55], v[140:143]\n"
"buffer_load_dwordx4 v92, s[0:3], s88 offen lds\n"
"v_mfma_f32_16x16x32_bf16 v[172:175], a[72:75], a[56:59], v[172:175]\n"
"ds_read_b64_tr_b16 a[112:113], v242 offset:8192\n"
"ds_read_b64_tr_b16 a[114:115], v242 offset:8448\n"
"v_mfma_f32_16x16x32_bf16 v[172:175], a[76:79], a[60:63], v[172:175]\n"
"ds_read_b64_tr_b16 a[120:121], v243 offset:8192\n"
"s_add_u32 m0, s90, 132096\n"
"v_mfma_f32_16x16x32_bf16 v[16:19], a[80:83], a[32:35], v[16:19]\n"
"ds_read_b64_tr_b16 a[122:123], v243 offset:8448\n"
"v_mov_b64 v[102:103], 0\n"
"v_mfma_f32_16x16x32_bf16 v[16:19], a[84:87], a[36:39], v[16:19]\n"
"ds_read_b64_tr_b16 a[128:129], v242 offset:8704\n"
"s_add_u32 s88, s54, 8192\n"
"v_mfma_f32_16x16x32_bf16 v[48:51], a[80:83], a[40:43], v[48:51]\n"
"buffer_load_dwordx4 v92, s[0:3], s88 offen lds\n"
"v_mfma_f32_16x16x32_bf16 v[48:51], a[84:87], a[44:47], v[48:51]\n"
"ds_read_b64_tr_b16 a[130:131], v242 offset:8960\n"
"ds_read_b64_tr_b16 a[136:137], v242 offset:0\n"
"v_mfma_f32_16x16x32_bf16 v[144:147], a[80:83], a[48:51], v[144:147]\n"
"ds_read_b64_tr_b16 a[138:139], v242 offset:256\n"
"s_add_u32 m0, s90, 133120\n"
"v_mfma_f32_16x16x32_bf16 v[144:147], a[84:87], a[52:55], v[144:147]\n"
"ds_read_b64_tr_b16 a[144:145], v243 offset:0\n"
"v_mov_b64 v[104:105], 0\n"
"v_mfma_f32_16x16x32_bf16 v[176:179], a[80:83], a[56:59], v[176:179]\n"
"ds_read_b64_tr_b16 a[146:147], v243 offset:256\n"
"s_add_u32 s88, s54, 16384\n"
"v_mfma_f32_16x16x32_bf16 v[176:179], a[84:87], a[60:63], v[176:179]\n"
"buffer_load_dwordx4 v92, s[0:3], s88 offen lds\n"
"v_mfma_f32_16x16x32_bf16 v[20:23], a[88:91], a[32:35], v[20:23]\n"
"ds_read_b64_tr_b16 a[116:117], v242 offset:24576\n"
"ds_read_b64_tr_b16 a[118:119], v242 offset:24832\n"
"v_mfma_f32_16x16x32_bf16 v[20:23], a[92:95], a[36:39], v[20:23]\n"
"ds_read_b64_tr_b16 a[124:125], v243 offset:24576\n"
"s_add_u32 m0, s90, 134144\n"
"v_mfma_f32_16x16x32_bf16 v[52:55], a[88:91], a[40:43], v[52:55]\n"
"ds_read_b64_tr_b16 a[126:127], v243 offset:24832\n"
"s_add_u32 s88, s54, 24576\n"
"v_mfma_f32_16x16x32_bf16 v[52:55], a[92:95], a[44:47], v[52:55]\n"
"ds_read_b64_tr_b16 a[132:133], v242 offset:25088\n"
"v_mov_b64 v[106:107], 0\n"
"v_mfma_f32_16x16x32_bf16 v[148:151], a[88:91], a[48:51], v[148:151]\n"
"buffer_load_dwordx4 v92, s[0:3], s88 offen lds\n"
"v_mfma_f32_16x16x32_bf16 v[148:151], a[92:95], a[52:55], v[148:151]\n"
"ds_read_b64_tr_b16 a[134:135], v242 offset:25344\n"
"ds_read_b64_tr_b16 a[140:141], v242 offset:16384\n"
"v_mfma_f32_16x16x32_bf16 v[180:183], a[88:91], a[56:59], v[180:183]\n"
"ds_read_b64_tr_b16 a[142:143], v242 offset:16640\n"
"s_mov_b64 s[0:1], s[52:53]\n"
"v_mfma_f32_16x16x32_bf16 v[180:183], a[92:95], a[60:63], v[180:183]\n"
"ds_read_b64_tr_b16 a[148:149], v243 offset:16384\n"
"s_mov_b32 s2, s56\n"
"v_mfma_f32_16x16x32_bf16 v[24:27], a[96:99], a[32:35], v[24:27]\n"
"ds_read_b64_tr_b16 a[150:151], v243 offset:16640\n"
"v_mov_b64 v[108:109], 0\n"
"v_mfma_f32_16x16x32_bf16 v[24:27], a[100:103], a[36:39], v[24:27]\n"
"s_add_u32 m0, s90, 147456\n"
"v_mov_b64 v[110:111], 0\n"
"v_mfma_f32_16x16x32_bf16 v[56:59], a[96:99], a[40:43], v[56:59]\n"
"v_mov_b64 v[112:113], 0\n"
"v_mov_b64 v[114:115], 0\n"
"v_mfma_f32_16x16x32_bf16 v[56:59], a[100:103], a[44:47], v[56:59]\n"
"s_add_u32 s88, s55, 0\n"
"v_mov_b64 v[116:117], 0\n"
"v_mfma_f32_16x16x32_bf16 v[152:155], a[96:99], a[48:51], v[152:155]\n"
"buffer_load_dwordx4 v92, s[0:3], s88 offen lds\n"
"v_mfma_f32_16x16x32_bf16 v[152:155], a[100:103], a[52:55], v[152:155]\n"
"v_mov_b64 v[118:119], 0\n"
"v_mov_b64 v[120:121], 0\n"
"v_mfma_f32_16x16x32_bf16 v[184:187], a[96:99], a[56:59], v[184:187]\n"
"s_add_u32 m0, s90, 148480\n"
"v_mov_b64 v[122:123], 0\n"
"v_mfma_f32_16x16x32_bf16 v[184:187], a[100:103], a[60:63], v[184:187]\n"
"s_add_u32 s88, s55, 8192\n"
"v_mov_b64 v[124:125], 0\n"
"v_mfma_f32_16x16x32_bf16 v[28:31], a[104:107], a[32:35], v[28:31]\n"
"v_mov_b64 v[126:127], 0\n"
"v_mfma_f32_16x16x32_bf16 v[28:31], a[108:111], a[36:39], v[28:31]\n"
"buffer_load_dwordx4 v92, s[0:3], s88 offen lds\n"
"v_mfma_f32_16x16x32_bf16 v[60:63], a[104:107], a[40:43], v[60:63]\n"
"v_mfma_f32_16x16x32_bf16 v[60:63], a[108:111], a[44:47], v[60:63]\n"
"s_add_u32 m0, s90, 149504\n"
"v_mfma_f32_16x16x32_bf16 v[156:159], a[104:107], a[48:51], v[156:159]\n"
"s_add_u32 s88, s55, 16384\n"
"v_mfma_f32_16x16x32_bf16 v[156:159], a[108:111], a[52:55], v[156:159]\n"
"v_mfma_f32_16x16x32_bf16 v[188:191], a[104:107], a[56:59], v[188:191]\n"
"buffer_load_dwordx4 v92, s[0:3], s88 offen lds\n"
"v_mfma_f32_16x16x32_bf16 v[188:191], a[108:111], a[60:63], v[188:191]\n"
"s_waitcnt vmcnt(25) lgkmcnt(0)\n"
"v_cmp_ne_u64 vcc, s[98:99], v[252:253]\n"
"s_cbranch_vccnz Lfused_ps0\n"
"Lfused_pd0:\n"
"v_mfma_f32_16x16x32_bf16 v[192:195], a[112:115], a[0:3], v[192:195]\n"
"s_barrier\n"
"v_mfma_f32_16x16x32_bf16 v[192:195], a[116:119], a[4:7], v[192:195]\n"
"ds_read_b64_tr_b16 a[64:65], v242 offset:512\n"
"s_add_u32 m0, s90, 150528\n"
"v_mfma_f32_16x16x32_bf16 v[196:199], a[112:115], a[8:11], v[196:199]\n"
"ds_read_b64_tr_b16 a[66:67], v242 offset:768\n"
"v_mfma_f32_16x16x32_bf16 v[196:199], a[116:119], a[12:15], v[196:199]\n"
"ds_read_b64_tr_b16 a[72:73], v243 offset:512\n"
"s_add_u32 s88, s55, 24576\n"
"v_mfma_f32_16x16x32_bf16 v[200:203], a[112:115], a[16:19], v[200:203]\n"
"ds_read_b64_tr_b16 a[74:75], v243 offset:768\n"
"v_mfma_f32_16x16x32_bf16 v[200:203], a[116:119], a[20:23], v[200:203]\n"
"ds_read_b64_tr_b16 a[80:81], v242 offset:1024\n"
"v_mfma_f32_16x16x32_bf16 v[204:207], a[112:115], a[24:27], v[204:207]\n"
"buffer_load_dwordx4 v92, s[0:3], s88 offen lds\n"
"v_mfma_f32_16x16x32_bf16 v[204:207], a[116:119], a[28:31], v[204:207]\n"
"ds_read_b64_tr_b16 a[82:83], v242 offset:1280\n"
"ds_read_b64_tr_b16 a[88:89], v243 offset:1024\n"
"v_mfma_f32_16x16x32_bf16 v[208:211], a[0:3], a[120:123], v[208:211]\n"
"ds_read_b64_tr_b16 a[90:91], v243 offset:1280\n"
"v_mfma_f32_16x16x32_bf16 v[208:211], a[4:7], a[124:127], v[208:211]\n"
"s_add_u32 m0, s90, 0\n"
"v_mfma_f32_16x16x32_bf16 v[212:215], a[8:11], a[120:123], v[212:215]\n"
"ds_read_b64_tr_b16 a[96:97], v242 offset:1536\n"
"v_mfma_f32_16x16x32_bf16 v[212:215], a[12:15], a[124:127], v[212:215]\n"
"ds_read_b64_tr_b16 a[98:99], v242 offset:1792\n"
"v_mfma_f32_16x16x32_bf16 v[216:219], a[16:19], a[120:123], v[216:219]\n"
"ds_read_b64_tr_b16 a[104:105], v243 offset:1536\n"
"s_add_u32 s89, s37, 0\n"
"v_mfma_f32_16x16x32_bf16 v[216:219], a[20:23], a[124:127], v[216:219]\n"
"ds_read_b64_tr_b16 a[106:107], v243 offset:1792\n"
"v_mfma_f32_16x16x32_bf16 v[220:223], a[24:27], a[120:123], v[220:223]\n"
"buffer_load_dwordx4 v248, s[4:7], s89 offen offset:0 sc1 lds\n"
"v_mfma_f32_16x16x32_bf16 v[220:223], a[28:31], a[124:127], v[220:223]\n"
"ds_read_b64_tr_b16 a[68:69], v242 offset:16896\n"
"ds_read_b64_tr_b16 a[70:71], v242 offset:17152\n"
"v_mfma_f32_16x16x32_bf16 v[224:227], a[0:3], a[128:131], v[224:227]\n"
"ds_read_b64_tr_b16 a[76:77], v243 offset:16896\n"
"v_mfma_f32_16x16x32_bf16 v[224:227], a[4:7], a[132:135], v[224:227]\n"
"buffer_load_dwordx4 v248, s[4:7], s89 offen offset:1024 sc1 lds\n"
"v_mfma_f32_16x16x32_bf16 v[228:231], a[8:11], a[128:131], v[228:231]\n"
"ds_read_b64_tr_b16 a[78:79], v243 offset:17152\n"
"v_mfma_f32_16x16x32_bf16 v[228:231], a[12:15], a[132:135], v[228:231]\n"
"ds_read_b64_tr_b16 a[84:85], v242 offset:17408\n"
"v_mfma_f32_16x16x32_bf16 v[232:235], a[16:19], a[128:131], v[232:235]\n"
"buffer_load_dwordx4 v248, s[4:7], s89 offen offset:2048 sc1 lds\n"
"v_mfma_f32_16x16x32_bf16 v[232:235], a[20:23], a[132:135], v[232:235]\n"
"ds_read_b64_tr_b16 a[86:87], v242 offset:17664\n"
"ds_read_b64_tr_b16 a[92:93], v243 offset:17408\n"
"v_mfma_f32_16x16x32_bf16 v[236:239], a[24:27], a[128:131], v[236:239]\n"
"ds_read_b64_tr_b16 a[94:95], v243 offset:17664\n"
"v_mfma_f32_16x16x32_bf16 v[236:239], a[28:31], a[132:135], v[236:239]\n"
"buffer_load_dwordx4 v248, s[4:7], s89 offen offset:3072 sc1 lds\n"
"v_mfma_f32_16x16x32_bf16 v[0:3], a[136:139], a[0:3], v[0:3]\n"
"ds_read_b64_tr_b16 a[100:101], v242 offset:17920\n"
"ds_read_b64_tr_b16 a[102:103], v242 offset:18176\n"
"v_mfma_f32_16x16x32_bf16 v[0:3], a[140:143], a[4:7], v[0:3]\n"
"ds_read_b64_tr_b16 a[108:109], v243 offset:17920\n"
"v_mfma_f32_16x16x32_bf16 v[32:35], a[136:139], a[8:11], v[32:35]\n"
"ds_read_b64_tr_b16 a[110:111], v243 offset:18176\n"
"s_add_u32 m0, s90, 16384\n"
"v_mfma_f32_16x16x32_bf16 v[32:35], a[140:143], a[12:15], v[32:35]\n"
"v_mfma_f32_16x16x32_bf16 v[128:131], a[136:139], a[16:19], v[128:131]\n"
"s_add_u32 s89, s37, 32768\n"
"v_mfma_f32_16x16x32_bf16 v[128:131], a[140:143], a[20:23], v[128:131]\n"
"v_mfma_f32_16x16x32_bf16 v[160:163], a[136:139], a[24:27], v[160:163]\n"
"v_mfma_f32_16x16x32_bf16 v[160:163], a[140:143], a[28:31], v[160:163]\n"
"buffer_load_dwordx4 v248, s[4:7], s89 offen offset:0 sc1 lds\n"
"v_mfma_f32_16x16x32_bf16 v[4:7], a[144:147], a[0:3], v[4:7]\n"
"v_mfma_f32_16x16x32_bf16 v[4:7], a[148:151], a[4:7], v[4:7]\n"
"v_mfma_f32_16x16x32_bf16 v[36:39], a[144:147], a[8:11], v[36:39]\n"
"buffer_load_dwordx4 v248, s[4:7], s89 offen offset:1024 sc1 lds\n"
"v_mfma_f32_16x16x32_bf16 v[36:39], a[148:151], a[12:15], v[36:39]\n"
"v_mfma_f32_16x16x32_bf16 v[132:135], a[144:147], a[16:19], v[132:135]\n"
"v_mfma_f32_16x16x32_bf16 v[132:135], a[148:151], a[20:23], v[132:135]\n"
"buffer_load_dwordx4 v248, s[4:7], s89 offen offset:2048 sc1 lds\n"
"v_mfma_f32_16x16x32_bf16 v[164:167], a[144:147], a[24:27], v[164:167]\n"
"v_mfma_f32_16x16x32_bf16 v[164:167], a[148:151], a[28:31], v[164:167]\n"
"s_waitcnt vmcnt(15) lgkmcnt(0)\n"
"v_mfma_f32_16x16x32_bf16 v[8:11], a[64:67], a[0:3], v[8:11]\n"
"s_barrier\n"
"v_mfma_f32_16x16x32_bf16 v[8:11], a[68:71], a[4:7], v[8:11]\n"
"buffer_load_dwordx4 v248, s[4:7], s89 offen offset:3072 sc1 lds\n"
"v_mfma_f32_16x16x32_bf16 v[40:43], a[64:67], a[8:11], v[40:43]\n"
"v_add_u32 v240, 65536, v240\n"
"v_add_u32 v241, 65536, v241\n"
"v_mfma_f32_16x16x32_bf16 v[40:43], a[68:71], a[12:15], v[40:43]\n"
"ds_read_b128 a[32:35], v240 offset:0\n"
"v_mfma_f32_16x16x32_bf16 v[136:139], a[64:67], a[16:19], v[136:139]\n"
"ds_read_b128 a[36:39], v241 offset:0\n"
"s_add_u32 m0, s90, 32768\n"
"v_mfma_f32_16x16x32_bf16 v[136:139], a[68:71], a[20:23], v[136:139]\n"
"ds_read_b128 a[40:43], v240 offset:2048\n"
"v_mfma_f32_16x16x32_bf16 v[168:171], a[64:67], a[24:27], v[168:171]\n"
"ds_read_b128 a[44:47], v241 offset:2048\n"
"v_mfma_f32_16x16x32_bf16 v[168:171], a[68:71], a[28:31], v[168:171]\n"
"ds_read_b128 a[48:51], v240 offset:16384\n"
"v_mfma_f32_16x16x32_bf16 v[12:15], a[72:75], a[0:3], v[12:15]\n"
"ds_read_b128 a[52:55], v241 offset:16384\n"
"s_add_u32 s89, s37, 8192\n"
"v_mfma_f32_16x16x32_bf16 v[12:15], a[76:79], a[4:7], v[12:15]\n"
"ds_read_b128 a[56:59], v240 offset:18432\n"
"v_mfma_f32_16x16x32_bf16 v[44:47], a[72:75], a[8:11], v[44:47]\n"
"ds_read_b128 a[60:63], v241 offset:18432\n"
"v_mfma_f32_16x16x32_bf16 v[44:47], a[76:79], a[12:15], v[44:47]\n"
"v_add_u32 v242, 65536, v242\n"
"v_mfma_f32_16x16x32_bf16 v[140:143], a[72:75], a[16:19], v[140:143]\n"
"buffer_load_dwordx4 v248, s[4:7], s89 offen offset:0 sc1 lds\n"
"v_mfma_f32_16x16x32_bf16 v[140:143], a[76:79], a[20:23], v[140:143]\n"
"v_add_u32 v243, 65536, v243\n"
"ds_read_b64_tr_b16 a[112:113], v242 offset:8192\n"
"v_mfma_f32_16x16x32_bf16 v[172:175], a[72:75], a[24:27], v[172:175]\n"
"ds_read_b64_tr_b16 a[114:115], v242 offset:8448\n"
"v_mfma_f32_16x16x32_bf16 v[172:175], a[76:79], a[28:31], v[172:175]\n"
"ds_read_b64_tr_b16 a[120:121], v243 offset:8192\n"
"v_mfma_f32_16x16x32_bf16 v[16:19], a[80:83], a[0:3], v[16:19]\n"
"buffer_load_dwordx4 v248, s[4:7], s89 offen offset:1024 sc1 lds\n"
"v_mfma_f32_16x16x32_bf16 v[16:19], a[84:87], a[4:7], v[16:19]\n"
"ds_read_b64_tr_b16 a[122:123], v243 offset:8448\n"
"ds_read_b64_tr_b16 a[128:129], v242 offset:8704\n"
"v_mfma_f32_16x16x32_bf16 v[48:51], a[80:83], a[8:11], v[48:51]\n"
"ds_read_b64_tr_b16 a[130:131], v242 offset:8960\n"
"v_mfma_f32_16x16x32_bf16 v[48:51], a[84:87], a[12:15], v[48:51]\n"
"ds_read_b64_tr_b16 a[136:137], v242 offset:0\n"
"v_mfma_f32_16x16x32_bf16 v[144:147], a[80:83], a[16:19], v[144:147]\n"
"buffer_load_dwordx4 v248, s[4:7], s89 offen offset:2048 sc1 lds\n"
"v_mfma_f32_16x16x32_bf16 v[144:147], a[84:87], a[20:23], v[144:147]\n"
"ds_read_b64_tr_b16 a[138:139], v242 offset:256\n"
"ds_read_b64_tr_b16 a[144:145], v243 offset:0\n"
"v_mfma_f32_16x16x32_bf16 v[176:179], a[80:83], a[24:27], v[176:179]\n"
"ds_read_b64_tr_b16 a[146:147], v243 offset:256\n"
"v_mfma_f32_16x16x32_bf16 v[176:179], a[84:87], a[28:31], v[176:179]\n"
"ds_read_b64_tr_b16 a[116:117], v242 offset:24576\n"
"v_mfma_f32_16x16x32_bf16 v[20:23], a[88:91], a[0:3], v[20:23]\n"
"buffer_load_dwordx4 v248, s[4:7], s89 offen offset:3072 sc1 lds\n"
"v_mfma_f32_16x16x32_bf16 v[20:23], a[92:95], a[4:7], v[20:23]\n"
"ds_read_b64_tr_b16 a[118:119], v242 offset:24832\n"
"ds_read_b64_tr_b16 a[124:125], v243 offset:24576\n"
"v_mfma_f32_16x16x32_bf16 v[52:55], a[88:91], a[8:11], v[52:55]\n"
"ds_read_b64_tr_b16 a[126:127], v243 offset:24832\n"
"v_mfma_f32_16x16x32_bf16 v[52:55], a[92:95], a[12:15], v[52:55]\n"
"ds_read_b64_tr_b16 a[132:133], v242 offset:25088\n"
"v_mfma_f32_16x16x32_bf16 v[148:151], a[88:91], a[16:19], v[148:151]\n"
"ds_read_b64_tr_b16 a[134:135], v242 offset:25344\n"
"s_add_u32 m0, s90, 49152\n"
"v_mfma_f32_16x16x32_bf16 v[148:151], a[92:95], a[20:23], v[148:151]\n"
"ds_read_b64_tr_b16 a[140:141], v242 offset:16384\n"
"v_mfma_f32_16x16x32_bf16 v[180:183], a[88:91], a[24:27], v[180:183]\n"
"ds_read_b64_tr_b16 a[142:143], v242 offset:16640\n"
"v_mfma_f32_16x16x32_bf16 v[180:183], a[92:95], a[28:31], v[180:183]\n"
"ds_read_b64_tr_b16 a[148:149], v243 offset:16384\n"
"v_mfma_f32_16x16x32_bf16 v[24:27], a[96:99], a[0:3], v[24:27]\n"
"ds_read_b64_tr_b16 a[150:151], v243 offset:16640\n"
"s_add_u32 s89, s37, 40960\n"
"v_mfma_f32_16x16x32_bf16 v[24:27], a[100:103], a[4:7], v[24:27]\n"
"v_mfma_f32_16x16x32_bf16 v[56:59], a[96:99], a[8:11], v[56:59]\n"
"v_mfma_f32_16x16x32_bf16 v[56:59], a[100:103], a[12:15], v[56:59]\n"
"v_mfma_f32_16x16x32_bf16 v[152:155], a[96:99], a[16:19], v[152:155]\n"
"buffer_load_dwordx4 v248, s[4:7], s89 offen offset:0 sc1 lds\n"
"v_mfma_f32_16x16x32_bf16 v[152:155], a[100:103], a[20:23], v[152:155]\n"
"v_mfma_f32_16x16x32_bf16 v[184:187], a[96:99], a[24:27], v[184:187]\n"
"v_mfma_f32_16x16x32_bf16 v[184:187], a[100:103], a[28:31], v[184:187]\n"
"v_mfma_f32_16x16x32_bf16 v[28:31], a[104:107], a[0:3], v[28:31]\n"
"buffer_load_dwordx4 v248, s[4:7], s89 offen offset:1024 sc1 lds\n"
"v_mfma_f32_16x16x32_bf16 v[28:31], a[108:111], a[4:7], v[28:31]\n"
"v_mfma_f32_16x16x32_bf16 v[60:63], a[104:107], a[8:11], v[60:63]\n"
"v_mfma_f32_16x16x32_bf16 v[60:63], a[108:111], a[12:15], v[60:63]\n"
"v_mfma_f32_16x16x32_bf16 v[156:159], a[104:107], a[16:19], v[156:159]\n"
"buffer_load_dwordx4 v248, s[4:7], s89 offen offset:2048 sc1 lds\n"
"v_mfma_f32_16x16x32_bf16 v[156:159], a[108:111], a[20:23], v[156:159]\n"
"v_mfma_f32_16x16x32_bf16 v[188:191], a[104:107], a[24:27], v[188:191]\n"
"v_mfma_f32_16x16x32_bf16 v[188:191], a[108:111], a[28:31], v[188:191]\n"
"s_waitcnt vmcnt(15) lgkmcnt(0)\n"
"v_mfma_f32_16x16x32_bf16 v[192:195], a[112:115], a[32:35], v[192:195]\n"
"s_barrier\n"
"v_mfma_f32_16x16x32_bf16 v[192:195], a[116:119], a[36:39], v[192:195]\n"
"buffer_load_dwordx4 v248, s[4:7], s89 offen offset:3072 sc1 lds\n"
"v_mfma_f32_16x16x32_bf16 v[196:199], a[112:115], a[40:43], v[196:199]\n"
"ds_read_b64_tr_b16 a[64:65], v242 offset:512\n"
"ds_read_b64_tr_b16 a[66:67], v242 offset:768\n"
"v_mfma_f32_16x16x32_bf16 v[196:199], a[116:119], a[44:47], v[196:199]\n"
"ds_read_b64_tr_b16 a[72:73], v243 offset:512\n"
"ds_read_b64_tr_b16 a[160:161], v88 offset:0\n"
"v_mfma_f32_16x16x32_bf16 v[200:203], a[112:115], a[48:51], v[200:203]\n"
"ds_read_b64_tr_b16 a[74:75], v243 offset:768\n"
"ds_read_b64_tr_b16 a[162:163], v88 offset:2048\n"
"v_mfma_f32_16x16x32_bf16 v[200:203], a[116:119], a[52:55], v[200:203]\n"
"ds_read_b64_tr_b16 a[80:81], v242 offset:1024\n"
"ds_read_b64_tr_b16 a[164:165], v88 offset:8\n"
"v_mfma_f32_16x16x32_bf16 v[204:207], a[112:115], a[56:59], v[204:207]\n"
"ds_read_b64_tr_b16 a[82:83], v242 offset:1280\n"
"ds_read_b64_tr_b16 a[166:167], v88 offset:2056\n"
"v_mfma_f32_16x16x32_bf16 v[204:207], a[116:119], a[60:63], v[204:207]\n"
"ds_read_b64_tr_b16 a[88:89], v243 offset:1024\n"
"ds_read_b64_tr_b16 a[168:169], v89 offset:0\n"
"v_mfma_f32_16x16x32_bf16 v[208:211], a[32:35], a[120:123], v[208:211]\n"
"ds_read_b64_tr_b16 a[90:91], v243 offset:1280\n"
"ds_read_b64_tr_b16 a[170:171], v89 offset:2048\n"
"v_mfma_f32_16x16x32_bf16 v[208:211], a[36:39], a[124:127], v[208:211]\n"
"ds_read_b64_tr_b16 a[172:173], v89 offset:8\n"
"ds_read_b64_tr_b16 a[174:175], v89 offset:2056\n"
"v_mfma_f32_16x16x32_bf16 v[212:215], a[40:43], a[120:123], v[212:215]\n"
"ds_read_b64_tr_b16 a[96:97], v242 offset:1536\n"
"ds_read_b64_tr_b16 a[176:177], v88 offset:16384\n"
"v_mfma_f32_16x16x32_bf16 v[212:215], a[44:47], a[124:127], v[212:215]\n"
"ds_read_b64_tr_b16 a[98:99], v242 offset:1792\n"
"ds_read_b64_tr_b16 a[178:179], v88 offset:18432\n"
"v_mfma_f32_16x16x32_bf16 v[216:219], a[48:51], a[120:123], v[216:219]\n"
"ds_read_b64_tr_b16 a[104:105], v243 offset:1536\n"
"ds_read_b64_tr_b16 a[180:181], v88 offset:16392\n"
"v_mfma_f32_16x16x32_bf16 v[216:219], a[52:55], a[124:127], v[216:219]\n"
"ds_read_b64_tr_b16 a[106:107], v243 offset:1792\n"
"ds_read_b64_tr_b16 a[182:183], v88 offset:18440\n"
"v_mfma_f32_16x16x32_bf16 v[220:223], a[56:59], a[120:123], v[220:223]\n"
"ds_read_b64_tr_b16 a[68:69], v242 offset:16896\n"
"ds_read_b64_tr_b16 a[184:185], v89 offset:16384\n"
"v_mfma_f32_16x16x32_bf16 v[220:223], a[60:63], a[124:127], v[220:223]\n"
"ds_read_b64_tr_b16 a[70:71], v242 offset:17152\n"
"ds_read_b64_tr_b16 a[186:187], v89 offset:18432\n"
"v_mfma_f32_16x16x32_bf16 v[224:227], a[32:35], a[128:131], v[224:227]\n"
"ds_read_b64_tr_b16 a[76:77], v243 offset:16896\n"
"ds_read_b64_tr_b16 a[188:189], v89 offset:16392\n"
"v_mfma_f32_16x16x32_bf16 v[224:227], a[36:39], a[132:135], v[224:227]\n"
"ds_read_b64_tr_b16 a[78:79], v243 offset:17152\n"
"ds_read_b64_tr_b16 a[190:191], v89 offset:18440\n"
"v_mfma_f32_16x16x32_bf16 v[228:231], a[40:43], a[128:131], v[228:231]\n"
"v_mfma_f32_16x16x32_bf16 v[228:231], a[44:47], a[132:135], v[228:231]\n"
"ds_read_b64_tr_b16 a[84:85], v242 offset:17408\n"
"v_mfma_f32_16x16x32_bf16 v[232:235], a[48:51], a[128:131], v[232:235]\n"
"ds_read_b64_tr_b16 a[86:87], v242 offset:17664\n"
"v_mfma_f32_16x16x32_bf16 v[232:235], a[52:55], a[132:135], v[232:235]\n"
"ds_read_b64_tr_b16 a[92:93], v243 offset:17408\n"
"v_mfma_f32_16x16x32_bf16 v[236:239], a[56:59], a[128:131], v[236:239]\n"
"ds_read_b64_tr_b16 a[94:95], v243 offset:17664\n"
"v_fma_f32 v68, v208, s42, v69\n"
"v_mfma_f32_16x16x32_bf16 v[236:239], a[60:63], a[132:135], v[236:239]\n"
"ds_read_b64_tr_b16 a[100:101], v242 offset:17920\n"
"v_mfma_f32_16x16x32_bf16 v[0:3], a[136:139], a[32:35], v[0:3]\n"
"ds_read_b64_tr_b16 a[102:103], v242 offset:18176\n"
"v_mfma_f32_16x16x32_bf16 v[0:3], a[140:143], a[36:39], v[0:3]\n"
"ds_read_b64_tr_b16 a[108:109], v243 offset:17920\n"
"v_mfma_f32_16x16x32_bf16 v[32:35], a[136:139], a[40:43], v[32:35]\n"
"ds_read_b64_tr_b16 a[110:111], v243 offset:18176\n"
"v_mfma_f32_16x16x32_bf16 v[32:35], a[140:143], a[44:47], v[32:35]\n"
"v_exp_f32 v68, v68 clamp\n"
"v_mfma_f32_16x16x32_bf16 v[128:131], a[136:139], a[48:51], v[128:131]\n"
"v_max_f32 v208, 897988541, v208\n"
"v_add_f32 v208, v208, v68\n"
"v_mfma_f32_16x16x32_bf16 v[128:131], a[140:143], a[52:55], v[128:131]\n"
"v_fma_f32 v68, v209, s42, v69\n"
"v_mfma_f32_16x16x32_bf16 v[160:163], a[136:139], a[56:59], v[160:163]\n"
"v_exp_f32 v68, v68 clamp\n"
"v_mfma_f32_16x16x32_bf16 v[160:163], a[140:143], a[60:63], v[160:163]\n"
"v_max_f32 v209, 897988541, v209\n"
"v_add_f32 v209, v209, v68\n"
"v_mfma_f32_16x16x32_bf16 v[4:7], a[144:147], a[32:35], v[4:7]\n"
"v_fma_f32 v68, v210, s42, v69\n"
"v_mfma_f32_16x16x32_bf16 v[4:7], a[148:151], a[36:39], v[4:7]\n"
"v_exp_f32 v68, v68 clamp\n"
"v_mfma_f32_16x16x32_bf16 v[36:39], a[144:147], a[40:43], v[36:39]\n"
"v_max_f32 v210, 897988541, v210\n"
"v_add_f32 v210, v210, v68\n"
"v_mfma_f32_16x16x32_bf16 v[36:39], a[148:151], a[44:47], v[36:39]\n"
"v_fma_f32 v68, v211, s42, v69\n"
"v_mfma_f32_16x16x32_bf16 v[132:135], a[144:147], a[48:51], v[132:135]\n"
"v_exp_f32 v68, v68 clamp\n"
"v_mfma_f32_16x16x32_bf16 v[132:135], a[148:151], a[52:55], v[132:135]\n"
"v_max_f32 v211, 897988541, v211\n"
"v_add_f32 v211, v211, v68\n"
"v_mfma_f32_16x16x32_bf16 v[164:167], a[144:147], a[56:59], v[164:167]\n"
"v_fma_f32 v68, v212, s42, v69\n"
"v_mfma_f32_16x16x32_bf16 v[164:167], a[148:151], a[60:63], v[164:167]\n"
"Lfused_hop7:\n"
"s_cbranch_execz Lfused_hop11\n"
"v_exp_f32 v68, v68 clamp\n"
"s_waitcnt vmcnt(12) lgkmcnt(0)\n"
"v_mfma_f32_16x16x32_bf16 v[8:11], a[64:67], a[32:35], v[8:11]\n"
"s_barrier\n"
"v_mfma_f32_16x16x32_bf16 v[8:11], a[68:71], a[36:39], v[8:11]\n"
"buffer_store_dwordx4 a[160:163], v248, s[72:75], s34 offen offset:0 sc1\n"
"v_mfma_f32_16x16x32_bf16 v[12:15], a[72:75], a[32:35], v[12:15]\n"
"ds_read_b128 a[128:131], v250 offset:0\n"
"v_max_f32 v212, 897988541, v212\n"
"v_mfma_f32_16x16x32_bf16 v[12:15], a[76:79], a[36:39], v[12:15]\n"
"v_add_f32 v212, v212, v68\n"
"v_fma_f32 v68, v213, s42, v69\n"
"v_mfma_f32_16x16x32_bf16 v[16:19], a[80:83], a[32:35], v[16:19]\n"
"v_exp_f32 v68, v68 clamp\n"
"v_mfma_f32_16x16x32_bf16 v[16:19], a[84:87], a[36:39], v[16:19]\n"
"ds_read_b128 a[132:135], v250 offset:1024\n"
"v_max_f32 v213, 897988541, v213\n"
"v_mfma_f32_16x16x32_bf16 v[20:23], a[88:91], a[32:35], v[20:23]\n"
"v_add_f32 v213, v213, v68\n"
"v_fma_f32 v68, v214, s42, v69\n"
"v_mfma_f32_16x16x32_bf16 v[20:23], a[92:95], a[36:39], v[20:23]\n"
"v_exp_f32 v68, v68 clamp\n"
"v_mfma_f32_16x16x32_bf16 v[24:27], a[96:99], a[32:35], v[24:27]\n"
"v_max_f32 v214, 897988541, v214\n"
"v_add_f32 v214, v214, v68\n"
"v_mfma_f32_16x16x32_bf16 v[24:27], a[100:103], a[36:39], v[24:27]\n"
"ds_read_b128 a[136:139], v250 offset:2048\n"
"v_fma_f32 v68, v215, s42, v69\n"
"v_mfma_f32_16x16x32_bf16 v[28:31], a[104:107], a[32:35], v[28:31]\n"
"v_exp_f32 v68, v68 clamp\n"
"v_mfma_f32_16x16x32_bf16 v[28:31], a[108:111], a[36:39], v[28:31]\n"
"v_max_f32 v215, 897988541, v215\n"
"v_add_f32 v215, v215, v68\n"
"v_mfma_f32_16x16x32_bf16 v[40:43], a[64:67], a[40:43], v[40:43]\n"
"buffer_store_dwordx4 a[164:167], v248, s[72:75], s34 offen offset:1024 sc1\n"
"v_mfma_f32_16x16x32_bf16 v[40:43], a[68:71], a[44:47], v[40:43]\n"
"ds_read_b128 a[140:143], v250 offset:3072\n"
"v_fma_f32 v68, v216, s42, v69\n"
"v_mfma_f32_16x16x32_bf16 v[44:47], a[72:75], a[40:43], v[44:47]\n"
"v_exp_f32 v68, v68 clamp\n"
"v_mfma_f32_16x16x32_bf16 v[44:47], a[76:79], a[44:47], v[44:47]\n"
"v_max_f32 v216, 897988541, v216\n"
"v_add_f32 v216, v216, v68\n"
"v_mfma_f32_16x16x32_bf16 v[48:51], a[80:83], a[40:43], v[48:51]\n"
"v_fma_f32 v68, v217, s42, v69\n"
"v_mfma_f32_16x16x32_bf16 v[48:51], a[84:87], a[44:47], v[48:51]\n"
"ds_read_b128 a[144:147], v250 offset:4096\n"
"v_mfma_f32_16x16x32_bf16 v[52:55], a[88:91], a[40:43], v[52:55]\n"
"v_exp_f32 v68, v68 clamp\n"
"v_mfma_f32_16x16x32_bf16 v[52:55], a[92:95], a[44:47], v[52:55]\n"
"v_max_f32 v217, 897988541, v217\n"
"v_add_f32 v217, v217, v68\n"
"v_mfma_f32_16x16x32_bf16 v[56:59], a[96:99], a[40:43], v[56:59]\n"
"v_fma_f32 v68, v218, s42, v69\n"
"v_mfma_f32_16x16x32_bf16 v[56:59], a[100:103], a[44:47], v[56:59]\n"
"ds_read_b128 a[148:151], v250 offset:5120\n"
"v_mfma_f32_16x16x32_bf16 v[60:63], a[104:107], a[40:43], v[60:63]\n"
"v_exp_f32 v68, v68 clamp\n"
"v_mfma_f32_16x16x32_bf16 v[60:63], a[108:111], a[44:47], v[60:63]\n"
"v_max_f32 v218, 897988541, v218\n"
"v_add_f32 v218, v218, v68\n"
"v_mfma_f32_16x16x32_bf16 v[136:139], a[64:67], a[48:51], v[136:139]\n"
"buffer_store_dwordx4 a[168:171], v248, s[72:75], s34 offen offset:2048 sc1\n"
"v_mfma_f32_16x16x32_bf16 v[136:139], a[68:71], a[52:55], v[136:139]\n"
"ds_read_b128 a[152:155], v250 offset:6144\n"
"v_fma_f32 v68, v219, s42, v69\n"
"v_mfma_f32_16x16x32_bf16 v[140:143], a[72:75], a[48:51], v[140:143]\n"
"v_exp_f32 v68, v68 clamp\n"
"v_mfma_f32_16x16x32_bf16 v[140:143], a[76:79], a[52:55], v[140:143]\n"
"v_max_f32 v219, 897988541, v219\n"
"v_add_f32 v219, v219, v68\n"
"v_mfma_f32_16x16x32_bf16 v[144:147], a[80:83], a[48:51], v[144:147]\n"
"v_fma_f32 v68, v220, s42, v69\n"
"v_mfma_f32_16x16x32_bf16 v[144:147], a[84:87], a[52:55], v[144:147]\n"
"ds_read_b128 a[156:159], v250 offset:7168\n"
"v_mfma_f32_16x16x32_bf16 v[148:151], a[88:91], a[48:51], v[148:151]\n"
"v_exp_f32 v68, v68 clamp\n"
"v_mfma_f32_16x16x32_bf16 v[148:151], a[92:95], a[52:55], v[148:151]\n"
"v_max_f32 v220, 897988541, v220\n"
"v_add_f32 v220, v220, v68\n"
"v_mfma_f32_16x16x32_bf16 v[152:155], a[96:99], a[48:51], v[152:155]\n"
"v_fma_f32 v68, v221, s42, v69\n"
"v_mfma_f32_16x16x32_bf16 v[152:155], a[100:103], a[52:55], v[152:155]\n"
"v_exp_f32 v68, v68 clamp\n"
"v_mfma_f32_16x16x32_bf16 v[156:159], a[104:107], a[48:51], v[156:159]\n"
"v_max_f32 v221, 897988541, v221\n"
"v_add_f32 v221, v221, v68\n"
"v_mfma_f32_16x16x32_bf16 v[156:159], a[108:111], a[52:55], v[156:159]\n"
"v_fma_f32 v68, v222, s42, v69\n"
"v_mfma_f32_16x16x32_bf16 v[168:171], a[64:67], a[56:59], v[168:171]\n"
"buffer_store_dwordx4 a[172:175], v248, s[72:75], s34 offen offset:3072 sc1\n"
"v_mfma_f32_16x16x32_bf16 v[168:171], a[68:71], a[60:63], v[168:171]\n"
"v_exp_f32 v68, v68 clamp\n"
"v_mfma_f32_16x16x32_bf16 v[172:175], a[72:75], a[56:59], v[172:175]\n"
"v_max_f32 v222, 897988541, v222\n"
"v_add_f32 v222, v222, v68\n"
"v_mfma_f32_16x16x32_bf16 v[172:175], a[76:79], a[60:63], v[172:175]\n"
"v_fma_f32 v68, v223, s42, v69\n"
"v_mfma_f32_16x16x32_bf16 v[176:179], a[80:83], a[56:59], v[176:179]\n"
"v_exp_f32 v68, v68 clamp\n"
"v_mfma_f32_16x16x32_bf16 v[176:179], a[84:87], a[60:63], v[176:179]\n"
"v_max_f32 v223, 897988541, v223\n"
"v_add_f32 v223, v223, v68\n"
"v_mfma_f32_16x16x32_bf16 v[180:183], a[88:91], a[56:59], v[180:183]\n"
"v_cvt_pk_bf16_f32 v224, v224, v225\n"
"v_cvt_pk_bf16_f32 v225, v226, v227\n"
"v_mfma_f32_16x16x32_bf16 v[180:183], a[92:95], a[60:63], v[180:183]\n"
"v_cvt_pk_bf16_f32 v226, v228, v229\n"
"v_cvt_pk_bf16_f32 v227, v230, v231\n"
"v_mfma_f32_16x16x32_bf16 v[184:187], a[96:99], a[56:59], v[184:187]\n"
"v_cvt_pk_bf16_f32 v228, v232, v233\n"
"v_cvt_pk_bf16_f32 v229, v234, v235\n"
"v_mfma_f32_16x16x32_bf16 v[184:187], a[100:103], a[60:63], v[184:187]\n"
"v_cvt_pk_bf16_f32 v230, v236, v237\n"
"v_cvt_pk_bf16_f32 v231, v238, v239\n"
"v_mfma_f32_16x16x32_bf16 v[188:191], a[104:107], a[56:59], v[188:191]\n"
"v_cvt_pk_bf16_f32 v208, v208, v209\n"
"v_cvt_pk_bf16_f32 v209, v210, v211\n"
"v_mfma_f32_16x16x32_bf16 v[188:191], a[108:111], a[60:63], v[188:191]\n"
"v_cvt_pk_bf16_f32 v210, v212, v213\n"
"v_cvt_pk_bf16_f32 v211, v214, v215\n"
"v_cvt_pk_bf16_f32 v212, v216, v217\n"
"v_cvt_pk_bf16_f32 v213, v218, v219\n"
"v_cvt_pk_bf16_f32 v214, v220, v221\n"
"v_cvt_pk_bf16_f32 v215, v222, v223\n"
"v_cvt_pk_bf16_f32 v192, v192, v193\n"
"v_accvgpr_write_b32 a224, v192\n"
"v_cvt_pk_bf16_f32 v193, v194, v195\n"
"v_accvgpr_write_b32 a225, v193\n"
"v_cvt_pk_bf16_f32 v194, v196, v197\n"
"v_accvgpr_write_b32 a226, v194\n"
"v_cvt_pk_bf16_f32 v195, v198, v199\n"
"v_accvgpr_write_b32 a227, v195\n"
"v_cvt_pk_bf16_f32 v196, v200, v201\n"
"v_accvgpr_write_b32 a228, v196\n"
"v_cvt_pk_bf16_f32 v197, v202, v203\n"
"v_accvgpr_write_b32 a229, v197\n"
"v_cvt_pk_bf16_f32 v198, v204, v205\n"
"v_accvgpr_write_b32 a230, v198\n"
"v_cvt_pk_bf16_f32 v199, v206, v207\n"
"v_accvgpr_write_b32 a231, v199\n"
"v_lshlrev_b32 v84, 1, v248\n"
"s_lshl_b32 s54, s12, 11\n"
"v_mfma_f32_16x16x32_bf16 v[76:79], v[208:211], v[224:227], 0\n"
"v_mfma_f32_16x16x32_bf16 v[80:83], v[208:211], v[72:75], 0\n"
"v_mfma_f32_16x16x32_bf16 v[76:79], v[212:215], v[228:231], v[76:79]\n"
"v_mfma_f32_16x16x32_bf16 v[80:83], v[212:215], v[72:75], v[80:83]\n"
"s_add_u32 s54, s54, 139264\n"
"v_add_u32 v85, s54, v84\n"
"s_waitcnt vmcnt(4)\n"
"v_cvt_pk_bf16_f32 v0, v0, v1\n"
"v_cvt_pk_bf16_f32 v1, v2, v3\n"
"v_cvt_pk_bf16_f32 v2, v4, v5\n"
"v_cvt_pk_bf16_f32 v3, v6, v7\n"
"ds_write_b128 v66, v[0:3] offset:0\n"
"v_accvgpr_write_b32 a0, v0\n"
"v_accvgpr_write_b32 a1, v1\n"
"v_accvgpr_write_b32 a2, v2\n"
"v_accvgpr_write_b32 a3, v3\n"
"v_cvt_pk_bf16_f32 v8, v8, v9\n"
"v_cvt_pk_bf16_f32 v9, v10, v11\n"
"v_cvt_pk_bf16_f32 v10, v12, v13\n"
"v_cvt_pk_bf16_f32 v11, v14, v15\n"
"ds_write_b128 v66, v[8:11] offset:1024\n"
"v_accvgpr_write_b32 a4, v8\n"
"v_accvgpr_write_b32 a5, v9\n"
"v_accvgpr_write_b32 a6, v10\n"
"v_accvgpr_write_b32 a7, v11\n"
"v_cvt_pk_bf16_f32 v16, v16, v17\n"
"v_cvt_pk_bf16_f32 v17, v18, v19\n"
"v_cvt_pk_bf16_f32 v18, v20, v21\n"
"v_cvt_pk_bf16_f32 v19, v22, v23\n"
"ds_write_b128 v66, v[16:19] offset:2048\n"
"v_accvgpr_write_b32 a8, v16\n"
"v_accvgpr_write_b32 a9, v17\n"
"v_accvgpr_write_b32 a10, v18\n"
"v_accvgpr_write_b32 a11, v19\n"
"v_cvt_pk_bf16_f32 v24, v24, v25\n"
"v_cvt_pk_bf16_f32 v25, v26, v27\n"
"v_cvt_pk_bf16_f32 v26, v28, v29\n"
"v_cvt_pk_bf16_f32 v27, v30, v31\n"
"ds_write_b128 v66, v[24:27] offset:3072\n"
"v_accvgpr_write_b32 a12, v24\n"
"v_accvgpr_write_b32 a13, v25\n"
"v_accvgpr_write_b32 a14, v26\n"
"v_accvgpr_write_b32 a15, v27\n"
"ds_write_b128 v85, v[76:79]\n"
"ds_write_b128 v85, v[80:83] offset:16\n"
"v_cvt_pk_bf16_f32 v32, v32, v33\n"
"v_cvt_pk_bf16_f32 v33, v34, v35\n"
"v_cvt_pk_bf16_f32 v34, v36, v37\n"
"v_cvt_pk_bf16_f32 v35, v38, v39\n"
"ds_write_b128 v66, v[32:35] offset:4096\n"
"v_accvgpr_write_b32 a16, v32\n"
"v_accvgpr_write_b32 a17, v33\n"
"v_accvgpr_write_b32 a18, v34\n"
"v_accvgpr_write_b32 a19, v35\n"
"v_cvt_pk_bf16_f32 v40, v40, v41\n"
"v_cvt_pk_bf16_f32 v41, v42, v43\n"
"v_cvt_pk_bf16_f32 v42, v44, v45\n"
"v_cvt_pk_bf16_f32 v43, v46, v47\n"
"ds_write_b128 v66, v[40:43] offset:5120\n"
"v_accvgpr_write_b32 a20, v40\n"
"v_accvgpr_write_b32 a21, v41\n"
"v_accvgpr_write_b32 a22, v42\n"
"v_accvgpr_write_b32 a23, v43\n"
"v_cvt_pk_bf16_f32 v48, v48, v49\n"
"v_cvt_pk_bf16_f32 v49, v50, v51\n"
"v_cvt_pk_bf16_f32 v50, v52, v53\n"
"v_cvt_pk_bf16_f32 v51, v54, v55\n"
"ds_write_b128 v66, v[48:51] offset:6144\n"
"v_accvgpr_write_b32 a24, v48\n"
"v_accvgpr_write_b32 a25, v49\n"
"v_accvgpr_write_b32 a26, v50\n"
"v_accvgpr_write_b32 a27, v51\n"
"v_cvt_pk_bf16_f32 v56, v56, v57\n"
"v_cvt_pk_bf16_f32 v57, v58, v59\n"
"v_cvt_pk_bf16_f32 v58, v60, v61\n"
"v_cvt_pk_bf16_f32 v59, v62, v63\n"
"ds_write_b128 v66, v[56:59] offset:7168\n"
"v_accvgpr_write_b32 a28, v56\n"
"v_accvgpr_write_b32 a29, v57\n"
"v_accvgpr_write_b32 a30, v58\n"
"v_accvgpr_write_b32 a31, v59\n"
"s_cmp_eq_u32 s56, 0\n"
"s_cbranch_scc1 Lfused_rt\n"
"buffer_store_dwordx4 a[176:179], v248, s[72:75], s35 offen offset:0 sc1\n"
"buffer_store_dwordx4 a[180:183], v248, s[72:75], s35 offen offset:1024 sc1\n"
"buffer_store_dwordx4 a[184:187], v248, s[72:75], s35 offen offset:2048 sc1\n"
"buffer_store_dwordx4 a[188:191], v248, s[72:75], s35 offen offset:3072 sc1\n"
"Lfused_rt:\n"
"s_lshl_b32 s54, s12, 3\n"
"v_add_u32 v84, s54, v84\n"
"v_add_u32 v85, 139264, v84\n"
"v_xor_b32 v66, 0x2000, v66\n"
"v_cvt_pk_bf16_f32 v128, v128, v129\n"
"v_cvt_pk_bf16_f32 v129, v130, v131\n"
"v_cvt_pk_bf16_f32 v130, v132, v133\n"
"v_cvt_pk_bf16_f32 v131, v134, v135\n"
"v_accvgpr_write_b32 a32, v128\n"
"v_accvgpr_write_b32 a33, v129\n"
"v_accvgpr_write_b32 a34, v130\n"
"v_accvgpr_write_b32 a35, v131\n"
"v_cvt_pk_bf16_f32 v136, v136, v137\n"
"v_cvt_pk_bf16_f32 v137, v138, v139\n"
"v_cvt_pk_bf16_f32 v138, v140, v141\n"
"v_cvt_pk_bf16_f32 v139, v142, v143\n"
"v_accvgpr_write_b32 a36, v136\n"
"v_accvgpr_write_b32 a37, v137\n"
"v_accvgpr_write_b32 a38, v138\n"
"v_accvgpr_write_b32 a39, v139\n"
"v_cvt_pk_bf16_f32 v144, v144, v145\n"
"v_cvt_pk_bf16_f32 v145, v146, v147\n"
"v_cvt_pk_bf16_f32 v146, v148, v149\n"
"v_cvt_pk_bf16_f32 v147, v150, v151\n"
"v_accvgpr_write_b32 a40, v144\n"
"v_accvgpr_write_b32 a41, v145\n"
"v_accvgpr_write_b32 a42, v146\n"
"v_accvgpr_write_b32 a43, v147\n"
"v_cvt_pk_bf16_f32 v152, v152, v153\n"
"v_cvt_pk_bf16_f32 v153, v154, v155\n"
"v_cvt_pk_bf16_f32 v154, v156, v157\n"
"v_cvt_pk_bf16_f32 v155, v158, v159\n"
"v_accvgpr_write_b32 a44, v152\n"
"v_accvgpr_write_b32 a45, v153\n"
"v_accvgpr_write_b32 a46, v154\n"
"v_accvgpr_write_b32 a47, v155\n"
"s_waitcnt lgkmcnt(0)\n"
"s_barrier\n"
"ds_read_b64 v[76:77], v85 offset:0\n"
"ds_read_b64 v[78:79], v85 offset:2048\n"
"ds_read_b64 v[80:81], v85 offset:4096\n"
"ds_read_b64 v[82:83], v85 offset:6144\n"
"ds_read_b128 a[64:67], v66 offset:0\n"
"ds_read_b128 a[68:71], v66 offset:1024\n"
"ds_read_b128 a[72:75], v66 offset:2048\n"
"ds_read_b128 a[76:79], v66 offset:3072\n"
"ds_read_b128 a[80:83], v66 offset:4096\n"
"ds_read_b128 a[84:87], v66 offset:5120\n"
"ds_read_b128 a[88:91], v66 offset:6144\n"
"ds_read_b128 a[92:95], v66 offset:7168\n"
"v_cvt_pk_bf16_f32 v160, v160, v161\n"
"v_cvt_pk_bf16_f32 v161, v162, v163\n"
"v_cvt_pk_bf16_f32 v162, v164, v165\n"
"v_cvt_pk_bf16_f32 v163, v166, v167\n"
"v_accvgpr_write_b32 a48, v160\n"
"v_accvgpr_write_b32 a49, v161\n"
"v_accvgpr_write_b32 a50, v162\n"
"v_accvgpr_write_b32 a51, v163\n"
"v_cvt_pk_bf16_f32 v168, v168, v169\n"
"v_cvt_pk_bf16_f32 v169, v170, v171\n"
"v_cvt_pk_bf16_f32 v170, v172, v173\n"
"v_cvt_pk_bf16_f32 v171, v174, v175\n"
"v_accvgpr_write_b32 a52, v168\n"
"v_accvgpr_write_b32 a53, v169\n"
"v_accvgpr_write_b32 a54, v170\n"
"v_accvgpr_write_b32 a55, v171\n"
"v_cvt_pk_bf16_f32 v176, v176, v177\n"
"v_cvt_pk_bf16_f32 v177, v178, v179\n"
"v_cvt_pk_bf16_f32 v178, v180, v181\n"
"v_cvt_pk_bf16_f32 v179, v182, v183\n"
"v_accvgpr_write_b32 a56, v176\n"
"v_accvgpr_write_b32 a57, v177\n"
"v_accvgpr_write_b32 a58, v178\n"
"v_accvgpr_write_b32 a59, v179\n"
"v_cvt_pk_bf16_f32 v184, v184, v185\n"
"v_cvt_pk_bf16_f32 v185, v186, v187\n"
"v_cvt_pk_bf16_f32 v186, v188, v189\n"
"v_cvt_pk_bf16_f32 v187, v190, v191\n"
"v_accvgpr_write_b32 a60, v184\n"
"v_accvgpr_write_b32 a61, v185\n"
"v_accvgpr_write_b32 a62, v186\n"
"v_accvgpr_write_b32 a63, v187\n"
"s_lshl_b32 s50, s10, 3\n"
"s_add_u32 s50, s50, s11\n"
"s_lshl_b32 s50, s50, 11\n"
"s_add_u32 s50, s50, 54525952\n"
"s_add_u32 s52, s8, s50\n"
"s_addc_u32 s53, s9, 0\n"
"v_mov_b64 v[200:201], 0\n"
"v_mov_b64 v[202:203], 0\n"
"v_mov_b64 v[204:205], 0\n"
"v_mov_b64 v[206:207], 0\n"
"s_waitcnt lgkmcnt(0)\n"
"v_add_f32 v76, v76, v78\n"
"v_add_f32 v80, v80, v82\n"
"v_add_f32 v77, v77, v79\n"
"v_add_f32 v81, v81, v83\n"
"v_add_f32 v76, v76, v80\n"
"v_add_f32 v77, v77, v81\n"
"global_store_dwordx2 v84, v[76:77], s[52:53] sc1\n"
"s_mov_b32 m0, s30\n"
"v_mfma_f32_16x16x32_bf16 v[128:131], a[128:131], a[0:3], 0\n"
"s_barrier\n"
"s_cmp_ge_u32 s14, s39\n"
"v_mfma_f32_16x16x32_bf16 v[128:131], a[136:139], a[4:7], v[128:131]\n"
"s_cselect_b32 s34, 0xff800000, 0\n"
"s_cmp_gt_u32 s14, s39\n"
"v_mfma_f32_16x16x32_bf16 v[128:131], a[144:147], a[8:11], v[128:131]\n"
"s_cselect_b32 s35, 0xff800000, 0\n"
"v_add_f32 v224, s35, v200\n"
"v_mfma_f32_16x16x32_bf16 v[128:131], a[152:155], a[12:15], v[128:131]\n"
"ds_read_b128 a[96:99], v250 offset:32768\n"
"v_add_f32 v225, s34, v204\n"
"v_mfma_f32_16x16x32_bf16 v[132:135], a[132:135], a[0:3], 0\n"
"ds_read_b128 a[100:103], v250 offset:33792\n"
"v_add_f32 v148, s34, v200\n"
"v_mfma_f32_16x16x32_bf16 v[132:135], a[140:143], a[4:7], v[132:135]\n"
"v_add_f32 v149, s34, v200\n"
"v_add_f32 v150, s34, v200\n"
"v_mfma_f32_16x16x32_bf16 v[132:135], a[148:151], a[8:11], v[132:135]\n"
"ds_read_b128 a[104:107], v250 offset:34816\n"
"v_add_f32 v151, s34, v200\n"
"v_mfma_f32_16x16x32_bf16 v[132:135], a[156:159], a[12:15], v[132:135]\n"
"ds_read_b128 a[108:111], v250 offset:35840\n"
"v_cndmask_b32 v144, v224, v148, s[64:65]\n"
"v_mfma_f32_16x16x32_bf16 v[136:139], a[128:131], a[16:19], 0\n"
"v_cndmask_b32 v145, v224, v148, s[66:67]\n"
"v_cndmask_b32 v146, v224, v148, s[68:69]\n"
"v_mfma_f32_16x16x32_bf16 v[136:139], a[136:139], a[20:23], v[136:139]\n"
"ds_read_b128 a[112:115], v250 offset:36864\n"
"v_cndmask_b32 v147, v224, v148, s[70:71]\n"
"v_mfma_f32_16x16x32_bf16 v[136:139], a[144:147], a[24:27], v[136:139]\n"
"v_add_f32 v152, s35, v204\n"
"v_add_f32 v153, s35, v204\n"
"v_mfma_f32_16x16x32_bf16 v[136:139], a[152:155], a[28:31], v[136:139]\n"
"ds_read_b128 a[116:119], v250 offset:37888\n"
"v_add_f32 v154, s35, v204\n"
"v_mfma_f32_16x16x32_bf16 v[140:143], a[132:135], a[16:19], 0\n"
"ds_read_b128 a[120:123], v250 offset:38912\n"
"v_add_f32 v155, s35, v204\n"
"v_mfma_f32_16x16x32_bf16 v[140:143], a[140:143], a[20:23], v[140:143]\n"
"v_cndmask_b32 v156, v152, v225, s[64:65]\n"
"v_cndmask_b32 v157, v152, v225, s[66:67]\n"
"v_mfma_f32_16x16x32_bf16 v[140:143], a[148:151], a[24:27], v[140:143]\n"
"ds_read_b128 a[124:127], v250 offset:39936\n"
"v_cndmask_b32 v158, v152, v225, s[68:69]\n"
"v_mfma_f32_16x16x32_bf16 v[140:143], a[156:159], a[28:31], v[140:143]\n"
"v_cndmask_b32 v159, v152, v225, s[70:71]\n"
"v_mfma_f32_16x16x32_bf16 v[144:147], a[128:131], a[32:35], v[144:147]\n"
"buffer_load_dwordx4 v248, s[16:19], s28 offen offset:0 sc1 lds\n"
"v_mfma_f32_16x16x32_bf16 v[144:147], a[136:139], a[36:39], v[144:147]\n"
"buffer_load_dwordx4 v248, s[16:19], s28 offen offset:1024 sc1 lds\n"
"v_mfma_f32_16x16x32_bf16 v[144:147], a[144:147], a[40:43], v[144:147]\n"
"buffer_load_dwordx4 v248, s[16:19], s28 offen offset:2048 sc1 lds\n"
"v_mfma_f32_16x16x32_bf16 v[144:147], a[152:155], a[44:47], v[144:147]\n"
"ds_read_b128 a[160:163], v250 offset:8192\n"
"ds_read_b128 a[164:167], v250 offset:9216\n"
"v_mfma_f32_16x16x32_bf16 v[148:151], a[132:135], a[32:35], v[148:151]\n"
"buffer_load_dwordx4 v248, s[16:19], s28 offen offset:3072 sc1 lds\n"
"v_mfma_f32_16x16x32_bf16 v[148:151], a[140:143], a[36:39], v[148:151]\n"
"ds_read_b128 a[168:171], v250 offset:10240\n"
"s_add_u32 m0, m0, 0x10000\n"
"v_mfma_f32_16x16x32_bf16 v[148:151], a[148:151], a[40:43], v[148:151]\n"
"buffer_load_dwordx4 v248, s[20:23], s28 offen offset:0 sc1 lds\n"
"v_mfma_f32_16x16x32_bf16 v[148:151], a[156:159], a[44:47], v[148:151]\n"
"ds_read_b128 a[172:175], v250 offset:11264\n"
"v_mfma_f32_16x16x32_bf16 v[152:155], a[128:131], a[48:51], v[152:155]\n"
"buffer_load_dwordx4 v248, s[20:23], s28 offen offset:1024 sc1 lds\n"
"v_mfma_f32_16x16x32_bf16 v[152:155], a[136:139], a[52:55], v[152:155]\n"
"buffer_load_dwordx4 v248, s[20:23], s28 offen offset:2048 sc1 lds\n"
"v_mfma_f32_16x16x32_bf16 v[152:155], a[144:147], a[56:59], v[152:155]\n"
"buffer_load_dwordx4 v248, s[20:23], s28 offen offset:3072 sc1 lds\n"
"v_mfma_f32_16x16x32_bf16 v[152:155], a[152:155], a[60:63], v[152:155]\n"
"ds_read_b128 a[176:179], v250 offset:12288\n"
"ds_read_b128 a[180:183], v250 offset:13312\n"
"v_mfma_f32_16x16x32_bf16 v[156:159], a[132:135], a[48:51], v[156:159]\n"
"ds_read_b128 a[184:187], v250 offset:14336\n"
"s_add_u32 s28, s28, 0x4000\n"
"v_mfma_f32_16x16x32_bf16 v[156:159], a[140:143], a[52:55], v[156:159]\n"
"ds_read_b128 a[188:191], v250 offset:15360\n"
"s_xor_b32 s30, s30, 0x4000\n"
"v_mfma_f32_16x16x32_bf16 v[156:159], a[148:151], a[56:59], v[156:159]\n"
"s_mov_b32 s22, s18\n"
"v_mfma_f32_16x16x32_bf16 v[156:159], a[156:159], a[60:63], v[156:159]\n"
"v_max3_f32 v226, v128, v129, v130\n"
"v_max3_f32 v228, v131, v132, v133\n"
"v_max3_f32 v227, v136, v137, v138\n"
"v_max3_f32 v229, v139, v140, v141\n"
"v_max3_f32 v226, v226, v134, v135\n"
"v_max3_f32 v227, v227, v142, v143\n"
"v_max_f32 v226, v226, v228\n"
"v_max_f32 v227, v227, v229\n"
"v_mov_b32 v228, v226\n"
"v_mov_b32 v229, v227\n"
"s_nop 0\n"
"v_permlane16_swap_b32 v226, v228\n"
"v_permlane16_swap_b32 v227, v229\n"
"v_max_f32 v226, v226, v228\n"
"v_max_f32 v227, v227, v229\n"
"v_mov_b32 v228, v226\n"
"v_mov_b32 v229, v227\n"
"s_nop 0\n"
"v_permlane32_swap_b32 v226, v228\n"
"v_permlane32_swap_b32 v227, v229\n"
"v_max_f32 v226, v226, v228\n"
"v_max_f32 v227, v227, v229\n"
"v_sub_f32 v192, 0, v226\n"
"v_sub_f32 v193, 0, v226\n"
"v_sub_f32 v194, 0, v226\n"
"v_sub_f32 v195, 0, v226\n"
"v_sub_f32 v196, 0, v227\n"
"v_sub_f32 v197, 0, v227\n"
"v_sub_f32 v198, 0, v227\n"
"v_sub_f32 v199, 0, v227\n"
"v_lshrrev_b32 v224, 1, v248\n"
"s_lshl_b32 s50, s12, 9\n"
"s_add_u32 s50, s50, 131072\n"
"v_add_u32 v224, s50, v224\n"
"ds_write2_b32 v224, v192, v196 offset0:0 offset1:1\n"
"v_sub_f32 v128, v128, v226\n"
"v_sub_f32 v129, v129, v226\n"
"v_sub_f32 v130, v130, v226\n"
"v_sub_f32 v131, v131, v226\n"
"v_sub_f32 v132, v132, v226\n"
"v_sub_f32 v133, v133, v226\n"
"v_sub_f32 v134, v134, v226\n"
"v_sub_f32 v135, v135, v226\n"
"v_sub_f32 v136, v136, v227\n"
"v_sub_f32 v137, v137, v227\n"
"v_sub_f32 v138, v138, v227\n"
"v_sub_f32 v139, v139, v227\n"
"v_sub_f32 v140, v140, v227\n"
"v_sub_f32 v141, v141, v227\n"
"v_sub_f32 v142, v142, v227\n"
"v_sub_f32 v143, v143, v227\n"
"v_mfma_f32_32x32x16_bf16 v[0:15], v[96:99], v[96:99], 0\n"
"v_exp_f32 v128, v128\n"
"v_exp_f32 v129, v129\n"
"v_exp_f32 v130, v130\n"
"v_mfma_f32_32x32x16_bf16 v[16:31], v[96:99], v[96:99], 0\n"
"v_exp_f32 v131, v131\n"
"v_exp_f32 v132, v132\n"
"v_exp_f32 v133, v133\n"
"v_mfma_f32_32x32x16_bf16 v[32:47], v[96:99], v[96:99], 0\n"
"v_exp_f32 v134, v134\n"
"v_exp_f32 v135, v135\n"
"v_exp_f32 v136, v136\n"
"v_mfma_f32_32x32x16_bf16 v[48:63], v[96:99], v[96:99], 0\n"
"v_exp_f32 v137, v137\n"
"v_exp_f32 v138, v138\n"
"v_exp_f32 v139, v139\n"
"v_mfma_f32_32x32x16_bf16 v[64:79], v[96:99], v[96:99], 0\n"
"v_exp_f32 v140, v140\n"
"v_exp_f32 v141, v141\n"
"v_exp_f32 v142, v142\n"
"v_mfma_f32_32x32x16_bf16 v[80:95], v[96:99], v[96:99], 0\n"
"v_exp_f32 v143, v143\n"
"v_max3_f32 v226, v144, v145, v146\n"
"v_max3_f32 v228, v147, v148, v149\n"
"v_max3_f32 v227, v152, v153, v154\n"
"v_max3_f32 v229, v155, v156, v157\n"
"v_max3_f32 v226, v226, v150, v151\n"
"v_max3_f32 v227, v227, v158, v159\n"
"v_max_f32 v226, v226, v228\n"
"v_max_f32 v227, v227, v229\n"
"v_mov_b32 v228, v226\n"
"v_mov_b32 v229, v227\n"
"s_nop 0\n"
"v_permlane16_swap_b32 v226, v228\n"
"v_permlane16_swap_b32 v227, v229\n"
"v_max_f32 v226, v226, v228\n"
"v_max_f32 v227, v227, v229\n"
"v_mov_b32 v228, v226\n"
"v_mov_b32 v229, v227\n"
"s_nop 0\n"
"v_permlane32_swap_b32 v226, v228\n"
"v_permlane32_swap_b32 v227, v229\n"
"v_max_f32 v226, v226, v228\n"
"v_max_f32 v227, v227, v229\n"
"v_cvt_pk_bf16_f32 v128, v128, v129\n"
"v_cvt_pk_bf16_f32 v129, v130, v131\n"
"v_cvt_pk_bf16_f32 v130, v132, v133\n"
"v_cvt_pk_bf16_f32 v131, v134, v135\n"
"v_cvt_pk_bf16_f32 v132, v136, v137\n"
"v_cvt_pk_bf16_f32 v133, v138, v139\n"
"v_cvt_pk_bf16_f32 v134, v140, v141\n"
"v_cvt_pk_bf16_f32 v135, v142, v143\n"
"v_sub_f32 v144, v144, v226\n"
"v_sub_f32 v145, v145, v226\n"
"v_sub_f32 v146, v146, v226\n"
"v_sub_f32 v147, v147, v226\n"
"v_sub_f32 v148, v148, v226\n"
"v_sub_f32 v149, v149, v226\n"
"v_sub_f32 v150, v150, v226\n"
"v_sub_f32 v151, v151, v226\n"
"v_sub_f32 v152, v152, v227\n"
"v_sub_f32 v153, v153, v227\n"
"v_sub_f32 v154, v154, v227\n"
"v_sub_f32 v155, v155, v227\n"
"v_sub_f32 v156, v156, v227\n"
"v_sub_f32 v157, v157, v227\n"
"v_sub_f32 v158, v158, v227\n"
"v_sub_f32 v159, v159, v227\n"
"v_mfma_f32_16x16x32_bf16 v[208:211], a[232:235], v[128:131], 0\n"
"v_mfma_f32_16x16x32_bf16 v[208:211], a[236:239], v[132:135], v[208:211]\n"
"v_exp_f32 v144, v144\n"
"v_exp_f32 v145, v145\n"
"v_exp_f32 v146, v146\n"
"v_exp_f32 v147, v147\n"
"v_exp_f32 v148, v148\n"
"v_exp_f32 v149, v149\n"
"v_exp_f32 v150, v150\n"
"v_exp_f32 v151, v151\n"
"v_sub_f32 v200, 0, v226\n"
"v_sub_f32 v201, 0, v226\n"
"v_sub_f32 v202, 0, v226\n"
"v_sub_f32 v203, 0, v226\n"
"v_sub_f32 v204, 0, v227\n"
"v_sub_f32 v205, 0, v227\n"
"v_sub_f32 v206, 0, v227\n"
"v_sub_f32 v207, 0, v227\n"
"v_cvt_pk_bf16_f32 v144, v144, v145\n"
"v_cvt_pk_bf16_f32 v145, v146, v147\n"
"v_cvt_pk_bf16_f32 v146, v148, v149\n"
"v_cvt_pk_bf16_f32 v147, v150, v151\n"
"v_mov_b64 v[220:221], 0\n"
"v_mov_b64 v[222:223], 0\n"
"s_waitcnt vmcnt(8)\n"
"s_lshl_b32 s58, s13, 5\n"
"s_lshl_b32 s59, s12, 3\n"
"s_add_u32 s58, s58, s59\n"
"s_add_u32 s58, s58, 8192\n"
"v_mov_b32 v224, s98\n"
"v_mov_b32 v225, s99\n"
"v_mov_b32 v226, s58\n"
"s_mov_b64 exec, 1\n"
"global_store_dwordx2 v226, v[224:225], s[62:63] sc1\n"
"s_mov_b64 exec, -1\n"
"s_mov_b32 s14, 1\n"
"s_mov_b32 s29, 1\n"
"s_cmp_eq_u32 s11, 0\n"
"s_cbranch_scc1 Lfused_B2\n"
"s_lshl_b32 s29, s11, 1\n"
"s_sub_u32 s29, s29, 1\n"
"s_branch Lfused_A2\n"
"Lfused_last7:\n"
"s_cmp_ge_u32 s14, s38\n"
"s_cselect_b32 s34, 0xff800000, 0\n"
"s_cmp_gt_u32 s14, s38\n"
"s_cselect_b32 s35, 0xff800000, 0\n"
"v_add_f32 v224, s35, v192\n"
"v_add_f32 v225, s34, v196\n"
"v_add_f32 v132, s34, v192\n"
"v_add_f32 v133, s34, v192\n"
"v_add_f32 v134, s34, v192\n"
"v_add_f32 v135, s34, v192\n"
"v_cndmask_b32 v128, v224, v132, s[64:65]\n"
"v_cndmask_b32 v129, v224, v132, s[66:67]\n"
"v_cndmask_b32 v130, v224, v132, s[68:69]\n"
"v_cndmask_b32 v131, v224, v132, s[70:71]\n"
"v_add_f32 v136, s35, v196\n"
"v_add_f32 v137, s35, v196\n"
"v_add_f32 v138, s35, v196\n"
"v_add_f32 v139, s35, v196\n"
"v_cndmask_b32 v140, v136, v225, s[64:65]\n"
"v_cndmask_b32 v141, v136, v225, s[66:67]\n"
"v_cndmask_b32 v142, v136, v225, s[68:69]\n"
"v_cndmask_b32 v143, v136, v225, s[70:71]\n"
"Lfused_G0:\n"
"s_waitcnt vmcnt(16) lgkmcnt(0)\n"
"v_mfma_f32_16x16x32_bf16 v[128:131], a[128:131], a[0:3], v[128:131]\n"
"s_barrier\n"
"s_add_u32 m0, s44, 81920\n"
"s_add_u32 s55, s45, 32768\n"
"v_mfma_f32_16x16x32_bf16 v[128:131], a[136:139], a[4:7], v[128:131]\n"
"v_exp_f32 v184, v184\n"
"v_mfma_f32_16x16x32_bf16 v[128:131], a[144:147], a[8:11], v[128:131]\n"
"ds_read_b128 a[96:99], v250 offset:32768\n"
"v_mfma_f32_16x16x32_bf16 v[128:131], a[152:155], a[12:15], v[128:131]\n"
"v_exp_f32 v185, v185\n"
"v_mfma_f32_16x16x32_bf16 v[220:223], a[232:235], v[176:179], v[216:219]\n"
"ds_read_b128 a[100:103], v250 offset:33792\n"
"v_cvt_pk_bf16_f32 v180, v184, v185\n"
"v_mfma_f32_16x16x32_bf16 v[132:135], a[132:135], a[0:3], v[132:135]\n"
"Lfused_hop8:\n"
"s_cbranch_execz Lfused_hop12\n"
"v_exp_f32 v186, v186\n"
"v_mfma_f32_16x16x32_bf16 v[132:135], a[140:143], a[4:7], v[132:135]\n"
"buffer_load_dwordx4 v248, s[72:75], s55 offen offset:0 sc1 lds\n"
"v_mfma_f32_16x16x32_bf16 v[132:135], a[148:151], a[8:11], v[132:135]\n"
"v_exp_f32 v187, v187\n"
"v_mfma_f32_16x16x32_bf16 v[132:135], a[156:159], a[12:15], v[132:135]\n"
"ds_read_b128 a[104:107], v250 offset:34816\n"
"v_cvt_pk_bf16_f32 v181, v186, v187\n"
"v_mfma_f32_16x16x32_bf16 v[136:139], a[128:131], a[16:19], v[136:139]\n"
"v_exp_f32 v188, v188\n"
"v_mfma_f32_16x16x32_bf16 v[136:139], a[136:139], a[20:23], v[136:139]\n"
"buffer_load_dwordx4 v248, s[72:75], s55 offen offset:1024 sc1 lds\n"
"v_mfma_f32_16x16x32_bf16 v[136:139], a[144:147], a[24:27], v[136:139]\n"
"v_exp_f32 v189, v189\n"
"v_mfma_f32_16x16x32_bf16 v[136:139], a[152:155], a[28:31], v[136:139]\n"
"ds_read_b128 a[108:111], v250 offset:35840\n"
"v_cvt_pk_bf16_f32 v182, v188, v189\n"
"v_mfma_f32_16x16x32_bf16 v[140:143], a[132:135], a[16:19], v[140:143]\n"
"v_exp_f32 v190, v190\n"
"v_mfma_f32_16x16x32_bf16 v[140:143], a[140:143], a[20:23], v[140:143]\n"
"buffer_load_dwordx4 v248, s[72:75], s55 offen offset:2048 sc1 lds\n"
"v_mfma_f32_16x16x32_bf16 v[140:143], a[148:151], a[24:27], v[140:143]\n"
"v_exp_f32 v191, v191\n"
"v_mfma_f32_16x16x32_bf16 v[140:143], a[156:159], a[28:31], v[140:143]\n"
"ds_read_b128 a[112:115], v250 offset:36864\n"
"v_cvt_pk_bf16_f32 v183, v190, v191\n"
"v_mfma_f32_16x16x32_bf16 v[0:3], a[192:195], v[160:163], v[0:3]\n"
"v_exp_f32 v128, v128\n"
"v_mfma_f32_16x16x32_bf16 v[220:223], a[236:239], v[180:183], v[220:223]\n"
"buffer_load_dwordx4 v248, s[72:75], s55 offen offset:3072 sc1 lds\n"
"v_mfma_f32_16x16x32_bf16 v[4:7], a[196:199], v[160:163], v[4:7]\n"
"v_exp_f32 v129, v129\n"
"v_mfma_f32_16x16x32_bf16 v[8:11], a[200:203], v[160:163], v[8:11]\n"
"ds_read_b128 a[116:119], v250 offset:37888\n"
"v_cvt_pk_bf16_f32 v128, v128, v129\n"
"v_mfma_f32_16x16x32_bf16 v[12:15], a[204:207], v[160:163], v[12:15]\n"
"v_exp_f32 v130, v130\n"
"v_mfma_f32_16x16x32_bf16 v[16:19], a[208:211], v[160:163], v[16:19]\n"
"v_cmp_lt_f32 vcc, s43, v222\n"
"v_mfma_f32_16x16x32_bf16 v[20:23], a[212:215], v[160:163], v[20:23]\n"
"v_exp_f32 v131, v131\n"
"v_mfma_f32_16x16x32_bf16 v[24:27], a[216:219], v[160:163], v[24:27]\n"
"ds_read_b128 a[120:123], v250 offset:38912\n"
"v_cvt_pk_bf16_f32 v129, v130, v131\n"
"s_cmp_ge_u32 s14, s40\n"
"s_cselect_b32 s34, 0xff800000, 0\n"
"v_mfma_f32_16x16x32_bf16 v[28:31], a[220:223], v[160:163], v[28:31]\n"
"v_exp_f32 v132, v132\n"
"v_mfma_f32_16x16x32_bf16 v[32:35], a[192:195], v[164:167], v[32:35]\n"
"s_cbranch_vccnz Lfused_xG0\n"
"Lfused_rG0:\n"
"s_cmp_gt_u32 s14, s40\n"
"s_cselect_b32 s35, 0xff800000, 0\n"
"v_mfma_f32_16x16x32_bf16 v[36:39], a[196:199], v[164:167], v[36:39]\n"
"v_exp_f32 v133, v133\n"
"v_mfma_f32_16x16x32_bf16 v[40:43], a[200:203], v[164:167], v[40:43]\n"
"ds_read_b128 a[124:127], v250 offset:39936\n"
"v_cvt_pk_bf16_f32 v130, v132, v133\n"
"v_mov_b32 v200, v244\n"
"v_mfma_f32_16x16x32_bf16 v[44:47], a[204:207], v[164:167], v[44:47]\n"
"v_exp_f32 v134, v134\n"
"v_mfma_f32_16x16x32_bf16 v[48:51], a[208:211], v[164:167], v[48:51]\n"
"v_mov_b32 v201, v244\n"
"v_mov_b32 v202, v244\n"
"v_mov_b32 v203, v244\n"
"v_mov_b32 v204, v245\n"
"v_mov_b32 v205, v245\n"
"v_mov_b32 v206, v245\n"
"v_mov_b32 v207, v245\n"
"v_add_f32 v224, s35, v200\n"
"v_add_f32 v225, s34, v204\n"
"v_add_f32 v148, s34, v200\n"
"v_add_f32 v149, s34, v200\n"
"v_add_f32 v150, s34, v200\n"
"v_mfma_f32_16x16x32_bf16 v[52:55], a[212:215], v[164:167], v[52:55]\n"
"v_exp_f32 v135, v135\n"
"v_mfma_f32_16x16x32_bf16 v[56:59], a[216:219], v[164:167], v[56:59]\n"
"v_cvt_pk_bf16_f32 v131, v134, v135\n"
"v_add_f32 v151, s34, v200\n"
"v_cndmask_b32 v144, v224, v148, s[64:65]\n"
"v_cndmask_b32 v145, v224, v148, s[66:67]\n"
"v_cndmask_b32 v146, v224, v148, s[68:69]\n"
"v_cndmask_b32 v147, v224, v148, s[70:71]\n"
"v_add_f32 v152, s35, v204\n"
"v_add_f32 v153, s35, v204\n"
"v_add_f32 v154, s35, v204\n"
"v_add_f32 v155, s35, v204\n"
"v_cndmask_b32 v156, v152, v225, s[64:65]\n"
"v_cndmask_b32 v157, v152, v225, s[66:67]\n"
"v_cndmask_b32 v158, v152, v225, s[68:69]\n"
"v_cndmask_b32 v159, v152, v225, s[70:71]\n"
"v_mfma_f32_16x16x32_bf16 v[60:63], a[220:223], v[164:167], v[60:63]\n"
"Lfused_G1:\n"
"v_mfma_f32_16x16x32_bf16 v[144:147], a[128:131], a[64:67], v[144:147]\n"
"ds_read_b128 a[160:163], v250 offset:8192\n"
"s_add_u32 m0, s44, 90112\n"
"s_add_u32 s55, s45, 40960\n"
"v_mfma_f32_16x16x32_bf16 v[144:147], a[136:139], a[68:71], v[144:147]\n"
"v_exp_f32 v136, v136\n"
"v_mfma_f32_16x16x32_bf16 v[144:147], a[144:147], a[72:75], v[144:147]\n"
"ds_read_b128 a[164:167], v250 offset:9216\n"
"v_mfma_f32_16x16x32_bf16 v[144:147], a[152:155], a[76:79], v[144:147]\n"
"v_exp_f32 v137, v137\n"
"v_mfma_f32_16x16x32_bf16 v[208:211], a[232:235], v[128:131], v[212:215]\n"
"ds_read_b128 a[168:171], v250 offset:10240\n"
"v_cvt_pk_bf16_f32 v132, v136, v137\n"
"v_mfma_f32_16x16x32_bf16 v[148:151], a[132:135], a[64:67], v[148:151]\n"
"v_exp_f32 v138, v138\n"
"v_mfma_f32_16x16x32_bf16 v[148:151], a[140:143], a[68:71], v[148:151]\n"
"buffer_load_dwordx4 v248, s[72:75], s55 offen offset:0 sc1 lds\n"
"v_mfma_f32_16x16x32_bf16 v[148:151], a[148:151], a[72:75], v[148:151]\n"
"v_exp_f32 v139, v139\n"
"v_mfma_f32_16x16x32_bf16 v[148:151], a[156:159], a[76:79], v[148:151]\n"
"ds_read_b128 a[172:175], v250 offset:11264\n"
"v_cvt_pk_bf16_f32 v133, v138, v139\n"
"v_mfma_f32_16x16x32_bf16 v[152:155], a[128:131], a[80:83], v[152:155]\n"
"v_exp_f32 v140, v140\n"
"v_mfma_f32_16x16x32_bf16 v[152:155], a[136:139], a[84:87], v[152:155]\n"
"buffer_load_dwordx4 v248, s[72:75], s55 offen offset:1024 sc1 lds\n"
"v_mfma_f32_16x16x32_bf16 v[152:155], a[144:147], a[88:91], v[152:155]\n"
"v_exp_f32 v141, v141\n"
"v_mfma_f32_16x16x32_bf16 v[152:155], a[152:155], a[92:95], v[152:155]\n"
"ds_read_b128 a[176:179], v250 offset:12288\n"
"v_cvt_pk_bf16_f32 v134, v140, v141\n"
"v_mfma_f32_16x16x32_bf16 v[156:159], a[132:135], a[80:83], v[156:159]\n"
"v_exp_f32 v142, v142\n"
"v_mfma_f32_16x16x32_bf16 v[156:159], a[140:143], a[84:87], v[156:159]\n"
"buffer_load_dwordx4 v248, s[72:75], s55 offen offset:2048 sc1 lds\n"
"v_mfma_f32_16x16x32_bf16 v[156:159], a[148:151], a[88:91], v[156:159]\n"
"v_exp_f32 v143, v143\n"
"v_mfma_f32_16x16x32_bf16 v[156:159], a[156:159], a[92:95], v[156:159]\n"
"ds_read_b128 a[180:183], v250 offset:13312\n"
"v_cvt_pk_bf16_f32 v135, v142, v143\n"
"v_mfma_f32_16x16x32_bf16 v[64:67], a[192:195], v[176:179], v[64:67]\n"
"v_exp_f32 v144, v144\n"
"v_mfma_f32_16x16x32_bf16 v[208:211], a[236:239], v[132:135], v[208:211]\n"
"buffer_load_dwordx4 v248, s[72:75], s55 offen offset:3072 sc1 lds\n"
"v_mfma_f32_16x16x32_bf16 v[68:71], a[196:199], v[176:179], v[68:71]\n"
"v_exp_f32 v145, v145\n"
"v_mfma_f32_16x16x32_bf16 v[72:75], a[200:203], v[176:179], v[72:75]\n"
"ds_read_b128 a[184:187], v250 offset:14336\n"
"v_cvt_pk_bf16_f32 v144, v144, v145\n"
"v_mfma_f32_16x16x32_bf16 v[76:79], a[204:207], v[176:179], v[76:79]\n"
"v_exp_f32 v146, v146\n"
"v_mfma_f32_16x16x32_bf16 v[80:83], a[208:211], v[176:179], v[80:83]\n"
"v_cmp_lt_f32 vcc, s43, v210\n"
"v_mfma_f32_16x16x32_bf16 v[84:87], a[212:215], v[176:179], v[84:87]\n"
"v_exp_f32 v147, v147\n"
"v_mfma_f32_16x16x32_bf16 v[88:91], a[216:219], v[176:179], v[88:91]\n"
"ds_read_b128 a[188:191], v250 offset:15360\n"
"v_cvt_pk_bf16_f32 v145, v146, v147\n"
"s_add_u32 s14, s14, 1\n"
"s_cmp_ge_u32 s14, s38\n"
"s_cselect_b32 s34, 0xff800000, 0\n"
"v_mfma_f32_16x16x32_bf16 v[92:95], a[220:223], v[176:179], v[92:95]\n"
"v_exp_f32 v148, v148\n"
"v_mfma_f32_16x16x32_bf16 v[96:99], a[192:195], v[180:183], v[96:99]\n"
"s_cbranch_vccnz Lfused_xG1\n"
"Lfused_rG1:\n"
"s_cmp_gt_u32 s14, s38\n"
"s_cselect_b32 s35, 0xff800000, 0\n"
"v_mfma_f32_16x16x32_bf16 v[100:103], a[196:199], v[180:183], v[100:103]\n"
"v_exp_f32 v149, v149\n"
"v_mfma_f32_16x16x32_bf16 v[104:107], a[200:203], v[180:183], v[104:107]\n"
"v_cvt_pk_bf16_f32 v146, v148, v149\n"
"v_add_f32 v224, s35, v192\n"
"v_mfma_f32_16x16x32_bf16 v[108:111], a[204:207], v[180:183], v[108:111]\n"
"v_exp_f32 v150, v150\n"
"v_mfma_f32_16x16x32_bf16 v[112:115], a[208:211], v[180:183], v[112:115]\n"
"v_add_f32 v225, s34, v196\n"
"v_add_f32 v164, s34, v192\n"
"v_add_f32 v165, s34, v192\n"
"v_add_f32 v166, s34, v192\n"
"v_add_f32 v167, s34, v192\n"
"v_cndmask_b32 v160, v224, v164, s[64:65]\n"
"v_cndmask_b32 v161, v224, v164, s[66:67]\n"
"v_cndmask_b32 v162, v224, v164, s[68:69]\n"
"v_mfma_f32_16x16x32_bf16 v[116:119], a[212:215], v[180:183], v[116:119]\n"
"v_exp_f32 v151, v151\n"
"v_mfma_f32_16x16x32_bf16 v[120:123], a[216:219], v[180:183], v[120:123]\n"
"v_cvt_pk_bf16_f32 v147, v150, v151\n"
"v_cndmask_b32 v163, v224, v164, s[70:71]\n"
"v_add_f32 v168, s35, v196\n"
"v_add_f32 v169, s35, v196\n"
"v_add_f32 v170, s35, v196\n"
"v_add_f32 v171, s35, v196\n"
"v_cndmask_b32 v172, v168, v225, s[64:65]\n"
"v_cndmask_b32 v173, v168, v225, s[66:67]\n"
"v_cndmask_b32 v174, v168, v225, s[68:69]\n"
"v_cndmask_b32 v175, v168, v225, s[70:71]\n"
"v_mfma_f32_16x16x32_bf16 v[124:127], a[220:223], v[180:183], v[124:127]\n"
"v_rcp_f32 v226, v220\n"
"v_rcp_f32 v227, v221\n"
"v_mov_b64 v[220:221], 0\n"
"v_mov_b64 v[222:223], 0\n"
"v_mul_f32 v64, v64, v226\n"
"v_mul_f32 v65, v65, v226\n"
"v_mul_f32 v66, v66, v226\n"
"v_mul_f32 v67, v67, v226\n"
"v_mul_f32 v68, v68, v226\n"
"v_mul_f32 v69, v69, v226\n"
"v_mul_f32 v70, v70, v226\n"
"v_mul_f32 v71, v71, v226\n"
"v_cvt_pk_bf16_f32 v64, v64, v65\n"
"v_cvt_pk_bf16_f32 v65, v66, v67\n"
"v_cvt_pk_bf16_f32 v66, v68, v69\n"
"v_cvt_pk_bf16_f32 v67, v70, v71\n"
"buffer_store_dwordx4 v[64:67], v248, s[24:27], s48 offen offset:0 sc1\n"
"v_mul_f32 v72, v72, v226\n"
"v_mul_f32 v73, v73, v226\n"
"v_mul_f32 v74, v74, v226\n"
"v_mul_f32 v75, v75, v226\n"
"v_mul_f32 v76, v76, v226\n"
"v_mul_f32 v77, v77, v226\n"
"v_mul_f32 v78, v78, v226\n"
"v_mul_f32 v79, v79, v226\n"
"v_cvt_pk_bf16_f32 v72, v72, v73\n"
"v_cvt_pk_bf16_f32 v73, v74, v75\n"
"v_cvt_pk_bf16_f32 v74, v76, v77\n"
"v_cvt_pk_bf16_f32 v75, v78, v79\n"
"buffer_store_dwordx4 v[72:75], v248, s[24:27], s48 offen offset:1024 sc1\n"
"v_mul_f32 v80, v80, v226\n"
"v_mul_f32 v81, v81, v226\n"
"v_mul_f32 v82, v82, v226\n"
"v_mul_f32 v83, v83, v226\n"
"v_mul_f32 v84, v84, v226\n"
"v_mul_f32 v85, v85, v226\n"
"v_mul_f32 v86, v86, v226\n"
"v_mul_f32 v87, v87, v226\n"
"v_cvt_pk_bf16_f32 v80, v80, v81\n"
"v_cvt_pk_bf16_f32 v81, v82, v83\n"
"v_cvt_pk_bf16_f32 v82, v84, v85\n"
"v_cvt_pk_bf16_f32 v83, v86, v87\n"
"buffer_store_dwordx4 v[80:83], v248, s[24:27], s48 offen offset:2048 sc1\n"
"v_mul_f32 v88, v88, v226\n"
"v_mul_f32 v89, v89, v226\n"
"v_mul_f32 v90, v90, v226\n"
"v_mul_f32 v91, v91, v226\n"
"v_mul_f32 v92, v92, v226\n"
"v_mul_f32 v93, v93, v226\n"
"v_mul_f32 v94, v94, v226\n"
"v_mul_f32 v95, v95, v226\n"
"v_cvt_pk_bf16_f32 v88, v88, v89\n"
"v_cvt_pk_bf16_f32 v89, v90, v91\n"
"v_cvt_pk_bf16_f32 v90, v92, v93\n"
"v_cvt_pk_bf16_f32 v91, v94, v95\n"
"buffer_store_dwordx4 v[88:91], v248, s[24:27], s48 offen offset:3072 sc1\n"
"s_add_u32 s48, s48, 147456\n"
"v_mul_f32 v96, v96, v227\n"
"v_mul_f32 v97, v97, v227\n"
"v_mul_f32 v98, v98, v227\n"
"v_mul_f32 v99, v99, v227\n"
"v_mul_f32 v100, v100, v227\n"
"v_mul_f32 v101, v101, v227\n"
"v_mul_f32 v102, v102, v227\n"
"v_mul_f32 v103, v103, v227\n"
"v_cvt_pk_bf16_f32 v96, v96, v97\n"
"v_cvt_pk_bf16_f32 v97, v98, v99\n"
"v_cvt_pk_bf16_f32 v98, v100, v101\n"
"v_cvt_pk_bf16_f32 v99, v102, v103\n"
"buffer_store_dwordx4 v[96:99], v248, s[24:27], s48 offen offset:0 sc1\n"
"v_mul_f32 v104, v104, v227\n"
"v_mul_f32 v105, v105, v227\n"
"v_mul_f32 v106, v106, v227\n"
"v_mul_f32 v107, v107, v227\n"
"v_mul_f32 v108, v108, v227\n"
"v_mul_f32 v109, v109, v227\n"
"v_mul_f32 v110, v110, v227\n"
"v_mul_f32 v111, v111, v227\n"
"v_cvt_pk_bf16_f32 v104, v104, v105\n"
"v_cvt_pk_bf16_f32 v105, v106, v107\n"
"v_cvt_pk_bf16_f32 v106, v108, v109\n"
"v_cvt_pk_bf16_f32 v107, v110, v111\n"
"buffer_store_dwordx4 v[104:107], v248, s[24:27], s48 offen offset:1024 sc1\n"
"v_mul_f32 v112, v112, v227\n"
"v_mul_f32 v113, v113, v227\n"
"v_mul_f32 v114, v114, v227\n"
"v_mul_f32 v115, v115, v227\n"
"v_mul_f32 v116, v116, v227\n"
"v_mul_f32 v117, v117, v227\n"
"v_mul_f32 v118, v118, v227\n"
"v_mul_f32 v119, v119, v227\n"
"v_cvt_pk_bf16_f32 v112, v112, v113\n"
"v_cvt_pk_bf16_f32 v113, v114, v115\n"
"v_cvt_pk_bf16_f32 v114, v116, v117\n"
"v_cvt_pk_bf16_f32 v115, v118, v119\n"
"buffer_store_dwordx4 v[112:115], v248, s[24:27], s48 offen offset:2048 sc1\n"
"v_mul_f32 v120, v120, v227\n"
"v_mul_f32 v121, v121, v227\n"
"v_mul_f32 v122, v122, v227\n"
"v_mul_f32 v123, v123, v227\n"
"v_mul_f32 v124, v124, v227\n"
"v_mul_f32 v125, v125, v227\n"
"v_mul_f32 v126, v126, v227\n"
"v_mul_f32 v127, v127, v227\n"
"v_cvt_pk_bf16_f32 v120, v120, v121\n"
"v_cvt_pk_bf16_f32 v121, v122, v123\n"
"v_cvt_pk_bf16_f32 v122, v124, v125\n"
"v_cvt_pk_bf16_f32 v123, v126, v127\n"
"buffer_store_dwordx4 v[120:123], v248, s[24:27], s48 offen offset:3072 sc1\n"
"Lfused_G2:\n"
"s_waitcnt vmcnt(16) lgkmcnt(0)\n"
"v_mfma_f32_16x16x32_bf16 v[160:163], a[160:163], a[0:3], v[160:163]\n"
"s_barrier\n"
"s_add_u32 m0, s44, 65536\n"
"s_add_u32 s55, s45, 49152\n"
"v_mfma_f32_16x16x32_bf16 v[160:163], a[168:171], a[4:7], v[160:163]\n"
"v_exp_f32 v152, v152\n"
"v_mfma_f32_16x16x32_bf16 v[160:163], a[176:179], a[8:11], v[160:163]\n"
"ds_read_b128 a[192:195], v250 offset:40960\n"
"v_mfma_f32_16x16x32_bf16 v[160:163], a[184:187], a[12:15], v[160:163]\n"
"v_exp_f32 v153, v153\n"
"v_mfma_f32_16x16x32_bf16 v[216:219], a[232:235], v[144:147], v[220:223]\n"
"ds_read_b128 a[196:199], v250 offset:41984\n"
"v_cvt_pk_bf16_f32 v148, v152, v153\n"
"v_mfma_f32_16x16x32_bf16 v[164:167], a[164:167], a[0:3], v[164:167]\n"
"v_exp_f32 v154, v154\n"
"v_mfma_f32_16x16x32_bf16 v[164:167], a[172:175], a[4:7], v[164:167]\n"
"buffer_load_dwordx4 v248, s[72:75], s55 offen offset:0 sc1 lds\n"
"v_mfma_f32_16x16x32_bf16 v[164:167], a[180:183], a[8:11], v[164:167]\n"
"v_exp_f32 v155, v155\n"
"v_mfma_f32_16x16x32_bf16 v[164:167], a[188:191], a[12:15], v[164:167]\n"
"ds_read_b128 a[200:203], v250 offset:43008\n"
"v_cvt_pk_bf16_f32 v149, v154, v155\n"
"v_mfma_f32_16x16x32_bf16 v[168:171], a[160:163], a[16:19], v[168:171]\n"
"v_exp_f32 v156, v156\n"
"v_mfma_f32_16x16x32_bf16 v[168:171], a[168:171], a[20:23], v[168:171]\n"
"buffer_load_dwordx4 v248, s[72:75], s55 offen offset:1024 sc1 lds\n"
"v_mfma_f32_16x16x32_bf16 v[168:171], a[176:179], a[24:27], v[168:171]\n"
"v_exp_f32 v157, v157\n"
"v_mfma_f32_16x16x32_bf16 v[168:171], a[184:187], a[28:31], v[168:171]\n"
"ds_read_b128 a[204:207], v250 offset:44032\n"
"v_cvt_pk_bf16_f32 v150, v156, v157\n"
"v_mfma_f32_16x16x32_bf16 v[172:175], a[164:167], a[16:19], v[172:175]\n"
"v_exp_f32 v158, v158\n"
"v_mfma_f32_16x16x32_bf16 v[172:175], a[172:175], a[20:23], v[172:175]\n"
"buffer_load_dwordx4 v248, s[72:75], s55 offen offset:2048 sc1 lds\n"
"v_mfma_f32_16x16x32_bf16 v[172:175], a[180:183], a[24:27], v[172:175]\n"
"v_exp_f32 v159, v159\n"
"v_mfma_f32_16x16x32_bf16 v[172:175], a[188:191], a[28:31], v[172:175]\n"
"ds_read_b128 a[208:211], v250 offset:45056\n"
"v_cvt_pk_bf16_f32 v151, v158, v159\n"
"v_mfma_f32_16x16x32_bf16 v[0:3], a[96:99], v[128:131], v[0:3]\n"
"v_exp_f32 v160, v160\n"
"v_mfma_f32_16x16x32_bf16 v[216:219], a[236:239], v[148:151], v[216:219]\n"
"buffer_load_dwordx4 v248, s[72:75], s55 offen offset:3072 sc1 lds\n"
"v_mfma_f32_16x16x32_bf16 v[4:7], a[100:103], v[128:131], v[4:7]\n"
"v_exp_f32 v161, v161\n"
"v_mfma_f32_16x16x32_bf16 v[8:11], a[104:107], v[128:131], v[8:11]\n"
"ds_read_b128 a[212:215], v250 offset:46080\n"
"v_cvt_pk_bf16_f32 v160, v160, v161\n"
"v_mfma_f32_16x16x32_bf16 v[12:15], a[108:111], v[128:131], v[12:15]\n"
"v_exp_f32 v162, v162\n"
"v_mfma_f32_16x16x32_bf16 v[16:19], a[112:115], v[128:131], v[16:19]\n"
"v_cmp_lt_f32 vcc, s43, v218\n"
"v_mfma_f32_16x16x32_bf16 v[20:23], a[116:119], v[128:131], v[20:23]\n"
"v_exp_f32 v163, v163\n"
"v_mfma_f32_16x16x32_bf16 v[24:27], a[120:123], v[128:131], v[24:27]\n"
"ds_read_b128 a[216:219], v250 offset:47104\n"
"v_cvt_pk_bf16_f32 v161, v162, v163\n"
"s_cmp_ge_u32 s14, s40\n"
"s_cselect_b32 s34, 0xff800000, 0\n"
"v_mfma_f32_16x16x32_bf16 v[28:31], a[124:127], v[128:131], v[28:31]\n"
"v_exp_f32 v164, v164\n"
"v_mfma_f32_16x16x32_bf16 v[32:35], a[96:99], v[132:135], v[32:35]\n"
"s_cbranch_vccnz Lfused_xG2\n"
"Lfused_rG2:\n"
"s_cmp_gt_u32 s14, s40\n"
"s_cselect_b32 s35, 0xff800000, 0\n"
"v_mfma_f32_16x16x32_bf16 v[36:39], a[100:103], v[132:135], v[36:39]\n"
"v_exp_f32 v165, v165\n"
"v_mfma_f32_16x16x32_bf16 v[40:43], a[104:107], v[132:135], v[40:43]\n"
"ds_read_b128 a[220:223], v250 offset:48128\n"
"v_cvt_pk_bf16_f32 v162, v164, v165\n"
"v_add_f32 v224, s35, v200\n"
"v_mfma_f32_16x16x32_bf16 v[44:47], a[108:111], v[132:135], v[44:47]\n"
"v_exp_f32 v166, v166\n"
"v_mfma_f32_16x16x32_bf16 v[48:51], a[112:115], v[132:135], v[48:51]\n"
"v_add_f32 v225, s34, v204\n"
"v_add_f32 v180, s34, v200\n"
"v_add_f32 v181, s34, v200\n"
"v_add_f32 v182, s34, v200\n"
"v_add_f32 v183, s34, v200\n"
"v_cndmask_b32 v176, v224, v180, s[64:65]\n"
"v_cndmask_b32 v177, v224, v180, s[66:67]\n"
"v_cndmask_b32 v178, v224, v180, s[68:69]\n"
"v_mfma_f32_16x16x32_bf16 v[52:55], a[116:119], v[132:135], v[52:55]\n"
"v_exp_f32 v167, v167\n"
"v_mfma_f32_16x16x32_bf16 v[56:59], a[120:123], v[132:135], v[56:59]\n"
"v_cvt_pk_bf16_f32 v163, v166, v167\n"
"v_cndmask_b32 v179, v224, v180, s[70:71]\n"
"v_add_f32 v184, s35, v204\n"
"v_add_f32 v185, s35, v204\n"
"v_add_f32 v186, s35, v204\n"
"v_add_f32 v187, s35, v204\n"
"v_cndmask_b32 v188, v184, v225, s[64:65]\n"
"v_cndmask_b32 v189, v184, v225, s[66:67]\n"
"v_cndmask_b32 v190, v184, v225, s[68:69]\n"
"v_cndmask_b32 v191, v184, v225, s[70:71]\n"
"v_mfma_f32_16x16x32_bf16 v[60:63], a[124:127], v[132:135], v[60:63]\n"
"Lfused_G3:\n"
"v_mfma_f32_16x16x32_bf16 v[176:179], a[160:163], a[64:67], v[176:179]\n"
"s_add_u32 m0, s90, 147456\n"
"s_add_u32 s55, s45, 57344\n"
"v_mfma_f32_16x16x32_bf16 v[176:179], a[168:171], a[68:71], v[176:179]\n"
"v_exp_f32 v168, v168\n"
"v_mfma_f32_16x16x32_bf16 v[176:179], a[176:179], a[72:75], v[176:179]\n"
"buffer_load_dwordx4 v248, s[72:75], s55 offen offset:0 sc1 lds\n"
"v_mfma_f32_16x16x32_bf16 v[176:179], a[184:187], a[76:79], v[176:179]\n"
"v_exp_f32 v169, v169\n"
"v_mfma_f32_16x16x32_bf16 v[212:215], a[232:235], v[160:163], v[208:211]\n"
"v_cvt_pk_bf16_f32 v164, v168, v169\n"
"v_mfma_f32_16x16x32_bf16 v[180:183], a[164:167], a[64:67], v[180:183]\n"
"v_exp_f32 v170, v170\n"
"v_mfma_f32_16x16x32_bf16 v[180:183], a[172:175], a[68:71], v[180:183]\n"
"buffer_load_dwordx4 v248, s[72:75], s55 offen offset:1024 sc1 lds\n"
"v_mfma_f32_16x16x32_bf16 v[180:183], a[180:183], a[72:75], v[180:183]\n"
"v_exp_f32 v171, v171\n"
"v_mfma_f32_16x16x32_bf16 v[180:183], a[188:191], a[76:79], v[180:183]\n"
"v_cvt_pk_bf16_f32 v165, v170, v171\n"
"v_mfma_f32_16x16x32_bf16 v[184:187], a[160:163], a[80:83], v[184:187]\n"
"v_exp_f32 v172, v172\n"
"v_mfma_f32_16x16x32_bf16 v[184:187], a[168:171], a[84:87], v[184:187]\n"
"buffer_load_dwordx4 v248, s[72:75], s55 offen offset:2048 sc1 lds\n"
"v_mfma_f32_16x16x32_bf16 v[184:187], a[176:179], a[88:91], v[184:187]\n"
"v_exp_f32 v173, v173\n"
"v_mfma_f32_16x16x32_bf16 v[184:187], a[184:187], a[92:95], v[184:187]\n"
"v_cvt_pk_bf16_f32 v166, v172, v173\n"
"v_mfma_f32_16x16x32_bf16 v[188:191], a[164:167], a[80:83], v[188:191]\n"
"v_exp_f32 v174, v174\n"
"v_mfma_f32_16x16x32_bf16 v[188:191], a[172:175], a[84:87], v[188:191]\n"
"buffer_load_dwordx4 v248, s[72:75], s55 offen offset:3072 sc1 lds\n"
"v_mfma_f32_16x16x32_bf16 v[188:191], a[180:183], a[88:91], v[188:191]\n"
"v_exp_f32 v175, v175\n"
"v_mfma_f32_16x16x32_bf16 v[188:191], a[188:191], a[92:95], v[188:191]\n"
"s_add_u32 s55, s79, 8192\n"
"v_cvt_pk_bf16_f32 v167, v174, v175\n"
"v_mfma_f32_16x16x32_bf16 v[64:67], a[96:99], v[144:147], 0\n"
"v_exp_f32 v176, v176\n"
"v_mfma_f32_16x16x32_bf16 v[212:215], a[236:239], v[164:167], v[212:215]\n"
"buffer_load_dwordx4 a[128:131], v248, s[72:75], s55 offen offset:0 sc1\n"
"v_mfma_f32_16x16x32_bf16 v[68:71], a[100:103], v[144:147], 0\n"
"v_exp_f32 v177, v177\n"
"v_mfma_f32_16x16x32_bf16 v[72:75], a[104:107], v[144:147], 0\n"
"v_cvt_pk_bf16_f32 v176, v176, v177\n"
"v_mfma_f32_16x16x32_bf16 v[76:79], a[108:111], v[144:147], 0\n"
"v_exp_f32 v178, v178\n"
"v_mfma_f32_16x16x32_bf16 v[80:83], a[112:115], v[144:147], 0\n"
"v_cmp_lt_f32 vcc, s43, v214\n"
"v_mfma_f32_16x16x32_bf16 v[84:87], a[116:119], v[144:147], 0\n"
"v_exp_f32 v179, v179\n"
"v_mfma_f32_16x16x32_bf16 v[88:91], a[120:123], v[144:147], 0\n"
"v_cvt_pk_bf16_f32 v177, v178, v179\n"
"s_add_u32 s14, s14, 1\n"
"v_mfma_f32_16x16x32_bf16 v[92:95], a[124:127], v[144:147], 0\n"
"v_exp_f32 v180, v180\n"
"v_mfma_f32_16x16x32_bf16 v[96:99], a[96:99], v[148:151], 0\n"
"buffer_load_dwordx4 a[132:135], v248, s[72:75], s55 offen offset:1024 sc1\n"
"s_cbranch_vccnz Lfused_xG3\n"
"Lfused_rG3:\n"
"v_mfma_f32_16x16x32_bf16 v[100:103], a[100:103], v[148:151], 0\n"
"v_exp_f32 v181, v181\n"
"v_mfma_f32_16x16x32_bf16 v[104:107], a[104:107], v[148:151], 0\n"
"v_cvt_pk_bf16_f32 v178, v180, v181\n"
"v_mfma_f32_16x16x32_bf16 v[108:111], a[108:111], v[148:151], 0\n"
"v_exp_f32 v182, v182\n"
"v_mfma_f32_16x16x32_bf16 v[112:115], a[112:115], v[148:151], 0\n"
"buffer_load_dwordx4 a[136:139], v248, s[72:75], s55 offen offset:2048 sc1\n"
"v_mfma_f32_16x16x32_bf16 v[116:119], a[116:119], v[148:151], 0\n"
"v_exp_f32 v183, v183\n"
"v_mfma_f32_16x16x32_bf16 v[120:123], a[120:123], v[148:151], 0\n"
"v_cvt_pk_bf16_f32 v179, v182, v183\n"
"v_mfma_f32_16x16x32_bf16 v[124:127], a[124:127], v[148:151], 0\n"
"buffer_load_dwordx4 a[140:143], v248, s[72:75], s55 offen offset:3072 sc1\n"
"s_branch Lfused_drain\n"
".p2align 6\n"
"Lfused_A0:\n"
"s_waitcnt vmcnt(8) lgkmcnt(0)\n"
"v_mfma_f32_16x16x32_bf16 v[128:131], a[128:131], a[0:3], v[192:195]\n"
"s_barrier\n"
"s_mov_b32 m0, s30\n"
"v_mfma_f32_16x16x32_bf16 v[128:131], a[136:139], a[4:7], v[128:131]\n"
"v_exp_f32 v184, v184\n"
"v_mfma_f32_16x16x32_bf16 v[128:131], a[144:147], a[8:11], v[128:131]\n"
"ds_read_b128 a[96:99], v250 offset:32768\n"
"v_mfma_f32_16x16x32_bf16 v[128:131], a[152:155], a[12:15], v[128:131]\n"
"v_exp_f32 v185, v185\n"
"v_mfma_f32_16x16x32_bf16 v[220:223], a[232:235], v[176:179], v[216:219]\n"
"ds_read_b128 a[100:103], v250 offset:33792\n"
"v_cvt_pk_bf16_f32 v180, v184, v185\n"
"v_mfma_f32_16x16x32_bf16 v[132:135], a[132:135], a[0:3], v[192:195]\n"
"v_exp_f32 v186, v186\n"
"v_mfma_f32_16x16x32_bf16 v[132:135], a[140:143], a[4:7], v[132:135]\n"
"buffer_load_dwordx4 v248, s[16:19], s28 offen offset:0 sc1 lds\n"
"v_mfma_f32_16x16x32_bf16 v[132:135], a[148:151], a[8:11], v[132:135]\n"
"v_exp_f32 v187, v187\n"
"v_mfma_f32_16x16x32_bf16 v[132:135], a[156:159], a[12:15], v[132:135]\n"
"ds_read_b128 a[104:107], v250 offset:34816\n"
"v_cvt_pk_bf16_f32 v181, v186, v187\n"
"v_mfma_f32_16x16x32_bf16 v[136:139], a[128:131], a[16:19], v[196:199]\n"
"v_exp_f32 v188, v188\n"
"v_mfma_f32_16x16x32_bf16 v[136:139], a[136:139], a[20:23], v[136:139]\n"
"buffer_load_dwordx4 v248, s[16:19], s28 offen offset:1024 sc1 lds\n"
"v_mfma_f32_16x16x32_bf16 v[136:139], a[144:147], a[24:27], v[136:139]\n"
"v_exp_f32 v189, v189\n"
"v_mfma_f32_16x16x32_bf16 v[136:139], a[152:155], a[28:31], v[136:139]\n"
"ds_read_b128 a[108:111], v250 offset:35840\n"
"v_cvt_pk_bf16_f32 v182, v188, v189\n"
"v_mfma_f32_16x16x32_bf16 v[140:143], a[132:135], a[16:19], v[196:199]\n"
"v_exp_f32 v190, v190\n"
"v_mfma_f32_16x16x32_bf16 v[140:143], a[140:143], a[20:23], v[140:143]\n"
"buffer_load_dwordx4 v248, s[16:19], s28 offen offset:2048 sc1 lds\n"
"v_mfma_f32_16x16x32_bf16 v[140:143], a[148:151], a[24:27], v[140:143]\n"
"v_exp_f32 v191, v191\n"
"v_mfma_f32_16x16x32_bf16 v[140:143], a[156:159], a[28:31], v[140:143]\n"
"ds_read_b128 a[112:115], v250 offset:36864\n"
"v_cvt_pk_bf16_f32 v183, v190, v191\n"
"v_mfma_f32_16x16x32_bf16 v[0:3], a[192:195], v[160:163], v[0:3]\n"
"v_exp_f32 v128, v128\n"
"v_mfma_f32_16x16x32_bf16 v[220:223], a[236:239], v[180:183], v[220:223]\n"
"buffer_load_dwordx4 v248, s[16:19], s28 offen offset:3072 sc1 lds\n"
"v_mfma_f32_16x16x32_bf16 v[4:7], a[196:199], v[160:163], v[4:7]\n"
"v_exp_f32 v129, v129\n"
"v_mfma_f32_16x16x32_bf16 v[8:11], a[200:203], v[160:163], v[8:11]\n"
"ds_read_b128 a[116:119], v250 offset:37888\n"
"v_cvt_pk_bf16_f32 v128, v128, v129\n"
"v_mfma_f32_16x16x32_bf16 v[12:15], a[204:207], v[160:163], v[12:15]\n"
"v_exp_f32 v130, v130\n"
"v_mfma_f32_16x16x32_bf16 v[16:19], a[208:211], v[160:163], v[16:19]\n"
"v_cmp_lt_f32 vcc, s43, v222\n"
"v_mfma_f32_16x16x32_bf16 v[20:23], a[212:215], v[160:163], v[20:23]\n"
"v_exp_f32 v131, v131\n"
"v_mfma_f32_16x16x32_bf16 v[24:27], a[216:219], v[160:163], v[24:27]\n"
"ds_read_b128 a[120:123], v250 offset:38912\n"
"v_cvt_pk_bf16_f32 v129, v130, v131\n"
"v_mfma_f32_16x16x32_bf16 v[28:31], a[220:223], v[160:163], v[28:31]\n"
"v_exp_f32 v132, v132\n"
"v_mfma_f32_16x16x32_bf16 v[32:35], a[192:195], v[164:167], v[32:35]\n"
"s_cbranch_vccnz Lfused_xA0\n"
"Lfused_rA0:\n"
"v_mfma_f32_16x16x32_bf16 v[36:39], a[196:199], v[164:167], v[36:39]\n"
"v_exp_f32 v133, v133\n"
"v_mfma_f32_16x16x32_bf16 v[40:43], a[200:203], v[164:167], v[40:43]\n"
"ds_read_b128 a[124:127], v250 offset:39936\n"
"v_cvt_pk_bf16_f32 v130, v132, v133\n"
"v_mfma_f32_16x16x32_bf16 v[44:47], a[204:207], v[164:167], v[44:47]\n"
"v_exp_f32 v134, v134\n"
"v_mfma_f32_16x16x32_bf16 v[48:51], a[208:211], v[164:167], v[48:51]\n"
"v_mfma_f32_16x16x32_bf16 v[52:55], a[212:215], v[164:167], v[52:55]\n"
"v_exp_f32 v135, v135\n"
"v_mfma_f32_16x16x32_bf16 v[56:59], a[216:219], v[164:167], v[56:59]\n"
"v_cvt_pk_bf16_f32 v131, v134, v135\n"
"v_mfma_f32_16x16x32_bf16 v[60:63], a[220:223], v[164:167], v[60:63]\n"
"Lfused_A1:\n"
"v_mfma_f32_16x16x32_bf16 v[144:147], a[128:131], a[32:35], v[200:203]\n"
"ds_read_b128 a[160:163], v250 offset:8192\n"
"v_mfma_f32_16x16x32_bf16 v[144:147], a[136:139], a[36:39], v[144:147]\n"
"v_exp_f32 v136, v136\n"
"v_mfma_f32_16x16x32_bf16 v[144:147], a[144:147], a[40:43], v[144:147]\n"
"ds_read_b128 a[164:167], v250 offset:9216\n"
"v_mfma_f32_16x16x32_bf16 v[144:147], a[152:155], a[44:47], v[144:147]\n"
"v_exp_f32 v137, v137\n"
"v_mfma_f32_16x16x32_bf16 v[208:211], a[232:235], v[128:131], v[212:215]\n"
"ds_read_b128 a[168:171], v250 offset:10240\n"
"v_cvt_pk_bf16_f32 v132, v136, v137\n"
"v_mfma_f32_16x16x32_bf16 v[148:151], a[132:135], a[32:35], v[200:203]\n"
"v_exp_f32 v138, v138\n"
"v_mfma_f32_16x16x32_bf16 v[148:151], a[140:143], a[36:39], v[148:151]\n"
"v_mfma_f32_16x16x32_bf16 v[148:151], a[148:151], a[40:43], v[148:151]\n"
"v_exp_f32 v139, v139\n"
"v_mfma_f32_16x16x32_bf16 v[148:151], a[156:159], a[44:47], v[148:151]\n"
"ds_read_b128 a[172:175], v250 offset:11264\n"
"v_cvt_pk_bf16_f32 v133, v138, v139\n"
"v_mfma_f32_16x16x32_bf16 v[152:155], a[128:131], a[48:51], v[204:207]\n"
"v_exp_f32 v140, v140\n"
"v_mfma_f32_16x16x32_bf16 v[152:155], a[136:139], a[52:55], v[152:155]\n"
"v_mfma_f32_16x16x32_bf16 v[152:155], a[144:147], a[56:59], v[152:155]\n"
"v_exp_f32 v141, v141\n"
"v_mfma_f32_16x16x32_bf16 v[152:155], a[152:155], a[60:63], v[152:155]\n"
"ds_read_b128 a[176:179], v250 offset:12288\n"
"v_cvt_pk_bf16_f32 v134, v140, v141\n"
"v_mfma_f32_16x16x32_bf16 v[156:159], a[132:135], a[48:51], v[204:207]\n"
"v_exp_f32 v142, v142\n"
"v_mfma_f32_16x16x32_bf16 v[156:159], a[140:143], a[52:55], v[156:159]\n"
"v_mfma_f32_16x16x32_bf16 v[156:159], a[148:151], a[56:59], v[156:159]\n"
"v_exp_f32 v143, v143\n"
"v_mfma_f32_16x16x32_bf16 v[156:159], a[156:159], a[60:63], v[156:159]\n"
"ds_read_b128 a[180:183], v250 offset:13312\n"
"v_cvt_pk_bf16_f32 v135, v142, v143\n"
"v_mfma_f32_16x16x32_bf16 v[64:67], a[192:195], v[176:179], v[64:67]\n"
"v_exp_f32 v144, v144\n"
"v_mfma_f32_16x16x32_bf16 v[208:211], a[236:239], v[132:135], v[208:211]\n"
"v_mfma_f32_16x16x32_bf16 v[68:71], a[196:199], v[176:179], v[68:71]\n"
"v_exp_f32 v145, v145\n"
"v_mfma_f32_16x16x32_bf16 v[72:75], a[200:203], v[176:179], v[72:75]\n"
"ds_read_b128 a[184:187], v250 offset:14336\n"
"v_cvt_pk_bf16_f32 v144, v144, v145\n"
"v_mfma_f32_16x16x32_bf16 v[76:79], a[204:207], v[176:179], v[76:79]\n"
"v_exp_f32 v146, v146\n"
"v_mfma_f32_16x16x32_bf16 v[80:83], a[208:211], v[176:179], v[80:83]\n"
"v_cmp_lt_f32 vcc, s43, v210\n"
"v_mfma_f32_16x16x32_bf16 v[84:87], a[212:215], v[176:179], v[84:87]\n"
"v_exp_f32 v147, v147\n"
"v_mfma_f32_16x16x32_bf16 v[88:91], a[216:219], v[176:179], v[88:91]\n"
"ds_read_b128 a[188:191], v250 offset:15360\n"
"v_cvt_pk_bf16_f32 v145, v146, v147\n"
"v_mfma_f32_16x16x32_bf16 v[92:95], a[220:223], v[176:179], v[92:95]\n"
"v_exp_f32 v148, v148\n"
"v_mfma_f32_16x16x32_bf16 v[96:99], a[192:195], v[180:183], v[96:99]\n"
"s_cbranch_vccnz Lfused_xA1\n"
"Lfused_rA1:\n"
"v_mfma_f32_16x16x32_bf16 v[100:103], a[196:199], v[180:183], v[100:103]\n"
"v_exp_f32 v149, v149\n"
"v_mfma_f32_16x16x32_bf16 v[104:107], a[200:203], v[180:183], v[104:107]\n"
"v_cvt_pk_bf16_f32 v146, v148, v149\n"
"s_add_u32 s28, s28, 0x4000\n"
"v_mfma_f32_16x16x32_bf16 v[108:111], a[204:207], v[180:183], v[108:111]\n"
"v_exp_f32 v150, v150\n"
"v_mfma_f32_16x16x32_bf16 v[112:115], a[208:211], v[180:183], v[112:115]\n"
"s_xor_b32 s30, s30, 0x4000\n"
"v_mfma_f32_16x16x32_bf16 v[116:119], a[212:215], v[180:183], v[116:119]\n"
"v_exp_f32 v151, v151\n"
"v_mfma_f32_16x16x32_bf16 v[120:123], a[216:219], v[180:183], v[120:123]\n"
"v_cvt_pk_bf16_f32 v147, v150, v151\n"
"v_mfma_f32_16x16x32_bf16 v[124:127], a[220:223], v[180:183], v[124:127]\n"
"Lfused_A2:\n"
"s_waitcnt vmcnt(8) lgkmcnt(0)\n"
"v_mfma_f32_16x16x32_bf16 v[160:163], a[160:163], a[0:3], v[192:195]\n"
"s_barrier\n"
"s_mov_b32 m0, s31\n"
"v_mfma_f32_16x16x32_bf16 v[160:163], a[168:171], a[4:7], v[160:163]\n"
"Lfused_hop9:\n"
"s_cbranch_execz Lfused_hop13\n"
"v_exp_f32 v152, v152\n"
"v_mfma_f32_16x16x32_bf16 v[160:163], a[176:179], a[8:11], v[160:163]\n"
"ds_read_b128 a[192:195], v250 offset:40960\n"
"v_mfma_f32_16x16x32_bf16 v[160:163], a[184:187], a[12:15], v[160:163]\n"
"v_exp_f32 v153, v153\n"
"v_mfma_f32_16x16x32_bf16 v[216:219], a[232:235], v[144:147], v[220:223]\n"
"ds_read_b128 a[196:199], v250 offset:41984\n"
"v_cvt_pk_bf16_f32 v148, v152, v153\n"
"v_mfma_f32_16x16x32_bf16 v[164:167], a[164:167], a[0:3], v[192:195]\n"
"v_exp_f32 v154, v154\n"
"v_mfma_f32_16x16x32_bf16 v[164:167], a[172:175], a[4:7], v[164:167]\n"
"buffer_load_dwordx4 v248, s[16:19], s28 offen offset:0 sc1 lds\n"
"v_mfma_f32_16x16x32_bf16 v[164:167], a[180:183], a[8:11], v[164:167]\n"
"v_exp_f32 v155, v155\n"
"v_mfma_f32_16x16x32_bf16 v[164:167], a[188:191], a[12:15], v[164:167]\n"
"ds_read_b128 a[200:203], v250 offset:43008\n"
"v_cvt_pk_bf16_f32 v149, v154, v155\n"
"v_mfma_f32_16x16x32_bf16 v[168:171], a[160:163], a[16:19], v[196:199]\n"
"v_exp_f32 v156, v156\n"
"v_mfma_f32_16x16x32_bf16 v[168:171], a[168:171], a[20:23], v[168:171]\n"
"buffer_load_dwordx4 v248, s[16:19], s28 offen offset:1024 sc1 lds\n"
"v_mfma_f32_16x16x32_bf16 v[168:171], a[176:179], a[24:27], v[168:171]\n"
"v_exp_f32 v157, v157\n"
"v_mfma_f32_16x16x32_bf16 v[168:171], a[184:187], a[28:31], v[168:171]\n"
"ds_read_b128 a[204:207], v250 offset:44032\n"
"v_cvt_pk_bf16_f32 v150, v156, v157\n"
"v_mfma_f32_16x16x32_bf16 v[172:175], a[164:167], a[16:19], v[196:199]\n"
"v_exp_f32 v158, v158\n"
"v_mfma_f32_16x16x32_bf16 v[172:175], a[172:175], a[20:23], v[172:175]\n"
"buffer_load_dwordx4 v248, s[16:19], s28 offen offset:2048 sc1 lds\n"
"v_mfma_f32_16x16x32_bf16 v[172:175], a[180:183], a[24:27], v[172:175]\n"
"v_exp_f32 v159, v159\n"
"v_mfma_f32_16x16x32_bf16 v[172:175], a[188:191], a[28:31], v[172:175]\n"
"ds_read_b128 a[208:211], v250 offset:45056\n"
"v_cvt_pk_bf16_f32 v151, v158, v159\n"
"v_mfma_f32_16x16x32_bf16 v[0:3], a[96:99], v[128:131], v[0:3]\n"
"v_exp_f32 v160, v160\n"
"v_mfma_f32_16x16x32_bf16 v[216:219], a[236:239], v[148:151], v[216:219]\n"
"buffer_load_dwordx4 v248, s[16:19], s28 offen offset:3072 sc1 lds\n"
"v_mfma_f32_16x16x32_bf16 v[4:7], a[100:103], v[128:131], v[4:7]\n"
"v_exp_f32 v161, v161\n"
"v_mfma_f32_16x16x32_bf16 v[8:11], a[104:107], v[128:131], v[8:11]\n"
"ds_read_b128 a[212:215], v250 offset:46080\n"
"v_cvt_pk_bf16_f32 v160, v160, v161\n"
"v_mfma_f32_16x16x32_bf16 v[12:15], a[108:111], v[128:131], v[12:15]\n"
"v_exp_f32 v162, v162\n"
"v_mfma_f32_16x16x32_bf16 v[16:19], a[112:115], v[128:131], v[16:19]\n"
"v_cmp_lt_f32 vcc, s43, v218\n"
"v_mfma_f32_16x16x32_bf16 v[20:23], a[116:119], v[128:131], v[20:23]\n"
"v_exp_f32 v163, v163\n"
"v_mfma_f32_16x16x32_bf16 v[24:27], a[120:123], v[128:131], v[24:27]\n"
"ds_read_b128 a[216:219], v250 offset:47104\n"
"v_cvt_pk_bf16_f32 v161, v162, v163\n"
"v_mfma_f32_16x16x32_bf16 v[28:31], a[124:127], v[128:131], v[28:31]\n"
"v_exp_f32 v164, v164\n"
"v_mfma_f32_16x16x32_bf16 v[32:35], a[96:99], v[132:135], v[32:35]\n"
"s_cbranch_vccnz Lfused_xA2\n"
"Lfused_rA2:\n"
"v_mfma_f32_16x16x32_bf16 v[36:39], a[100:103], v[132:135], v[36:39]\n"
"v_exp_f32 v165, v165\n"
"v_mfma_f32_16x16x32_bf16 v[40:43], a[104:107], v[132:135], v[40:43]\n"
"ds_read_b128 a[220:223], v250 offset:48128\n"
"v_cvt_pk_bf16_f32 v162, v164, v165\n"
"v_xor_b32 v250, 0x4000, v250\n"
"v_mfma_f32_16x16x32_bf16 v[44:47], a[108:111], v[132:135], v[44:47]\n"
"v_exp_f32 v166, v166\n"
"v_mfma_f32_16x16x32_bf16 v[48:51], a[112:115], v[132:135], v[48:51]\n"
"v_mfma_f32_16x16x32_bf16 v[52:55], a[116:119], v[132:135], v[52:55]\n"
"v_exp_f32 v167, v167\n"
"v_mfma_f32_16x16x32_bf16 v[56:59], a[120:123], v[132:135], v[56:59]\n"
"v_cvt_pk_bf16_f32 v163, v166, v167\n"
"v_mfma_f32_16x16x32_bf16 v[60:63], a[124:127], v[132:135], v[60:63]\n"
"Lfused_A3:\n"
"v_mfma_f32_16x16x32_bf16 v[176:179], a[160:163], a[32:35], v[200:203]\n"
"ds_read_b128 a[128:131], v250 offset:0\n"
"v_mfma_f32_16x16x32_bf16 v[176:179], a[168:171], a[36:39], v[176:179]\n"
"v_exp_f32 v168, v168\n"
"v_mfma_f32_16x16x32_bf16 v[176:179], a[176:179], a[40:43], v[176:179]\n"
"ds_read_b128 a[132:135], v250 offset:1024\n"
"v_mfma_f32_16x16x32_bf16 v[176:179], a[184:187], a[44:47], v[176:179]\n"
"v_exp_f32 v169, v169\n"
"v_mfma_f32_16x16x32_bf16 v[212:215], a[232:235], v[160:163], v[208:211]\n"
"ds_read_b128 a[136:139], v250 offset:2048\n"
"v_cvt_pk_bf16_f32 v164, v168, v169\n"
"v_mfma_f32_16x16x32_bf16 v[180:183], a[164:167], a[32:35], v[200:203]\n"
"v_exp_f32 v170, v170\n"
"v_mfma_f32_16x16x32_bf16 v[180:183], a[172:175], a[36:39], v[180:183]\n"
"v_mfma_f32_16x16x32_bf16 v[180:183], a[180:183], a[40:43], v[180:183]\n"
"v_exp_f32 v171, v171\n"
"v_mfma_f32_16x16x32_bf16 v[180:183], a[188:191], a[44:47], v[180:183]\n"
"ds_read_b128 a[140:143], v250 offset:3072\n"
"v_cvt_pk_bf16_f32 v165, v170, v171\n"
"v_mfma_f32_16x16x32_bf16 v[184:187], a[160:163], a[48:51], v[204:207]\n"
"v_exp_f32 v172, v172\n"
"v_mfma_f32_16x16x32_bf16 v[184:187], a[168:171], a[52:55], v[184:187]\n"
"v_mfma_f32_16x16x32_bf16 v[184:187], a[176:179], a[56:59], v[184:187]\n"
"v_exp_f32 v173, v173\n"
"v_mfma_f32_16x16x32_bf16 v[184:187], a[184:187], a[60:63], v[184:187]\n"
"ds_read_b128 a[144:147], v250 offset:4096\n"
"v_cvt_pk_bf16_f32 v166, v172, v173\n"
"v_mfma_f32_16x16x32_bf16 v[188:191], a[164:167], a[48:51], v[204:207]\n"
"v_exp_f32 v174, v174\n"
"v_mfma_f32_16x16x32_bf16 v[188:191], a[172:175], a[52:55], v[188:191]\n"
"v_mfma_f32_16x16x32_bf16 v[188:191], a[180:183], a[56:59], v[188:191]\n"
"v_exp_f32 v175, v175\n"
"v_mfma_f32_16x16x32_bf16 v[188:191], a[188:191], a[60:63], v[188:191]\n"
"ds_read_b128 a[148:151], v250 offset:5120\n"
"v_cvt_pk_bf16_f32 v167, v174, v175\n"
"v_mfma_f32_16x16x32_bf16 v[64:67], a[96:99], v[144:147], v[64:67]\n"
"v_exp_f32 v176, v176\n"
"v_mfma_f32_16x16x32_bf16 v[212:215], a[236:239], v[164:167], v[212:215]\n"
"v_mfma_f32_16x16x32_bf16 v[68:71], a[100:103], v[144:147], v[68:71]\n"
"v_exp_f32 v177, v177\n"
"v_mfma_f32_16x16x32_bf16 v[72:75], a[104:107], v[144:147], v[72:75]\n"
"ds_read_b128 a[152:155], v250 offset:6144\n"
"v_cvt_pk_bf16_f32 v176, v176, v177\n"
"v_mfma_f32_16x16x32_bf16 v[76:79], a[108:111], v[144:147], v[76:79]\n"
"v_exp_f32 v178, v178\n"
"v_mfma_f32_16x16x32_bf16 v[80:83], a[112:115], v[144:147], v[80:83]\n"
"v_cmp_lt_f32 vcc, s43, v214\n"
"v_mfma_f32_16x16x32_bf16 v[84:87], a[116:119], v[144:147], v[84:87]\n"
"v_exp_f32 v179, v179\n"
"v_mfma_f32_16x16x32_bf16 v[88:91], a[120:123], v[144:147], v[88:91]\n"
"ds_read_b128 a[156:159], v250 offset:7168\n"
"v_cvt_pk_bf16_f32 v177, v178, v179\n"
"v_mfma_f32_16x16x32_bf16 v[92:95], a[124:127], v[144:147], v[92:95]\n"
"v_exp_f32 v180, v180\n"
"v_mfma_f32_16x16x32_bf16 v[96:99], a[96:99], v[148:151], v[96:99]\n"
"s_cbranch_vccnz Lfused_xA3\n"
"Lfused_rA3:\n"
"v_mfma_f32_16x16x32_bf16 v[100:103], a[100:103], v[148:151], v[100:103]\n"
"v_exp_f32 v181, v181\n"
"v_mfma_f32_16x16x32_bf16 v[104:107], a[104:107], v[148:151], v[104:107]\n"
"v_cvt_pk_bf16_f32 v178, v180, v181\n"
"s_add_u32 s28, s28, 0x4000\n"
"v_mfma_f32_16x16x32_bf16 v[108:111], a[108:111], v[148:151], v[108:111]\n"
"v_exp_f32 v182, v182\n"
"v_mfma_f32_16x16x32_bf16 v[112:115], a[112:115], v[148:151], v[112:115]\n"
"s_xor_b32 s31, s31, 0x4000\n"
"v_mfma_f32_16x16x32_bf16 v[116:119], a[116:119], v[148:151], v[116:119]\n"
"v_exp_f32 v183, v183\n"
"v_mfma_f32_16x16x32_bf16 v[120:123], a[120:123], v[148:151], v[120:123]\n"
"v_cvt_pk_bf16_f32 v179, v182, v183\n"
"s_sub_u32 s29, s29, 1\n"
"v_mfma_f32_16x16x32_bf16 v[124:127], a[124:127], v[148:151], v[124:127]\n"
"s_cbranch_scc0 Lfused_A0\n"
"global_load_dwordx2 v[242:243], v251, s[62:63] sc1\n"
"s_lshl_b32 s14, s11, 2\n"
"s_mov_b32 s29, 1\n"
"Lfused_B0:\n"
"s_waitcnt vmcnt(8) lgkmcnt(0)\n"
"v_mfma_f32_16x16x32_bf16 v[128:131], a[128:131], a[0:3], v[192:195]\n"
"s_barrier\n"
"s_mov_b32 m0, s30\n"
"v_mfma_f32_16x16x32_bf16 v[128:131], a[136:139], a[4:7], v[128:131]\n"
"v_exp_f32 v184, v184\n"
"v_mfma_f32_16x16x32_bf16 v[128:131], a[144:147], a[8:11], v[128:131]\n"
"ds_read_b128 a[96:99], v250 offset:32768\n"
"v_mfma_f32_16x16x32_bf16 v[128:131], a[152:155], a[12:15], v[128:131]\n"
"v_exp_f32 v185, v185\n"
"v_mfma_f32_16x16x32_bf16 v[220:223], a[232:235], v[176:179], v[216:219]\n"
"ds_read_b128 a[100:103], v250 offset:33792\n"
"v_cvt_pk_bf16_f32 v180, v184, v185\n"
"v_mfma_f32_16x16x32_bf16 v[132:135], a[132:135], a[0:3], v[192:195]\n"
"v_exp_f32 v186, v186\n"
"v_mfma_f32_16x16x32_bf16 v[132:135], a[140:143], a[4:7], v[132:135]\n"
"buffer_load_dwordx4 v248, s[16:19], s28 offen offset:0 sc1 lds\n"
"v_mfma_f32_16x16x32_bf16 v[132:135], a[148:151], a[8:11], v[132:135]\n"
"v_exp_f32 v187, v187\n"
"v_mfma_f32_16x16x32_bf16 v[132:135], a[156:159], a[12:15], v[132:135]\n"
"ds_read_b128 a[104:107], v250 offset:34816\n"
"v_cvt_pk_bf16_f32 v181, v186, v187\n"
"v_mfma_f32_16x16x32_bf16 v[136:139], a[128:131], a[16:19], v[196:199]\n"
"v_exp_f32 v188, v188\n"
"v_mfma_f32_16x16x32_bf16 v[136:139], a[136:139], a[20:23], v[136:139]\n"
"buffer_load_dwordx4 v248, s[16:19], s28 offen offset:1024 sc1 lds\n"
"v_mfma_f32_16x16x32_bf16 v[136:139], a[144:147], a[24:27], v[136:139]\n"
"v_exp_f32 v189, v189\n"
"v_mfma_f32_16x16x32_bf16 v[136:139], a[152:155], a[28:31], v[136:139]\n"
"ds_read_b128 a[108:111], v250 offset:35840\n"
"v_cvt_pk_bf16_f32 v182, v188, v189\n"
"v_mfma_f32_16x16x32_bf16 v[140:143], a[132:135], a[16:19], v[196:199]\n"
"v_exp_f32 v190, v190\n"
"v_mfma_f32_16x16x32_bf16 v[140:143], a[140:143], a[20:23], v[140:143]\n"
"buffer_load_dwordx4 v248, s[16:19], s28 offen offset:2048 sc1 lds\n"
"v_mfma_f32_16x16x32_bf16 v[140:143], a[148:151], a[24:27], v[140:143]\n"
"v_exp_f32 v191, v191\n"
"v_mfma_f32_16x16x32_bf16 v[140:143], a[156:159], a[28:31], v[140:143]\n"
"ds_read_b128 a[112:115], v250 offset:36864\n"
"v_cvt_pk_bf16_f32 v183, v190, v191\n"
"v_mfma_f32_16x16x32_bf16 v[0:3], a[192:195], v[160:163], v[0:3]\n"
"v_exp_f32 v128, v128\n"
"v_mfma_f32_16x16x32_bf16 v[220:223], a[236:239], v[180:183], v[220:223]\n"
"buffer_load_dwordx4 v248, s[16:19], s28 offen offset:3072 sc1 lds\n"
"v_mfma_f32_16x16x32_bf16 v[4:7], a[196:199], v[160:163], v[4:7]\n"
"v_exp_f32 v129, v129\n"
"v_mfma_f32_16x16x32_bf16 v[8:11], a[200:203], v[160:163], v[8:11]\n"
"ds_read_b128 a[116:119], v250 offset:37888\n"
"v_cvt_pk_bf16_f32 v128, v128, v129\n"
"v_mfma_f32_16x16x32_bf16 v[12:15], a[204:207], v[160:163], v[12:15]\n"
"v_exp_f32 v130, v130\n"
"v_mfma_f32_16x16x32_bf16 v[16:19], a[208:211], v[160:163], v[16:19]\n"
"v_cmp_lt_f32 vcc, s43, v222\n"
"v_mfma_f32_16x16x32_bf16 v[20:23], a[212:215], v[160:163], v[20:23]\n"
"v_exp_f32 v131, v131\n"
"v_mfma_f32_16x16x32_bf16 v[24:27], a[216:219], v[160:163], v[24:27]\n"
"ds_read_b128 a[120:123], v250 offset:38912\n"
"v_cvt_pk_bf16_f32 v129, v130, v131\n"
"s_cmp_ge_u32 s14, s39\n"
"s_cselect_b32 s34, 0xff800000, 0\n"
"v_mfma_f32_16x16x32_bf16 v[28:31], a[220:223], v[160:163], v[28:31]\n"
"v_exp_f32 v132, v132\n"
"v_mfma_f32_16x16x32_bf16 v[32:35], a[192:195], v[164:167], v[32:35]\n"
"s_cbranch_vccnz Lfused_xB0\n"
"Lfused_rB0:\n"
"s_cmp_gt_u32 s14, s39\n"
"s_cselect_b32 s35, 0xff800000, 0\n"
"v_mfma_f32_16x16x32_bf16 v[36:39], a[196:199], v[164:167], v[36:39]\n"
"v_exp_f32 v133, v133\n"
"v_mfma_f32_16x16x32_bf16 v[40:43], a[200:203], v[164:167], v[40:43]\n"
"ds_read_b128 a[124:127], v250 offset:39936\n"
"v_cvt_pk_bf16_f32 v130, v132, v133\n"
"v_add_f32 v224, s35, v200\n"
"v_mfma_f32_16x16x32_bf16 v[44:47], a[204:207], v[164:167], v[44:47]\n"
"v_exp_f32 v134, v134\n"
"v_mfma_f32_16x16x32_bf16 v[48:51], a[208:211], v[164:167], v[48:51]\n"
"v_add_f32 v225, s34, v204\n"
"v_add_f32 v148, s34, v200\n"
"v_add_f32 v149, s34, v200\n"
"v_add_f32 v150, s34, v200\n"
"v_add_f32 v151, s34, v200\n"
"v_cndmask_b32 v144, v224, v148, s[64:65]\n"
"v_cndmask_b32 v145, v224, v148, s[66:67]\n"
"v_cndmask_b32 v146, v224, v148, s[68:69]\n"
"v_mfma_f32_16x16x32_bf16 v[52:55], a[212:215], v[164:167], v[52:55]\n"
"v_exp_f32 v135, v135\n"
"v_mfma_f32_16x16x32_bf16 v[56:59], a[216:219], v[164:167], v[56:59]\n"
"v_cvt_pk_bf16_f32 v131, v134, v135\n"
"v_cndmask_b32 v147, v224, v148, s[70:71]\n"
"v_add_f32 v152, s35, v204\n"
"v_add_f32 v153, s35, v204\n"
"v_add_f32 v154, s35, v204\n"
"v_add_f32 v155, s35, v204\n"
"v_cndmask_b32 v156, v152, v225, s[64:65]\n"
"v_cndmask_b32 v157, v152, v225, s[66:67]\n"
"v_cndmask_b32 v158, v152, v225, s[68:69]\n"
"v_cndmask_b32 v159, v152, v225, s[70:71]\n"
"v_mfma_f32_16x16x32_bf16 v[60:63], a[220:223], v[164:167], v[60:63]\n"
"Lfused_B1:\n"
"v_mfma_f32_16x16x32_bf16 v[144:147], a[128:131], a[32:35], v[144:147]\n"
"ds_read_b128 a[160:163], v250 offset:8192\n"
"s_add_u32 m0, m0, 0x10000\n"
"v_mfma_f32_16x16x32_bf16 v[144:147], a[136:139], a[36:39], v[144:147]\n"
"v_exp_f32 v136, v136\n"
"v_mfma_f32_16x16x32_bf16 v[144:147], a[144:147], a[40:43], v[144:147]\n"
"ds_read_b128 a[164:167], v250 offset:9216\n"
"v_mfma_f32_16x16x32_bf16 v[144:147], a[152:155], a[44:47], v[144:147]\n"
"v_exp_f32 v137, v137\n"
"v_mfma_f32_16x16x32_bf16 v[208:211], a[232:235], v[128:131], v[212:215]\n"
"ds_read_b128 a[168:171], v250 offset:10240\n"
"v_cvt_pk_bf16_f32 v132, v136, v137\n"
"v_mfma_f32_16x16x32_bf16 v[148:151], a[132:135], a[32:35], v[148:151]\n"
"v_exp_f32 v138, v138\n"
"v_mfma_f32_16x16x32_bf16 v[148:151], a[140:143], a[36:39], v[148:151]\n"
"buffer_load_dwordx4 v248, s[20:23], s28 offen offset:0 sc1 lds\n"
"v_mfma_f32_16x16x32_bf16 v[148:151], a[148:151], a[40:43], v[148:151]\n"
"v_exp_f32 v139, v139\n"
"v_mfma_f32_16x16x32_bf16 v[148:151], a[156:159], a[44:47], v[148:151]\n"
"ds_read_b128 a[172:175], v250 offset:11264\n"
"v_cvt_pk_bf16_f32 v133, v138, v139\n"
"v_mfma_f32_16x16x32_bf16 v[152:155], a[128:131], a[48:51], v[152:155]\n"
"v_exp_f32 v140, v140\n"
"v_mfma_f32_16x16x32_bf16 v[152:155], a[136:139], a[52:55], v[152:155]\n"
"buffer_load_dwordx4 v248, s[20:23], s28 offen offset:1024 sc1 lds\n"
"v_mfma_f32_16x16x32_bf16 v[152:155], a[144:147], a[56:59], v[152:155]\n"
"v_exp_f32 v141, v141\n"
"v_mfma_f32_16x16x32_bf16 v[152:155], a[152:155], a[60:63], v[152:155]\n"
"ds_read_b128 a[176:179], v250 offset:12288\n"
"v_cvt_pk_bf16_f32 v134, v140, v141\n"
"v_mfma_f32_16x16x32_bf16 v[156:159], a[132:135], a[48:51], v[156:159]\n"
"v_exp_f32 v142, v142\n"
"v_mfma_f32_16x16x32_bf16 v[156:159], a[140:143], a[52:55], v[156:159]\n"
"buffer_load_dwordx4 v248, s[20:23], s28 offen offset:2048 sc1 lds\n"
"v_mfma_f32_16x16x32_bf16 v[156:159], a[148:151], a[56:59], v[156:159]\n"
"v_exp_f32 v143, v143\n"
"v_mfma_f32_16x16x32_bf16 v[156:159], a[156:159], a[60:63], v[156:159]\n"
"ds_read_b128 a[180:183], v250 offset:13312\n"
"v_cvt_pk_bf16_f32 v135, v142, v143\n"
"v_mfma_f32_16x16x32_bf16 v[64:67], a[192:195], v[176:179], v[64:67]\n"
"v_exp_f32 v144, v144\n"
"v_mfma_f32_16x16x32_bf16 v[208:211], a[236:239], v[132:135], v[208:211]\n"
"buffer_load_dwordx4 v248, s[20:23], s28 offen offset:3072 sc1 lds\n"
"v_mfma_f32_16x16x32_bf16 v[68:71], a[196:199], v[176:179], v[68:71]\n"
"v_exp_f32 v145, v145\n"
"v_mfma_f32_16x16x32_bf16 v[72:75], a[200:203], v[176:179], v[72:75]\n"
"ds_read_b128 a[184:187], v250 offset:14336\n"
"v_cvt_pk_bf16_f32 v144, v144, v145\n"
"v_mfma_f32_16x16x32_bf16 v[76:79], a[204:207], v[176:179], v[76:79]\n"
"v_exp_f32 v146, v146\n"
"v_mfma_f32_16x16x32_bf16 v[80:83], a[208:211], v[176:179], v[80:83]\n"
"v_cmp_lt_f32 vcc, s43, v210\n"
"v_mfma_f32_16x16x32_bf16 v[84:87], a[212:215], v[176:179], v[84:87]\n"
"v_exp_f32 v147, v147\n"
"v_mfma_f32_16x16x32_bf16 v[88:91], a[216:219], v[176:179], v[88:91]\n"
"ds_read_b128 a[188:191], v250 offset:15360\n"
"v_cvt_pk_bf16_f32 v145, v146, v147\n"
"s_add_u32 s14, s14, 1\n"
"v_mfma_f32_16x16x32_bf16 v[92:95], a[220:223], v[176:179], v[92:95]\n"
"v_exp_f32 v148, v148\n"
"v_mfma_f32_16x16x32_bf16 v[96:99], a[192:195], v[180:183], v[96:99]\n"
"s_cbranch_vccnz Lfused_xB1\n"
"Lfused_rB1:\n"
"v_mfma_f32_16x16x32_bf16 v[100:103], a[196:199], v[180:183], v[100:103]\n"
"v_exp_f32 v149, v149\n"
"v_mfma_f32_16x16x32_bf16 v[104:107], a[200:203], v[180:183], v[104:107]\n"
"v_cvt_pk_bf16_f32 v146, v148, v149\n"
"s_add_u32 s28, s28, 0x4000\n"
"v_mfma_f32_16x16x32_bf16 v[108:111], a[204:207], v[180:183], v[108:111]\n"
"v_exp_f32 v150, v150\n"
"v_mfma_f32_16x16x32_bf16 v[112:115], a[208:211], v[180:183], v[112:115]\n"
"s_xor_b32 s30, s30, 0x4000\n"
"v_mfma_f32_16x16x32_bf16 v[116:119], a[212:215], v[180:183], v[116:119]\n"
"v_exp_f32 v151, v151\n"
"v_mfma_f32_16x16x32_bf16 v[120:123], a[216:219], v[180:183], v[120:123]\n"
"v_cvt_pk_bf16_f32 v147, v150, v151\n"
"v_mfma_f32_16x16x32_bf16 v[124:127], a[220:223], v[180:183], v[124:127]\n"
"Lfused_B2:\n"
"s_waitcnt vmcnt(12) lgkmcnt(0)\n"
"v_mfma_f32_16x16x32_bf16 v[160:163], a[160:163], a[0:3], v[192:195]\n"
"s_barrier\n"
"s_mov_b32 m0, s31\n"
"v_mfma_f32_16x16x32_bf16 v[160:163], a[168:171], a[4:7], v[160:163]\n"
"v_exp_f32 v152, v152\n"
"v_mfma_f32_16x16x32_bf16 v[160:163], a[176:179], a[8:11], v[160:163]\n"
"ds_read_b128 a[192:195], v250 offset:40960\n"
"v_mfma_f32_16x16x32_bf16 v[160:163], a[184:187], a[12:15], v[160:163]\n"
"v_exp_f32 v153, v153\n"
"v_mfma_f32_16x16x32_bf16 v[216:219], a[232:235], v[144:147], v[220:223]\n"
"ds_read_b128 a[196:199], v250 offset:41984\n"
"v_cvt_pk_bf16_f32 v148, v152, v153\n"
"v_mfma_f32_16x16x32_bf16 v[164:167], a[164:167], a[0:3], v[192:195]\n"
"v_exp_f32 v154, v154\n"
"v_mfma_f32_16x16x32_bf16 v[164:167], a[172:175], a[4:7], v[164:167]\n"
"buffer_load_dwordx4 v248, s[16:19], s28 offen offset:0 sc1 lds\n"
"v_mfma_f32_16x16x32_bf16 v[164:167], a[180:183], a[8:11], v[164:167]\n"
"v_exp_f32 v155, v155\n"
"v_mfma_f32_16x16x32_bf16 v[164:167], a[188:191], a[12:15], v[164:167]\n"
"ds_read_b128 a[200:203], v250 offset:43008\n"
"v_cvt_pk_bf16_f32 v149, v154, v155\n"
"v_mfma_f32_16x16x32_bf16 v[168:171], a[160:163], a[16:19], v[196:199]\n"
"v_exp_f32 v156, v156\n"
"v_mfma_f32_16x16x32_bf16 v[168:171], a[168:171], a[20:23], v[168:171]\n"
"buffer_load_dwordx4 v248, s[16:19], s28 offen offset:1024 sc1 lds\n"
"v_mfma_f32_16x16x32_bf16 v[168:171], a[176:179], a[24:27], v[168:171]\n"
"v_exp_f32 v157, v157\n"
"v_mfma_f32_16x16x32_bf16 v[168:171], a[184:187], a[28:31], v[168:171]\n"
"ds_read_b128 a[204:207], v250 offset:44032\n"
"v_cvt_pk_bf16_f32 v150, v156, v157\n"
"v_mfma_f32_16x16x32_bf16 v[172:175], a[164:167], a[16:19], v[196:199]\n"
"v_exp_f32 v158, v158\n"
"v_mfma_f32_16x16x32_bf16 v[172:175], a[172:175], a[20:23], v[172:175]\n"
"buffer_load_dwordx4 v248, s[16:19], s28 offen offset:2048 sc1 lds\n"
"v_mfma_f32_16x16x32_bf16 v[172:175], a[180:183], a[24:27], v[172:175]\n"
"v_exp_f32 v159, v159\n"
"v_mfma_f32_16x16x32_bf16 v[172:175], a[188:191], a[28:31], v[172:175]\n"
"ds_read_b128 a[208:211], v250 offset:45056\n"
"v_cvt_pk_bf16_f32 v151, v158, v159\n"
"v_mfma_f32_16x16x32_bf16 v[0:3], a[96:99], v[128:131], v[0:3]\n"
"v_exp_f32 v160, v160\n"
"v_mfma_f32_16x16x32_bf16 v[216:219], a[236:239], v[148:151], v[216:219]\n"
"buffer_load_dwordx4 v248, s[16:19], s28 offen offset:3072 sc1 lds\n"
"v_mfma_f32_16x16x32_bf16 v[4:7], a[100:103], v[128:131], v[4:7]\n"
"v_exp_f32 v161, v161\n"
"v_mfma_f32_16x16x32_bf16 v[8:11], a[104:107], v[128:131], v[8:11]\n"
"ds_read_b128 a[212:215], v250 offset:46080\n"
"v_cvt_pk_bf16_f32 v160, v160, v161\n"
"v_mfma_f32_16x16x32_bf16 v[12:15], a[108:111], v[128:131], v[12:15]\n"
"v_exp_f32 v162, v162\n"
"v_mfma_f32_16x16x32_bf16 v[16:19], a[112:115], v[128:131], v[16:19]\n"
"v_cmp_lt_f32 vcc, s43, v218\n"
"v_mfma_f32_16x16x32_bf16 v[20:23], a[116:119], v[128:131], v[20:23]\n"
"v_exp_f32 v163, v163\n"
"v_mfma_f32_16x16x32_bf16 v[24:27], a[120:123], v[128:131], v[24:27]\n"
"ds_read_b128 a[216:219], v250 offset:47104\n"
"v_cvt_pk_bf16_f32 v161, v162, v163\n"
"s_cmp_eq_u32 s29, 0\n"
"s_cselect_b32 s42, s41, 0x4000\n"
"s_cmp_ge_u32 s14, s39\n"
"s_cselect_b32 s34, 0xff800000, 0\n"
"v_mfma_f32_16x16x32_bf16 v[28:31], a[124:127], v[128:131], v[28:31]\n"
"v_exp_f32 v164, v164\n"
"v_mfma_f32_16x16x32_bf16 v[32:35], a[96:99], v[132:135], v[32:35]\n"
"s_cbranch_vccnz Lfused_xB2\n"
"Lfused_rB2:\n"
"s_cmp_gt_u32 s14, s39\n"
"s_cselect_b32 s35, 0xff800000, 0\n"
"v_mfma_f32_16x16x32_bf16 v[36:39], a[100:103], v[132:135], v[36:39]\n"
"v_exp_f32 v165, v165\n"
"v_mfma_f32_16x16x32_bf16 v[40:43], a[104:107], v[132:135], v[40:43]\n"
"ds_read_b128 a[220:223], v250 offset:48128\n"
"v_cvt_pk_bf16_f32 v162, v164, v165\n"
"v_xor_b32 v250, s42, v250\n"
"v_add_f32 v224, s35, v200\n"
"v_mfma_f32_16x16x32_bf16 v[44:47], a[108:111], v[132:135], v[44:47]\n"
"v_exp_f32 v166, v166\n"
"v_mfma_f32_16x16x32_bf16 v[48:51], a[112:115], v[132:135], v[48:51]\n"
"v_add_f32 v225, s34, v204\n"
"v_add_f32 v180, s34, v200\n"
"v_add_f32 v181, s34, v200\n"
"v_add_f32 v182, s34, v200\n"
"v_add_f32 v183, s34, v200\n"
"v_cndmask_b32 v176, v224, v180, s[64:65]\n"
"v_cndmask_b32 v177, v224, v180, s[66:67]\n"
"v_cndmask_b32 v178, v224, v180, s[68:69]\n"
"v_mfma_f32_16x16x32_bf16 v[52:55], a[116:119], v[132:135], v[52:55]\n"
"v_exp_f32 v167, v167\n"
"v_mfma_f32_16x16x32_bf16 v[56:59], a[120:123], v[132:135], v[56:59]\n"
"v_cvt_pk_bf16_f32 v163, v166, v167\n"
"v_cndmask_b32 v179, v224, v180, s[70:71]\n"
"v_add_f32 v184, s35, v204\n"
"v_add_f32 v185, s35, v204\n"
"v_add_f32 v186, s35, v204\n"
"v_add_f32 v187, s35, v204\n"
"v_cndmask_b32 v188, v184, v225, s[64:65]\n"
"v_cndmask_b32 v189, v184, v225, s[66:67]\n"
"v_cndmask_b32 v190, v184, v225, s[68:69]\n"
"v_cndmask_b32 v191, v184, v225, s[70:71]\n"
"v_mfma_f32_16x16x32_bf16 v[60:63], a[124:127], v[132:135], v[60:63]\n"
"Lfused_B3:\n"
"v_mfma_f32_16x16x32_bf16 v[176:179], a[160:163], a[32:35], v[176:179]\n"
"ds_read_b128 a[128:131], v250 offset:0\n"
"s_add_u32 m0, m0, 0x10000\n"
"v_mfma_f32_16x16x32_bf16 v[176:179], a[168:171], a[36:39], v[176:179]\n"
"v_exp_f32 v168, v168\n"
"v_mfma_f32_16x16x32_bf16 v[176:179], a[176:179], a[40:43], v[176:179]\n"
"ds_read_b128 a[132:135], v250 offset:1024\n"
"v_mfma_f32_16x16x32_bf16 v[176:179], a[184:187], a[44:47], v[176:179]\n"
"v_exp_f32 v169, v169\n"
"v_mfma_f32_16x16x32_bf16 v[212:215], a[232:235], v[160:163], v[208:211]\n"
"ds_read_b128 a[136:139], v250 offset:2048\n"
"v_cvt_pk_bf16_f32 v164, v168, v169\n"
"v_mfma_f32_16x16x32_bf16 v[180:183], a[164:167], a[32:35], v[180:183]\n"
"v_exp_f32 v170, v170\n"
"v_mfma_f32_16x16x32_bf16 v[180:183], a[172:175], a[36:39], v[180:183]\n"
"buffer_load_dwordx4 v248, s[20:23], s28 offen offset:0 sc1 lds\n"
"v_mfma_f32_16x16x32_bf16 v[180:183], a[180:183], a[40:43], v[180:183]\n"
"v_exp_f32 v171, v171\n"
"v_mfma_f32_16x16x32_bf16 v[180:183], a[188:191], a[44:47], v[180:183]\n"
"ds_read_b128 a[140:143], v250 offset:3072\n"
"v_cvt_pk_bf16_f32 v165, v170, v171\n"
"v_mfma_f32_16x16x32_bf16 v[184:187], a[160:163], a[48:51], v[184:187]\n"
"v_exp_f32 v172, v172\n"
"v_mfma_f32_16x16x32_bf16 v[184:187], a[168:171], a[52:55], v[184:187]\n"
"buffer_load_dwordx4 v248, s[20:23], s28 offen offset:1024 sc1 lds\n"
"v_mfma_f32_16x16x32_bf16 v[184:187], a[176:179], a[56:59], v[184:187]\n"
"v_exp_f32 v173, v173\n"
"v_mfma_f32_16x16x32_bf16 v[184:187], a[184:187], a[60:63], v[184:187]\n"
"ds_read_b128 a[144:147], v250 offset:4096\n"
"v_cvt_pk_bf16_f32 v166, v172, v173\n"
"v_mfma_f32_16x16x32_bf16 v[188:191], a[164:167], a[48:51], v[188:191]\n"
"v_exp_f32 v174, v174\n"
"v_mfma_f32_16x16x32_bf16 v[188:191], a[172:175], a[52:55], v[188:191]\n"
"buffer_load_dwordx4 v248, s[20:23], s28 offen offset:2048 sc1 lds\n"
"v_mfma_f32_16x16x32_bf16 v[188:191], a[180:183], a[56:59], v[188:191]\n"
"v_exp_f32 v175, v175\n"
"v_mfma_f32_16x16x32_bf16 v[188:191], a[188:191], a[60:63], v[188:191]\n"
"ds_read_b128 a[148:151], v250 offset:5120\n"
"v_cvt_pk_bf16_f32 v167, v174, v175\n"
"v_mfma_f32_16x16x32_bf16 v[64:67], a[96:99], v[144:147], v[64:67]\n"
"v_exp_f32 v176, v176\n"
"v_mfma_f32_16x16x32_bf16 v[212:215], a[236:239], v[164:167], v[212:215]\n"
"buffer_load_dwordx4 v248, s[20:23], s28 offen offset:3072 sc1 lds\n"
"v_mfma_f32_16x16x32_bf16 v[68:71], a[100:103], v[144:147], v[68:71]\n"
"v_exp_f32 v177, v177\n"
"v_mfma_f32_16x16x32_bf16 v[72:75], a[104:107], v[144:147], v[72:75]\n"
"ds_read_b128 a[152:155], v250 offset:6144\n"
"v_cvt_pk_bf16_f32 v176, v176, v177\n"
"v_mfma_f32_16x16x32_bf16 v[76:79], a[108:111], v[144:147], v[76:79]\n"
"v_exp_f32 v178, v178\n"
"v_mfma_f32_16x16x32_bf16 v[80:83], a[112:115], v[144:147], v[80:83]\n"
"v_cmp_lt_f32 vcc, s43, v214\n"
"v_mfma_f32_16x16x32_bf16 v[84:87], a[116:119], v[144:147], v[84:87]\n"
"v_exp_f32 v179, v179\n"
"v_mfma_f32_16x16x32_bf16 v[88:91], a[120:123], v[144:147], v[88:91]\n"
"ds_read_b128 a[156:159], v250 offset:7168\n"
"v_cvt_pk_bf16_f32 v177, v178, v179\n"
"s_add_u32 s14, s14, 1\n"
"v_mfma_f32_16x16x32_bf16 v[92:95], a[124:127], v[144:147], v[92:95]\n"
"v_exp_f32 v180, v180\n"
"v_mfma_f32_16x16x32_bf16 v[96:99], a[96:99], v[148:151], v[96:99]\n"
"s_cbranch_vccnz Lfused_xB3\n"
"Lfused_rB3:\n"
"v_mfma_f32_16x16x32_bf16 v[100:103], a[100:103], v[148:151], v[100:103]\n"
"v_exp_f32 v181, v181\n"
"v_mfma_f32_16x16x32_bf16 v[104:107], a[104:107], v[148:151], v[104:107]\n"
"v_cvt_pk_bf16_f32 v178, v180, v181\n"
"s_add_u32 s28, s28, 0x4000\n"
"v_mfma_f32_16x16x32_bf16 v[108:111], a[108:111], v[148:151], v[108:111]\n"
"v_exp_f32 v182, v182\n"
"v_mfma_f32_16x16x32_bf16 v[112:115], a[112:115], v[148:151], v[112:115]\n"
"s_xor_b32 s31, s31, 0x4000\n"
"v_mfma_f32_16x16x32_bf16 v[116:119], a[116:119], v[148:151], v[116:119]\n"
"v_exp_f32 v183, v183\n"
"v_mfma_f32_16x16x32_bf16 v[120:123], a[120:123], v[148:151], v[120:123]\n"
"v_cvt_pk_bf16_f32 v179, v182, v183\n"
"s_sub_u32 s29, s29, 1\n"
"v_mfma_f32_16x16x32_bf16 v[124:127], a[124:127], v[148:151], v[124:127]\n"
"s_cbranch_scc0 Lfused_B0\n"
"v_lshrrev_b32 v226, 1, v248\n"
"s_xor_b32 s50, s12, 1\n"
"s_lshl_b32 s50, s50, 9\n"
"s_add_u32 s50, s50, 131072\n"
"v_add_u32 v226, s50, v226\n"
"ds_read_b64 v[244:245], v226\n"
"s_cmp_eq_u32 s11, 0\n"
"s_cbranch_scc0 Lfused_a1c\n"
"global_load_dwordx2 v[242:243], v251, s[62:63] sc1\n"
"s_branch Lfused_a1e\n"
"Lfused_a1c:\n"
"s_waitcnt vmcnt(16)\n"
"v_cmp_ne_u64 vcc, s[98:99], v[242:243]\n"
"s_cbranch_vccnz Lfused_ps1\n"
"Lfused_pd1:\n"
"s_lshl_b32 s50, s10, 14\n"
"s_add_u32 s50, s50, s90\n"
"s_add_u32 s50, s50, 54525952\n"
"s_add_u32 s52, s8, s50\n"
"s_addc_u32 s53, s9, 0\n"
"v_lshlrev_b32 v224, 1, v248\n"
"global_load_dwordx4 a[240:243], v224, s[52:53] offset:0 sc1\n"
"global_load_dwordx4 a[244:247], v224, s[52:53] offset:16 sc1\n"
"global_load_dwordx4 a[248:251], v224, s[52:53] offset:2048 sc1\n"
"global_load_dwordx4 a[252:255], v224, s[52:53] offset:2064 sc1\n"
"Lfused_a1e:\n"
"s_cmp_eq_u32 s11, 7\n"
"s_cbranch_scc1 Lfused_last7\n"
"Lfused_C0:\n"
"s_waitcnt vmcnt(16) lgkmcnt(0)\n"
"v_mfma_f32_16x16x32_bf16 v[128:131], a[128:131], a[0:3], v[192:195]\n"
"s_barrier\n"
"s_mov_b32 m0, s30\n"
"v_mfma_f32_16x16x32_bf16 v[128:131], a[136:139], a[4:7], v[128:131]\n"
"v_exp_f32 v184, v184\n"
"v_mfma_f32_16x16x32_bf16 v[128:131], a[144:147], a[8:11], v[128:131]\n"
"ds_read_b128 a[96:99], v250 offset:32768\n"
"v_mfma_f32_16x16x32_bf16 v[128:131], a[152:155], a[12:15], v[128:131]\n"
"v_exp_f32 v185, v185\n"
"v_mfma_f32_16x16x32_bf16 v[220:223], a[232:235], v[176:179], v[216:219]\n"
"ds_read_b128 a[100:103], v250 offset:33792\n"
"v_cvt_pk_bf16_f32 v180, v184, v185\n"
"v_mfma_f32_16x16x32_bf16 v[132:135], a[132:135], a[0:3], v[192:195]\n"
"v_exp_f32 v186, v186\n"
"v_mfma_f32_16x16x32_bf16 v[132:135], a[140:143], a[4:7], v[132:135]\n"
"buffer_load_dwordx4 v248, s[16:19], s28 offen offset:0 sc1 lds\n"
"v_mfma_f32_16x16x32_bf16 v[132:135], a[148:151], a[8:11], v[132:135]\n"
"v_exp_f32 v187, v187\n"
"v_mfma_f32_16x16x32_bf16 v[132:135], a[156:159], a[12:15], v[132:135]\n"
"ds_read_b128 a[104:107], v250 offset:34816\n"
"v_cvt_pk_bf16_f32 v181, v186, v187\n"
"v_mfma_f32_16x16x32_bf16 v[136:139], a[128:131], a[16:19], v[196:199]\n"
"v_exp_f32 v188, v188\n"
"v_mfma_f32_16x16x32_bf16 v[136:139], a[136:139], a[20:23], v[136:139]\n"
"buffer_load_dwordx4 v248, s[16:19], s28 offen offset:1024 sc1 lds\n"
"v_mfma_f32_16x16x32_bf16 v[136:139], a[144:147], a[24:27], v[136:139]\n"
"v_exp_f32 v189, v189\n"
"v_mfma_f32_16x16x32_bf16 v[136:139], a[152:155], a[28:31], v[136:139]\n"
"ds_read_b128 a[108:111], v250 offset:35840\n"
"v_cvt_pk_bf16_f32 v182, v188, v189\n"
"v_mfma_f32_16x16x32_bf16 v[140:143], a[132:135], a[16:19], v[196:199]\n"
"v_exp_f32 v190, v190\n"
"v_mfma_f32_16x16x32_bf16 v[140:143], a[140:143], a[20:23], v[140:143]\n"
"buffer_load_dwordx4 v248, s[16:19], s28 offen offset:2048 sc1 lds\n"
"v_mfma_f32_16x16x32_bf16 v[140:143], a[148:151], a[24:27], v[140:143]\n"
"v_exp_f32 v191, v191\n"
"v_mfma_f32_16x16x32_bf16 v[140:143], a[156:159], a[28:31], v[140:143]\n"
"ds_read_b128 a[112:115], v250 offset:36864\n"
"v_cvt_pk_bf16_f32 v183, v190, v191\n"
"v_mfma_f32_16x16x32_bf16 v[0:3], a[192:195], v[160:163], v[0:3]\n"
"v_exp_f32 v128, v128\n"
"v_mfma_f32_16x16x32_bf16 v[220:223], a[236:239], v[180:183], v[220:223]\n"
"buffer_load_dwordx4 v248, s[16:19], s28 offen offset:3072 sc1 lds\n"
"v_mfma_f32_16x16x32_bf16 v[4:7], a[196:199], v[160:163], v[4:7]\n"
"v_exp_f32 v129, v129\n"
"v_mfma_f32_16x16x32_bf16 v[8:11], a[200:203], v[160:163], v[8:11]\n"
"ds_read_b128 a[116:119], v250 offset:37888\n"
"v_cvt_pk_bf16_f32 v128, v128, v129\n"
"v_mfma_f32_16x16x32_bf16 v[12:15], a[204:207], v[160:163], v[12:15]\n"
"v_exp_f32 v130, v130\n"
"v_mfma_f32_16x16x32_bf16 v[16:19], a[208:211], v[160:163], v[16:19]\n"
"v_cmp_lt_f32 vcc, s43, v222\n"
"v_mfma_f32_16x16x32_bf16 v[20:23], a[212:215], v[160:163], v[20:23]\n"
"v_exp_f32 v131, v131\n"
"v_mfma_f32_16x16x32_bf16 v[24:27], a[216:219], v[160:163], v[24:27]\n"
"ds_read_b128 a[120:123], v250 offset:38912\n"
"v_cvt_pk_bf16_f32 v129, v130, v131\n"
"v_mfma_f32_16x16x32_bf16 v[28:31], a[220:223], v[160:163], v[28:31]\n"
"v_exp_f32 v132, v132\n"
"v_mfma_f32_16x16x32_bf16 v[32:35], a[192:195], v[164:167], v[32:35]\n"
"s_cbranch_vccnz Lfused_xC0\n"
"Lfused_rC0:\n"
"v_mfma_f32_16x16x32_bf16 v[36:39], a[196:199], v[164:167], v[36:39]\n"
"v_exp_f32 v133, v133\n"
"v_mfma_f32_16x16x32_bf16 v[40:43], a[200:203], v[164:167], v[40:43]\n"
"ds_read_b128 a[124:127], v250 offset:39936\n"
"v_cvt_pk_bf16_f32 v130, v132, v133\n"
"v_mov_b32 v200, v244\n"
"v_mfma_f32_16x16x32_bf16 v[44:47], a[204:207], v[164:167], v[44:47]\n"
"v_exp_f32 v134, v134\n"
"v_mfma_f32_16x16x32_bf16 v[48:51], a[208:211], v[164:167], v[48:51]\n"
"v_mov_b32 v201, v244\n"
"v_mov_b32 v202, v244\n"
"v_mov_b32 v203, v244\n"
"v_mfma_f32_16x16x32_bf16 v[52:55], a[212:215], v[164:167], v[52:55]\n"
"v_exp_f32 v135, v135\n"
"v_mfma_f32_16x16x32_bf16 v[56:59], a[216:219], v[164:167], v[56:59]\n"
"v_cvt_pk_bf16_f32 v131, v134, v135\n"
"v_mov_b32 v204, v245\n"
"v_mov_b32 v205, v245\n"
"v_mov_b32 v206, v245\n"
"v_mov_b32 v207, v245\n"
"v_mfma_f32_16x16x32_bf16 v[60:63], a[220:223], v[164:167], v[60:63]\n"
"Lfused_C1:\n"
"v_mfma_f32_16x16x32_bf16 v[144:147], a[128:131], a[64:67], v[200:203]\n"
"ds_read_b128 a[160:163], v250 offset:8192\n"
"s_add_u32 m0, m0, 0x10000\n"
"v_mfma_f32_16x16x32_bf16 v[144:147], a[136:139], a[68:71], v[144:147]\n"
"Lfused_hop10:\n"
"s_cbranch_execz Lfused_hop14\n"
"v_exp_f32 v136, v136\n"
"v_mfma_f32_16x16x32_bf16 v[144:147], a[144:147], a[72:75], v[144:147]\n"
"ds_read_b128 a[164:167], v250 offset:9216\n"
"v_mfma_f32_16x16x32_bf16 v[144:147], a[152:155], a[76:79], v[144:147]\n"
"v_exp_f32 v137, v137\n"
"v_mfma_f32_16x16x32_bf16 v[208:211], a[232:235], v[128:131], v[212:215]\n"
"ds_read_b128 a[168:171], v250 offset:10240\n"
"v_cvt_pk_bf16_f32 v132, v136, v137\n"
"v_mfma_f32_16x16x32_bf16 v[148:151], a[132:135], a[64:67], v[200:203]\n"
"v_exp_f32 v138, v138\n"
"v_mfma_f32_16x16x32_bf16 v[148:151], a[140:143], a[68:71], v[148:151]\n"
"buffer_load_dwordx4 v248, s[20:23], s28 offen offset:0 sc1 lds\n"
"v_mfma_f32_16x16x32_bf16 v[148:151], a[148:151], a[72:75], v[148:151]\n"
"v_exp_f32 v139, v139\n"
"v_mfma_f32_16x16x32_bf16 v[148:151], a[156:159], a[76:79], v[148:151]\n"
"ds_read_b128 a[172:175], v250 offset:11264\n"
"v_cvt_pk_bf16_f32 v133, v138, v139\n"
"v_mfma_f32_16x16x32_bf16 v[152:155], a[128:131], a[80:83], v[204:207]\n"
"v_exp_f32 v140, v140\n"
"v_mfma_f32_16x16x32_bf16 v[152:155], a[136:139], a[84:87], v[152:155]\n"
"buffer_load_dwordx4 v248, s[20:23], s28 offen offset:1024 sc1 lds\n"
"v_mfma_f32_16x16x32_bf16 v[152:155], a[144:147], a[88:91], v[152:155]\n"
"v_exp_f32 v141, v141\n"
"v_mfma_f32_16x16x32_bf16 v[152:155], a[152:155], a[92:95], v[152:155]\n"
"ds_read_b128 a[176:179], v250 offset:12288\n"
"v_cvt_pk_bf16_f32 v134, v140, v141\n"
"v_mfma_f32_16x16x32_bf16 v[156:159], a[132:135], a[80:83], v[204:207]\n"
"v_exp_f32 v142, v142\n"
"v_mfma_f32_16x16x32_bf16 v[156:159], a[140:143], a[84:87], v[156:159]\n"
"buffer_load_dwordx4 v248, s[20:23], s28 offen offset:2048 sc1 lds\n"
"v_mfma_f32_16x16x32_bf16 v[156:159], a[148:151], a[88:91], v[156:159]\n"
"v_exp_f32 v143, v143\n"
"v_mfma_f32_16x16x32_bf16 v[156:159], a[156:159], a[92:95], v[156:159]\n"
"ds_read_b128 a[180:183], v250 offset:13312\n"
"v_cvt_pk_bf16_f32 v135, v142, v143\n"
"v_mfma_f32_16x16x32_bf16 v[64:67], a[192:195], v[176:179], v[64:67]\n"
"v_exp_f32 v144, v144\n"
"v_mfma_f32_16x16x32_bf16 v[208:211], a[236:239], v[132:135], v[208:211]\n"
"buffer_load_dwordx4 v248, s[20:23], s28 offen offset:3072 sc1 lds\n"
"v_mfma_f32_16x16x32_bf16 v[68:71], a[196:199], v[176:179], v[68:71]\n"
"v_exp_f32 v145, v145\n"
"v_mfma_f32_16x16x32_bf16 v[72:75], a[200:203], v[176:179], v[72:75]\n"
"ds_read_b128 a[184:187], v250 offset:14336\n"
"v_cvt_pk_bf16_f32 v144, v144, v145\n"
"v_mfma_f32_16x16x32_bf16 v[76:79], a[204:207], v[176:179], v[76:79]\n"
"v_exp_f32 v146, v146\n"
"v_mfma_f32_16x16x32_bf16 v[80:83], a[208:211], v[176:179], v[80:83]\n"
"v_cmp_lt_f32 vcc, s43, v210\n"
"v_mfma_f32_16x16x32_bf16 v[84:87], a[212:215], v[176:179], v[84:87]\n"
"v_exp_f32 v147, v147\n"
"v_mfma_f32_16x16x32_bf16 v[88:91], a[216:219], v[176:179], v[88:91]\n"
"ds_read_b128 a[188:191], v250 offset:15360\n"
"v_cvt_pk_bf16_f32 v145, v146, v147\n"
"v_mfma_f32_16x16x32_bf16 v[92:95], a[220:223], v[176:179], v[92:95]\n"
"v_exp_f32 v148, v148\n"
"v_mfma_f32_16x16x32_bf16 v[96:99], a[192:195], v[180:183], v[96:99]\n"
"s_cbranch_vccnz Lfused_xC1\n"
"Lfused_rC1:\n"
"v_mfma_f32_16x16x32_bf16 v[100:103], a[196:199], v[180:183], v[100:103]\n"
"v_exp_f32 v149, v149\n"
"v_mfma_f32_16x16x32_bf16 v[104:107], a[200:203], v[180:183], v[104:107]\n"
"v_cvt_pk_bf16_f32 v146, v148, v149\n"
"s_add_u32 s28, s28, 0x4000\n"
"v_mfma_f32_16x16x32_bf16 v[108:111], a[204:207], v[180:183], v[108:111]\n"
"v_exp_f32 v150, v150\n"
"v_mfma_f32_16x16x32_bf16 v[112:115], a[208:211], v[180:183], v[112:115]\n"
"s_xor_b32 s30, s30, 0x4000\n"
"v_mfma_f32_16x16x32_bf16 v[116:119], a[212:215], v[180:183], v[116:119]\n"
"v_exp_f32 v151, v151\n"
"v_mfma_f32_16x16x32_bf16 v[120:123], a[216:219], v[180:183], v[120:123]\n"
"v_cvt_pk_bf16_f32 v147, v150, v151\n"
"v_mfma_f32_16x16x32_bf16 v[124:127], a[220:223], v[180:183], v[124:127]\n"
"v_rcp_f32 v226, v220\n"
"v_rcp_f32 v227, v221\n"
"v_mov_b64 v[220:221], 0\n"
"v_mov_b64 v[222:223], 0\n"
"v_mul_f32 v64, v64, v226\n"
"v_mul_f32 v65, v65, v226\n"
"v_mul_f32 v66, v66, v226\n"
"v_mul_f32 v67, v67, v226\n"
"v_mul_f32 v68, v68, v226\n"
"v_mul_f32 v69, v69, v226\n"
"v_mul_f32 v70, v70, v226\n"
"v_mul_f32 v71, v71, v226\n"
"v_cvt_pk_bf16_f32 v64, v64, v65\n"
"v_cvt_pk_bf16_f32 v65, v66, v67\n"
"v_cvt_pk_bf16_f32 v66, v68, v69\n"
"v_cvt_pk_bf16_f32 v67, v70, v71\n"
"buffer_store_dwordx4 v[64:67], v248, s[24:27], s48 offen offset:0 sc1\n"
"v_mul_f32 v72, v72, v226\n"
"v_mul_f32 v73, v73, v226\n"
"v_mul_f32 v74, v74, v226\n"
"v_mul_f32 v75, v75, v226\n"
"v_mul_f32 v76, v76, v226\n"
"v_mul_f32 v77, v77, v226\n"
"v_mul_f32 v78, v78, v226\n"
"v_mul_f32 v79, v79, v226\n"
"v_cvt_pk_bf16_f32 v72, v72, v73\n"
"v_cvt_pk_bf16_f32 v73, v74, v75\n"
"v_cvt_pk_bf16_f32 v74, v76, v77\n"
"v_cvt_pk_bf16_f32 v75, v78, v79\n"
"buffer_store_dwordx4 v[72:75], v248, s[24:27], s48 offen offset:1024 sc1\n"
"v_mul_f32 v80, v80, v226\n"
"v_mul_f32 v81, v81, v226\n"
"v_mul_f32 v82, v82, v226\n"
"v_mul_f32 v83, v83, v226\n"
"v_mul_f32 v84, v84, v226\n"
"v_mul_f32 v85, v85, v226\n"
"v_mul_f32 v86, v86, v226\n"
"v_mul_f32 v87, v87, v226\n"
"v_cvt_pk_bf16_f32 v80, v80, v81\n"
"v_cvt_pk_bf16_f32 v81, v82, v83\n"
"v_cvt_pk_bf16_f32 v82, v84, v85\n"
"v_cvt_pk_bf16_f32 v83, v86, v87\n"
"buffer_store_dwordx4 v[80:83], v248, s[24:27], s48 offen offset:2048 sc1\n"
"v_mul_f32 v88, v88, v226\n"
"v_mul_f32 v89, v89, v226\n"
"v_mul_f32 v90, v90, v226\n"
"v_mul_f32 v91, v91, v226\n"
"v_mul_f32 v92, v92, v226\n"
"v_mul_f32 v93, v93, v226\n"
"v_mul_f32 v94, v94, v226\n"
"v_mul_f32 v95, v95, v226\n"
"v_cvt_pk_bf16_f32 v88, v88, v89\n"
"v_cvt_pk_bf16_f32 v89, v90, v91\n"
"v_cvt_pk_bf16_f32 v90, v92, v93\n"
"v_cvt_pk_bf16_f32 v91, v94, v95\n"
"buffer_store_dwordx4 v[88:91], v248, s[24:27], s48 offen offset:3072 sc1\n"
"s_add_u32 s48, s48, 147456\n"
"v_mul_f32 v96, v96, v227\n"
"v_mul_f32 v97, v97, v227\n"
"v_mul_f32 v98, v98, v227\n"
"v_mul_f32 v99, v99, v227\n"
"v_mul_f32 v100, v100, v227\n"
"v_mul_f32 v101, v101, v227\n"
"v_mul_f32 v102, v102, v227\n"
"v_mul_f32 v103, v103, v227\n"
"v_cvt_pk_bf16_f32 v96, v96, v97\n"
"v_cvt_pk_bf16_f32 v97, v98, v99\n"
"v_cvt_pk_bf16_f32 v98, v100, v101\n"
"v_cvt_pk_bf16_f32 v99, v102, v103\n"
"buffer_store_dwordx4 v[96:99], v248, s[24:27], s48 offen offset:0 sc1\n"
"v_mul_f32 v104, v104, v227\n"
"v_mul_f32 v105, v105, v227\n"
"v_mul_f32 v106, v106, v227\n"
"v_mul_f32 v107, v107, v227\n"
"v_mul_f32 v108, v108, v227\n"
"v_mul_f32 v109, v109, v227\n"
"v_mul_f32 v110, v110, v227\n"
"v_mul_f32 v111, v111, v227\n"
"v_cvt_pk_bf16_f32 v104, v104, v105\n"
"v_cvt_pk_bf16_f32 v105, v106, v107\n"
"v_cvt_pk_bf16_f32 v106, v108, v109\n"
"v_cvt_pk_bf16_f32 v107, v110, v111\n"
"buffer_store_dwordx4 v[104:107], v248, s[24:27], s48 offen offset:1024 sc1\n"
"v_mul_f32 v112, v112, v227\n"
"v_mul_f32 v113, v113, v227\n"
"v_mul_f32 v114, v114, v227\n"
"v_mul_f32 v115, v115, v227\n"
"v_mul_f32 v116, v116, v227\n"
"v_mul_f32 v117, v117, v227\n"
"v_mul_f32 v118, v118, v227\n"
"v_mul_f32 v119, v119, v227\n"
"v_cvt_pk_bf16_f32 v112, v112, v113\n"
"v_cvt_pk_bf16_f32 v113, v114, v115\n"
"v_cvt_pk_bf16_f32 v114, v116, v117\n"
"v_cvt_pk_bf16_f32 v115, v118, v119\n"
"buffer_store_dwordx4 v[112:115], v248, s[24:27], s48 offen offset:2048 sc1\n"
"v_mul_f32 v120, v120, v227\n"
"v_mul_f32 v121, v121, v227\n"
"v_mul_f32 v122, v122, v227\n"
"v_mul_f32 v123, v123, v227\n"
"v_mul_f32 v124, v124, v227\n"
"v_mul_f32 v125, v125, v227\n"
"v_mul_f32 v126, v126, v227\n"
"v_mul_f32 v127, v127, v227\n"
"v_cvt_pk_bf16_f32 v120, v120, v121\n"
"v_cvt_pk_bf16_f32 v121, v122, v123\n"
"v_cvt_pk_bf16_f32 v122, v124, v125\n"
"v_cvt_pk_bf16_f32 v123, v126, v127\n"
"buffer_store_dwordx4 v[120:123], v248, s[24:27], s48 offen offset:3072 sc1\n"
"Lfused_C2:\n"
"s_waitcnt vmcnt(16) lgkmcnt(0)\n"
"v_mfma_f32_16x16x32_bf16 v[160:163], a[160:163], a[0:3], v[192:195]\n"
"s_barrier\n"
"s_mov_b32 m0, s31\n"
"v_mfma_f32_16x16x32_bf16 v[160:163], a[168:171], a[4:7], v[160:163]\n"
"v_exp_f32 v152, v152\n"
"v_mfma_f32_16x16x32_bf16 v[160:163], a[176:179], a[8:11], v[160:163]\n"
"ds_read_b128 a[192:195], v250 offset:40960\n"
"v_mfma_f32_16x16x32_bf16 v[160:163], a[184:187], a[12:15], v[160:163]\n"
"v_exp_f32 v153, v153\n"
"v_mfma_f32_16x16x32_bf16 v[216:219], a[232:235], v[144:147], v[220:223]\n"
"ds_read_b128 a[196:199], v250 offset:41984\n"
"v_cvt_pk_bf16_f32 v148, v152, v153\n"
"v_mfma_f32_16x16x32_bf16 v[164:167], a[164:167], a[0:3], v[192:195]\n"
"v_exp_f32 v154, v154\n"
"v_mfma_f32_16x16x32_bf16 v[164:167], a[172:175], a[4:7], v[164:167]\n"
"buffer_load_dwordx4 v248, s[16:19], s28 offen offset:0 sc1 lds\n"
"v_mfma_f32_16x16x32_bf16 v[164:167], a[180:183], a[8:11], v[164:167]\n"
"v_exp_f32 v155, v155\n"
"v_mfma_f32_16x16x32_bf16 v[164:167], a[188:191], a[12:15], v[164:167]\n"
"ds_read_b128 a[200:203], v250 offset:43008\n"
"v_cvt_pk_bf16_f32 v149, v154, v155\n"
"v_mfma_f32_16x16x32_bf16 v[168:171], a[160:163], a[16:19], v[196:199]\n"
"v_exp_f32 v156, v156\n"
"v_mfma_f32_16x16x32_bf16 v[168:171], a[168:171], a[20:23], v[168:171]\n"
"buffer_load_dwordx4 v248, s[16:19], s28 offen offset:1024 sc1 lds\n"
"v_mfma_f32_16x16x32_bf16 v[168:171], a[176:179], a[24:27], v[168:171]\n"
"v_exp_f32 v157, v157\n"
"v_mfma_f32_16x16x32_bf16 v[168:171], a[184:187], a[28:31], v[168:171]\n"
"ds_read_b128 a[204:207], v250 offset:44032\n"
"v_cvt_pk_bf16_f32 v150, v156, v157\n"
"v_mfma_f32_16x16x32_bf16 v[172:175], a[164:167], a[16:19], v[196:199]\n"
"v_exp_f32 v158, v158\n"
"v_mfma_f32_16x16x32_bf16 v[172:175], a[172:175], a[20:23], v[172:175]\n"
"buffer_load_dwordx4 v248, s[16:19], s28 offen offset:2048 sc1 lds\n"
"v_mfma_f32_16x16x32_bf16 v[172:175], a[180:183], a[24:27], v[172:175]\n"
"v_exp_f32 v159, v159\n"
"v_mfma_f32_16x16x32_bf16 v[172:175], a[188:191], a[28:31], v[172:175]\n"
"ds_read_b128 a[208:211], v250 offset:45056\n"
"v_cvt_pk_bf16_f32 v151, v158, v159\n"
"v_mfma_f32_16x16x32_bf16 v[0:3], a[96:99], v[128:131], v[0:3]\n"
"v_exp_f32 v160, v160\n"
"v_mfma_f32_16x16x32_bf16 v[216:219], a[236:239], v[148:151], v[216:219]\n"
"buffer_load_dwordx4 v248, s[16:19], s28 offen offset:3072 sc1 lds\n"
"v_mfma_f32_16x16x32_bf16 v[4:7], a[100:103], v[128:131], v[4:7]\n"
"v_exp_f32 v161, v161\n"
"v_mfma_f32_16x16x32_bf16 v[8:11], a[104:107], v[128:131], v[8:11]\n"
"ds_read_b128 a[212:215], v250 offset:46080\n"
"v_cvt_pk_bf16_f32 v160, v160, v161\n"
"v_mfma_f32_16x16x32_bf16 v[12:15], a[108:111], v[128:131], v[12:15]\n"
"v_exp_f32 v162, v162\n"
"v_mfma_f32_16x16x32_bf16 v[16:19], a[112:115], v[128:131], v[16:19]\n"
"v_cmp_lt_f32 vcc, s43, v218\n"
"v_mfma_f32_16x16x32_bf16 v[20:23], a[116:119], v[128:131], v[20:23]\n"
"v_exp_f32 v163, v163\n"
"v_mfma_f32_16x16x32_bf16 v[24:27], a[120:123], v[128:131], v[24:27]\n"
"ds_read_b128 a[216:219], v250 offset:47104\n"
"v_cvt_pk_bf16_f32 v161, v162, v163\n"
"v_mfma_f32_16x16x32_bf16 v[28:31], a[124:127], v[128:131], v[28:31]\n"
"v_exp_f32 v164, v164\n"
"v_mfma_f32_16x16x32_bf16 v[32:35], a[96:99], v[132:135], v[32:35]\n"
"s_cbranch_vccnz Lfused_xC2\n"
"Lfused_rC2:\n"
"v_mfma_f32_16x16x32_bf16 v[36:39], a[100:103], v[132:135], v[36:39]\n"
"v_exp_f32 v165, v165\n"
"v_mfma_f32_16x16x32_bf16 v[40:43], a[104:107], v[132:135], v[40:43]\n"
"ds_read_b128 a[220:223], v250 offset:48128\n"
"v_cvt_pk_bf16_f32 v162, v164, v165\n"
"v_xor_b32 v250, 0x4000, v250\n"
"v_mfma_f32_16x16x32_bf16 v[44:47], a[108:111], v[132:135], v[44:47]\n"
"v_exp_f32 v166, v166\n"
"v_mfma_f32_16x16x32_bf16 v[48:51], a[112:115], v[132:135], v[48:51]\n"
"v_mfma_f32_16x16x32_bf16 v[52:55], a[116:119], v[132:135], v[52:55]\n"
"v_exp_f32 v167, v167\n"
"v_mfma_f32_16x16x32_bf16 v[56:59], a[120:123], v[132:135], v[56:59]\n"
"v_cvt_pk_bf16_f32 v163, v166, v167\n"
"v_mfma_f32_16x16x32_bf16 v[60:63], a[124:127], v[132:135], v[60:63]\n"
"Lfused_C3:\n"
"v_mfma_f32_16x16x32_bf16 v[176:179], a[160:163], a[64:67], v[200:203]\n"
"ds_read_b128 a[128:131], v250 offset:0\n"
"s_add_u32 m0, m0, 0x10000\n"
"v_mfma_f32_16x16x32_bf16 v[176:179], a[168:171], a[68:71], v[176:179]\n"
"v_exp_f32 v168, v168\n"
"v_mfma_f32_16x16x32_bf16 v[176:179], a[176:179], a[72:75], v[176:179]\n"
"ds_read_b128 a[132:135], v250 offset:1024\n"
"v_mfma_f32_16x16x32_bf16 v[176:179], a[184:187], a[76:79], v[176:179]\n"
"v_exp_f32 v169, v169\n"
"v_mfma_f32_16x16x32_bf16 v[212:215], a[232:235], v[160:163], v[208:211]\n"
"ds_read_b128 a[136:139], v250 offset:2048\n"
"v_cvt_pk_bf16_f32 v164, v168, v169\n"
"v_mfma_f32_16x16x32_bf16 v[180:183], a[164:167], a[64:67], v[200:203]\n"
"v_exp_f32 v170, v170\n"
"v_mfma_f32_16x16x32_bf16 v[180:183], a[172:175], a[68:71], v[180:183]\n"
"buffer_load_dwordx4 v248, s[20:23], s28 offen offset:0 sc1 lds\n"
"v_mfma_f32_16x16x32_bf16 v[180:183], a[180:183], a[72:75], v[180:183]\n"
"v_exp_f32 v171, v171\n"
"v_mfma_f32_16x16x32_bf16 v[180:183], a[188:191], a[76:79], v[180:183]\n"
"ds_read_b128 a[140:143], v250 offset:3072\n"
"v_cvt_pk_bf16_f32 v165, v170, v171\n"
"v_mfma_f32_16x16x32_bf16 v[184:187], a[160:163], a[80:83], v[204:207]\n"
"v_exp_f32 v172, v172\n"
"v_mfma_f32_16x16x32_bf16 v[184:187], a[168:171], a[84:87], v[184:187]\n"
"buffer_load_dwordx4 v248, s[20:23], s28 offen offset:1024 sc1 lds\n"
"v_mfma_f32_16x16x32_bf16 v[184:187], a[176:179], a[88:91], v[184:187]\n"
"v_exp_f32 v173, v173\n"
"v_mfma_f32_16x16x32_bf16 v[184:187], a[184:187], a[92:95], v[184:187]\n"
"ds_read_b128 a[144:147], v250 offset:4096\n"
"v_cvt_pk_bf16_f32 v166, v172, v173\n"
"v_mfma_f32_16x16x32_bf16 v[188:191], a[164:167], a[80:83], v[204:207]\n"
"v_exp_f32 v174, v174\n"
"v_mfma_f32_16x16x32_bf16 v[188:191], a[172:175], a[84:87], v[188:191]\n"
"buffer_load_dwordx4 v248, s[20:23], s28 offen offset:2048 sc1 lds\n"
"v_mfma_f32_16x16x32_bf16 v[188:191], a[180:183], a[88:91], v[188:191]\n"
"v_exp_f32 v175, v175\n"
"v_mfma_f32_16x16x32_bf16 v[188:191], a[188:191], a[92:95], v[188:191]\n"
"ds_read_b128 a[148:151], v250 offset:5120\n"
"v_cvt_pk_bf16_f32 v167, v174, v175\n"
"v_mfma_f32_16x16x32_bf16 v[64:67], a[96:99], v[144:147], 0\n"
"v_exp_f32 v176, v176\n"
"v_mfma_f32_16x16x32_bf16 v[212:215], a[236:239], v[164:167], v[212:215]\n"
"buffer_load_dwordx4 v248, s[20:23], s28 offen offset:3072 sc1 lds\n"
"v_mfma_f32_16x16x32_bf16 v[68:71], a[100:103], v[144:147], 0\n"
"v_exp_f32 v177, v177\n"
"v_mfma_f32_16x16x32_bf16 v[72:75], a[104:107], v[144:147], 0\n"
"ds_read_b128 a[152:155], v250 offset:6144\n"
"v_cvt_pk_bf16_f32 v176, v176, v177\n"
"v_mfma_f32_16x16x32_bf16 v[76:79], a[108:111], v[144:147], 0\n"
"v_exp_f32 v178, v178\n"
"v_mfma_f32_16x16x32_bf16 v[80:83], a[112:115], v[144:147], 0\n"
"v_cmp_lt_f32 vcc, s43, v214\n"
"v_mfma_f32_16x16x32_bf16 v[84:87], a[116:119], v[144:147], 0\n"
"v_exp_f32 v179, v179\n"
"v_mfma_f32_16x16x32_bf16 v[88:91], a[120:123], v[144:147], 0\n"
"ds_read_b128 a[156:159], v250 offset:7168\n"
"v_cvt_pk_bf16_f32 v177, v178, v179\n"
"v_mfma_f32_16x16x32_bf16 v[92:95], a[124:127], v[144:147], 0\n"
"v_exp_f32 v180, v180\n"
"v_mfma_f32_16x16x32_bf16 v[96:99], a[96:99], v[148:151], 0\n"
"s_cbranch_vccnz Lfused_xC3\n"
"Lfused_rC3:\n"
"v_mfma_f32_16x16x32_bf16 v[100:103], a[100:103], v[148:151], 0\n"
"v_exp_f32 v181, v181\n"
"v_mfma_f32_16x16x32_bf16 v[104:107], a[104:107], v[148:151], 0\n"
"v_cvt_pk_bf16_f32 v178, v180, v181\n"
"s_add_u32 s28, s28, 0x4000\n"
"v_mfma_f32_16x16x32_bf16 v[108:111], a[108:111], v[148:151], 0\n"
"v_exp_f32 v182, v182\n"
"v_mfma_f32_16x16x32_bf16 v[112:115], a[112:115], v[148:151], 0\n"
"s_xor_b32 s31, s31, 0x4000\n"
"v_mfma_f32_16x16x32_bf16 v[116:119], a[116:119], v[148:151], 0\n"
"v_exp_f32 v183, v183\n"
"v_mfma_f32_16x16x32_bf16 v[120:123], a[120:123], v[148:151], 0\n"
"v_cvt_pk_bf16_f32 v179, v182, v183\n"
"v_mfma_f32_16x16x32_bf16 v[124:127], a[124:127], v[148:151], 0\n"
"s_mov_b32 s14, 30\n"
"s_cmp_eq_u32 s11, 6\n"
"s_cbranch_scc1 Lfused_E0\n"
"s_cmp_eq_u32 s11, 0\n"
"s_cbranch_scc0 Lfused_a2e\n"
"Lfused_a2c:\n"
"s_waitcnt vmcnt(16)\n"
"v_cmp_ne_u64 vcc, s[98:99], v[242:243]\n"
"s_cbranch_vccnz Lfused_ps2\n"
"Lfused_pd2:\n"
"s_lshl_b32 s50, s10, 14\n"
"s_add_u32 s50, s50, s90\n"
"s_add_u32 s50, s50, 54525952\n"
"s_add_u32 s52, s8, s50\n"
"s_addc_u32 s53, s9, 0\n"
"v_lshlrev_b32 v224, 1, v248\n"
"global_load_dwordx4 a[240:243], v224, s[52:53] offset:0 sc1\n"
"global_load_dwordx4 a[244:247], v224, s[52:53] offset:16 sc1\n"
"global_load_dwordx4 a[248:251], v224, s[52:53] offset:2048 sc1\n"
"global_load_dwordx4 a[252:255], v224, s[52:53] offset:2064 sc1\n"
"Lfused_a2e:\n"
"s_lshl_b32 s29, s11, 1\n"
"s_sub_u32 s29, 11, s29\n"
"Lfused_D0:\n"
"s_waitcnt vmcnt(16) lgkmcnt(0)\n"
"v_mfma_f32_16x16x32_bf16 v[128:131], a[128:131], a[0:3], v[192:195]\n"
"s_barrier\n"
"s_mov_b32 m0, s30\n"
"v_mfma_f32_16x16x32_bf16 v[128:131], a[136:139], a[4:7], v[128:131]\n"
"v_exp_f32 v184, v184\n"
"v_mfma_f32_16x16x32_bf16 v[128:131], a[144:147], a[8:11], v[128:131]\n"
"ds_read_b128 a[96:99], v250 offset:32768\n"
"v_mfma_f32_16x16x32_bf16 v[128:131], a[152:155], a[12:15], v[128:131]\n"
"v_exp_f32 v185, v185\n"
"v_mfma_f32_16x16x32_bf16 v[220:223], a[232:235], v[176:179], v[216:219]\n"
"ds_read_b128 a[100:103], v250 offset:33792\n"
"v_cvt_pk_bf16_f32 v180, v184, v185\n"
"v_mfma_f32_16x16x32_bf16 v[132:135], a[132:135], a[0:3], v[192:195]\n"
"v_exp_f32 v186, v186\n"
"v_mfma_f32_16x16x32_bf16 v[132:135], a[140:143], a[4:7], v[132:135]\n"
"buffer_load_dwordx4 v248, s[16:19], s28 offen offset:0 sc1 lds\n"
"v_mfma_f32_16x16x32_bf16 v[132:135], a[148:151], a[8:11], v[132:135]\n"
"v_exp_f32 v187, v187\n"
"v_mfma_f32_16x16x32_bf16 v[132:135], a[156:159], a[12:15], v[132:135]\n"
"ds_read_b128 a[104:107], v250 offset:34816\n"
"v_cvt_pk_bf16_f32 v181, v186, v187\n"
"v_mfma_f32_16x16x32_bf16 v[136:139], a[128:131], a[16:19], v[196:199]\n"
"v_exp_f32 v188, v188\n"
"v_mfma_f32_16x16x32_bf16 v[136:139], a[136:139], a[20:23], v[136:139]\n"
"buffer_load_dwordx4 v248, s[16:19], s28 offen offset:1024 sc1 lds\n"
"v_mfma_f32_16x16x32_bf16 v[136:139], a[144:147], a[24:27], v[136:139]\n"
"v_exp_f32 v189, v189\n"
"v_mfma_f32_16x16x32_bf16 v[136:139], a[152:155], a[28:31], v[136:139]\n"
"ds_read_b128 a[108:111], v250 offset:35840\n"
"v_cvt_pk_bf16_f32 v182, v188, v189\n"
"v_mfma_f32_16x16x32_bf16 v[140:143], a[132:135], a[16:19], v[196:199]\n"
"v_exp_f32 v190, v190\n"
"v_mfma_f32_16x16x32_bf16 v[140:143], a[140:143], a[20:23], v[140:143]\n"
"buffer_load_dwordx4 v248, s[16:19], s28 offen offset:2048 sc1 lds\n"
"v_mfma_f32_16x16x32_bf16 v[140:143], a[148:151], a[24:27], v[140:143]\n"
"v_exp_f32 v191, v191\n"
"v_mfma_f32_16x16x32_bf16 v[140:143], a[156:159], a[28:31], v[140:143]\n"
"ds_read_b128 a[112:115], v250 offset:36864\n"
"v_cvt_pk_bf16_f32 v183, v190, v191\n"
"v_mfma_f32_16x16x32_bf16 v[0:3], a[192:195], v[160:163], v[0:3]\n"
"v_exp_f32 v128, v128\n"
"v_mfma_f32_16x16x32_bf16 v[220:223], a[236:239], v[180:183], v[220:223]\n"
"buffer_load_dwordx4 v248, s[16:19], s28 offen offset:3072 sc1 lds\n"
"v_mfma_f32_16x16x32_bf16 v[4:7], a[196:199], v[160:163], v[4:7]\n"
"v_exp_f32 v129, v129\n"
"v_mfma_f32_16x16x32_bf16 v[8:11], a[200:203], v[160:163], v[8:11]\n"
"ds_read_b128 a[116:119], v250 offset:37888\n"
"v_cvt_pk_bf16_f32 v128, v128, v129\n"
"v_mfma_f32_16x16x32_bf16 v[12:15], a[204:207], v[160:163], v[12:15]\n"
"v_exp_f32 v130, v130\n"
"v_mfma_f32_16x16x32_bf16 v[16:19], a[208:211], v[160:163], v[16:19]\n"
"v_cmp_lt_f32 vcc, s43, v222\n"
"v_mfma_f32_16x16x32_bf16 v[20:23], a[212:215], v[160:163], v[20:23]\n"
"v_exp_f32 v131, v131\n"
"v_mfma_f32_16x16x32_bf16 v[24:27], a[216:219], v[160:163], v[24:27]\n"
"ds_read_b128 a[120:123], v250 offset:38912\n"
"v_cvt_pk_bf16_f32 v129, v130, v131\n"
"v_mfma_f32_16x16x32_bf16 v[28:31], a[220:223], v[160:163], v[28:31]\n"
"v_exp_f32 v132, v132\n"
"v_mfma_f32_16x16x32_bf16 v[32:35], a[192:195], v[164:167], v[32:35]\n"
"s_cbranch_vccnz Lfused_xD0\n"
"Lfused_rD0:\n"
"v_mfma_f32_16x16x32_bf16 v[36:39], a[196:199], v[164:167], v[36:39]\n"
"v_exp_f32 v133, v133\n"
"v_mfma_f32_16x16x32_bf16 v[40:43], a[200:203], v[164:167], v[40:43]\n"
"ds_read_b128 a[124:127], v250 offset:39936\n"
"v_cvt_pk_bf16_f32 v130, v132, v133\n"
"v_mfma_f32_16x16x32_bf16 v[44:47], a[204:207], v[164:167], v[44:47]\n"
"v_exp_f32 v134, v134\n"
"v_mfma_f32_16x16x32_bf16 v[48:51], a[208:211], v[164:167], v[48:51]\n"
"v_mfma_f32_16x16x32_bf16 v[52:55], a[212:215], v[164:167], v[52:55]\n"
"v_exp_f32 v135, v135\n"
"v_mfma_f32_16x16x32_bf16 v[56:59], a[216:219], v[164:167], v[56:59]\n"
"v_cvt_pk_bf16_f32 v131, v134, v135\n"
"v_mfma_f32_16x16x32_bf16 v[60:63], a[220:223], v[164:167], v[60:63]\n"
"Lfused_D1:\n"
"v_mfma_f32_16x16x32_bf16 v[144:147], a[128:131], a[64:67], v[200:203]\n"
"ds_read_b128 a[160:163], v250 offset:8192\n"
"s_add_u32 m0, m0, 0x10000\n"
"v_mfma_f32_16x16x32_bf16 v[144:147], a[136:139], a[68:71], v[144:147]\n"
"v_exp_f32 v136, v136\n"
"v_mfma_f32_16x16x32_bf16 v[144:147], a[144:147], a[72:75], v[144:147]\n"
"ds_read_b128 a[164:167], v250 offset:9216\n"
"v_mfma_f32_16x16x32_bf16 v[144:147], a[152:155], a[76:79], v[144:147]\n"
"v_exp_f32 v137, v137\n"
"v_mfma_f32_16x16x32_bf16 v[208:211], a[232:235], v[128:131], v[212:215]\n"
"ds_read_b128 a[168:171], v250 offset:10240\n"
"v_cvt_pk_bf16_f32 v132, v136, v137\n"
"v_mfma_f32_16x16x32_bf16 v[148:151], a[132:135], a[64:67], v[200:203]\n"
"v_exp_f32 v138, v138\n"
"v_mfma_f32_16x16x32_bf16 v[148:151], a[140:143], a[68:71], v[148:151]\n"
"buffer_load_dwordx4 v248, s[20:23], s28 offen offset:0 sc1 lds\n"
"v_mfma_f32_16x16x32_bf16 v[148:151], a[148:151], a[72:75], v[148:151]\n"
"v_exp_f32 v139, v139\n"
"v_mfma_f32_16x16x32_bf16 v[148:151], a[156:159], a[76:79], v[148:151]\n"
"ds_read_b128 a[172:175], v250 offset:11264\n"
"v_cvt_pk_bf16_f32 v133, v138, v139\n"
"v_mfma_f32_16x16x32_bf16 v[152:155], a[128:131], a[80:83], v[204:207]\n"
"v_exp_f32 v140, v140\n"
"v_mfma_f32_16x16x32_bf16 v[152:155], a[136:139], a[84:87], v[152:155]\n"
"buffer_load_dwordx4 v248, s[20:23], s28 offen offset:1024 sc1 lds\n"
"v_mfma_f32_16x16x32_bf16 v[152:155], a[144:147], a[88:91], v[152:155]\n"
"v_exp_f32 v141, v141\n"
"v_mfma_f32_16x16x32_bf16 v[152:155], a[152:155], a[92:95], v[152:155]\n"
"ds_read_b128 a[176:179], v250 offset:12288\n"
"v_cvt_pk_bf16_f32 v134, v140, v141\n"
"v_mfma_f32_16x16x32_bf16 v[156:159], a[132:135], a[80:83], v[204:207]\n"
"v_exp_f32 v142, v142\n"
"v_mfma_f32_16x16x32_bf16 v[156:159], a[140:143], a[84:87], v[156:159]\n"
"buffer_load_dwordx4 v248, s[20:23], s28 offen offset:2048 sc1 lds\n"
"v_mfma_f32_16x16x32_bf16 v[156:159], a[148:151], a[88:91], v[156:159]\n"
"v_exp_f32 v143, v143\n"
"v_mfma_f32_16x16x32_bf16 v[156:159], a[156:159], a[92:95], v[156:159]\n"
"ds_read_b128 a[180:183], v250 offset:13312\n"
"v_cvt_pk_bf16_f32 v135, v142, v143\n"
"v_mfma_f32_16x16x32_bf16 v[64:67], a[192:195], v[176:179], v[64:67]\n"
"v_exp_f32 v144, v144\n"
"v_mfma_f32_16x16x32_bf16 v[208:211], a[236:239], v[132:135], v[208:211]\n"
"buffer_load_dwordx4 v248, s[20:23], s28 offen offset:3072 sc1 lds\n"
"v_mfma_f32_16x16x32_bf16 v[68:71], a[196:199], v[176:179], v[68:71]\n"
"v_exp_f32 v145, v145\n"
"v_mfma_f32_16x16x32_bf16 v[72:75], a[200:203], v[176:179], v[72:75]\n"
"ds_read_b128 a[184:187], v250 offset:14336\n"
"v_cvt_pk_bf16_f32 v144, v144, v145\n"
"v_mfma_f32_16x16x32_bf16 v[76:79], a[204:207], v[176:179], v[76:79]\n"
"v_exp_f32 v146, v146\n"
"v_mfma_f32_16x16x32_bf16 v[80:83], a[208:211], v[176:179], v[80:83]\n"
"v_cmp_lt_f32 vcc, s43, v210\n"
"v_mfma_f32_16x16x32_bf16 v[84:87], a[212:215], v[176:179], v[84:87]\n"
"v_exp_f32 v147, v147\n"
"v_mfma_f32_16x16x32_bf16 v[88:91], a[216:219], v[176:179], v[88:91]\n"
"ds_read_b128 a[188:191], v250 offset:15360\n"
"v_cvt_pk_bf16_f32 v145, v146, v147\n"
"v_mfma_f32_16x16x32_bf16 v[92:95], a[220:223], v[176:179], v[92:95]\n"
"v_exp_f32 v148, v148\n"
"v_mfma_f32_16x16x32_bf16 v[96:99], a[192:195], v[180:183], v[96:99]\n"
"s_cbranch_vccnz Lfused_xD1\n"
"Lfused_rD1:\n"
"v_mfma_f32_16x16x32_bf16 v[100:103], a[196:199], v[180:183], v[100:103]\n"
"v_exp_f32 v149, v149\n"
"v_mfma_f32_16x16x32_bf16 v[104:107], a[200:203], v[180:183], v[104:107]\n"
"v_cvt_pk_bf16_f32 v146, v148, v149\n"
"s_add_u32 s28, s28, 0x4000\n"
"v_mfma_f32_16x16x32_bf16 v[108:111], a[204:207], v[180:183], v[108:111]\n"
"v_exp_f32 v150, v150\n"
"v_mfma_f32_16x16x32_bf16 v[112:115], a[208:211], v[180:183], v[112:115]\n"
"s_xor_b32 s30, s30, 0x4000\n"
"v_mfma_f32_16x16x32_bf16 v[116:119], a[212:215], v[180:183], v[116:119]\n"
"v_exp_f32 v151, v151\n"
"v_mfma_f32_16x16x32_bf16 v[120:123], a[216:219], v[180:183], v[120:123]\n"
"v_cvt_pk_bf16_f32 v147, v150, v151\n"
"v_mfma_f32_16x16x32_bf16 v[124:127], a[220:223], v[180:183], v[124:127]\n"
"Lfused_D2:\n"
"s_waitcnt vmcnt(16) lgkmcnt(0)\n"
"v_mfma_f32_16x16x32_bf16 v[160:163], a[160:163], a[0:3], v[192:195]\n"
"s_barrier\n"
"s_mov_b32 m0, s31\n"
"v_mfma_f32_16x16x32_bf16 v[160:163], a[168:171], a[4:7], v[160:163]\n"
"v_exp_f32 v152, v152\n"
"v_mfma_f32_16x16x32_bf16 v[160:163], a[176:179], a[8:11], v[160:163]\n"
"ds_read_b128 a[192:195], v250 offset:40960\n"
"v_mfma_f32_16x16x32_bf16 v[160:163], a[184:187], a[12:15], v[160:163]\n"
"v_exp_f32 v153, v153\n"
"v_mfma_f32_16x16x32_bf16 v[216:219], a[232:235], v[144:147], v[220:223]\n"
"ds_read_b128 a[196:199], v250 offset:41984\n"
"v_cvt_pk_bf16_f32 v148, v152, v153\n"
"v_mfma_f32_16x16x32_bf16 v[164:167], a[164:167], a[0:3], v[192:195]\n"
"v_exp_f32 v154, v154\n"
"v_mfma_f32_16x16x32_bf16 v[164:167], a[172:175], a[4:7], v[164:167]\n"
"buffer_load_dwordx4 v248, s[16:19], s28 offen offset:0 sc1 lds\n"
"v_mfma_f32_16x16x32_bf16 v[164:167], a[180:183], a[8:11], v[164:167]\n"
"v_exp_f32 v155, v155\n"
"v_mfma_f32_16x16x32_bf16 v[164:167], a[188:191], a[12:15], v[164:167]\n"
"ds_read_b128 a[200:203], v250 offset:43008\n"
"v_cvt_pk_bf16_f32 v149, v154, v155\n"
"v_mfma_f32_16x16x32_bf16 v[168:171], a[160:163], a[16:19], v[196:199]\n"
"v_exp_f32 v156, v156\n"
"v_mfma_f32_16x16x32_bf16 v[168:171], a[168:171], a[20:23], v[168:171]\n"
"buffer_load_dwordx4 v248, s[16:19], s28 offen offset:1024 sc1 lds\n"
"v_mfma_f32_16x16x32_bf16 v[168:171], a[176:179], a[24:27], v[168:171]\n"
"v_exp_f32 v157, v157\n"
"v_mfma_f32_16x16x32_bf16 v[168:171], a[184:187], a[28:31], v[168:171]\n"
"ds_read_b128 a[204:207], v250 offset:44032\n"
"v_cvt_pk_bf16_f32 v150, v156, v157\n"
"v_mfma_f32_16x16x32_bf16 v[172:175], a[164:167], a[16:19], v[196:199]\n"
"v_exp_f32 v158, v158\n"
"v_mfma_f32_16x16x32_bf16 v[172:175], a[172:175], a[20:23], v[172:175]\n"
"buffer_load_dwordx4 v248, s[16:19], s28 offen offset:2048 sc1 lds\n"
"v_mfma_f32_16x16x32_bf16 v[172:175], a[180:183], a[24:27], v[172:175]\n"
"v_exp_f32 v159, v159\n"
"v_mfma_f32_16x16x32_bf16 v[172:175], a[188:191], a[28:31], v[172:175]\n"
"ds_read_b128 a[208:211], v250 offset:45056\n"
"v_cvt_pk_bf16_f32 v151, v158, v159\n"
"v_mfma_f32_16x16x32_bf16 v[0:3], a[96:99], v[128:131], v[0:3]\n"
"v_exp_f32 v160, v160\n"
"v_mfma_f32_16x16x32_bf16 v[216:219], a[236:239], v[148:151], v[216:219]\n"
"buffer_load_dwordx4 v248, s[16:19], s28 offen offset:3072 sc1 lds\n"
"v_mfma_f32_16x16x32_bf16 v[4:7], a[100:103], v[128:131], v[4:7]\n"
"v_exp_f32 v161, v161\n"
"v_mfma_f32_16x16x32_bf16 v[8:11], a[104:107], v[128:131], v[8:11]\n"
"ds_read_b128 a[212:215], v250 offset:46080\n"
"v_cvt_pk_bf16_f32 v160, v160, v161\n"
"v_mfma_f32_16x16x32_bf16 v[12:15], a[108:111], v[128:131], v[12:15]\n"
"v_exp_f32 v162, v162\n"
"v_mfma_f32_16x16x32_bf16 v[16:19], a[112:115], v[128:131], v[16:19]\n"
"v_cmp_lt_f32 vcc, s43, v218\n"
"v_mfma_f32_16x16x32_bf16 v[20:23], a[116:119], v[128:131], v[20:23]\n"
"v_exp_f32 v163, v163\n"
"v_mfma_f32_16x16x32_bf16 v[24:27], a[120:123], v[128:131], v[24:27]\n"
"ds_read_b128 a[216:219], v250 offset:47104\n"
"v_cvt_pk_bf16_f32 v161, v162, v163\n"
"v_mfma_f32_16x16x32_bf16 v[28:31], a[124:127], v[128:131], v[28:31]\n"
"v_exp_f32 v164, v164\n"
"v_mfma_f32_16x16x32_bf16 v[32:35], a[96:99], v[132:135], v[32:35]\n"
"s_cbranch_vccnz Lfused_xD2\n"
"Lfused_rD2:\n"
"v_mfma_f32_16x16x32_bf16 v[36:39], a[100:103], v[132:135], v[36:39]\n"
"v_exp_f32 v165, v165\n"
"v_mfma_f32_16x16x32_bf16 v[40:43], a[104:107], v[132:135], v[40:43]\n"
"ds_read_b128 a[220:223], v250 offset:48128\n"
"v_cvt_pk_bf16_f32 v162, v164, v165\n"
"v_xor_b32 v250, 0x4000, v250\n"
"v_mfma_f32_16x16x32_bf16 v[44:47], a[108:111], v[132:135], v[44:47]\n"
"v_exp_f32 v166, v166\n"
"v_mfma_f32_16x16x32_bf16 v[48:51], a[112:115], v[132:135], v[48:51]\n"
"v_mfma_f32_16x16x32_bf16 v[52:55], a[116:119], v[132:135], v[52:55]\n"
"v_exp_f32 v167, v167\n"
"v_mfma_f32_16x16x32_bf16 v[56:59], a[120:123], v[132:135], v[56:59]\n"
"v_cvt_pk_bf16_f32 v163, v166, v167\n"
"v_mfma_f32_16x16x32_bf16 v[60:63], a[124:127], v[132:135], v[60:63]\n"
"Lfused_D3:\n"
"v_mfma_f32_16x16x32_bf16 v[176:179], a[160:163], a[64:67], v[200:203]\n"
"ds_read_b128 a[128:131], v250 offset:0\n"
"s_add_u32 m0, m0, 0x10000\n"
"v_mfma_f32_16x16x32_bf16 v[176:179], a[168:171], a[68:71], v[176:179]\n"
"v_exp_f32 v168, v168\n"
"v_mfma_f32_16x16x32_bf16 v[176:179], a[176:179], a[72:75], v[176:179]\n"
"ds_read_b128 a[132:135], v250 offset:1024\n"
"v_mfma_f32_16x16x32_bf16 v[176:179], a[184:187], a[76:79], v[176:179]\n"
"v_exp_f32 v169, v169\n"
"v_mfma_f32_16x16x32_bf16 v[212:215], a[232:235], v[160:163], v[208:211]\n"
"ds_read_b128 a[136:139], v250 offset:2048\n"
"v_cvt_pk_bf16_f32 v164, v168, v169\n"
"v_mfma_f32_16x16x32_bf16 v[180:183], a[164:167], a[64:67], v[200:203]\n"
"v_exp_f32 v170, v170\n"
"v_mfma_f32_16x16x32_bf16 v[180:183], a[172:175], a[68:71], v[180:183]\n"
"buffer_load_dwordx4 v248, s[20:23], s28 offen offset:0 sc1 lds\n"
"v_mfma_f32_16x16x32_bf16 v[180:183], a[180:183], a[72:75], v[180:183]\n"
"v_exp_f32 v171, v171\n"
"v_mfma_f32_16x16x32_bf16 v[180:183], a[188:191], a[76:79], v[180:183]\n"
"ds_read_b128 a[140:143], v250 offset:3072\n"
"v_cvt_pk_bf16_f32 v165, v170, v171\n"
"v_mfma_f32_16x16x32_bf16 v[184:187], a[160:163], a[80:83], v[204:207]\n"
"v_exp_f32 v172, v172\n"
"v_mfma_f32_16x16x32_bf16 v[184:187], a[168:171], a[84:87], v[184:187]\n"
"buffer_load_dwordx4 v248, s[20:23], s28 offen offset:1024 sc1 lds\n"
"v_mfma_f32_16x16x32_bf16 v[184:187], a[176:179], a[88:91], v[184:187]\n"
"v_exp_f32 v173, v173\n"
"v_mfma_f32_16x16x32_bf16 v[184:187], a[184:187], a[92:95], v[184:187]\n"
"ds_read_b128 a[144:147], v250 offset:4096\n"
"v_cvt_pk_bf16_f32 v166, v172, v173\n"
"v_mfma_f32_16x16x32_bf16 v[188:191], a[164:167], a[80:83], v[204:207]\n"
"v_exp_f32 v174, v174\n"
"v_mfma_f32_16x16x32_bf16 v[188:191], a[172:175], a[84:87], v[188:191]\n"
"buffer_load_dwordx4 v248, s[20:23], s28 offen offset:2048 sc1 lds\n"
"v_mfma_f32_16x16x32_bf16 v[188:191], a[180:183], a[88:91], v[188:191]\n"
"v_exp_f32 v175, v175\n"
"v_mfma_f32_16x16x32_bf16 v[188:191], a[188:191], a[92:95], v[188:191]\n"
"Lfused_hop11:\n"
"s_cbranch_execz Lfused_hop15\n"
"ds_read_b128 a[148:151], v250 offset:5120\n"
"v_cvt_pk_bf16_f32 v167, v174, v175\n"
"v_mfma_f32_16x16x32_bf16 v[64:67], a[96:99], v[144:147], v[64:67]\n"
"v_exp_f32 v176, v176\n"
"v_mfma_f32_16x16x32_bf16 v[212:215], a[236:239], v[164:167], v[212:215]\n"
"buffer_load_dwordx4 v248, s[20:23], s28 offen offset:3072 sc1 lds\n"
"v_mfma_f32_16x16x32_bf16 v[68:71], a[100:103], v[144:147], v[68:71]\n"
"v_exp_f32 v177, v177\n"
"v_mfma_f32_16x16x32_bf16 v[72:75], a[104:107], v[144:147], v[72:75]\n"
"ds_read_b128 a[152:155], v250 offset:6144\n"
"v_cvt_pk_bf16_f32 v176, v176, v177\n"
"v_mfma_f32_16x16x32_bf16 v[76:79], a[108:111], v[144:147], v[76:79]\n"
"v_exp_f32 v178, v178\n"
"v_mfma_f32_16x16x32_bf16 v[80:83], a[112:115], v[144:147], v[80:83]\n"
"v_cmp_lt_f32 vcc, s43, v214\n"
"v_mfma_f32_16x16x32_bf16 v[84:87], a[116:119], v[144:147], v[84:87]\n"
"v_exp_f32 v179, v179\n"
"v_mfma_f32_16x16x32_bf16 v[88:91], a[120:123], v[144:147], v[88:91]\n"
"ds_read_b128 a[156:159], v250 offset:7168\n"
"v_cvt_pk_bf16_f32 v177, v178, v179\n"
"v_mfma_f32_16x16x32_bf16 v[92:95], a[124:127], v[144:147], v[92:95]\n"
"v_exp_f32 v180, v180\n"
"v_mfma_f32_16x16x32_bf16 v[96:99], a[96:99], v[148:151], v[96:99]\n"
"s_cbranch_vccnz Lfused_xD3\n"
"Lfused_rD3:\n"
"v_mfma_f32_16x16x32_bf16 v[100:103], a[100:103], v[148:151], v[100:103]\n"
"v_exp_f32 v181, v181\n"
"v_mfma_f32_16x16x32_bf16 v[104:107], a[104:107], v[148:151], v[104:107]\n"
"v_cvt_pk_bf16_f32 v178, v180, v181\n"
"s_add_u32 s28, s28, 0x4000\n"
"v_mfma_f32_16x16x32_bf16 v[108:111], a[108:111], v[148:151], v[108:111]\n"
"v_exp_f32 v182, v182\n"
"v_mfma_f32_16x16x32_bf16 v[112:115], a[112:115], v[148:151], v[112:115]\n"
"s_xor_b32 s31, s31, 0x4000\n"
"v_mfma_f32_16x16x32_bf16 v[116:119], a[116:119], v[148:151], v[116:119]\n"
"v_exp_f32 v183, v183\n"
"v_mfma_f32_16x16x32_bf16 v[120:123], a[120:123], v[148:151], v[120:123]\n"
"v_cvt_pk_bf16_f32 v179, v182, v183\n"
"s_sub_u32 s29, s29, 1\n"
"v_mfma_f32_16x16x32_bf16 v[124:127], a[124:127], v[148:151], v[124:127]\n"
"s_cbranch_scc0 Lfused_D0\n"
"s_lshr_b32 s58, s13, 3\n"
"s_lshl_b32 s58, s58, 2\n"
"s_add_u32 s58, s58, s12\n"
"v_bfe_u32 v226, v248, 4, 1\n"
"v_lshl_add_u32 v226, v226, 7, s58\n"
"v_lshlrev_b32 v226, 7, v226\n"
"s_getpc_b64 s[58:59]\n"
"Lfused_cte:\n"
"v_min_u32 v226, Lfused_fin-Lfused_cte, v226\n"
"global_load_dword v249, v226, s[58:59]\n"
"Lfused_E0:\n"
"s_waitcnt vmcnt(16) lgkmcnt(0)\n"
"v_mfma_f32_16x16x32_bf16 v[128:131], a[128:131], a[0:3], v[192:195]\n"
"s_barrier\n"
"s_mov_b32 m0, s30\n"
"v_mfma_f32_16x16x32_bf16 v[128:131], a[136:139], a[4:7], v[128:131]\n"
"v_exp_f32 v184, v184\n"
"v_mfma_f32_16x16x32_bf16 v[128:131], a[144:147], a[8:11], v[128:131]\n"
"ds_read_b128 a[96:99], v250 offset:32768\n"
"v_mfma_f32_16x16x32_bf16 v[128:131], a[152:155], a[12:15], v[128:131]\n"
"v_exp_f32 v185, v185\n"
"v_mfma_f32_16x16x32_bf16 v[220:223], a[232:235], v[176:179], v[216:219]\n"
"ds_read_b128 a[100:103], v250 offset:33792\n"
"v_cvt_pk_bf16_f32 v180, v184, v185\n"
"v_mfma_f32_16x16x32_bf16 v[132:135], a[132:135], a[0:3], v[192:195]\n"
"v_exp_f32 v186, v186\n"
"v_mfma_f32_16x16x32_bf16 v[132:135], a[140:143], a[4:7], v[132:135]\n"
"buffer_load_dwordx4 v248, s[16:19], s28 offen offset:0 sc1 lds\n"
"v_mfma_f32_16x16x32_bf16 v[132:135], a[148:151], a[8:11], v[132:135]\n"
"v_exp_f32 v187, v187\n"
"v_mfma_f32_16x16x32_bf16 v[132:135], a[156:159], a[12:15], v[132:135]\n"
"ds_read_b128 a[104:107], v250 offset:34816\n"
"v_cvt_pk_bf16_f32 v181, v186, v187\n"
"v_mfma_f32_16x16x32_bf16 v[136:139], a[128:131], a[16:19], v[196:199]\n"
"v_exp_f32 v188, v188\n"
"v_mfma_f32_16x16x32_bf16 v[136:139], a[136:139], a[20:23], v[136:139]\n"
"buffer_load_dwordx4 v248, s[16:19], s28 offen offset:1024 sc1 lds\n"
"v_mfma_f32_16x16x32_bf16 v[136:139], a[144:147], a[24:27], v[136:139]\n"
"v_exp_f32 v189, v189\n"
"v_mfma_f32_16x16x32_bf16 v[136:139], a[152:155], a[28:31], v[136:139]\n"
"ds_read_b128 a[108:111], v250 offset:35840\n"
"v_cvt_pk_bf16_f32 v182, v188, v189\n"
"v_mfma_f32_16x16x32_bf16 v[140:143], a[132:135], a[16:19], v[196:199]\n"
"v_exp_f32 v190, v190\n"
"v_mfma_f32_16x16x32_bf16 v[140:143], a[140:143], a[20:23], v[140:143]\n"
"buffer_load_dwordx4 v248, s[16:19], s28 offen offset:2048 sc1 lds\n"
"v_mfma_f32_16x16x32_bf16 v[140:143], a[148:151], a[24:27], v[140:143]\n"
"v_exp_f32 v191, v191\n"
"v_mfma_f32_16x16x32_bf16 v[140:143], a[156:159], a[28:31], v[140:143]\n"
"ds_read_b128 a[112:115], v250 offset:36864\n"
"v_cvt_pk_bf16_f32 v183, v190, v191\n"
"v_mfma_f32_16x16x32_bf16 v[0:3], a[192:195], v[160:163], v[0:3]\n"
"v_exp_f32 v128, v128\n"
"v_mfma_f32_16x16x32_bf16 v[220:223], a[236:239], v[180:183], v[220:223]\n"
"buffer_load_dwordx4 v248, s[16:19], s28 offen offset:3072 sc1 lds\n"
"v_mfma_f32_16x16x32_bf16 v[4:7], a[196:199], v[160:163], v[4:7]\n"
"v_exp_f32 v129, v129\n"
"v_mfma_f32_16x16x32_bf16 v[8:11], a[200:203], v[160:163], v[8:11]\n"
"ds_read_b128 a[116:119], v250 offset:37888\n"
"v_cvt_pk_bf16_f32 v128, v128, v129\n"
"v_mfma_f32_16x16x32_bf16 v[12:15], a[204:207], v[160:163], v[12:15]\n"
"v_exp_f32 v130, v130\n"
"v_mfma_f32_16x16x32_bf16 v[16:19], a[208:211], v[160:163], v[16:19]\n"
"v_cmp_lt_f32 vcc, s43, v222\n"
"v_mfma_f32_16x16x32_bf16 v[20:23], a[212:215], v[160:163], v[20:23]\n"
"v_exp_f32 v131, v131\n"
"v_mfma_f32_16x16x32_bf16 v[24:27], a[216:219], v[160:163], v[24:27]\n"
"ds_read_b128 a[120:123], v250 offset:38912\n"
"v_cvt_pk_bf16_f32 v129, v130, v131\n"
"s_cmp_ge_u32 s14, s40\n"
"s_cselect_b32 s34, 0xff800000, 0\n"
"v_mfma_f32_16x16x32_bf16 v[28:31], a[220:223], v[160:163], v[28:31]\n"
"v_exp_f32 v132, v132\n"
"v_mfma_f32_16x16x32_bf16 v[32:35], a[192:195], v[164:167], v[32:35]\n"
"s_cbranch_vccnz Lfused_xE0\n"
"Lfused_rE0:\n"
"s_cmp_gt_u32 s14, s40\n"
"s_cselect_b32 s35, 0xff800000, 0\n"
"v_mfma_f32_16x16x32_bf16 v[36:39], a[196:199], v[164:167], v[36:39]\n"
"v_exp_f32 v133, v133\n"
"v_mfma_f32_16x16x32_bf16 v[40:43], a[200:203], v[164:167], v[40:43]\n"
"ds_read_b128 a[124:127], v250 offset:39936\n"
"v_cvt_pk_bf16_f32 v130, v132, v133\n"
"v_add_f32 v224, s35, v200\n"
"v_mfma_f32_16x16x32_bf16 v[44:47], a[204:207], v[164:167], v[44:47]\n"
"v_exp_f32 v134, v134\n"
"v_mfma_f32_16x16x32_bf16 v[48:51], a[208:211], v[164:167], v[48:51]\n"
"v_add_f32 v225, s34, v204\n"
"v_add_f32 v148, s34, v200\n"
"v_add_f32 v149, s34, v200\n"
"v_add_f32 v150, s34, v200\n"
"v_add_f32 v151, s34, v200\n"
"v_cndmask_b32 v144, v224, v148, s[64:65]\n"
"v_cndmask_b32 v145, v224, v148, s[66:67]\n"
"v_cndmask_b32 v146, v224, v148, s[68:69]\n"
"v_mfma_f32_16x16x32_bf16 v[52:55], a[212:215], v[164:167], v[52:55]\n"
"v_exp_f32 v135, v135\n"
"v_mfma_f32_16x16x32_bf16 v[56:59], a[216:219], v[164:167], v[56:59]\n"
"v_cvt_pk_bf16_f32 v131, v134, v135\n"
"v_cndmask_b32 v147, v224, v148, s[70:71]\n"
"v_add_f32 v152, s35, v204\n"
"v_add_f32 v153, s35, v204\n"
"v_add_f32 v154, s35, v204\n"
"v_add_f32 v155, s35, v204\n"
"v_cndmask_b32 v156, v152, v225, s[64:65]\n"
"v_cndmask_b32 v157, v152, v225, s[66:67]\n"
"v_cndmask_b32 v158, v152, v225, s[68:69]\n"
"v_cndmask_b32 v159, v152, v225, s[70:71]\n"
"v_mfma_f32_16x16x32_bf16 v[60:63], a[220:223], v[164:167], v[60:63]\n"
"Lfused_E1:\n"
"v_mfma_f32_16x16x32_bf16 v[144:147], a[128:131], a[64:67], v[144:147]\n"
"ds_read_b128 a[160:163], v250 offset:8192\n"
"s_add_u32 m0, m0, 0x10000\n"
"v_mfma_f32_16x16x32_bf16 v[144:147], a[136:139], a[68:71], v[144:147]\n"
"v_exp_f32 v136, v136\n"
"v_mfma_f32_16x16x32_bf16 v[144:147], a[144:147], a[72:75], v[144:147]\n"
"ds_read_b128 a[164:167], v250 offset:9216\n"
"v_mfma_f32_16x16x32_bf16 v[144:147], a[152:155], a[76:79], v[144:147]\n"
"v_exp_f32 v137, v137\n"
"v_mfma_f32_16x16x32_bf16 v[208:211], a[232:235], v[128:131], v[212:215]\n"
"ds_read_b128 a[168:171], v250 offset:10240\n"
"v_cvt_pk_bf16_f32 v132, v136, v137\n"
"v_mfma_f32_16x16x32_bf16 v[148:151], a[132:135], a[64:67], v[148:151]\n"
"v_exp_f32 v138, v138\n"
"v_mfma_f32_16x16x32_bf16 v[148:151], a[140:143], a[68:71], v[148:151]\n"
"buffer_load_dwordx4 v248, s[20:23], s28 offen offset:0 sc1 lds\n"
"v_mfma_f32_16x16x32_bf16 v[148:151], a[148:151], a[72:75], v[148:151]\n"
"v_exp_f32 v139, v139\n"
"v_mfma_f32_16x16x32_bf16 v[148:151], a[156:159], a[76:79], v[148:151]\n"
"ds_read_b128 a[172:175], v250 offset:11264\n"
"v_cvt_pk_bf16_f32 v133, v138, v139\n"
"v_mfma_f32_16x16x32_bf16 v[152:155], a[128:131], a[80:83], v[152:155]\n"
"v_exp_f32 v140, v140\n"
"v_mfma_f32_16x16x32_bf16 v[152:155], a[136:139], a[84:87], v[152:155]\n"
"buffer_load_dwordx4 v248, s[20:23], s28 offen offset:1024 sc1 lds\n"
"v_mfma_f32_16x16x32_bf16 v[152:155], a[144:147], a[88:91], v[152:155]\n"
"v_exp_f32 v141, v141\n"
"v_mfma_f32_16x16x32_bf16 v[152:155], a[152:155], a[92:95], v[152:155]\n"
"ds_read_b128 a[176:179], v250 offset:12288\n"
"v_cvt_pk_bf16_f32 v134, v140, v141\n"
"v_mfma_f32_16x16x32_bf16 v[156:159], a[132:135], a[80:83], v[156:159]\n"
"v_exp_f32 v142, v142\n"
"v_mfma_f32_16x16x32_bf16 v[156:159], a[140:143], a[84:87], v[156:159]\n"
"buffer_load_dwordx4 v248, s[20:23], s28 offen offset:2048 sc1 lds\n"
"v_mfma_f32_16x16x32_bf16 v[156:159], a[148:151], a[88:91], v[156:159]\n"
"v_exp_f32 v143, v143\n"
"v_mfma_f32_16x16x32_bf16 v[156:159], a[156:159], a[92:95], v[156:159]\n"
"ds_read_b128 a[180:183], v250 offset:13312\n"
"v_cvt_pk_bf16_f32 v135, v142, v143\n"
"v_mfma_f32_16x16x32_bf16 v[64:67], a[192:195], v[176:179], v[64:67]\n"
"v_exp_f32 v144, v144\n"
"v_mfma_f32_16x16x32_bf16 v[208:211], a[236:239], v[132:135], v[208:211]\n"
"buffer_load_dwordx4 v248, s[20:23], s28 offen offset:3072 sc1 lds\n"
"v_mfma_f32_16x16x32_bf16 v[68:71], a[196:199], v[176:179], v[68:71]\n"
"v_exp_f32 v145, v145\n"
"v_mfma_f32_16x16x32_bf16 v[72:75], a[200:203], v[176:179], v[72:75]\n"
"ds_read_b128 a[184:187], v250 offset:14336\n"
"v_cvt_pk_bf16_f32 v144, v144, v145\n"
"v_mfma_f32_16x16x32_bf16 v[76:79], a[204:207], v[176:179], v[76:79]\n"
"v_exp_f32 v146, v146\n"
"v_mfma_f32_16x16x32_bf16 v[80:83], a[208:211], v[176:179], v[80:83]\n"
"v_cmp_lt_f32 vcc, s43, v210\n"
"v_mfma_f32_16x16x32_bf16 v[84:87], a[212:215], v[176:179], v[84:87]\n"
"v_exp_f32 v147, v147\n"
"v_mfma_f32_16x16x32_bf16 v[88:91], a[216:219], v[176:179], v[88:91]\n"
"ds_read_b128 a[188:191], v250 offset:15360\n"
"v_cvt_pk_bf16_f32 v145, v146, v147\n"
"s_add_u32 s14, s14, 1\n"
"s_cmp_ge_u32 s14, s38\n"
"s_cselect_b32 s34, 0xff800000, 0\n"
"v_mfma_f32_16x16x32_bf16 v[92:95], a[220:223], v[176:179], v[92:95]\n"
"v_exp_f32 v148, v148\n"
"v_mfma_f32_16x16x32_bf16 v[96:99], a[192:195], v[180:183], v[96:99]\n"
"s_cbranch_vccnz Lfused_xE1\n"
"Lfused_rE1:\n"
"s_cmp_gt_u32 s14, s38\n"
"s_cselect_b32 s35, 0xff800000, 0\n"
"v_mfma_f32_16x16x32_bf16 v[100:103], a[196:199], v[180:183], v[100:103]\n"
"v_exp_f32 v149, v149\n"
"v_mfma_f32_16x16x32_bf16 v[104:107], a[200:203], v[180:183], v[104:107]\n"
"v_cvt_pk_bf16_f32 v146, v148, v149\n"
"v_add_f32 v224, s35, v192\n"
"v_mfma_f32_16x16x32_bf16 v[108:111], a[204:207], v[180:183], v[108:111]\n"
"v_exp_f32 v150, v150\n"
"v_mfma_f32_16x16x32_bf16 v[112:115], a[208:211], v[180:183], v[112:115]\n"
"v_add_f32 v225, s34, v196\n"
"v_add_f32 v164, s34, v192\n"
"v_add_f32 v165, s34, v192\n"
"v_add_f32 v166, s34, v192\n"
"v_add_f32 v167, s34, v192\n"
"v_cndmask_b32 v160, v224, v164, s[64:65]\n"
"v_cndmask_b32 v161, v224, v164, s[66:67]\n"
"v_cndmask_b32 v162, v224, v164, s[68:69]\n"
"v_mfma_f32_16x16x32_bf16 v[116:119], a[212:215], v[180:183], v[116:119]\n"
"v_exp_f32 v151, v151\n"
"v_mfma_f32_16x16x32_bf16 v[120:123], a[216:219], v[180:183], v[120:123]\n"
"v_cvt_pk_bf16_f32 v147, v150, v151\n"
"v_cndmask_b32 v163, v224, v164, s[70:71]\n"
"v_add_f32 v168, s35, v196\n"
"v_add_f32 v169, s35, v196\n"
"v_add_f32 v170, s35, v196\n"
"v_add_f32 v171, s35, v196\n"
"v_cndmask_b32 v172, v168, v225, s[64:65]\n"
"v_cndmask_b32 v173, v168, v225, s[66:67]\n"
"v_cndmask_b32 v174, v168, v225, s[68:69]\n"
"v_cndmask_b32 v175, v168, v225, s[70:71]\n"
"v_mfma_f32_16x16x32_bf16 v[124:127], a[220:223], v[180:183], v[124:127]\n"
"Lfused_E2:\n"
"s_waitcnt vmcnt(16) lgkmcnt(0)\n"
"v_mfma_f32_16x16x32_bf16 v[160:163], a[160:163], a[0:3], v[160:163]\n"
"s_barrier\n"
"s_add_u32 m0, s44, 81920\n"
"s_add_u32 s55, s45, 32768\n"
"v_mfma_f32_16x16x32_bf16 v[160:163], a[168:171], a[4:7], v[160:163]\n"
"v_exp_f32 v152, v152\n"
"v_mfma_f32_16x16x32_bf16 v[160:163], a[176:179], a[8:11], v[160:163]\n"
"ds_read_b128 a[192:195], v250 offset:40960\n"
"v_mfma_f32_16x16x32_bf16 v[160:163], a[184:187], a[12:15], v[160:163]\n"
"v_exp_f32 v153, v153\n"
"v_mfma_f32_16x16x32_bf16 v[216:219], a[232:235], v[144:147], v[220:223]\n"
"ds_read_b128 a[196:199], v250 offset:41984\n"
"v_cvt_pk_bf16_f32 v148, v152, v153\n"
"v_mfma_f32_16x16x32_bf16 v[164:167], a[164:167], a[0:3], v[164:167]\n"
"v_exp_f32 v154, v154\n"
"v_mfma_f32_16x16x32_bf16 v[164:167], a[172:175], a[4:7], v[164:167]\n"
"buffer_load_dwordx4 v248, s[72:75], s55 offen offset:0 sc1 lds\n"
"v_mfma_f32_16x16x32_bf16 v[164:167], a[180:183], a[8:11], v[164:167]\n"
"v_exp_f32 v155, v155\n"
"v_mfma_f32_16x16x32_bf16 v[164:167], a[188:191], a[12:15], v[164:167]\n"
"ds_read_b128 a[200:203], v250 offset:43008\n"
"v_cvt_pk_bf16_f32 v149, v154, v155\n"
"v_mfma_f32_16x16x32_bf16 v[168:171], a[160:163], a[16:19], v[168:171]\n"
"v_exp_f32 v156, v156\n"
"v_mfma_f32_16x16x32_bf16 v[168:171], a[168:171], a[20:23], v[168:171]\n"
"buffer_load_dwordx4 v248, s[72:75], s55 offen offset:1024 sc1 lds\n"
"v_mfma_f32_16x16x32_bf16 v[168:171], a[176:179], a[24:27], v[168:171]\n"
"v_exp_f32 v157, v157\n"
"v_mfma_f32_16x16x32_bf16 v[168:171], a[184:187], a[28:31], v[168:171]\n"
"ds_read_b128 a[204:207], v250 offset:44032\n"
"v_cvt_pk_bf16_f32 v150, v156, v157\n"
"v_mfma_f32_16x16x32_bf16 v[172:175], a[164:167], a[16:19], v[172:175]\n"
"v_exp_f32 v158, v158\n"
"v_mfma_f32_16x16x32_bf16 v[172:175], a[172:175], a[20:23], v[172:175]\n"
"buffer_load_dwordx4 v248, s[72:75], s55 offen offset:2048 sc1 lds\n"
"v_mfma_f32_16x16x32_bf16 v[172:175], a[180:183], a[24:27], v[172:175]\n"
"v_exp_f32 v159, v159\n"
"v_mfma_f32_16x16x32_bf16 v[172:175], a[188:191], a[28:31], v[172:175]\n"
"ds_read_b128 a[208:211], v250 offset:45056\n"
"v_cvt_pk_bf16_f32 v151, v158, v159\n"
"v_mfma_f32_16x16x32_bf16 v[0:3], a[96:99], v[128:131], v[0:3]\n"
"v_exp_f32 v160, v160\n"
"v_mfma_f32_16x16x32_bf16 v[216:219], a[236:239], v[148:151], v[216:219]\n"
"buffer_load_dwordx4 v248, s[72:75], s55 offen offset:3072 sc1 lds\n"
"v_mfma_f32_16x16x32_bf16 v[4:7], a[100:103], v[128:131], v[4:7]\n"
"v_exp_f32 v161, v161\n"
"v_mfma_f32_16x16x32_bf16 v[8:11], a[104:107], v[128:131], v[8:11]\n"
"ds_read_b128 a[212:215], v250 offset:46080\n"
"v_cvt_pk_bf16_f32 v160, v160, v161\n"
"v_mfma_f32_16x16x32_bf16 v[12:15], a[108:111], v[128:131], v[12:15]\n"
"v_exp_f32 v162, v162\n"
"v_mfma_f32_16x16x32_bf16 v[16:19], a[112:115], v[128:131], v[16:19]\n"
"v_cmp_lt_f32 vcc, s43, v218\n"
"v_mfma_f32_16x16x32_bf16 v[20:23], a[116:119], v[128:131], v[20:23]\n"
"v_exp_f32 v163, v163\n"
"v_mfma_f32_16x16x32_bf16 v[24:27], a[120:123], v[128:131], v[24:27]\n"
"ds_read_b128 a[216:219], v250 offset:47104\n"
"v_cvt_pk_bf16_f32 v161, v162, v163\n"
"s_cmp_ge_u32 s14, s40\n"
"s_cselect_b32 s34, 0xff800000, 0\n"
"v_mfma_f32_16x16x32_bf16 v[28:31], a[124:127], v[128:131], v[28:31]\n"
"v_exp_f32 v164, v164\n"
"v_mfma_f32_16x16x32_bf16 v[32:35], a[96:99], v[132:135], v[32:35]\n"
"s_cbranch_vccnz Lfused_xE2\n"
"Lfused_rE2:\n"
"s_cmp_gt_u32 s14, s40\n"
"s_cselect_b32 s35, 0xff800000, 0\n"
"v_mfma_f32_16x16x32_bf16 v[36:39], a[100:103], v[132:135], v[36:39]\n"
"v_exp_f32 v165, v165\n"
"v_mfma_f32_16x16x32_bf16 v[40:43], a[104:107], v[132:135], v[40:43]\n"
"ds_read_b128 a[220:223], v250 offset:48128\n"
"v_cvt_pk_bf16_f32 v162, v164, v165\n"
"v_xor_b32 v250, 0x4000, v250\n"
"v_add_f32 v224, s35, v200\n"
"v_mfma_f32_16x16x32_bf16 v[44:47], a[108:111], v[132:135], v[44:47]\n"
"v_exp_f32 v166, v166\n"
"v_mfma_f32_16x16x32_bf16 v[48:51], a[112:115], v[132:135], v[48:51]\n"
"v_add_f32 v225, s34, v204\n"
"v_add_f32 v180, s34, v200\n"
"v_add_f32 v181, s34, v200\n"
"v_add_f32 v182, s34, v200\n"
"v_add_f32 v183, s34, v200\n"
"v_cndmask_b32 v176, v224, v180, s[64:65]\n"
"v_cndmask_b32 v177, v224, v180, s[66:67]\n"
"v_cndmask_b32 v178, v224, v180, s[68:69]\n"
"v_mfma_f32_16x16x32_bf16 v[52:55], a[116:119], v[132:135], v[52:55]\n"
"v_exp_f32 v167, v167\n"
"v_mfma_f32_16x16x32_bf16 v[56:59], a[120:123], v[132:135], v[56:59]\n"
"v_cvt_pk_bf16_f32 v163, v166, v167\n"
"v_cndmask_b32 v179, v224, v180, s[70:71]\n"
"v_add_f32 v184, s35, v204\n"
"v_add_f32 v185, s35, v204\n"
"v_add_f32 v186, s35, v204\n"
"v_add_f32 v187, s35, v204\n"
"v_cndmask_b32 v188, v184, v225, s[64:65]\n"
"v_cndmask_b32 v189, v184, v225, s[66:67]\n"
"v_cndmask_b32 v190, v184, v225, s[68:69]\n"
"v_cndmask_b32 v191, v184, v225, s[70:71]\n"
"v_mfma_f32_16x16x32_bf16 v[60:63], a[124:127], v[132:135], v[60:63]\n"
"Lfused_E3:\n"
"v_mfma_f32_16x16x32_bf16 v[176:179], a[160:163], a[64:67], v[176:179]\n"
"ds_read_b128 a[128:131], v250 offset:0\n"
"s_add_u32 s55, s79, 0\n"
"v_mfma_f32_16x16x32_bf16 v[176:179], a[168:171], a[68:71], v[176:179]\n"
"v_exp_f32 v168, v168\n"
"v_mfma_f32_16x16x32_bf16 v[176:179], a[176:179], a[72:75], v[176:179]\n"
"ds_read_b128 a[132:135], v250 offset:1024\n"
"v_mfma_f32_16x16x32_bf16 v[176:179], a[184:187], a[76:79], v[176:179]\n"
"v_exp_f32 v169, v169\n"
"v_mfma_f32_16x16x32_bf16 v[212:215], a[232:235], v[160:163], v[208:211]\n"
"ds_read_b128 a[136:139], v250 offset:2048\n"
"v_cvt_pk_bf16_f32 v164, v168, v169\n"
"v_mfma_f32_16x16x32_bf16 v[180:183], a[164:167], a[64:67], v[180:183]\n"
"v_exp_f32 v170, v170\n"
"v_mfma_f32_16x16x32_bf16 v[180:183], a[172:175], a[68:71], v[180:183]\n"
"buffer_load_dwordx4 a[32:35], v248, s[72:75], s55 offen offset:0 sc1\n"
"v_mfma_f32_16x16x32_bf16 v[180:183], a[180:183], a[72:75], v[180:183]\n"
"v_exp_f32 v171, v171\n"
"v_mfma_f32_16x16x32_bf16 v[180:183], a[188:191], a[76:79], v[180:183]\n"
"ds_read_b128 a[140:143], v250 offset:3072\n"
"v_cvt_pk_bf16_f32 v165, v170, v171\n"
"v_mfma_f32_16x16x32_bf16 v[184:187], a[160:163], a[80:83], v[184:187]\n"
"v_exp_f32 v172, v172\n"
"v_mfma_f32_16x16x32_bf16 v[184:187], a[168:171], a[84:87], v[184:187]\n"
"buffer_load_dwordx4 a[36:39], v248, s[72:75], s55 offen offset:1024 sc1\n"
"v_mfma_f32_16x16x32_bf16 v[184:187], a[176:179], a[88:91], v[184:187]\n"
"v_exp_f32 v173, v173\n"
"v_mfma_f32_16x16x32_bf16 v[184:187], a[184:187], a[92:95], v[184:187]\n"
"ds_read_b128 a[144:147], v250 offset:4096\n"
"v_cvt_pk_bf16_f32 v166, v172, v173\n"
"v_mfma_f32_16x16x32_bf16 v[188:191], a[164:167], a[80:83], v[188:191]\n"
"v_exp_f32 v174, v174\n"
"v_mfma_f32_16x16x32_bf16 v[188:191], a[172:175], a[84:87], v[188:191]\n"
"buffer_load_dwordx4 a[40:43], v248, s[72:75], s55 offen offset:2048 sc1\n"
"v_mfma_f32_16x16x32_bf16 v[188:191], a[180:183], a[88:91], v[188:191]\n"
"v_exp_f32 v175, v175\n"
"v_mfma_f32_16x16x32_bf16 v[188:191], a[188:191], a[92:95], v[188:191]\n"
"ds_read_b128 a[148:151], v250 offset:5120\n"
"v_cvt_pk_bf16_f32 v167, v174, v175\n"
"v_mfma_f32_16x16x32_bf16 v[64:67], a[96:99], v[144:147], v[64:67]\n"
"v_exp_f32 v176, v176\n"
"v_mfma_f32_16x16x32_bf16 v[212:215], a[236:239], v[164:167], v[212:215]\n"
"buffer_load_dwordx4 a[44:47], v248, s[72:75], s55 offen offset:3072 sc1\n"
"v_mfma_f32_16x16x32_bf16 v[68:71], a[100:103], v[144:147], v[68:71]\n"
"v_exp_f32 v177, v177\n"
"v_mfma_f32_16x16x32_bf16 v[72:75], a[104:107], v[144:147], v[72:75]\n"
"ds_read_b128 a[152:155], v250 offset:6144\n"
"v_cvt_pk_bf16_f32 v176, v176, v177\n"
"v_mfma_f32_16x16x32_bf16 v[76:79], a[108:111], v[144:147], v[76:79]\n"
"v_exp_f32 v178, v178\n"
"v_mfma_f32_16x16x32_bf16 v[80:83], a[112:115], v[144:147], v[80:83]\n"
"v_cmp_lt_f32 vcc, s43, v214\n"
"v_mfma_f32_16x16x32_bf16 v[84:87], a[116:119], v[144:147], v[84:87]\n"
"v_exp_f32 v179, v179\n"
"v_mfma_f32_16x16x32_bf16 v[88:91], a[120:123], v[144:147], v[88:91]\n"
"ds_read_b128 a[156:159], v250 offset:7168\n"
"v_cvt_pk_bf16_f32 v177, v178, v179\n"
"s_add_u32 s14, s14, 1\n"
"s_cmp_ge_u32 s14, s38\n"
"s_cselect_b32 s34, 0xff800000, 0\n"
"v_mfma_f32_16x16x32_bf16 v[92:95], a[124:127], v[144:147], v[92:95]\n"
"v_exp_f32 v180, v180\n"
"v_mfma_f32_16x16x32_bf16 v[96:99], a[96:99], v[148:151], v[96:99]\n"
"s_cbranch_vccnz Lfused_xE3\n"
"Lfused_rE3:\n"
"s_cmp_gt_u32 s14, s38\n"
"s_cselect_b32 s35, 0xff800000, 0\n"
"v_mfma_f32_16x16x32_bf16 v[100:103], a[100:103], v[148:151], v[100:103]\n"
"v_exp_f32 v181, v181\n"
"v_mfma_f32_16x16x32_bf16 v[104:107], a[104:107], v[148:151], v[104:107]\n"
"v_cvt_pk_bf16_f32 v178, v180, v181\n"
"v_add_f32 v224, s35, v192\n"
"v_mfma_f32_16x16x32_bf16 v[108:111], a[108:111], v[148:151], v[108:111]\n"
"v_exp_f32 v182, v182\n"
"v_mfma_f32_16x16x32_bf16 v[112:115], a[112:115], v[148:151], v[112:115]\n"
"v_add_f32 v225, s34, v196\n"
"v_add_f32 v132, s34, v192\n"
"v_add_f32 v133, s34, v192\n"
"v_add_f32 v134, s34, v192\n"
"v_add_f32 v135, s34, v192\n"
"v_cndmask_b32 v128, v224, v132, s[64:65]\n"
"v_cndmask_b32 v129, v224, v132, s[66:67]\n"
"v_cndmask_b32 v130, v224, v132, s[68:69]\n"
"v_mfma_f32_16x16x32_bf16 v[116:119], a[116:119], v[148:151], v[116:119]\n"
"v_exp_f32 v183, v183\n"
"v_mfma_f32_16x16x32_bf16 v[120:123], a[120:123], v[148:151], v[120:123]\n"
"v_cvt_pk_bf16_f32 v179, v182, v183\n"
"v_cndmask_b32 v131, v224, v132, s[70:71]\n"
"v_add_f32 v136, s35, v196\n"
"v_add_f32 v137, s35, v196\n"
"v_add_f32 v138, s35, v196\n"
"v_add_f32 v139, s35, v196\n"
"v_cndmask_b32 v140, v136, v225, s[64:65]\n"
"v_cndmask_b32 v141, v136, v225, s[66:67]\n"
"v_cndmask_b32 v142, v136, v225, s[68:69]\n"
"v_cndmask_b32 v143, v136, v225, s[70:71]\n"
"v_mfma_f32_16x16x32_bf16 v[124:127], a[124:127], v[148:151], v[124:127]\n"
"Lfused_F0:\n"
"s_waitcnt vmcnt(16) lgkmcnt(0)\n"
"v_mfma_f32_16x16x32_bf16 v[128:131], a[128:131], a[0:3], v[128:131]\n"
"s_barrier\n"
"s_add_u32 m0, s44, 90112\n"
"s_add_u32 s55, s45, 40960\n"
"v_mfma_f32_16x16x32_bf16 v[128:131], a[136:139], a[4:7], v[128:131]\n"
"v_exp_f32 v184, v184\n"
"v_mfma_f32_16x16x32_bf16 v[128:131], a[144:147], a[8:11], v[128:131]\n"
"ds_read_b128 a[96:99], v250 offset:32768\n"
"v_mfma_f32_16x16x32_bf16 v[128:131], a[152:155], a[12:15], v[128:131]\n"
"v_exp_f32 v185, v185\n"
"v_mfma_f32_16x16x32_bf16 v[220:223], a[232:235], v[176:179], v[216:219]\n"
"ds_read_b128 a[100:103], v250 offset:33792\n"
"v_cvt_pk_bf16_f32 v180, v184, v185\n"
"v_mfma_f32_16x16x32_bf16 v[132:135], a[132:135], a[0:3], v[132:135]\n"
"v_exp_f32 v186, v186\n"
"v_mfma_f32_16x16x32_bf16 v[132:135], a[140:143], a[4:7], v[132:135]\n"
"buffer_load_dwordx4 v248, s[72:75], s55 offen offset:0 sc1 lds\n"
"v_mfma_f32_16x16x32_bf16 v[132:135], a[148:151], a[8:11], v[132:135]\n"
"v_exp_f32 v187, v187\n"
"v_mfma_f32_16x16x32_bf16 v[132:135], a[156:159], a[12:15], v[132:135]\n"
"ds_read_b128 a[104:107], v250 offset:34816\n"
"v_cvt_pk_bf16_f32 v181, v186, v187\n"
"v_mfma_f32_16x16x32_bf16 v[136:139], a[128:131], a[16:19], v[136:139]\n"
"v_exp_f32 v188, v188\n"
"v_mfma_f32_16x16x32_bf16 v[136:139], a[136:139], a[20:23], v[136:139]\n"
"buffer_load_dwordx4 v248, s[72:75], s55 offen offset:1024 sc1 lds\n"
"v_mfma_f32_16x16x32_bf16 v[136:139], a[144:147], a[24:27], v[136:139]\n"
"v_exp_f32 v189, v189\n"
"v_mfma_f32_16x16x32_bf16 v[136:139], a[152:155], a[28:31], v[136:139]\n"
"ds_read_b128 a[108:111], v250 offset:35840\n"
"v_cvt_pk_bf16_f32 v182, v188, v189\n"
"v_mfma_f32_16x16x32_bf16 v[140:143], a[132:135], a[16:19], v[140:143]\n"
"v_exp_f32 v190, v190\n"
"v_mfma_f32_16x16x32_bf16 v[140:143], a[140:143], a[20:23], v[140:143]\n"
"buffer_load_dwordx4 v248, s[72:75], s55 offen offset:2048 sc1 lds\n"
"v_mfma_f32_16x16x32_bf16 v[140:143], a[148:151], a[24:27], v[140:143]\n"
"v_exp_f32 v191, v191\n"
"v_mfma_f32_16x16x32_bf16 v[140:143], a[156:159], a[28:31], v[140:143]\n"
"ds_read_b128 a[112:115], v250 offset:36864\n"
"v_cvt_pk_bf16_f32 v183, v190, v191\n"
"v_mfma_f32_16x16x32_bf16 v[0:3], a[192:195], v[160:163], v[0:3]\n"
"v_exp_f32 v128, v128\n"
"v_mfma_f32_16x16x32_bf16 v[220:223], a[236:239], v[180:183], v[220:223]\n"
"buffer_load_dwordx4 v248, s[72:75], s55 offen offset:3072 sc1 lds\n"
"v_mfma_f32_16x16x32_bf16 v[4:7], a[196:199], v[160:163], v[4:7]\n"
"v_exp_f32 v129, v129\n"
"v_mfma_f32_16x16x32_bf16 v[8:11], a[200:203], v[160:163], v[8:11]\n"
"ds_read_b128 a[116:119], v250 offset:37888\n"
"v_cvt_pk_bf16_f32 v128, v128, v129\n"
"v_mfma_f32_16x16x32_bf16 v[12:15], a[204:207], v[160:163], v[12:15]\n"
"v_exp_f32 v130, v130\n"
"v_mfma_f32_16x16x32_bf16 v[16:19], a[208:211], v[160:163], v[16:19]\n"
"v_cmp_lt_f32 vcc, s43, v222\n"
"v_mfma_f32_16x16x32_bf16 v[20:23], a[212:215], v[160:163], v[20:23]\n"
"v_exp_f32 v131, v131\n"
"v_mfma_f32_16x16x32_bf16 v[24:27], a[216:219], v[160:163], v[24:27]\n"
"ds_read_b128 a[120:123], v250 offset:38912\n"
"v_cvt_pk_bf16_f32 v129, v130, v131\n"
"s_cmp_ge_u32 s14, s40\n"
"s_cselect_b32 s34, 0xff800000, 0\n"
"v_mfma_f32_16x16x32_bf16 v[28:31], a[220:223], v[160:163], v[28:31]\n"
"v_exp_f32 v132, v132\n"
"v_mfma_f32_16x16x32_bf16 v[32:35], a[192:195], v[164:167], v[32:35]\n"
"s_cbranch_vccnz Lfused_xF0\n"
"Lfused_rF0:\n"
"s_cmp_gt_u32 s14, s40\n"
"s_cselect_b32 s35, 0xff800000, 0\n"
"v_mfma_f32_16x16x32_bf16 v[36:39], a[196:199], v[164:167], v[36:39]\n"
"v_exp_f32 v133, v133\n"
"v_mfma_f32_16x16x32_bf16 v[40:43], a[200:203], v[164:167], v[40:43]\n"
"ds_read_b128 a[124:127], v250 offset:39936\n"
"v_cvt_pk_bf16_f32 v130, v132, v133\n"
"v_add_f32 v224, s35, v200\n"
"v_mfma_f32_16x16x32_bf16 v[44:47], a[204:207], v[164:167], v[44:47]\n"
"v_exp_f32 v134, v134\n"
"v_mfma_f32_16x16x32_bf16 v[48:51], a[208:211], v[164:167], v[48:51]\n"
"v_add_f32 v225, s34, v204\n"
"v_add_f32 v148, s34, v200\n"
"v_add_f32 v149, s34, v200\n"
"v_add_f32 v150, s34, v200\n"
"v_add_f32 v151, s34, v200\n"
"v_cndmask_b32 v144, v224, v148, s[64:65]\n"
"v_cndmask_b32 v145, v224, v148, s[66:67]\n"
"v_cndmask_b32 v146, v224, v148, s[68:69]\n"
"v_mfma_f32_16x16x32_bf16 v[52:55], a[212:215], v[164:167], v[52:55]\n"
"v_exp_f32 v135, v135\n"
"v_mfma_f32_16x16x32_bf16 v[56:59], a[216:219], v[164:167], v[56:59]\n"
"v_cvt_pk_bf16_f32 v131, v134, v135\n"
"v_cndmask_b32 v147, v224, v148, s[70:71]\n"
"v_add_f32 v152, s35, v204\n"
"v_add_f32 v153, s35, v204\n"
"v_add_f32 v154, s35, v204\n"
"v_add_f32 v155, s35, v204\n"
"v_cndmask_b32 v156, v152, v225, s[64:65]\n"
"v_cndmask_b32 v157, v152, v225, s[66:67]\n"
"v_cndmask_b32 v158, v152, v225, s[68:69]\n"
"v_cndmask_b32 v159, v152, v225, s[70:71]\n"
"v_mfma_f32_16x16x32_bf16 v[60:63], a[220:223], v[164:167], v[60:63]\n"
"Lfused_F1:\n"
"v_mfma_f32_16x16x32_bf16 v[144:147], a[128:131], a[64:67], v[144:147]\n"
"ds_read_b128 a[160:163], v250 offset:8192\n"
"s_add_u32 s55, s79, 4096\n"
"v_mfma_f32_16x16x32_bf16 v[144:147], a[136:139], a[68:71], v[144:147]\n"
"v_exp_f32 v136, v136\n"
"v_mfma_f32_16x16x32_bf16 v[144:147], a[144:147], a[72:75], v[144:147]\n"
"ds_read_b128 a[164:167], v250 offset:9216\n"
"v_mfma_f32_16x16x32_bf16 v[144:147], a[152:155], a[76:79], v[144:147]\n"
"v_exp_f32 v137, v137\n"
"v_mfma_f32_16x16x32_bf16 v[208:211], a[232:235], v[128:131], v[212:215]\n"
"ds_read_b128 a[168:171], v250 offset:10240\n"
"v_cvt_pk_bf16_f32 v132, v136, v137\n"
"v_mfma_f32_16x16x32_bf16 v[148:151], a[132:135], a[64:67], v[148:151]\n"
"v_exp_f32 v138, v138\n"
"v_mfma_f32_16x16x32_bf16 v[148:151], a[140:143], a[68:71], v[148:151]\n"
"buffer_load_dwordx4 a[48:51], v248, s[72:75], s55 offen offset:0 sc1\n"
"v_mfma_f32_16x16x32_bf16 v[148:151], a[148:151], a[72:75], v[148:151]\n"
"v_exp_f32 v139, v139\n"
"v_mfma_f32_16x16x32_bf16 v[148:151], a[156:159], a[76:79], v[148:151]\n"
"ds_read_b128 a[172:175], v250 offset:11264\n"
"v_cvt_pk_bf16_f32 v133, v138, v139\n"
"v_mfma_f32_16x16x32_bf16 v[152:155], a[128:131], a[80:83], v[152:155]\n"
"v_exp_f32 v140, v140\n"
"v_mfma_f32_16x16x32_bf16 v[152:155], a[136:139], a[84:87], v[152:155]\n"
"buffer_load_dwordx4 a[52:55], v248, s[72:75], s55 offen offset:1024 sc1\n"
"v_mfma_f32_16x16x32_bf16 v[152:155], a[144:147], a[88:91], v[152:155]\n"
"v_exp_f32 v141, v141\n"
"v_mfma_f32_16x16x32_bf16 v[152:155], a[152:155], a[92:95], v[152:155]\n"
"ds_read_b128 a[176:179], v250 offset:12288\n"
"v_cvt_pk_bf16_f32 v134, v140, v141\n"
"v_mfma_f32_16x16x32_bf16 v[156:159], a[132:135], a[80:83], v[156:159]\n"
"v_exp_f32 v142, v142\n"
"v_mfma_f32_16x16x32_bf16 v[156:159], a[140:143], a[84:87], v[156:159]\n"
"buffer_load_dwordx4 a[56:59], v248, s[72:75], s55 offen offset:2048 sc1\n"
"v_mfma_f32_16x16x32_bf16 v[156:159], a[148:151], a[88:91], v[156:159]\n"
"v_exp_f32 v143, v143\n"
"v_mfma_f32_16x16x32_bf16 v[156:159], a[156:159], a[92:95], v[156:159]\n"
"ds_read_b128 a[180:183], v250 offset:13312\n"
"v_cvt_pk_bf16_f32 v135, v142, v143\n"
"v_mfma_f32_16x16x32_bf16 v[64:67], a[192:195], v[176:179], v[64:67]\n"
"v_exp_f32 v144, v144\n"
"v_mfma_f32_16x16x32_bf16 v[208:211], a[236:239], v[132:135], v[208:211]\n"
"buffer_load_dwordx4 a[60:63], v248, s[72:75], s55 offen offset:3072 sc1\n"
"v_mfma_f32_16x16x32_bf16 v[68:71], a[196:199], v[176:179], v[68:71]\n"
"v_exp_f32 v145, v145\n"
"v_mfma_f32_16x16x32_bf16 v[72:75], a[200:203], v[176:179], v[72:75]\n"
"ds_read_b128 a[184:187], v250 offset:14336\n"
"v_cvt_pk_bf16_f32 v144, v144, v145\n"
"v_mfma_f32_16x16x32_bf16 v[76:79], a[204:207], v[176:179], v[76:79]\n"
"v_exp_f32 v146, v146\n"
"v_mfma_f32_16x16x32_bf16 v[80:83], a[208:211], v[176:179], v[80:83]\n"
"v_cmp_lt_f32 vcc, s43, v210\n"
"v_mfma_f32_16x16x32_bf16 v[84:87], a[212:215], v[176:179], v[84:87]\n"
"v_exp_f32 v147, v147\n"
"v_mfma_f32_16x16x32_bf16 v[88:91], a[216:219], v[176:179], v[88:91]\n"
"ds_read_b128 a[188:191], v250 offset:15360\n"
"v_cvt_pk_bf16_f32 v145, v146, v147\n"
"s_add_u32 s14, s14, 1\n"
"s_cmp_ge_u32 s14, s38\n"
"s_cselect_b32 s34, 0xff800000, 0\n"
"v_mfma_f32_16x16x32_bf16 v[92:95], a[220:223], v[176:179], v[92:95]\n"
"v_exp_f32 v148, v148\n"
"v_mfma_f32_16x16x32_bf16 v[96:99], a[192:195], v[180:183], v[96:99]\n"
"s_cbranch_vccnz Lfused_xF1\n"
"Lfused_rF1:\n"
"s_cmp_gt_u32 s14, s38\n"
"s_cselect_b32 s35, 0xff800000, 0\n"
"v_mfma_f32_16x16x32_bf16 v[100:103], a[196:199], v[180:183], v[100:103]\n"
"v_exp_f32 v149, v149\n"
"v_mfma_f32_16x16x32_bf16 v[104:107], a[200:203], v[180:183], v[104:107]\n"
"v_cvt_pk_bf16_f32 v146, v148, v149\n"
"v_add_f32 v224, s35, v192\n"
"v_mfma_f32_16x16x32_bf16 v[108:111], a[204:207], v[180:183], v[108:111]\n"
"v_exp_f32 v150, v150\n"
"v_mfma_f32_16x16x32_bf16 v[112:115], a[208:211], v[180:183], v[112:115]\n"
"v_add_f32 v225, s34, v196\n"
"v_add_f32 v164, s34, v192\n"
"v_add_f32 v165, s34, v192\n"
"v_add_f32 v166, s34, v192\n"
"v_add_f32 v167, s34, v192\n"
"v_cndmask_b32 v160, v224, v164, s[64:65]\n"
"v_cndmask_b32 v161, v224, v164, s[66:67]\n"
"v_cndmask_b32 v162, v224, v164, s[68:69]\n"
"v_mfma_f32_16x16x32_bf16 v[116:119], a[212:215], v[180:183], v[116:119]\n"
"Lfused_hop12:\n"
"s_cbranch_execz Lfused_hopend\n"
"v_exp_f32 v151, v151\n"
"v_mfma_f32_16x16x32_bf16 v[120:123], a[216:219], v[180:183], v[120:123]\n"
"v_cvt_pk_bf16_f32 v147, v150, v151\n"
"v_cndmask_b32 v163, v224, v164, s[70:71]\n"
"v_add_f32 v168, s35, v196\n"
"v_add_f32 v169, s35, v196\n"
"v_add_f32 v170, s35, v196\n"
"v_add_f32 v171, s35, v196\n"
"v_cndmask_b32 v172, v168, v225, s[64:65]\n"
"v_cndmask_b32 v173, v168, v225, s[66:67]\n"
"v_cndmask_b32 v174, v168, v225, s[68:69]\n"
"v_cndmask_b32 v175, v168, v225, s[70:71]\n"
"v_mfma_f32_16x16x32_bf16 v[124:127], a[220:223], v[180:183], v[124:127]\n"
"Lfused_F2:\n"
"s_waitcnt vmcnt(16) lgkmcnt(0)\n"
"v_mfma_f32_16x16x32_bf16 v[160:163], a[160:163], a[0:3], v[160:163]\n"
"s_barrier\n"
"s_add_u32 m0, s44, 65536\n"
"s_add_u32 s55, s45, 49152\n"
"v_mfma_f32_16x16x32_bf16 v[160:163], a[168:171], a[4:7], v[160:163]\n"
"v_exp_f32 v152, v152\n"
"v_mfma_f32_16x16x32_bf16 v[160:163], a[176:179], a[8:11], v[160:163]\n"
"ds_read_b128 a[192:195], v250 offset:40960\n"
"v_mfma_f32_16x16x32_bf16 v[160:163], a[184:187], a[12:15], v[160:163]\n"
"v_exp_f32 v153, v153\n"
"v_mfma_f32_16x16x32_bf16 v[216:219], a[232:235], v[144:147], v[220:223]\n"
"ds_read_b128 a[196:199], v250 offset:41984\n"
"v_cvt_pk_bf16_f32 v148, v152, v153\n"
"v_mfma_f32_16x16x32_bf16 v[164:167], a[164:167], a[0:3], v[164:167]\n"
"v_exp_f32 v154, v154\n"
"v_mfma_f32_16x16x32_bf16 v[164:167], a[172:175], a[4:7], v[164:167]\n"
"buffer_load_dwordx4 v248, s[72:75], s55 offen offset:0 sc1 lds\n"
"v_mfma_f32_16x16x32_bf16 v[164:167], a[180:183], a[8:11], v[164:167]\n"
"v_exp_f32 v155, v155\n"
"v_mfma_f32_16x16x32_bf16 v[164:167], a[188:191], a[12:15], v[164:167]\n"
"ds_read_b128 a[200:203], v250 offset:43008\n"
"v_cvt_pk_bf16_f32 v149, v154, v155\n"
"v_mfma_f32_16x16x32_bf16 v[168:171], a[160:163], a[16:19], v[168:171]\n"
"v_exp_f32 v156, v156\n"
"v_mfma_f32_16x16x32_bf16 v[168:171], a[168:171], a[20:23], v[168:171]\n"
"buffer_load_dwordx4 v248, s[72:75], s55 offen offset:1024 sc1 lds\n"
"v_mfma_f32_16x16x32_bf16 v[168:171], a[176:179], a[24:27], v[168:171]\n"
"v_exp_f32 v157, v157\n"
"v_mfma_f32_16x16x32_bf16 v[168:171], a[184:187], a[28:31], v[168:171]\n"
"ds_read_b128 a[204:207], v250 offset:44032\n"
"v_cvt_pk_bf16_f32 v150, v156, v157\n"
"v_mfma_f32_16x16x32_bf16 v[172:175], a[164:167], a[16:19], v[172:175]\n"
"v_exp_f32 v158, v158\n"
"v_mfma_f32_16x16x32_bf16 v[172:175], a[172:175], a[20:23], v[172:175]\n"
"buffer_load_dwordx4 v248, s[72:75], s55 offen offset:2048 sc1 lds\n"
"v_mfma_f32_16x16x32_bf16 v[172:175], a[180:183], a[24:27], v[172:175]\n"
"v_exp_f32 v159, v159\n"
"v_mfma_f32_16x16x32_bf16 v[172:175], a[188:191], a[28:31], v[172:175]\n"
"ds_read_b128 a[208:211], v250 offset:45056\n"
"v_cvt_pk_bf16_f32 v151, v158, v159\n"
"v_mfma_f32_16x16x32_bf16 v[0:3], a[96:99], v[128:131], v[0:3]\n"
"v_exp_f32 v160, v160\n"
"v_mfma_f32_16x16x32_bf16 v[216:219], a[236:239], v[148:151], v[216:219]\n"
"buffer_load_dwordx4 v248, s[72:75], s55 offen offset:3072 sc1 lds\n"
"v_mfma_f32_16x16x32_bf16 v[4:7], a[100:103], v[128:131], v[4:7]\n"
"v_exp_f32 v161, v161\n"
"v_mfma_f32_16x16x32_bf16 v[8:11], a[104:107], v[128:131], v[8:11]\n"
"ds_read_b128 a[212:215], v250 offset:46080\n"
"v_cvt_pk_bf16_f32 v160, v160, v161\n"
"v_mfma_f32_16x16x32_bf16 v[12:15], a[108:111], v[128:131], v[12:15]\n"
"v_exp_f32 v162, v162\n"
"v_mfma_f32_16x16x32_bf16 v[16:19], a[112:115], v[128:131], v[16:19]\n"
"v_cmp_lt_f32 vcc, s43, v218\n"
"v_mfma_f32_16x16x32_bf16 v[20:23], a[116:119], v[128:131], v[20:23]\n"
"v_exp_f32 v163, v163\n"
"v_mfma_f32_16x16x32_bf16 v[24:27], a[120:123], v[128:131], v[24:27]\n"
"ds_read_b128 a[216:219], v250 offset:47104\n"
"v_cvt_pk_bf16_f32 v161, v162, v163\n"
"s_cmp_ge_u32 s14, s40\n"
"s_cselect_b32 s34, 0xff800000, 0\n"
"v_mfma_f32_16x16x32_bf16 v[28:31], a[124:127], v[128:131], v[28:31]\n"
"v_exp_f32 v164, v164\n"
"v_mfma_f32_16x16x32_bf16 v[32:35], a[96:99], v[132:135], v[32:35]\n"
"s_cbranch_vccnz Lfused_xF2\n"
"Lfused_rF2:\n"
"s_cmp_gt_u32 s14, s40\n"
"s_cselect_b32 s35, 0xff800000, 0\n"
"v_mfma_f32_16x16x32_bf16 v[36:39], a[100:103], v[132:135], v[36:39]\n"
"v_exp_f32 v165, v165\n"
"v_mfma_f32_16x16x32_bf16 v[40:43], a[104:107], v[132:135], v[40:43]\n"
"ds_read_b128 a[220:223], v250 offset:48128\n"
"v_cvt_pk_bf16_f32 v162, v164, v165\n"
"v_add_f32 v224, s35, v200\n"
"v_mfma_f32_16x16x32_bf16 v[44:47], a[108:111], v[132:135], v[44:47]\n"
"v_exp_f32 v166, v166\n"
"v_mfma_f32_16x16x32_bf16 v[48:51], a[112:115], v[132:135], v[48:51]\n"
"v_add_f32 v225, s34, v204\n"
"v_add_f32 v180, s34, v200\n"
"v_add_f32 v181, s34, v200\n"
"v_add_f32 v182, s34, v200\n"
"v_add_f32 v183, s34, v200\n"
"v_cndmask_b32 v176, v224, v180, s[64:65]\n"
"v_cndmask_b32 v177, v224, v180, s[66:67]\n"
"v_cndmask_b32 v178, v224, v180, s[68:69]\n"
"v_mfma_f32_16x16x32_bf16 v[52:55], a[116:119], v[132:135], v[52:55]\n"
"v_exp_f32 v167, v167\n"
"v_mfma_f32_16x16x32_bf16 v[56:59], a[120:123], v[132:135], v[56:59]\n"
"v_cvt_pk_bf16_f32 v163, v166, v167\n"
"v_cndmask_b32 v179, v224, v180, s[70:71]\n"
"v_add_f32 v184, s35, v204\n"
"v_add_f32 v185, s35, v204\n"
"v_add_f32 v186, s35, v204\n"
"v_add_f32 v187, s35, v204\n"
"v_cndmask_b32 v188, v184, v225, s[64:65]\n"
"v_cndmask_b32 v189, v184, v225, s[66:67]\n"
"v_cndmask_b32 v190, v184, v225, s[68:69]\n"
"v_cndmask_b32 v191, v184, v225, s[70:71]\n"
"v_mfma_f32_16x16x32_bf16 v[60:63], a[124:127], v[132:135], v[60:63]\n"
"Lfused_F3:\n"
"v_mfma_f32_16x16x32_bf16 v[176:179], a[160:163], a[64:67], v[176:179]\n"
"s_add_u32 m0, s90, 147456\n"
"s_add_u32 s55, s45, 57344\n"
"v_mfma_f32_16x16x32_bf16 v[176:179], a[168:171], a[68:71], v[176:179]\n"
"v_exp_f32 v168, v168\n"
"v_mfma_f32_16x16x32_bf16 v[176:179], a[176:179], a[72:75], v[176:179]\n"
"buffer_load_dwordx4 v248, s[72:75], s55 offen offset:0 sc1 lds\n"
"v_mfma_f32_16x16x32_bf16 v[176:179], a[184:187], a[76:79], v[176:179]\n"
"v_exp_f32 v169, v169\n"
"v_mfma_f32_16x16x32_bf16 v[212:215], a[232:235], v[160:163], v[208:211]\n"
"v_cvt_pk_bf16_f32 v164, v168, v169\n"
"v_mfma_f32_16x16x32_bf16 v[180:183], a[164:167], a[64:67], v[180:183]\n"
"v_exp_f32 v170, v170\n"
"v_mfma_f32_16x16x32_bf16 v[180:183], a[172:175], a[68:71], v[180:183]\n"
"buffer_load_dwordx4 v248, s[72:75], s55 offen offset:1024 sc1 lds\n"
"v_mfma_f32_16x16x32_bf16 v[180:183], a[180:183], a[72:75], v[180:183]\n"
"v_exp_f32 v171, v171\n"
"v_mfma_f32_16x16x32_bf16 v[180:183], a[188:191], a[76:79], v[180:183]\n"
"v_cvt_pk_bf16_f32 v165, v170, v171\n"
"v_mfma_f32_16x16x32_bf16 v[184:187], a[160:163], a[80:83], v[184:187]\n"
"v_exp_f32 v172, v172\n"
"v_mfma_f32_16x16x32_bf16 v[184:187], a[168:171], a[84:87], v[184:187]\n"
"buffer_load_dwordx4 v248, s[72:75], s55 offen offset:2048 sc1 lds\n"
"v_mfma_f32_16x16x32_bf16 v[184:187], a[176:179], a[88:91], v[184:187]\n"
"v_exp_f32 v173, v173\n"
"v_mfma_f32_16x16x32_bf16 v[184:187], a[184:187], a[92:95], v[184:187]\n"
"v_cvt_pk_bf16_f32 v166, v172, v173\n"
"v_mfma_f32_16x16x32_bf16 v[188:191], a[164:167], a[80:83], v[188:191]\n"
"v_exp_f32 v174, v174\n"
"v_mfma_f32_16x16x32_bf16 v[188:191], a[172:175], a[84:87], v[188:191]\n"
"buffer_load_dwordx4 v248, s[72:75], s55 offen offset:3072 sc1 lds\n"
"v_mfma_f32_16x16x32_bf16 v[188:191], a[180:183], a[88:91], v[188:191]\n"
"v_exp_f32 v175, v175\n"
"v_mfma_f32_16x16x32_bf16 v[188:191], a[188:191], a[92:95], v[188:191]\n"
"s_add_u32 s55, s79, 8192\n"
"v_cvt_pk_bf16_f32 v167, v174, v175\n"
"v_mfma_f32_16x16x32_bf16 v[64:67], a[96:99], v[144:147], v[64:67]\n"
"v_exp_f32 v176, v176\n"
"v_mfma_f32_16x16x32_bf16 v[212:215], a[236:239], v[164:167], v[212:215]\n"
"buffer_load_dwordx4 a[128:131], v248, s[72:75], s55 offen offset:0 sc1\n"
"v_mfma_f32_16x16x32_bf16 v[68:71], a[100:103], v[144:147], v[68:71]\n"
"v_exp_f32 v177, v177\n"
"v_mfma_f32_16x16x32_bf16 v[72:75], a[104:107], v[144:147], v[72:75]\n"
"v_cvt_pk_bf16_f32 v176, v176, v177\n"
"v_mfma_f32_16x16x32_bf16 v[76:79], a[108:111], v[144:147], v[76:79]\n"
"v_exp_f32 v178, v178\n"
"v_mfma_f32_16x16x32_bf16 v[80:83], a[112:115], v[144:147], v[80:83]\n"
"v_cmp_lt_f32 vcc, s43, v214\n"
"v_mfma_f32_16x16x32_bf16 v[84:87], a[116:119], v[144:147], v[84:87]\n"
"v_exp_f32 v179, v179\n"
"v_mfma_f32_16x16x32_bf16 v[88:91], a[120:123], v[144:147], v[88:91]\n"
"v_cvt_pk_bf16_f32 v177, v178, v179\n"
"s_add_u32 s14, s14, 1\n"
"v_mfma_f32_16x16x32_bf16 v[92:95], a[124:127], v[144:147], v[92:95]\n"
"v_exp_f32 v180, v180\n"
"v_mfma_f32_16x16x32_bf16 v[96:99], a[96:99], v[148:151], v[96:99]\n"
"buffer_load_dwordx4 a[132:135], v248, s[72:75], s55 offen offset:1024 sc1\n"
"s_cbranch_vccnz Lfused_xF3\n"
"Lfused_rF3:\n"
"v_mfma_f32_16x16x32_bf16 v[100:103], a[100:103], v[148:151], v[100:103]\n"
"v_exp_f32 v181, v181\n"
"v_mfma_f32_16x16x32_bf16 v[104:107], a[104:107], v[148:151], v[104:107]\n"
"v_cvt_pk_bf16_f32 v178, v180, v181\n"
"v_mfma_f32_16x16x32_bf16 v[108:111], a[108:111], v[148:151], v[108:111]\n"
"v_exp_f32 v182, v182\n"
"v_mfma_f32_16x16x32_bf16 v[112:115], a[112:115], v[148:151], v[112:115]\n"
"buffer_load_dwordx4 a[136:139], v248, s[72:75], s55 offen offset:2048 sc1\n"
"v_mfma_f32_16x16x32_bf16 v[116:119], a[116:119], v[148:151], v[116:119]\n"
"v_exp_f32 v183, v183\n"
"v_mfma_f32_16x16x32_bf16 v[120:123], a[120:123], v[148:151], v[120:123]\n"
"v_cvt_pk_bf16_f32 v179, v182, v183\n"
"v_mfma_f32_16x16x32_bf16 v[124:127], a[124:127], v[148:151], v[124:127]\n"
"buffer_load_dwordx4 a[140:143], v248, s[72:75], s55 offen offset:3072 sc1\n"
"Lfused_drain:\n"
"s_waitcnt lgkmcnt(0)\n"
"s_lshl_b32 s50, s12, 10\n"
"s_add_u32 s50, s50, 135168\n"
"v_add_u32 v232, s50, v248\n"
"v_mov_b32 v214, v192\n"
"v_mov_b32 v215, v196\n"
"ds_write_b128 v232, v[212:215]\n"
"s_lshl_b32 s50, s12, 14\n"
"v_add_u32 v233, s50, v248\n"
"v_xor_b32 v232, 0x400, v232\n"
"v_mfma_f32_16x16x32_bf16 v[220:223], a[232:235], v[176:179], v[216:219]\n"
"v_exp_f32 v184, v184\n"
"v_mfma_f32_16x16x32_bf16 v[0:3], a[192:195], v[160:163], v[0:3]\n"
"v_exp_f32 v185, v185\n"
"v_mfma_f32_16x16x32_bf16 v[4:7], a[196:199], v[160:163], v[4:7]\n"
"v_cvt_pk_bf16_f32 v180, v184, v185\n"
"v_mfma_f32_16x16x32_bf16 v[8:11], a[200:203], v[160:163], v[8:11]\n"
"v_exp_f32 v186, v186\n"
"v_mfma_f32_16x16x32_bf16 v[12:15], a[204:207], v[160:163], v[12:15]\n"
"s_waitcnt lgkmcnt(0)\n"
"s_barrier\n"
"ds_read_b128 v[236:239], v232\n"
"v_exp_f32 v187, v187\n"
"v_mfma_f32_16x16x32_bf16 v[16:19], a[208:211], v[160:163], v[16:19]\n"
"ds_write_b128 v233, v[0:3] offset:0\n"
"v_cvt_pk_bf16_f32 v181, v186, v187\n"
"v_mfma_f32_16x16x32_bf16 v[20:23], a[212:215], v[160:163], v[20:23]\n"
"v_exp_f32 v188, v188\n"
"v_mfma_f32_16x16x32_bf16 v[24:27], a[216:219], v[160:163], v[24:27]\n"
"v_exp_f32 v189, v189\n"
"v_mfma_f32_16x16x32_bf16 v[28:31], a[220:223], v[160:163], v[28:31]\n"
"ds_write_b128 v233, v[4:7] offset:1024\n"
"v_cvt_pk_bf16_f32 v182, v188, v189\n"
"v_mfma_f32_16x16x32_bf16 v[32:35], a[192:195], v[164:167], v[32:35]\n"
"v_exp_f32 v190, v190\n"
"v_mfma_f32_16x16x32_bf16 v[36:39], a[196:199], v[164:167], v[36:39]\n"
"v_exp_f32 v191, v191\n"
"v_mfma_f32_16x16x32_bf16 v[40:43], a[200:203], v[164:167], v[40:43]\n"
"ds_write_b128 v233, v[8:11] offset:2048\n"
"v_cvt_pk_bf16_f32 v183, v190, v191\n"
"v_mfma_f32_16x16x32_bf16 v[44:47], a[204:207], v[164:167], v[44:47]\n"
"s_add_u32 s55, s79, 12288\n"
"buffer_load_dwordx4 a[144:147], v248, s[72:75], s55 offen offset:0 sc1\n"
"v_mfma_f32_16x16x32_bf16 v[220:223], a[236:239], v[180:183], v[220:223]\n"
"ds_write_b128 v233, v[12:15] offset:3072\n"
"v_mfma_f32_16x16x32_bf16 v[48:51], a[208:211], v[164:167], v[48:51]\n"
"buffer_load_dwordx4 a[148:151], v248, s[72:75], s55 offen offset:1024 sc1\n"
"v_mfma_f32_16x16x32_bf16 v[52:55], a[212:215], v[164:167], v[52:55]\n"
"ds_write_b128 v233, v[16:19] offset:4096\n"
"v_mfma_f32_16x16x32_bf16 v[56:59], a[216:219], v[164:167], v[56:59]\n"
"buffer_load_dwordx4 a[152:155], v248, s[72:75], s55 offen offset:2048 sc1\n"
"v_mfma_f32_16x16x32_bf16 v[60:63], a[220:223], v[164:167], v[60:63]\n"
"ds_write_b128 v233, v[20:23] offset:5120\n"
"v_cmp_lt_f32 vcc, s43, v222\n"
"ds_write_b128 v233, v[24:27] offset:6144\n"
"buffer_load_dwordx4 a[156:159], v248, s[72:75], s55 offen offset:3072 sc1\n"
"ds_write_b128 v233, v[28:31] offset:7168\n"
"s_cbranch_vccnz Lfused_xZ\n"
"Lfused_rZ:\n"
"s_waitcnt lgkmcnt(8)\n"
"v_mfma_f32_16x16x32_bf16 v[64:67], a[192:195], v[176:179], v[64:67]\n"
"ds_write_b128 v233, v[32:35] offset:8192\n"
"v_min_f32 v228, v200, v238\n"
"v_min_f32 v229, v204, v239\n"
"v_mfma_f32_16x16x32_bf16 v[68:71], a[196:199], v[176:179], v[68:71]\n"
"s_add_u32 s55, s79, 16384\n"
"buffer_load_dwordx4 a[0:3], v248, s[72:75], s55 offen offset:0 sc1\n"
"v_mfma_f32_16x16x32_bf16 v[72:75], a[200:203], v[176:179], v[72:75]\n"
"v_sub_f32 v226, v228, v200\n"
"v_sub_f32 v227, v229, v204\n"
"v_mfma_f32_16x16x32_bf16 v[76:79], a[204:207], v[176:179], v[76:79]\n"
"ds_write_b128 v233, v[36:39] offset:9216\n"
"v_sub_f32 v228, v228, v238\n"
"v_mfma_f32_16x16x32_bf16 v[80:83], a[208:211], v[176:179], v[80:83]\n"
"ds_write_b128 v233, v[40:43] offset:10240\n"
"v_sub_f32 v229, v229, v239\n"
"v_mfma_f32_16x16x32_bf16 v[84:87], a[212:215], v[176:179], v[84:87]\n"
"v_exp_f32 v226, v226\n"
"buffer_load_dwordx4 a[4:7], v248, s[72:75], s55 offen offset:1024 sc1\n"
"v_mfma_f32_16x16x32_bf16 v[88:91], a[216:219], v[176:179], v[88:91]\n"
"v_exp_f32 v227, v227\n"
"v_exp_f32 v228, v228\n"
"v_mfma_f32_16x16x32_bf16 v[92:95], a[220:223], v[176:179], v[92:95]\n"
"ds_write_b128 v233, v[44:47] offset:11264\n"
"v_exp_f32 v229, v229\n"
"v_mfma_f32_16x16x32_bf16 v[96:99], a[192:195], v[180:183], v[96:99]\n"
"ds_write_b128 v233, v[48:51] offset:12288\n"
"v_mul_f32 v220, v220, v226\n"
"v_mfma_f32_16x16x32_bf16 v[100:103], a[196:199], v[180:183], v[100:103]\n"
"v_mul_f32 v221, v221, v227\n"
"buffer_load_dwordx4 a[8:11], v248, s[72:75], s55 offen offset:2048 sc1\n"
"v_mfma_f32_16x16x32_bf16 v[104:107], a[200:203], v[180:183], v[104:107]\n"
"v_fmac_f32 v220, v236, v228\n"
"v_fmac_f32 v221, v237, v229\n"
"v_mfma_f32_16x16x32_bf16 v[108:111], a[204:207], v[180:183], v[108:111]\n"
"ds_write_b128 v233, v[52:55] offset:13312\n"
"v_rcp_f32 v220, v220\n"
"v_mfma_f32_16x16x32_bf16 v[112:115], a[208:211], v[180:183], v[112:115]\n"
"ds_write_b128 v233, v[56:59] offset:14336\n"
"v_rcp_f32 v221, v221\n"
"v_mfma_f32_16x16x32_bf16 v[116:119], a[212:215], v[180:183], v[116:119]\n"
"v_mul_f32 v226, v226, v220\n"
"buffer_load_dwordx4 a[12:15], v248, s[72:75], s55 offen offset:3072 sc1\n"
"v_mfma_f32_16x16x32_bf16 v[120:123], a[216:219], v[180:183], v[120:123]\n"
"v_mul_f32 v227, v227, v221\n"
"v_mul_f32 v228, v228, v220\n"
"v_mfma_f32_16x16x32_bf16 v[124:127], a[220:223], v[180:183], v[124:127]\n"
"ds_write_b128 v233, v[60:63] offset:15360\n"
"v_mul_f32 v229, v229, v221\n"
"v_accvgpr_read_b32 v128, a240\n"
"s_add_u32 s55, s79, 20480\n"
"buffer_load_dwordx4 a[16:19], v248, s[72:75], s55 offen offset:0 sc1\n"
"v_accvgpr_read_b32 v129, a241\n"
"v_accvgpr_read_b32 v130, a242\n"
"v_accvgpr_read_b32 v131, a243\n"
"v_accvgpr_read_b32 v132, a244\n"
"v_accvgpr_read_b32 v133, a245\n"
"v_accvgpr_read_b32 v134, a246\n"
"v_accvgpr_read_b32 v135, a247\n"
"v_accvgpr_read_b32 v136, a248\n"
"buffer_load_dwordx4 a[20:23], v248, s[72:75], s55 offen offset:1024 sc1\n"
"v_accvgpr_read_b32 v137, a249\n"
"v_accvgpr_read_b32 v138, a250\n"
"v_accvgpr_read_b32 v139, a251\n"
"v_accvgpr_read_b32 v140, a252\n"
"v_accvgpr_read_b32 v141, a253\n"
"v_accvgpr_read_b32 v142, a254\n"
"v_accvgpr_read_b32 v143, a255\n"
"v_add_f32 v128, v128, v136\n"
"v_add_f32 v129, v129, v137\n"
"buffer_load_dwordx4 a[24:27], v248, s[72:75], s55 offen offset:2048 sc1\n"
"v_add_f32 v130, v130, v138\n"
"v_add_f32 v131, v131, v139\n"
"v_add_f32 v132, v132, v140\n"
"v_add_f32 v133, v133, v141\n"
"v_add_f32 v134, v134, v142\n"
"v_add_f32 v135, v135, v143\n"
"v_add_u32 v234, 139264, v248\n"
"s_lshl_b32 s50, s12, 11\n"
"buffer_load_dwordx4 a[28:31], v248, s[72:75], s55 offen offset:3072 sc1\n"
"v_add_u32 v232, s50, v234\n"
"ds_write_b128 v232, v[128:131]\n"
"ds_write_b128 v232, v[132:135] offset:1024\n"
"v_xor_b32 v233, 0x4000, v233\n"
"s_and_b32 s50, s12, 1\n"
"s_cselect_b32 s50, 589824, 0\n"
"s_add_u32 s14, s47, 294912\n"
"s_sub_u32 s14, s14, s50\n"
"s_add_u32 s47, s47, 147456\n"
"s_waitcnt lgkmcnt(0)\n"
"s_barrier\n"
"ds_read_b128 v[0:3], v233 offset:0\n"
"ds_read_b128 v[4:7], v233 offset:1024\n"
"ds_read_b128 v[8:11], v233 offset:2048\n"
"ds_read_b128 v[12:15], v233 offset:3072\n"
"ds_read_b128 v[16:19], v233 offset:4096\n"
"ds_read_b128 v[20:23], v233 offset:5120\n"
"ds_read_b128 v[24:27], v233 offset:6144\n"
"ds_read_b128 v[28:31], v233 offset:7168\n"
"ds_read_b128 v[32:35], v233 offset:8192\n"
"ds_read_b128 v[36:39], v233 offset:9216\n"
"ds_read_b128 v[40:43], v233 offset:10240\n"
"ds_read_b128 v[44:47], v233 offset:11264\n"
"ds_read_b128 v[48:51], v233 offset:12288\n"
"ds_read_b128 v[52:55], v233 offset:13312\n"
"ds_read_b128 v[56:59], v233 offset:14336\n"
"ds_read_b128 v[60:63], v233 offset:15360\n"
"v_mul_f32 v64, v64, v226\n"
"v_mul_f32 v65, v65, v226\n"
"v_mul_f32 v66, v66, v226\n"
"v_mul_f32 v67, v67, v226\n"
"v_mul_f32 v68, v68, v226\n"
"v_mul_f32 v69, v69, v226\n"
"v_mul_f32 v70, v70, v226\n"
"v_mul_f32 v71, v71, v226\n"
"s_waitcnt lgkmcnt(14)\n"
"v_fmac_f32 v64, v0, v228\n"
"v_fmac_f32 v65, v1, v228\n"
"v_fmac_f32 v66, v2, v228\n"
"v_fmac_f32 v67, v3, v228\n"
"v_fmac_f32 v68, v4, v228\n"
"v_fmac_f32 v69, v5, v228\n"
"v_fmac_f32 v70, v6, v228\n"
"v_fmac_f32 v71, v7, v228\n"
"v_cvt_pk_bf16_f32 v64, v64, v65\n"
"v_cvt_pk_bf16_f32 v65, v66, v67\n"
"v_cvt_pk_bf16_f32 v66, v68, v69\n"
"v_cvt_pk_bf16_f32 v67, v70, v71\n"
"buffer_store_dwordx4 v[64:67], v248, s[24:27], s14 offen offset:0 sc1\n"
"v_mul_f32 v72, v72, v226\n"
"v_mul_f32 v73, v73, v226\n"
"v_mul_f32 v74, v74, v226\n"
"v_mul_f32 v75, v75, v226\n"
"v_mul_f32 v76, v76, v226\n"
"v_mul_f32 v77, v77, v226\n"
"v_mul_f32 v78, v78, v226\n"
"v_mul_f32 v79, v79, v226\n"
"s_waitcnt lgkmcnt(12)\n"
"v_fmac_f32 v72, v8, v228\n"
"v_fmac_f32 v73, v9, v228\n"
"v_fmac_f32 v74, v10, v228\n"
"v_fmac_f32 v75, v11, v228\n"
"v_fmac_f32 v76, v12, v228\n"
"v_fmac_f32 v77, v13, v228\n"
"v_fmac_f32 v78, v14, v228\n"
"v_fmac_f32 v79, v15, v228\n"
"v_cvt_pk_bf16_f32 v72, v72, v73\n"
"v_cvt_pk_bf16_f32 v73, v74, v75\n"
"v_cvt_pk_bf16_f32 v74, v76, v77\n"
"v_cvt_pk_bf16_f32 v75, v78, v79\n"
"buffer_store_dwordx4 v[72:75], v248, s[24:27], s14 offen offset:1024 sc1\n"
"v_mul_f32 v80, v80, v226\n"
"v_mul_f32 v81, v81, v226\n"
"v_mul_f32 v82, v82, v226\n"
"v_mul_f32 v83, v83, v226\n"
"v_mul_f32 v84, v84, v226\n"
"v_mul_f32 v85, v85, v226\n"
"v_mul_f32 v86, v86, v226\n"
"v_mul_f32 v87, v87, v226\n"
"s_waitcnt lgkmcnt(10)\n"
"v_fmac_f32 v80, v16, v228\n"
"v_fmac_f32 v81, v17, v228\n"
"v_fmac_f32 v82, v18, v228\n"
"v_fmac_f32 v83, v19, v228\n"
"v_fmac_f32 v84, v20, v228\n"
"v_fmac_f32 v85, v21, v228\n"
"v_fmac_f32 v86, v22, v228\n"
"v_fmac_f32 v87, v23, v228\n"
"v_cvt_pk_bf16_f32 v80, v80, v81\n"
"v_cvt_pk_bf16_f32 v81, v82, v83\n"
"v_cvt_pk_bf16_f32 v82, v84, v85\n"
"v_cvt_pk_bf16_f32 v83, v86, v87\n"
"buffer_store_dwordx4 v[80:83], v248, s[24:27], s14 offen offset:2048 sc1\n"
"v_mul_f32 v88, v88, v226\n"
"v_mul_f32 v89, v89, v226\n"
"v_mul_f32 v90, v90, v226\n"
"v_mul_f32 v91, v91, v226\n"
"v_mul_f32 v92, v92, v226\n"
"v_mul_f32 v93, v93, v226\n"
"v_mul_f32 v94, v94, v226\n"
"v_mul_f32 v95, v95, v226\n"
"s_waitcnt lgkmcnt(8)\n"
"v_fmac_f32 v88, v24, v228\n"
"v_fmac_f32 v89, v25, v228\n"
"v_fmac_f32 v90, v26, v228\n"
"v_fmac_f32 v91, v27, v228\n"
"v_fmac_f32 v92, v28, v228\n"
"v_fmac_f32 v93, v29, v228\n"
"v_fmac_f32 v94, v30, v228\n"
"v_fmac_f32 v95, v31, v228\n"
"v_cvt_pk_bf16_f32 v88, v88, v89\n"
"v_cvt_pk_bf16_f32 v89, v90, v91\n"
"v_cvt_pk_bf16_f32 v90, v92, v93\n"
"v_cvt_pk_bf16_f32 v91, v94, v95\n"
"buffer_store_dwordx4 v[88:91], v248, s[24:27], s14 offen offset:3072 sc1\n"
"s_add_u32 s14, s14, 147456\n"
"v_mul_f32 v96, v96, v227\n"
"v_mul_f32 v97, v97, v227\n"
"v_mul_f32 v98, v98, v227\n"
"v_mul_f32 v99, v99, v227\n"
"v_mul_f32 v100, v100, v227\n"
"v_mul_f32 v101, v101, v227\n"
"v_mul_f32 v102, v102, v227\n"
"v_mul_f32 v103, v103, v227\n"
"s_waitcnt lgkmcnt(6)\n"
"v_fmac_f32 v96, v32, v229\n"
"v_fmac_f32 v97, v33, v229\n"
"v_fmac_f32 v98, v34, v229\n"
"v_fmac_f32 v99, v35, v229\n"
"v_fmac_f32 v100, v36, v229\n"
"v_fmac_f32 v101, v37, v229\n"
"v_fmac_f32 v102, v38, v229\n"
"v_fmac_f32 v103, v39, v229\n"
"v_cvt_pk_bf16_f32 v96, v96, v97\n"
"v_cvt_pk_bf16_f32 v97, v98, v99\n"
"v_cvt_pk_bf16_f32 v98, v100, v101\n"
"v_cvt_pk_bf16_f32 v99, v102, v103\n"
"buffer_store_dwordx4 v[96:99], v248, s[24:27], s14 offen offset:0 sc1\n"
"v_mul_f32 v104, v104, v227\n"
"v_mul_f32 v105, v105, v227\n"
"v_mul_f32 v106, v106, v227\n"
"v_mul_f32 v107, v107, v227\n"
"v_mul_f32 v108, v108, v227\n"
"v_mul_f32 v109, v109, v227\n"
"v_mul_f32 v110, v110, v227\n"
"v_mul_f32 v111, v111, v227\n"
"s_waitcnt lgkmcnt(4)\n"
"v_fmac_f32 v104, v40, v229\n"
"v_fmac_f32 v105, v41, v229\n"
"v_fmac_f32 v106, v42, v229\n"
"v_fmac_f32 v107, v43, v229\n"
"v_fmac_f32 v108, v44, v229\n"
"v_fmac_f32 v109, v45, v229\n"
"v_fmac_f32 v110, v46, v229\n"
"v_fmac_f32 v111, v47, v229\n"
"v_cvt_pk_bf16_f32 v104, v104, v105\n"
"v_cvt_pk_bf16_f32 v105, v106, v107\n"
"v_cvt_pk_bf16_f32 v106, v108, v109\n"
"v_cvt_pk_bf16_f32 v107, v110, v111\n"
"buffer_store_dwordx4 v[104:107], v248, s[24:27], s14 offen offset:1024 sc1\n"
"v_mul_f32 v112, v112, v227\n"
"v_mul_f32 v113, v113, v227\n"
"v_mul_f32 v114, v114, v227\n"
"v_mul_f32 v115, v115, v227\n"
"v_mul_f32 v116, v116, v227\n"
"v_mul_f32 v117, v117, v227\n"
"v_mul_f32 v118, v118, v227\n"
"v_mul_f32 v119, v119, v227\n"
"s_waitcnt lgkmcnt(2)\n"
"v_fmac_f32 v112, v48, v229\n"
"v_fmac_f32 v113, v49, v229\n"
"v_fmac_f32 v114, v50, v229\n"
"v_fmac_f32 v115, v51, v229\n"
"v_fmac_f32 v116, v52, v229\n"
"v_fmac_f32 v117, v53, v229\n"
"v_fmac_f32 v118, v54, v229\n"
"v_fmac_f32 v119, v55, v229\n"
"v_cvt_pk_bf16_f32 v112, v112, v113\n"
"v_cvt_pk_bf16_f32 v113, v114, v115\n"
"v_cvt_pk_bf16_f32 v114, v116, v117\n"
"v_cvt_pk_bf16_f32 v115, v118, v119\n"
"buffer_store_dwordx4 v[112:115], v248, s[24:27], s14 offen offset:2048 sc1\n"
"v_mul_f32 v120, v120, v227\n"
"v_mul_f32 v121, v121, v227\n"
"v_mul_f32 v122, v122, v227\n"
"v_mul_f32 v123, v123, v227\n"
"v_mul_f32 v124, v124, v227\n"
"v_mul_f32 v125, v125, v227\n"
"v_mul_f32 v126, v126, v227\n"
"v_mul_f32 v127, v127, v227\n"
"s_waitcnt lgkmcnt(0)\n"
"v_fmac_f32 v120, v56, v229\n"
"v_fmac_f32 v121, v57, v229\n"
"v_fmac_f32 v122, v58, v229\n"
"v_fmac_f32 v123, v59, v229\n"
"v_fmac_f32 v124, v60, v229\n"
"v_fmac_f32 v125, v61, v229\n"
"v_fmac_f32 v126, v62, v229\n"
"v_fmac_f32 v127, v63, v229\n"
"v_cvt_pk_bf16_f32 v120, v120, v121\n"
"v_cvt_pk_bf16_f32 v121, v122, v123\n"
"v_cvt_pk_bf16_f32 v122, v124, v125\n"
"v_cvt_pk_bf16_f32 v123, v126, v127\n"
"buffer_store_dwordx4 v[120:123], v248, s[24:27], s14 offen offset:3072 sc1\n"
"ds_read_b128 v[128:131], v234 offset:0\n"
"ds_read_b128 v[132:135], v234 offset:1024\n"
"ds_read_b128 v[136:139], v234 offset:2048\n"
"ds_read_b128 v[140:143], v234 offset:3072\n"
"ds_read_b128 v[144:147], v234 offset:4096\n"
"ds_read_b128 v[148:151], v234 offset:5120\n"
"ds_read_b128 v[152:155], v234 offset:6144\n"
"ds_read_b128 v[156:159], v234 offset:7168\n"
"v_accvgpr_read_b32 v177, a224\n"
"v_lshlrev_b32 v176, 16, v177\n"
"v_and_b32 v177, 0xffff0000, v177\n"
"v_accvgpr_read_b32 v179, a225\n"
"v_lshlrev_b32 v178, 16, v179\n"
"v_and_b32 v179, 0xffff0000, v179\n"
"v_accvgpr_read_b32 v181, a226\n"
"v_lshlrev_b32 v180, 16, v181\n"
"v_and_b32 v181, 0xffff0000, v181\n"
"v_accvgpr_read_b32 v183, a227\n"
"v_lshlrev_b32 v182, 16, v183\n"
"v_and_b32 v183, 0xffff0000, v183\n"
"v_accvgpr_read_b32 v185, a228\n"
"v_lshlrev_b32 v184, 16, v185\n"
"v_and_b32 v185, 0xffff0000, v185\n"
"v_accvgpr_read_b32 v187, a229\n"
"v_lshlrev_b32 v186, 16, v187\n"
"v_and_b32 v187, 0xffff0000, v187\n"
"v_accvgpr_read_b32 v189, a230\n"
"v_lshlrev_b32 v188, 16, v189\n"
"v_and_b32 v189, 0xffff0000, v189\n"
"v_accvgpr_read_b32 v191, a231\n"
"v_lshlrev_b32 v190, 16, v191\n"
"v_and_b32 v191, 0xffff0000, v191\n"
"v_mul_f32 v224, 1069066811, v176\n"
"v_exp_f32 v224, v224 clamp\n"
"v_max_f32 v176, 0, v176\n"
"v_add_f32 v224, 897988541, v224\n"
"v_add_f32 v176, v176, v224\n"
"v_mul_f32 v224, 1069066811, v177\n"
"v_exp_f32 v224, v224 clamp\n"
"v_max_f32 v177, 0, v177\n"
"v_add_f32 v224, 897988541, v224\n"
"v_add_f32 v177, v177, v224\n"
"v_mul_f32 v224, 1069066811, v178\n"
"v_exp_f32 v224, v224 clamp\n"
"v_max_f32 v178, 0, v178\n"
"v_add_f32 v224, 897988541, v224\n"
"v_add_f32 v178, v178, v224\n"
"v_mul_f32 v224, 1069066811, v179\n"
"v_exp_f32 v224, v224 clamp\n"
"v_max_f32 v179, 0, v179\n"
"v_add_f32 v224, 897988541, v224\n"
"v_add_f32 v179, v179, v224\n"
"v_mul_f32 v224, 1069066811, v180\n"
"v_exp_f32 v224, v224 clamp\n"
"v_max_f32 v180, 0, v180\n"
"v_add_f32 v224, 897988541, v224\n"
"v_add_f32 v180, v180, v224\n"
"v_mul_f32 v224, 1069066811, v181\n"
"v_exp_f32 v224, v224 clamp\n"
"v_max_f32 v181, 0, v181\n"
"v_add_f32 v224, 897988541, v224\n"
"v_add_f32 v181, v181, v224\n"
"v_mul_f32 v224, 1069066811, v182\n"
"v_exp_f32 v224, v224 clamp\n"
"v_max_f32 v182, 0, v182\n"
"v_add_f32 v224, 897988541, v224\n"
"v_add_f32 v182, v182, v224\n"
"v_mul_f32 v224, 1069066811, v183\n"
"v_exp_f32 v224, v224 clamp\n"
"v_max_f32 v183, 0, v183\n"
"v_add_f32 v224, 897988541, v224\n"
"v_add_f32 v183, v183, v224\n"
"v_mul_f32 v224, 1069066811, v184\n"
"v_exp_f32 v224, v224 clamp\n"
"v_max_f32 v184, 0, v184\n"
"v_add_f32 v224, 897988541, v224\n"
"v_add_f32 v184, v184, v224\n"
"v_mul_f32 v224, 1069066811, v185\n"
"v_exp_f32 v224, v224 clamp\n"
"v_max_f32 v185, 0, v185\n"
"v_add_f32 v224, 897988541, v224\n"
"v_add_f32 v185, v185, v224\n"
"v_mul_f32 v224, 1069066811, v186\n"
"v_exp_f32 v224, v224 clamp\n"
"v_max_f32 v186, 0, v186\n"
"v_add_f32 v224, 897988541, v224\n"
"v_add_f32 v186, v186, v224\n"
"v_mul_f32 v224, 1069066811, v187\n"
"v_exp_f32 v224, v224 clamp\n"
"v_max_f32 v187, 0, v187\n"
"v_add_f32 v224, 897988541, v224\n"
"v_add_f32 v187, v187, v224\n"
"v_mul_f32 v224, 1069066811, v188\n"
"v_exp_f32 v224, v224 clamp\n"
"v_max_f32 v188, 0, v188\n"
"v_add_f32 v224, 897988541, v224\n"
"v_add_f32 v188, v188, v224\n"
"v_mul_f32 v224, 1069066811, v189\n"
"v_exp_f32 v224, v224 clamp\n"
"v_max_f32 v189, 0, v189\n"
"v_add_f32 v224, 897988541, v224\n"
"v_add_f32 v189, v189, v224\n"
"v_mul_f32 v224, 1069066811, v190\n"
"v_exp_f32 v224, v224 clamp\n"
"v_max_f32 v190, 0, v190\n"
"v_add_f32 v224, 897988541, v224\n"
"v_add_f32 v190, v190, v224\n"
"v_mul_f32 v224, 1069066811, v191\n"
"v_exp_f32 v224, v224 clamp\n"
"v_max_f32 v191, 0, v191\n"
"v_add_f32 v224, 897988541, v224\n"
"v_add_f32 v191, v191, v224\n"
"s_waitcnt vmcnt(0) lgkmcnt(0)\n"
"s_barrier\n"
"s_cmp_eq_u32 s12, 0\n"
"s_cbranch_scc0 Lfused_p3a\n"
"s_lshl_b32 s58, s11, 8\n"
"s_lshl_b32 s59, s10, 3\n"
"s_add_u32 s58, s58, s59\n"
"s_add_u32 s58, s58, 24576\n"
"v_mov_b32 v250, s98\n"
"v_mov_b32 v251, s99\n"
"v_mov_b32 v252, s58\n"
"s_mov_b64 exec, 1\n"
"global_store_dwordx2 v252, v[250:251], s[62:63] sc1\n"
"s_mov_b64 exec, -1\n"
"Lfused_p3a:\n"
"s_add_u32 s55, s79, 24576\n"
"buffer_load_dwordx4 a[96:99], v248, s[72:75], s55 offen offset:0 sc1\n"
"buffer_load_dwordx4 a[100:103], v248, s[72:75], s55 offen offset:1024 sc1\n"
"buffer_load_dwordx4 a[104:107], v248, s[72:75], s55 offen offset:2048 sc1\n"
"buffer_load_dwordx4 a[108:111], v248, s[72:75], s55 offen offset:3072 sc1\n"
"s_add_u32 s55, s79, 28672\n"
"buffer_load_dwordx4 a[112:115], v248, s[72:75], s55 offen offset:0 sc1\n"
"buffer_load_dwordx4 a[116:119], v248, s[72:75], s55 offen offset:1024 sc1\n"
"buffer_load_dwordx4 a[120:123], v248, s[72:75], s55 offen offset:2048 sc1\n"
"Lfused_hop13:\n"
"s_cbranch_execz Lfused_hopend\n"
"buffer_load_dwordx4 a[124:127], v248, s[72:75], s55 offen offset:3072 sc1\n"
"v_cvt_pk_bf16_f32 v176, v176, v177\n"
"v_cvt_pk_bf16_f32 v177, v178, v179\n"
"v_cvt_pk_bf16_f32 v178, v180, v181\n"
"v_cvt_pk_bf16_f32 v179, v182, v183\n"
"v_cvt_pk_bf16_f32 v180, v184, v185\n"
"v_cvt_pk_bf16_f32 v181, v186, v187\n"
"v_cvt_pk_bf16_f32 v182, v188, v189\n"
"v_cvt_pk_bf16_f32 v183, v190, v191\n"
"s_lshr_b32 s50, s10, 4\n"
"s_mul_i32 s50, s50, 131072\n"
"s_mul_i32 s51, s10, 3072\n"
"s_sub_u32 s50, s50, s51\n"
"s_sub_u32 s50, s50, 16384\n"
"s_add_u32 s51, s48, s50\n"
"s_add_u32 s50, s47, s50\n"
"s_waitcnt lgkmcnt(0)\n"
"v_add_f32 v128, v128, v136\n"
"v_add_f32 v129, v129, v137\n"
"v_add_f32 v130, v130, v138\n"
"v_add_f32 v131, v131, v139\n"
"v_add_f32 v132, v132, v140\n"
"v_add_f32 v133, v133, v141\n"
"v_add_f32 v134, v134, v142\n"
"v_add_f32 v135, v135, v143\n"
"v_add_f32 v144, v144, v152\n"
"v_add_f32 v145, v145, v153\n"
"v_add_f32 v146, v146, v154\n"
"v_add_f32 v147, v147, v155\n"
"v_add_f32 v148, v148, v156\n"
"v_add_f32 v149, v149, v157\n"
"v_add_f32 v150, v150, v158\n"
"v_add_f32 v151, v151, v159\n"
"v_add_f32 v128, v128, v144\n"
"v_add_f32 v129, v129, v145\n"
"v_add_f32 v130, v130, v146\n"
"v_add_f32 v131, v131, v147\n"
"v_add_f32 v132, v132, v148\n"
"v_add_f32 v133, v133, v149\n"
"v_add_f32 v134, v134, v150\n"
"v_add_f32 v135, v135, v151\n"
"v_cvt_pk_bf16_f32 v128, v128, v129\n"
"v_cvt_pk_bf16_f32 v129, v130, v131\n"
"v_cvt_pk_bf16_f32 v130, v132, v133\n"
"v_cvt_pk_bf16_f32 v131, v134, v135\n"
"s_nop 1\n"
"v_mfma_f32_16x16x16_bf16 v[140:143], v[130:131], v[176:177], 0\n"
"v_mfma_f32_16x16x16_bf16 v[136:139], v[128:129], v[176:177], 0\n"
"v_mfma_f32_16x16x16_bf16 v[148:151], v[130:131], v[178:179], 0\n"
"v_mfma_f32_16x16x16_bf16 v[144:147], v[128:129], v[178:179], 0\n"
"v_mfma_f32_16x16x16_bf16 v[156:159], v[130:131], v[180:181], 0\n"
"v_mfma_f32_16x16x16_bf16 v[152:155], v[128:129], v[180:181], 0\n"
"v_mfma_f32_16x16x16_bf16 v[164:167], v[130:131], v[182:183], 0\n"
"v_mfma_f32_16x16x16_bf16 v[160:163], v[128:129], v[182:183], 0\n"
"s_nop 0\n"
"v_add_f32 v140, 897988541, v140\n"
"s_nop 0\n"
"v_add_f32 v148, 897988541, v148\n"
"s_nop 0\n"
"v_add_f32 v156, 897988541, v156\n"
"s_nop 0\n"
"v_add_f32 v164, 897988541, v164\n"
"v_rcp_f32 v140, v140\n"
"v_rcp_f32 v148, v148\n"
"v_rcp_f32 v156, v156\n"
"v_rcp_f32 v164, v164\n"
"v_mul_f32 v136, v136, v140\n"
"v_mul_f32 v137, v137, v140\n"
"v_mul_f32 v138, v138, v140\n"
"v_mul_f32 v139, v139, v140\n"
"v_cvt_pk_bf16_f32 v128, v136, v137\n"
"v_cvt_pk_bf16_f32 v129, v138, v139\n"
"v_mul_f32 v144, v144, v148\n"
"v_mul_f32 v145, v145, v148\n"
"v_mul_f32 v146, v146, v148\n"
"v_mul_f32 v147, v147, v148\n"
"v_cvt_pk_bf16_f32 v130, v144, v145\n"
"v_cvt_pk_bf16_f32 v131, v146, v147\n"
"buffer_store_dwordx4 v[128:131], v248, s[24:27], s50 offen sc1\n"
"v_mul_f32 v152, v152, v156\n"
"v_mul_f32 v153, v153, v156\n"
"v_mul_f32 v154, v154, v156\n"
"v_mul_f32 v155, v155, v156\n"
"v_cvt_pk_bf16_f32 v132, v152, v153\n"
"v_cvt_pk_bf16_f32 v133, v154, v155\n"
"v_mul_f32 v160, v160, v164\n"
"v_mul_f32 v161, v161, v164\n"
"v_mul_f32 v162, v162, v164\n"
"v_mul_f32 v163, v163, v164\n"
"v_cvt_pk_bf16_f32 v134, v160, v161\n"
"v_cvt_pk_bf16_f32 v135, v162, v163\n"
"buffer_store_dwordx4 v[132:135], v248, s[24:27], s51 offen sc1\n"
"s_and_b32 s50, s90, 0x1000\n"
"v_add_u32 v246, s50, v248\n"
"v_add_u32 v250, 0x10000, v246\n"
"v_add_u32 v251, 131072, v246\n"
"s_add_u32 m0, s90, 0\n"
"ds_read_b128 v[64:67], v250 offset:16384\n"
"s_lshl_b32 s50, s76, 2\n"
"s_add_u32 s50, s50, s12\n"
"ds_read_b128 v[68:71], v250 offset:17408\n"
"s_add_u32 s55, s45, 65536\n"
"s_mul_i32 s64, s50, 294912\n"
"ds_read_b128 v[72:75], v250 offset:18432\n"
"s_lshr_b32 s50, s78, 1\n"
"ds_read_b128 v[76:79], v250 offset:19456\n"
"buffer_load_dwordx4 v248, s[72:75], s55 offen offset:0 sc1 lds\n"
"s_mul_i32 s50, s50, 131072\n"
"ds_read_b128 v[80:83], v250 offset:49152\n"
"s_add_u32 s65, s64, s50\n"
"s_lshl_b32 s50, s78, 13\n"
"buffer_load_dwordx4 v248, s[72:75], s55 offen offset:1024 sc1 lds\n"
"ds_read_b128 v[84:87], v250 offset:50176\n"
"s_add_u32 s65, s65, s50\n"
"ds_read_b128 v[88:91], v250 offset:51200\n"
"s_add_u32 s65, s65, 131072\n"
"buffer_load_dwordx4 v248, s[72:75], s55 offen offset:2048 sc1 lds\n"
"ds_read_b128 v[92:95], v250 offset:52224\n"
"s_lshl_b32 s50, s78, 15\n"
"ds_read_b128 v[96:99], v250 offset:24576\n"
"s_add_u32 s64, s64, s50\n"
"buffer_load_dwordx4 v248, s[72:75], s55 offen offset:3072 sc1 lds\n"
"s_sub_u32 s50, 15, s76\n"
"ds_read_b128 v[100:103], v250 offset:25600\n"
"s_min_u32 s50, s50, s76\n"
"ds_read_b128 v[104:107], v250 offset:26624\n"
"s_add_u32 m0, s90, 16384\n"
"s_lshl_b32 s51, s50, 3\n"
"ds_read_b128 v[108:111], v250 offset:27648\n"
"s_add_u32 s51, s51, 4096\n"
"ds_read_b128 v[112:115], v250 offset:57344\n"
"s_add_u32 s55, s45, 73728\n"
"v_lshl_add_u32 v244, v248, 3, s51\n"
"s_lshl_b32 s50, s50, 8\n"
"ds_read_b128 v[116:119], v250 offset:58368\n"
"s_lshl_b32 s51, s78, 6\n"
"buffer_load_dwordx4 v248, s[72:75], s55 offen offset:0 sc1 lds\n"
"ds_read_b128 v[120:123], v250 offset:59392\n"
"s_add_u32 s50, s50, s51\n"
"ds_read_b128 v[124:127], v250 offset:60416\n"
"s_add_u32 s50, s50, 24576\n"
"buffer_load_dwordx4 v248, s[72:75], s55 offen offset:1024 sc1 lds\n"
"ds_read_b128 a[64:67], v250 offset:0\n"
"v_bfe_u32 v245, v248, 4, 3\n"
"v_lshl_add_u32 v245, v245, 3, s50\n"
"ds_read_b128 a[68:71], v250 offset:1024\n"
"buffer_load_dwordx4 v248, s[72:75], s55 offen offset:2048 sc1 lds\n"
"v_cmp_gt_u32 vcc, 0x200, v248\n"
"ds_read_b128 a[72:75], v250 offset:2048\n"
"v_add_u32 v232, 4096, v245\n"
"ds_read_b128 a[76:79], v250 offset:3072\n"
"buffer_load_dwordx4 v248, s[72:75], s55 offen offset:3072 sc1 lds\n"
"v_cndmask_b32 v244, v232, v244, vcc\n"
"ds_read_b128 a[80:83], v250 offset:32768\n"
"s_and_b32 s50, s12, 2\n"
"s_lshl_b32 s50, s50, 1\n"
"s_add_u32 m0, s90, 32768\n"
"ds_read_b128 a[84:87], v250 offset:33792\n"
"s_add_u32 s56, s50, s78\n"
"ds_read_b128 a[88:91], v250 offset:34816\n"
"s_lshl_b32 s50, s56, 11\n"
"s_add_u32 s55, s45, 81920\n"
"ds_read_b128 a[92:95], v250 offset:35840\n"
"s_add_u32 s50, s50, 0x10000\n"
"ds_read_b128 a[160:163], v251 offset:16384\n"
"v_add_u32 v232, s50, v248\n"
"buffer_load_dwordx4 v248, s[72:75], s55 offen offset:0 sc1 lds\n"
"v_xor_b32 v233, 2048, v232\n"
"ds_read_b128 a[164:167], v251 offset:17408\n"
"v_xor_b32 v234, 4096, v232\n"
"ds_read_b128 a[168:171], v251 offset:18432\n"
"buffer_load_dwordx4 v248, s[72:75], s55 offen offset:1024 sc1 lds\n"
"v_xor_b32 v235, 6144, v232\n"
"ds_read_b128 a[172:175], v251 offset:19456\n"
"s_and_b32 s51, s78, 1\n"
"ds_read_b128 a[176:179], v251 offset:24576\n"
"buffer_load_dwordx4 v248, s[72:75], s55 offen offset:2048 sc1 lds\n"
"s_mul_i32 s51, s51, 2040\n"
"v_subrev_u32 v240, s51, v232\n"
"ds_read_b128 a[180:183], v251 offset:25600\n"
"v_xor_b32 v241, 8, v240\n"
"buffer_load_dwordx4 v248, s[72:75], s55 offen offset:3072 sc1 lds\n"
"ds_read_b128 a[184:187], v251 offset:26624\n"
"v_xor_b32 v242, 4096, v240\n"
"ds_read_b128 a[188:191], v251 offset:27648\n"
"v_xor_b32 v243, 4104, v240\n"
"s_waitcnt lgkmcnt(0)\n"
"s_barrier\n"
"s_cmp_eq_u32 s11, 7\n"
"s_cbranch_scc0 Lfused_pre7\n"
"s_add_u32 s55, s79, 0\n"
"buffer_load_dwordx4 a[32:35], v248, s[72:75], s55 offen offset:0 sc1\n"
"buffer_load_dwordx4 a[36:39], v248, s[72:75], s55 offen offset:1024 sc1\n"
"buffer_load_dwordx4 a[40:43], v248, s[72:75], s55 offen offset:2048 sc1\n"
"buffer_load_dwordx4 a[44:47], v248, s[72:75], s55 offen offset:3072 sc1\n"
"s_add_u32 s55, s79, 4096\n"
"buffer_load_dwordx4 a[48:51], v248, s[72:75], s55 offen offset:0 sc1\n"
"buffer_load_dwordx4 a[52:55], v248, s[72:75], s55 offen offset:1024 sc1\n"
"buffer_load_dwordx4 a[56:59], v248, s[72:75], s55 offen offset:2048 sc1\n"
"buffer_load_dwordx4 a[60:63], v248, s[72:75], s55 offen offset:3072 sc1\n"
"Lfused_pre7:\n"
"s_mov_b32 s58, 32768\n"
"Lfused_pl3:\n"
"global_load_dwordx2 v[250:251], v245, s[62:63] sc1\n"
"s_waitcnt vmcnt(0)\n"
"v_cmp_ne_u64 vcc, s[98:99], v[250:251]\n"
"s_cbranch_vccz Lfused_pd3\n"
"s_sleep 1\n"
"s_sub_u32 s58, s58, 1\n"
"s_cbranch_scc0 Lfused_pl3\n"
"Lfused_pd3:\n"
"s_add_u32 m0, s90, 65536\n"
"s_add_u32 s51, s64, 0\n"
"buffer_load_dwordx4 v248, s[24:27], s51 offen offset:0 sc1 lds\n"
"buffer_load_dwordx4 v248, s[24:27], s51 offen offset:1024 sc1 lds\n"
"s_add_u32 s51, s64, 145408\n"
"buffer_load_dwordx4 v248, s[24:27], s51 offen offset:2048 sc1 lds\n"
"buffer_load_dwordx4 v248, s[24:27], s51 offen offset:3072 sc1 lds\n"
"s_add_u32 m0, s90, 81920\n"
"s_add_u32 s51, s64, 2048\n"
"buffer_load_dwordx4 v248, s[24:27], s51 offen offset:0 sc1 lds\n"
"buffer_load_dwordx4 v248, s[24:27], s51 offen offset:1024 sc1 lds\n"
"s_add_u32 s51, s64, 147456\n"
"buffer_load_dwordx4 v248, s[24:27], s51 offen offset:2048 sc1 lds\n"
"buffer_load_dwordx4 v248, s[24:27], s51 offen offset:3072 sc1 lds\n"
"s_add_u32 m0, s90, 98304\n"
"s_add_u32 s51, s64, 4096\n"
"buffer_load_dwordx4 v248, s[24:27], s51 offen offset:0 sc1 lds\n"
"buffer_load_dwordx4 v248, s[24:27], s51 offen offset:1024 sc1 lds\n"
"s_add_u32 s51, s64, 149504\n"
"buffer_load_dwordx4 v248, s[24:27], s51 offen offset:2048 sc1 lds\n"
"buffer_load_dwordx4 v248, s[24:27], s51 offen offset:3072 sc1 lds\n"
"s_add_u32 m0, s90, 114688\n"
"s_add_u32 s51, s64, 6144\n"
"buffer_load_dwordx4 v248, s[24:27], s51 offen offset:0 sc1 lds\n"
"buffer_load_dwordx4 v248, s[24:27], s51 offen offset:1024 sc1 lds\n"
"s_add_u32 s51, s64, 151552\n"
"buffer_load_dwordx4 v248, s[24:27], s51 offen offset:2048 sc1 lds\n"
"buffer_load_dwordx4 v248, s[24:27], s51 offen offset:3072 sc1 lds\n"
"s_waitcnt vmcnt(12)\n"
"s_barrier\n"
"ds_read_b128 a[192:195], v233 offset:0\n"
"ds_read_b128 a[196:199], v233 offset:1024\n"
"ds_read_b128 a[200:203], v234 offset:0\n"
"ds_read_b128 a[204:207], v234 offset:1024\n"
"ds_read_b128 a[208:211], v235 offset:0\n"
"ds_read_b128 a[212:215], v235 offset:1024\n"
"ds_read_b128 a[216:219], v232 offset:0\n"
"ds_read_b128 a[220:223], v232 offset:1024\n"
"s_cmp_eq_u32 s12, 0\n"
"s_cbranch_scc0 Lfused_p3b\n"
"s_lshl_b32 s58, s11, 8\n"
"s_lshl_b32 s59, s10, 3\n"
"s_add_u32 s58, s58, s59\n"
"s_add_u32 s58, s58, 28672\n"
"v_mov_b32 v250, s98\n"
"v_mov_b32 v251, s99\n"
"v_mov_b32 v252, s58\n"
"s_mov_b64 exec, 1\n"
"global_store_dwordx2 v252, v[250:251], s[62:63] sc1\n"
"s_mov_b64 exec, -1\n"
"Lfused_p3b:\n"
"s_waitcnt vmcnt(8) lgkmcnt(0)\n"
"v_mfma_f32_16x16x32_bf16 v[16:19], a[32:35], a[192:195], 0\n"
"s_barrier\n"
"v_mfma_f32_16x16x32_bf16 v[16:19], a[48:51], a[196:199], v[16:19]\n"
"ds_read_b128 a[224:227], v233 offset:16384\n"
"s_add_u32 m0, s90, 131072\n"
"v_mfma_f32_16x16x32_bf16 v[20:23], a[36:39], a[192:195], 0\n"
"s_add_u32 s51, s64, 8192\n"
"v_mfma_f32_16x16x32_bf16 v[20:23], a[52:55], a[196:199], v[20:23]\n"
"ds_read_b128 a[228:231], v233 offset:17408\n"
"v_mfma_f32_16x16x32_bf16 v[24:27], a[40:43], a[192:195], 0\n"
"buffer_load_dwordx4 v248, s[24:27], s51 offen offset:0 sc1 lds\n"
"v_mfma_f32_16x16x32_bf16 v[24:27], a[56:59], a[196:199], v[24:27]\n"
"v_mfma_f32_16x16x32_bf16 v[28:31], a[44:47], a[192:195], 0\n"
"buffer_load_dwordx4 v248, s[24:27], s51 offen offset:1024 sc1 lds\n"
"v_mfma_f32_16x16x32_bf16 v[28:31], a[60:63], a[196:199], v[28:31]\n"
"ds_read_b128 a[232:235], v234 offset:16384\n"
"s_add_u32 s51, s64, 153600\n"
"v_mfma_f32_16x16x32_bf16 v[32:35], a[32:35], a[200:203], 0\n"
"ds_read_b128 a[236:239], v234 offset:17408\n"
"v_mfma_f32_16x16x32_bf16 v[32:35], a[48:51], a[204:207], v[32:35]\n"
"buffer_load_dwordx4 v248, s[24:27], s51 offen offset:2048 sc1 lds\n"
"v_mfma_f32_16x16x32_bf16 v[36:39], a[36:39], a[200:203], 0\n"
"v_mfma_f32_16x16x32_bf16 v[36:39], a[52:55], a[204:207], v[36:39]\n"
"buffer_load_dwordx4 v248, s[24:27], s51 offen offset:3072 sc1 lds\n"
"v_mfma_f32_16x16x32_bf16 v[40:43], a[40:43], a[200:203], 0\n"
"ds_read_b128 a[240:243], v235 offset:16384\n"
"v_mfma_f32_16x16x32_bf16 v[40:43], a[56:59], a[204:207], v[40:43]\n"
"ds_read_b128 a[244:247], v235 offset:17408\n"
"s_add_u32 m0, s90, 147456\n"
"v_mfma_f32_16x16x32_bf16 v[44:47], a[44:47], a[200:203], 0\n"
"s_add_u32 s51, s64, 10240\n"
"v_mfma_f32_16x16x32_bf16 v[44:47], a[60:63], a[204:207], v[44:47]\n"
"v_mfma_f32_16x16x32_bf16 v[48:51], a[32:35], a[208:211], 0\n"
"buffer_load_dwordx4 v248, s[24:27], s51 offen offset:0 sc1 lds\n"
"v_mfma_f32_16x16x32_bf16 v[48:51], a[48:51], a[212:215], v[48:51]\n"
"ds_read_b128 a[248:251], v232 offset:16384\n"
"v_mfma_f32_16x16x32_bf16 v[52:55], a[36:39], a[208:211], 0\n"
"buffer_load_dwordx4 v248, s[24:27], s51 offen offset:1024 sc1 lds\n"
"v_mfma_f32_16x16x32_bf16 v[52:55], a[52:55], a[212:215], v[52:55]\n"
"ds_read_b128 a[252:255], v232 offset:17408\n"
"s_add_u32 s51, s64, 155648\n"
"v_mfma_f32_16x16x32_bf16 v[56:59], a[40:43], a[208:211], 0\n"
"v_mfma_f32_16x16x32_bf16 v[56:59], a[56:59], a[212:215], v[56:59]\n"
"buffer_load_dwordx4 v248, s[24:27], s51 offen offset:2048 sc1 lds\n"
"v_mfma_f32_16x16x32_bf16 v[60:63], a[44:47], a[208:211], 0\n"
"v_mfma_f32_16x16x32_bf16 v[60:63], a[60:63], a[212:215], v[60:63]\n"
"buffer_load_dwordx4 v248, s[24:27], s51 offen offset:3072 sc1 lds\n"
"v_mfma_f32_16x16x32_bf16 v[0:3], a[32:35], a[216:219], 0\n"
"v_mfma_f32_16x16x32_bf16 v[0:3], a[48:51], a[220:223], v[0:3]\n"
"s_add_u32 m0, s90, 49152\n"
"v_mfma_f32_16x16x32_bf16 v[4:7], a[36:39], a[216:219], 0\n"
"s_add_u32 s55, s45, 90112\n"
"v_mfma_f32_16x16x32_bf16 v[4:7], a[52:55], a[220:223], v[4:7]\n"
"v_mfma_f32_16x16x32_bf16 v[8:11], a[40:43], a[216:219], 0\n"
"buffer_load_dwordx4 v248, s[72:75], s55 offen offset:0 sc1 lds\n"
"v_mfma_f32_16x16x32_bf16 v[8:11], a[56:59], a[220:223], v[8:11]\n"
"v_mfma_f32_16x16x32_bf16 v[12:15], a[44:47], a[216:219], 0\n"
"buffer_load_dwordx4 v248, s[72:75], s55 offen offset:1024 sc1 lds\n"
"v_mfma_f32_16x16x32_bf16 v[12:15], a[60:63], a[220:223], v[12:15]\n"
"s_waitcnt vmcnt(14) lgkmcnt(0)\n"
"v_mfma_f32_16x16x32_bf16 v[16:19], a[128:131], a[224:227], v[16:19]\n"
"s_barrier\n"
"v_mfma_f32_16x16x32_bf16 v[16:19], a[144:147], a[228:231], v[16:19]\n"
"ds_read_b128 v[128:131], v233 offset:32768\n"
"s_add_u32 m0, s90, 65536\n"
"v_mfma_f32_16x16x32_bf16 v[20:23], a[132:135], a[224:227], v[20:23]\n"
"ds_read_b128 v[132:135], v233 offset:33792\n"
"s_add_u32 s51, s64, 12288\n"
"v_mfma_f32_16x16x32_bf16 v[20:23], a[148:151], a[228:231], v[20:23]\n"
"ds_read_b128 v[136:139], v234 offset:32768\n"
"v_mfma_f32_16x16x32_bf16 v[24:27], a[136:139], a[224:227], v[24:27]\n"
"buffer_load_dwordx4 v248, s[24:27], s51 offen offset:0 sc1 lds\n"
"v_mfma_f32_16x16x32_bf16 v[24:27], a[152:155], a[228:231], v[24:27]\n"
"ds_read_b128 v[140:143], v234 offset:33792\n"
"v_mfma_f32_16x16x32_bf16 v[28:31], a[140:143], a[224:227], v[28:31]\n"
"buffer_load_dwordx4 v248, s[24:27], s51 offen offset:1024 sc1 lds\n"
"v_mfma_f32_16x16x32_bf16 v[28:31], a[156:159], a[228:231], v[28:31]\n"
"ds_read_b128 v[144:147], v235 offset:32768\n"
"s_add_u32 s51, s64, 157696\n"
"v_mfma_f32_16x16x32_bf16 v[32:35], a[128:131], a[232:235], v[32:35]\n"
"ds_read_b128 v[148:151], v235 offset:33792\n"
"ds_read_b128 v[152:155], v232 offset:32768\n"
"v_mfma_f32_16x16x32_bf16 v[32:35], a[144:147], a[236:239], v[32:35]\n"
"buffer_load_dwordx4 v248, s[24:27], s51 offen offset:2048 sc1 lds\n"
"v_mfma_f32_16x16x32_bf16 v[36:39], a[132:135], a[232:235], v[36:39]\n"
"ds_read_b128 v[156:159], v232 offset:33792\n"
"v_mfma_f32_16x16x32_bf16 v[36:39], a[148:151], a[236:239], v[36:39]\n"
"buffer_load_dwordx4 v248, s[24:27], s51 offen offset:3072 sc1 lds\n"
"v_mfma_f32_16x16x32_bf16 v[40:43], a[136:139], a[232:235], v[40:43]\n"
"ds_read_b128 v[160:163], v246 offset:0\n"
"ds_read_b128 v[164:167], v246 offset:1024\n"
"v_mfma_f32_16x16x32_bf16 v[40:43], a[152:155], a[236:239], v[40:43]\n"
"ds_read_b128 v[168:171], v246 offset:2048\n"
"s_add_u32 m0, s90, 81920\n"
"v_mfma_f32_16x16x32_bf16 v[44:47], a[140:143], a[232:235], v[44:47]\n"
"ds_read_b128 v[172:175], v246 offset:3072\n"
"s_add_u32 s51, s64, 14336\n"
"v_mfma_f32_16x16x32_bf16 v[44:47], a[156:159], a[236:239], v[44:47]\n"
"s_add_u32 s68, s8, 67108864\n"
"s_addc_u32 s69, s9, 0\n"
"v_mfma_f32_16x16x32_bf16 v[48:51], a[128:131], a[240:243], v[48:51]\n"
"buffer_load_dwordx4 v248, s[24:27], s51 offen offset:0 sc1 lds\n"
"v_mfma_f32_16x16x32_bf16 v[48:51], a[144:147], a[244:247], v[48:51]\n"
"ds_read_b128 v[176:179], v246 offset:8192\n"
"ds_read_b128 v[180:183], v246 offset:9216\n"
"v_mfma_f32_16x16x32_bf16 v[52:55], a[132:135], a[240:243], v[52:55]\n"
"buffer_load_dwordx4 v248, s[24:27], s51 offen offset:1024 sc1 lds\n"
"v_mfma_f32_16x16x32_bf16 v[52:55], a[148:151], a[244:247], v[52:55]\n"
"ds_read_b128 v[184:187], v246 offset:10240\n"
"s_add_u32 s51, s64, 159744\n"
"v_mfma_f32_16x16x32_bf16 v[56:59], a[136:139], a[240:243], v[56:59]\n"
"ds_read_b128 v[188:191], v246 offset:11264\n"
"s_and_b32 s69, s69, 0xffff\n"
"v_mfma_f32_16x16x32_bf16 v[56:59], a[152:155], a[244:247], v[56:59]\n"
"buffer_load_dwordx4 v248, s[24:27], s51 offen offset:2048 sc1 lds\n"
"v_mfma_f32_16x16x32_bf16 v[60:63], a[140:143], a[240:243], v[60:63]\n"
"s_mov_b32 s70, 8388608\n"
"s_mov_b32 s71, 0x20000\n"
"v_mfma_f32_16x16x32_bf16 v[60:63], a[156:159], a[244:247], v[60:63]\n"
"buffer_load_dwordx4 v248, s[24:27], s51 offen offset:3072 sc1 lds\n"
"v_mfma_f32_16x16x32_bf16 v[0:3], a[128:131], a[248:251], v[0:3]\n"
"s_lshl_b32 s16, s13, 15\n"
"s_lshl_b32 s17, s78, 13\n"
"v_mfma_f32_16x16x32_bf16 v[0:3], a[144:147], a[252:255], v[0:3]\n"
"s_add_u32 m0, s90, 49152\n"
"s_add_u32 s16, s16, s17\n"
"v_mfma_f32_16x16x32_bf16 v[4:7], a[132:135], a[248:251], v[4:7]\n"
"s_add_u32 s55, s45, 90112\n"
"s_lshr_b32 s17, s90, 1\n"
"v_mfma_f32_16x16x32_bf16 v[4:7], a[148:151], a[252:255], v[4:7]\n"
"s_add_u32 s16, s16, s17\n"
"s_xor_b32 s17, s16, 65536\n"
"v_mfma_f32_16x16x32_bf16 v[8:11], a[136:139], a[248:251], v[8:11]\n"
"buffer_load_dwordx4 v248, s[72:75], s55 offen offset:2048 sc1 lds\n"
"v_mfma_f32_16x16x32_bf16 v[8:11], a[152:155], a[252:255], v[8:11]\n"
"s_xor_b32 s20, s16, 8192\n"
"s_xor_b32 s18, s16, 131072\n"
"v_mfma_f32_16x16x32_bf16 v[12:15], a[140:143], a[248:251], v[12:15]\n"
"buffer_load_dwordx4 v248, s[72:75], s55 offen offset:3072 sc1 lds\n"
"v_mfma_f32_16x16x32_bf16 v[12:15], a[156:159], a[252:255], v[12:15]\n"
"s_xor_b32 s21, s16, 16384\n"
"s_xor_b32 s19, s16, 196608\n"
"s_waitcnt vmcnt(20) lgkmcnt(0)\n"
"v_mfma_f32_16x16x32_bf16 v[16:19], a[0:3], v[128:131], v[16:19]\n"
"s_barrier\n"
"v_mfma_f32_16x16x32_bf16 v[16:19], a[16:19], v[132:135], v[16:19]\n"
"ds_read_b128 v[192:195], v233 offset:49152\n"
"s_add_u32 m0, s90, 98304\n"
"v_mfma_f32_16x16x32_bf16 v[20:23], a[4:7], v[128:131], v[20:23]\n"
"v_mfma_f32_16x16x32_bf16 v[20:23], a[20:23], v[132:135], v[20:23]\n"
"ds_read_b128 v[196:199], v233 offset:50176\n"
"s_add_u32 s51, s64, 16384\n"
"v_mfma_f32_16x16x32_bf16 v[24:27], a[8:11], v[128:131], v[24:27]\n"
"s_xor_b32 s22, s16, 24576\n"
"s_lshr_b32 s16, s16, 8\n"
"v_mfma_f32_16x16x32_bf16 v[24:27], a[24:27], v[132:135], v[24:27]\n"
"s_add_u32 s16, s16, 32768\n"
"v_bfe_u32 v245, v248, 4, 2\n"
"v_mfma_f32_16x16x32_bf16 v[28:31], a[12:15], v[128:131], v[28:31]\n"
"buffer_load_dwordx4 v248, s[24:27], s51 offen offset:0 sc1 lds\n"
"v_mfma_f32_16x16x32_bf16 v[28:31], a[28:31], v[132:135], v[28:31]\n"
"ds_read_b128 v[200:203], v234 offset:49152\n"
"v_max_u32 v245, 1, v245\n"
"v_mfma_f32_16x16x32_bf16 v[32:35], a[0:3], v[136:139], v[32:35]\n"
"ds_read_b128 v[204:207], v234 offset:50176\n"
"v_lshlrev_b32 v247, 8, v245\n"
"v_mfma_f32_16x16x32_bf16 v[32:35], a[16:19], v[140:143], v[32:35]\n"
"buffer_load_dwordx4 v248, s[24:27], s51 offen offset:1024 sc1 lds\n"
"v_mfma_f32_16x16x32_bf16 v[36:39], a[4:7], v[136:139], v[36:39]\n"
"v_xor_b32 v247, s16, v247\n"
"v_lshlrev_b32 v245, 5, v245\n"
"v_mfma_f32_16x16x32_bf16 v[36:39], a[20:23], v[140:143], v[36:39]\n"
"ds_read_b128 v[208:211], v235 offset:49152\n"
"v_xor_b32 v245, s16, v245\n"
"v_mfma_f32_16x16x32_bf16 v[40:43], a[8:11], v[136:139], v[40:43]\n"
"s_add_u32 s51, s64, 161792\n"
"v_bfe_u32 v254, v248, 4, 4\n"
"v_mfma_f32_16x16x32_bf16 v[40:43], a[24:27], v[140:143], v[40:43]\n"
"ds_read_b128 v[212:215], v235 offset:50176\n"
"v_lshlrev_b32 v254, 10, v254\n"
"v_mfma_f32_16x16x32_bf16 v[44:47], a[12:15], v[136:139], v[44:47]\n"
"v_lshrrev_b32 v253, 8, v248\n"
"v_lshl_add_u32 v254, v253, 4, v254\n"
"v_mfma_f32_16x16x32_bf16 v[44:47], a[28:31], v[140:143], v[44:47]\n"
"buffer_load_dwordx4 v248, s[24:27], s51 offen offset:2048 sc1 lds\n"
"v_mfma_f32_16x16x32_bf16 v[48:51], a[0:3], v[144:147], v[48:51]\n"
"ds_read_b128 v[216:219], v232 offset:49152\n"
"v_mfma_f32_16x16x32_bf16 v[48:51], a[16:19], v[148:151], v[48:51]\n"
"buffer_load_dwordx4 v248, s[24:27], s51 offen offset:3072 sc1 lds\n"
"v_mfma_f32_16x16x32_bf16 v[52:55], a[4:7], v[144:147], v[52:55]\n"
"ds_read_b128 v[220:223], v232 offset:50176\n"
"v_mfma_f32_16x16x32_bf16 v[52:55], a[20:23], v[148:151], v[52:55]\n"
"s_add_u32 s52, s8, 55574528\n"
"s_addc_u32 s53, s9, 0\n"
"v_mfma_f32_16x16x32_bf16 v[56:59], a[8:11], v[144:147], v[56:59]\n"
"s_add_u32 m0, s90, 0\n"
"s_lshl_b32 s50, s76, 3\n"
"v_mfma_f32_16x16x32_bf16 v[56:59], a[24:27], v[148:151], v[56:59]\n"
"s_add_u32 s56, s56, s50\n"
"s_lshl_b32 s50, s56, 6\n"
"v_mfma_f32_16x16x32_bf16 v[60:63], a[12:15], v[144:147], v[60:63]\n"
"v_bfe_u32 v236, v248, 4, 4\n"
"v_lshl_add_u32 v236, v236, 2, s50\n"
"v_mfma_f32_16x16x32_bf16 v[60:63], a[28:31], v[148:151], v[60:63]\n"
"s_add_u32 s55, s46, 0\n"
"v_xor_b32 v237, 64, v236\n"
"v_mfma_f32_16x16x32_bf16 v[0:3], a[0:3], v[152:155], v[0:3]\n"
"v_xor_b32 v238, 128, v236\n"
"v_xor_b32 v239, 192, v236\n"
"v_mfma_f32_16x16x32_bf16 v[0:3], a[16:19], v[156:159], v[0:3]\n"
"v_mfma_f32_16x16x32_bf16 v[4:7], a[4:7], v[152:155], v[4:7]\n"
"buffer_load_dwordx4 v248, s[72:75], s55 offen offset:0 sc1 lds\n"
"v_mfma_f32_16x16x32_bf16 v[4:7], a[20:23], v[156:159], v[4:7]\n"
"v_mfma_f32_16x16x32_bf16 v[8:11], a[8:11], v[152:155], v[8:11]\n"
"v_mfma_f32_16x16x32_bf16 v[8:11], a[24:27], v[156:159], v[8:11]\n"
"buffer_load_dwordx4 v248, s[72:75], s55 offen offset:1024 sc1 lds\n"
"v_mfma_f32_16x16x32_bf16 v[12:15], a[12:15], v[152:155], v[12:15]\n"
"v_mfma_f32_16x16x32_bf16 v[12:15], a[28:31], v[156:159], v[12:15]\n"
"s_waitcnt vmcnt(22) lgkmcnt(0)\n"
"v_mfma_f32_16x16x32_bf16 v[16:19], a[96:99], v[192:195], v[16:19]\n"
"s_barrier\n"
"v_mfma_f32_16x16x32_bf16 v[16:19], a[112:115], v[196:199], v[16:19]\n"
"v_add_u32 v232, 32768, v232\n"
"s_add_u32 m0, s90, 114688\n"
"v_mfma_f32_16x16x32_bf16 v[20:23], a[100:103], v[192:195], v[20:23]\n"
"v_add_u32 v233, 32768, v233\n"
"v_mfma_f32_16x16x32_bf16 v[20:23], a[116:119], v[196:199], v[20:23]\n"
"v_add_u32 v234, 32768, v234\n"
"s_add_u32 s51, s64, 18432\n"
"v_mfma_f32_16x16x32_bf16 v[24:27], a[104:107], v[192:195], v[24:27]\n"
"v_add_u32 v235, 32768, v235\n"
"v_mfma_f32_16x16x32_bf16 v[24:27], a[120:123], v[196:199], v[24:27]\n"
"ds_read_b128 v[224:227], v233 offset:32768\n"
"v_mfma_f32_16x16x32_bf16 v[28:31], a[108:111], v[192:195], v[28:31]\n"
"buffer_load_dwordx4 v248, s[24:27], s51 offen offset:0 sc1 lds\n"
"v_mfma_f32_16x16x32_bf16 v[28:31], a[124:127], v[196:199], v[28:31]\n"
"ds_read_b128 v[228:231], v233 offset:33792\n"
"ds_read_b128 a[216:219], v234 offset:32768\n"
"v_mfma_f32_16x16x32_bf16 v[32:35], a[96:99], v[200:203], v[32:35]\n"
"ds_read_b128 a[192:195], v234 offset:33792\n"
"v_mfma_f32_16x16x32_bf16 v[32:35], a[112:115], v[204:207], v[32:35]\n"
"buffer_load_dwordx4 v248, s[24:27], s51 offen offset:1024 sc1 lds\n"
"v_mfma_f32_16x16x32_bf16 v[36:39], a[100:103], v[200:203], v[36:39]\n"
"ds_read_b128 a[200:203], v235 offset:32768\n"
"ds_read_b128 a[208:211], v235 offset:33792\n"
"v_mfma_f32_16x16x32_bf16 v[36:39], a[116:119], v[204:207], v[36:39]\n"
"ds_read_b128 a[32:35], v232 offset:32768\n"
"v_mfma_f32_16x16x32_bf16 v[40:43], a[104:107], v[200:203], v[40:43]\n"
"ds_read_b128 a[36:39], v232 offset:33792\n"
"s_add_u32 s51, s64, 163840\n"
"v_mfma_f32_16x16x32_bf16 v[40:43], a[120:123], v[204:207], v[40:43]\n"
"ds_read_b128 a[40:43], v246 offset:16384\n"
"v_mfma_f32_16x16x32_bf16 v[44:47], a[108:111], v[200:203], v[44:47]\n"
"ds_read_b128 a[44:47], v246 offset:17408\n"
"v_mfma_f32_16x16x32_bf16 v[44:47], a[124:127], v[204:207], v[44:47]\n"
"buffer_load_dwordx4 v248, s[24:27], s51 offen offset:2048 sc1 lds\n"
"v_mfma_f32_16x16x32_bf16 v[48:51], a[96:99], v[208:211], v[48:51]\n"
"ds_read_b128 a[220:223], v246 offset:18432\n"
"ds_read_b128 a[196:199], v246 offset:19456\n"
"v_mfma_f32_16x16x32_bf16 v[48:51], a[112:115], v[212:215], v[48:51]\n"
"buffer_load_dwordx4 v248, s[24:27], s51 offen offset:3072 sc1 lds\n"
"v_mfma_f32_16x16x32_bf16 v[52:55], a[100:103], v[208:211], v[52:55]\n"
"ds_read_b128 a[204:207], v246 offset:24576\n"
"ds_read_b128 a[212:215], v246 offset:25600\n"
"v_mfma_f32_16x16x32_bf16 v[52:55], a[116:119], v[212:215], v[52:55]\n"
"ds_read_b128 a[48:51], v246 offset:26624\n"
"v_mfma_f32_16x16x32_bf16 v[56:59], a[104:107], v[208:211], v[56:59]\n"
"ds_read_b128 a[52:55], v246 offset:27648\n"
"s_add_u32 m0, s90, 0\n"
"v_mfma_f32_16x16x32_bf16 v[56:59], a[120:123], v[212:215], v[56:59]\n"
"v_mfma_f32_16x16x32_bf16 v[60:63], a[108:111], v[208:211], v[60:63]\n"
"v_mfma_f32_16x16x32_bf16 v[60:63], a[124:127], v[212:215], v[60:63]\n"
"s_add_u32 s55, s46, 0\n"
"v_mfma_f32_16x16x32_bf16 v[0:3], a[96:99], v[216:219], v[0:3]\n"
"v_mfma_f32_16x16x32_bf16 v[0:3], a[112:115], v[220:223], v[0:3]\n"
"v_mfma_f32_16x16x32_bf16 v[4:7], a[100:103], v[216:219], v[4:7]\n"
"buffer_load_dwordx4 v248, s[72:75], s55 offen offset:2048 sc1 lds\n"
"v_mfma_f32_16x16x32_bf16 v[4:7], a[116:119], v[220:223], v[4:7]\n"
"v_mfma_f32_16x16x32_bf16 v[8:11], a[104:107], v[216:219], v[8:11]\n"
"v_mfma_f32_16x16x32_bf16 v[8:11], a[120:123], v[220:223], v[8:11]\n"
"buffer_load_dwordx4 v248, s[72:75], s55 offen offset:3072 sc1 lds\n"
"v_mfma_f32_16x16x32_bf16 v[12:15], a[108:111], v[216:219], v[12:15]\n"
"v_mfma_f32_16x16x32_bf16 v[12:15], a[124:127], v[220:223], v[12:15]\n"
"s_waitcnt vmcnt(24) lgkmcnt(0)\n"
"v_mfma_f32_16x16x32_bf16 v[16:19], v[64:67], v[224:227], v[16:19]\n"
"s_barrier\n"
"v_mfma_f32_16x16x32_bf16 v[16:19], v[80:83], v[228:231], v[16:19]\n"
"global_load_dwordx2 v[250:251], v244, s[62:63] sc1\n"
"v_mfma_f32_16x16x32_bf16 v[20:23], v[68:71], v[224:227], v[20:23]\n"
"ds_read_b128 a[56:59], v233 offset:49152\n"
"v_mfma_f32_16x16x32_bf16 v[20:23], v[84:87], v[228:231], v[20:23]\n"
"ds_read_b128 a[60:63], v233 offset:50176\n"
"s_add_u32 m0, s90, 131072\n"
"v_mfma_f32_16x16x32_bf16 v[24:27], v[72:75], v[224:227], v[24:27]\n"
"v_mfma_f32_16x16x32_bf16 v[24:27], v[88:91], v[228:231], v[24:27]\n"
"v_mfma_f32_16x16x32_bf16 v[28:31], v[76:79], v[224:227], v[28:31]\n"
"ds_read_b128 a[248:251], v234 offset:49152\n"
"s_add_u32 s51, s64, 20480\n"
"v_mfma_f32_16x16x32_bf16 v[28:31], v[92:95], v[228:231], v[28:31]\n"
"v_mfma_f32_16x16x32_bf16 v[32:35], v[64:67], a[216:219], v[32:35]\n"
"buffer_load_dwordx4 v248, s[24:27], s51 offen offset:0 sc1 lds\n"
"v_mfma_f32_16x16x32_bf16 v[32:35], v[80:83], a[192:195], v[32:35]\n"
"ds_read_b128 a[224:227], v234 offset:50176\n"
"v_mfma_f32_16x16x32_bf16 v[36:39], v[68:71], a[216:219], v[36:39]\n"
"v_mfma_f32_16x16x32_bf16 v[36:39], v[84:87], a[192:195], v[36:39]\n"
"buffer_load_dwordx4 v248, s[24:27], s51 offen offset:1024 sc1 lds\n"
"v_mfma_f32_16x16x32_bf16 v[40:43], v[72:75], a[216:219], v[40:43]\n"
"ds_read_b128 a[232:235], v235 offset:49152\n"
"v_mfma_f32_16x16x32_bf16 v[40:43], v[88:91], a[192:195], v[40:43]\n"
"ds_read_b128 a[240:243], v235 offset:50176\n"
"s_add_u32 s51, s64, 165888\n"
"v_mfma_f32_16x16x32_bf16 v[44:47], v[76:79], a[216:219], v[44:47]\n"
"v_mfma_f32_16x16x32_bf16 v[44:47], v[92:95], a[192:195], v[44:47]\n"
"v_mfma_f32_16x16x32_bf16 v[48:51], v[64:67], a[200:203], v[48:51]\n"
"buffer_load_dwordx4 v248, s[24:27], s51 offen offset:2048 sc1 lds\n"
"v_mfma_f32_16x16x32_bf16 v[48:51], v[80:83], a[208:211], v[48:51]\n"
"Lfused_hop14:\n"
"s_cbranch_execz Lfused_hopend\n"
"ds_read_b128 a[128:131], v232 offset:49152\n"
"v_mfma_f32_16x16x32_bf16 v[52:55], v[68:71], a[200:203], v[52:55]\n"
"ds_read_b128 a[132:135], v232 offset:50176\n"
"v_mfma_f32_16x16x32_bf16 v[52:55], v[84:87], a[208:211], v[52:55]\n"
"buffer_load_dwordx4 v248, s[24:27], s51 offen offset:3072 sc1 lds\n"
"v_mfma_f32_16x16x32_bf16 v[56:59], v[72:75], a[200:203], v[56:59]\n"
"v_mfma_f32_16x16x32_bf16 v[56:59], v[88:91], a[208:211], v[56:59]\n"
"s_add_u32 m0, s90, 16384\n"
"v_mfma_f32_16x16x32_bf16 v[60:63], v[76:79], a[200:203], v[60:63]\n"
"v_mfma_f32_16x16x32_bf16 v[60:63], v[92:95], a[208:211], v[60:63]\n"
"v_mfma_f32_16x16x32_bf16 v[0:3], v[64:67], a[32:35], v[0:3]\n"
"s_add_u32 s55, s46, 8192\n"
"v_mfma_f32_16x16x32_bf16 v[0:3], v[80:83], a[36:39], v[0:3]\n"
"v_mfma_f32_16x16x32_bf16 v[4:7], v[68:71], a[32:35], v[4:7]\n"
"buffer_load_dwordx4 v248, s[72:75], s55 offen offset:0 sc1 lds\n"
"v_mfma_f32_16x16x32_bf16 v[4:7], v[84:87], a[36:39], v[4:7]\n"
"v_mfma_f32_16x16x32_bf16 v[8:11], v[72:75], a[32:35], v[8:11]\n"
"v_mfma_f32_16x16x32_bf16 v[8:11], v[88:91], a[36:39], v[8:11]\n"
"buffer_load_dwordx4 v248, s[72:75], s55 offen offset:1024 sc1 lds\n"
"v_mfma_f32_16x16x32_bf16 v[12:15], v[76:79], a[32:35], v[12:15]\n"
"v_mfma_f32_16x16x32_bf16 v[12:15], v[92:95], a[36:39], v[12:15]\n"
"s_waitcnt vmcnt(25) lgkmcnt(0)\n"
"v_mfma_f32_16x16x32_bf16 v[16:19], v[96:99], a[56:59], v[16:19]\n"
"s_barrier\n"
"v_mfma_f32_16x16x32_bf16 v[16:19], v[112:115], a[60:63], v[16:19]\n"
"v_add_u32 v232, 4294934528, v232\n"
"s_add_u32 m0, s90, 147456\n"
"v_mfma_f32_16x16x32_bf16 v[20:23], v[100:103], a[56:59], v[20:23]\n"
"v_add_u32 v233, 4294934528, v233\n"
"v_mfma_f32_16x16x32_bf16 v[20:23], v[116:119], a[60:63], v[20:23]\n"
"v_add_u32 v234, 4294934528, v234\n"
"s_add_u32 s51, s64, 22528\n"
"v_mfma_f32_16x16x32_bf16 v[24:27], v[104:107], a[56:59], v[24:27]\n"
"v_add_u32 v235, 4294934528, v235\n"
"v_mfma_f32_16x16x32_bf16 v[24:27], v[120:123], a[60:63], v[24:27]\n"
"ds_read_b128 a[136:139], v233 offset:0\n"
"v_mfma_f32_16x16x32_bf16 v[28:31], v[108:111], a[56:59], v[28:31]\n"
"buffer_load_dwordx4 v248, s[24:27], s51 offen offset:0 sc1 lds\n"
"v_mfma_f32_16x16x32_bf16 v[28:31], v[124:127], a[60:63], v[28:31]\n"
"ds_read_b128 a[140:143], v233 offset:1024\n"
"ds_read_b128 a[252:255], v234 offset:0\n"
"v_mfma_f32_16x16x32_bf16 v[32:35], v[96:99], a[248:251], v[32:35]\n"
"ds_read_b128 a[228:231], v234 offset:1024\n"
"v_mfma_f32_16x16x32_bf16 v[32:35], v[112:115], a[224:227], v[32:35]\n"
"buffer_load_dwordx4 v248, s[24:27], s51 offen offset:1024 sc1 lds\n"
"v_mfma_f32_16x16x32_bf16 v[36:39], v[100:103], a[248:251], v[36:39]\n"
"ds_read_b128 a[236:239], v235 offset:0\n"
"ds_read_b128 a[244:247], v235 offset:1024\n"
"v_mfma_f32_16x16x32_bf16 v[36:39], v[116:119], a[224:227], v[36:39]\n"
"ds_read_b128 a[144:147], v232 offset:0\n"
"v_mfma_f32_16x16x32_bf16 v[40:43], v[104:107], a[248:251], v[40:43]\n"
"ds_read_b128 a[148:151], v232 offset:1024\n"
"s_add_u32 s51, s64, 167936\n"
"v_mfma_f32_16x16x32_bf16 v[40:43], v[120:123], a[224:227], v[40:43]\n"
"ds_read_b128 a[152:155], v246 offset:32768\n"
"v_mfma_f32_16x16x32_bf16 v[44:47], v[108:111], a[248:251], v[44:47]\n"
"ds_read_b128 a[156:159], v246 offset:33792\n"
"v_mfma_f32_16x16x32_bf16 v[44:47], v[124:127], a[224:227], v[44:47]\n"
"buffer_load_dwordx4 v248, s[24:27], s51 offen offset:2048 sc1 lds\n"
"v_mfma_f32_16x16x32_bf16 v[48:51], v[96:99], a[232:235], v[48:51]\n"
"ds_read_b128 v[152:155], v246 offset:34816\n"
"ds_read_b128 v[128:131], v246 offset:35840\n"
"v_mfma_f32_16x16x32_bf16 v[48:51], v[112:115], a[240:243], v[48:51]\n"
"buffer_load_dwordx4 v248, s[24:27], s51 offen offset:3072 sc1 lds\n"
"v_mfma_f32_16x16x32_bf16 v[52:55], v[100:103], a[232:235], v[52:55]\n"
"ds_read_b128 v[136:139], v246 offset:40960\n"
"ds_read_b128 v[144:147], v246 offset:41984\n"
"v_mfma_f32_16x16x32_bf16 v[52:55], v[116:119], a[240:243], v[52:55]\n"
"ds_read_b128 a[0:3], v246 offset:43008\n"
"v_mfma_f32_16x16x32_bf16 v[56:59], v[104:107], a[232:235], v[56:59]\n"
"ds_read_b128 a[4:7], v246 offset:44032\n"
"s_add_u32 m0, s90, 16384\n"
"v_mfma_f32_16x16x32_bf16 v[56:59], v[120:123], a[240:243], v[56:59]\n"
"v_mfma_f32_16x16x32_bf16 v[60:63], v[108:111], a[232:235], v[60:63]\n"
"v_mfma_f32_16x16x32_bf16 v[60:63], v[124:127], a[240:243], v[60:63]\n"
"s_add_u32 s55, s46, 8192\n"
"v_mfma_f32_16x16x32_bf16 v[0:3], v[96:99], a[128:131], v[0:3]\n"
"v_mfma_f32_16x16x32_bf16 v[0:3], v[112:115], a[132:135], v[0:3]\n"
"v_mfma_f32_16x16x32_bf16 v[4:7], v[100:103], a[128:131], v[4:7]\n"
"buffer_load_dwordx4 v248, s[72:75], s55 offen offset:2048 sc1 lds\n"
"v_mfma_f32_16x16x32_bf16 v[4:7], v[116:119], a[132:135], v[4:7]\n"
"v_mfma_f32_16x16x32_bf16 v[8:11], v[104:107], a[128:131], v[8:11]\n"
"v_mfma_f32_16x16x32_bf16 v[8:11], v[120:123], a[132:135], v[8:11]\n"
"buffer_load_dwordx4 v248, s[72:75], s55 offen offset:3072 sc1 lds\n"
"v_mfma_f32_16x16x32_bf16 v[12:15], v[108:111], a[128:131], v[12:15]\n"
"v_mfma_f32_16x16x32_bf16 v[12:15], v[124:127], a[132:135], v[12:15]\n"
"s_waitcnt vmcnt(12) lgkmcnt(0)\n"
"v_cmp_ne_u64 vcc, s[98:99], v[250:251]\n"
"s_cbranch_vccnz Lfused_ps4\n"
"Lfused_pd4:\n"
"v_mfma_f32_16x16x32_bf16 v[16:19], a[64:67], a[136:139], v[16:19]\n"
"s_barrier\n"
"v_mfma_f32_16x16x32_bf16 v[16:19], a[80:83], a[140:143], v[16:19]\n"
"global_load_dword v236, v236, s[52:53] sc1\n"
"v_mfma_f32_16x16x32_bf16 v[20:23], a[68:71], a[136:139], v[20:23]\n"
"ds_read_b128 a[8:11], v233 offset:16384\n"
"v_mfma_f32_16x16x32_bf16 v[20:23], a[84:87], a[140:143], v[20:23]\n"
"global_load_dword v237, v237, s[52:53] sc1\n"
"v_mfma_f32_16x16x32_bf16 v[24:27], a[72:75], a[136:139], v[24:27]\n"
"ds_read_b128 a[12:15], v233 offset:17408\n"
"v_mfma_f32_16x16x32_bf16 v[24:27], a[88:91], a[140:143], v[24:27]\n"
"global_load_dword v238, v238, s[52:53] sc1\n"
"v_mfma_f32_16x16x32_bf16 v[28:31], a[76:79], a[136:139], v[28:31]\n"
"ds_read_b128 v[156:159], v234 offset:16384\n"
"v_mfma_f32_16x16x32_bf16 v[28:31], a[92:95], a[140:143], v[28:31]\n"
"global_load_dword v239, v239, s[52:53] sc1\n"
"v_mfma_f32_16x16x32_bf16 v[32:35], a[64:67], a[252:255], v[32:35]\n"
"ds_read_b128 v[132:135], v234 offset:17408\n"
"v_mfma_f32_16x16x32_bf16 v[32:35], a[80:83], a[228:231], v[32:35]\n"
"s_add_u32 m0, s90, 65536\n"
"v_mfma_f32_16x16x32_bf16 v[36:39], a[68:71], a[252:255], v[36:39]\n"
"v_mfma_f32_16x16x32_bf16 v[36:39], a[84:87], a[228:231], v[36:39]\n"
"ds_read_b128 v[140:143], v235 offset:16384\n"
"v_mfma_f32_16x16x32_bf16 v[40:43], a[72:75], a[252:255], v[40:43]\n"
"s_add_u32 s51, s65, 0\n"
"v_mfma_f32_16x16x32_bf16 v[40:43], a[88:91], a[228:231], v[40:43]\n"
"ds_read_b128 v[148:151], v235 offset:17408\n"
"v_mfma_f32_16x16x32_bf16 v[44:47], a[76:79], a[252:255], v[44:47]\n"
"buffer_load_dwordx4 v248, s[24:27], s51 offen offset:0 sc1 lds\n"
"v_mfma_f32_16x16x32_bf16 v[44:47], a[92:95], a[228:231], v[44:47]\n"
"v_mfma_f32_16x16x32_bf16 v[48:51], a[64:67], a[236:239], v[48:51]\n"
"buffer_load_dwordx4 v248, s[24:27], s51 offen offset:1024 sc1 lds\n"
"v_mfma_f32_16x16x32_bf16 v[48:51], a[80:83], a[244:247], v[48:51]\n"
"ds_read_b128 a[16:19], v232 offset:16384\n"
"v_mfma_f32_16x16x32_bf16 v[52:55], a[68:71], a[236:239], v[52:55]\n"
"buffer_load_dwordx4 v248, s[24:27], s51 offen offset:2048 sc1 lds\n"
"v_mfma_f32_16x16x32_bf16 v[52:55], a[84:87], a[244:247], v[52:55]\n"
"ds_read_b128 a[20:23], v232 offset:17408\n"
"v_mfma_f32_16x16x32_bf16 v[56:59], a[72:75], a[236:239], v[56:59]\n"
"buffer_load_dwordx4 v248, s[24:27], s51 offen offset:3072 sc1 lds\n"
"v_mfma_f32_16x16x32_bf16 v[56:59], a[88:91], a[244:247], v[56:59]\n"
"v_mfma_f32_16x16x32_bf16 v[60:63], a[76:79], a[236:239], v[60:63]\n"
"v_mfma_f32_16x16x32_bf16 v[60:63], a[92:95], a[244:247], v[60:63]\n"
"s_add_u32 m0, s90, 32768\n"
"v_mfma_f32_16x16x32_bf16 v[0:3], a[64:67], a[144:147], v[0:3]\n"
"v_mfma_f32_16x16x32_bf16 v[0:3], a[80:83], a[148:151], v[0:3]\n"
"s_add_u32 s55, s45, 98304\n"
"v_mfma_f32_16x16x32_bf16 v[4:7], a[68:71], a[144:147], v[4:7]\n"
"v_mfma_f32_16x16x32_bf16 v[4:7], a[84:87], a[148:151], v[4:7]\n"
"buffer_load_dwordx4 v248, s[72:75], s55 offen offset:0 sc1 lds\n"
"v_mfma_f32_16x16x32_bf16 v[8:11], a[72:75], a[144:147], v[8:11]\n"
"v_mfma_f32_16x16x32_bf16 v[8:11], a[88:91], a[148:151], v[8:11]\n"
"buffer_load_dwordx4 v248, s[72:75], s55 offen offset:1024 sc1 lds\n"
"v_mfma_f32_16x16x32_bf16 v[12:15], a[76:79], a[144:147], v[12:15]\n"
"v_mfma_f32_16x16x32_bf16 v[12:15], a[92:95], a[148:151], v[12:15]\n"
"s_waitcnt vmcnt(31) lgkmcnt(0)\n"
"v_mfma_f32_16x16x32_bf16 v[16:19], a[160:163], a[8:11], v[16:19]\n"
"s_barrier\n"
"v_mfma_f32_16x16x32_bf16 v[16:19], a[176:179], a[12:15], v[16:19]\n"
"ds_read_b128 a[24:27], v233 offset:32768\n"
"s_add_u32 m0, s90, 81920\n"
"v_mfma_f32_16x16x32_bf16 v[20:23], a[164:167], a[8:11], v[20:23]\n"
"ds_read_b128 a[28:31], v233 offset:33792\n"
"v_mfma_f32_16x16x32_bf16 v[20:23], a[180:183], a[12:15], v[20:23]\n"
"ds_read_b128 v[216:219], v234 offset:32768\n"
"v_mfma_f32_16x16x32_bf16 v[24:27], a[168:171], a[8:11], v[24:27]\n"
"ds_read_b128 v[192:195], v234 offset:33792\n"
"s_add_u32 s51, s65, 4096\n"
"v_mfma_f32_16x16x32_bf16 v[24:27], a[184:187], a[12:15], v[24:27]\n"
"v_mfma_f32_16x16x32_bf16 v[28:31], a[172:175], a[8:11], v[28:31]\n"
"ds_read_b128 v[200:203], v235 offset:32768\n"
"v_mfma_f32_16x16x32_bf16 v[28:31], a[188:191], a[12:15], v[28:31]\n"
"buffer_load_dwordx4 v248, s[24:27], s51 offen offset:0 sc1 lds\n"
"v_mfma_f32_16x16x32_bf16 v[32:35], a[160:163], v[156:159], v[32:35]\n"
"ds_read_b128 v[208:211], v235 offset:33792\n"
"ds_read_b128 a[96:99], v232 offset:32768\n"
"v_mfma_f32_16x16x32_bf16 v[32:35], a[176:179], v[132:135], v[32:35]\n"
"ds_read_b128 a[100:103], v232 offset:33792\n"
"v_mfma_f32_16x16x32_bf16 v[36:39], a[164:167], v[156:159], v[36:39]\n"
"buffer_load_dwordx4 v248, s[24:27], s51 offen offset:1024 sc1 lds\n"
"v_mfma_f32_16x16x32_bf16 v[36:39], a[180:183], v[132:135], v[36:39]\n"
"ds_read_b128 a[104:107], v246 offset:49152\n"
"v_mfma_f32_16x16x32_bf16 v[40:43], a[168:171], v[156:159], v[40:43]\n"
"ds_read_b128 a[108:111], v246 offset:50176\n"
"v_mfma_f32_16x16x32_bf16 v[40:43], a[184:187], v[132:135], v[40:43]\n"
"buffer_load_dwordx4 v248, s[24:27], s51 offen offset:2048 sc1 lds\n"
"v_mfma_f32_16x16x32_bf16 v[44:47], a[172:175], v[156:159], v[44:47]\n"
"ds_read_b128 v[220:223], v246 offset:51200\n"
"ds_read_b128 v[196:199], v246 offset:52224\n"
"v_mfma_f32_16x16x32_bf16 v[44:47], a[188:191], v[132:135], v[44:47]\n"
"v_mfma_f32_16x16x32_bf16 v[48:51], a[160:163], v[140:143], v[48:51]\n"
"buffer_load_dwordx4 v248, s[24:27], s51 offen offset:3072 sc1 lds\n"
"v_mfma_f32_16x16x32_bf16 v[48:51], a[176:179], v[148:151], v[48:51]\n"
"ds_read_b128 v[204:207], v246 offset:57344\n"
"ds_read_b128 v[212:215], v246 offset:58368\n"
"v_mfma_f32_16x16x32_bf16 v[52:55], a[164:167], v[140:143], v[52:55]\n"
"ds_read_b128 a[112:115], v246 offset:59392\n"
"v_mfma_f32_16x16x32_bf16 v[52:55], a[180:183], v[148:151], v[52:55]\n"
"ds_read_b128 a[116:119], v246 offset:60416\n"
"s_add_u32 m0, s90, 32768\n"
"v_mfma_f32_16x16x32_bf16 v[56:59], a[168:171], v[140:143], v[56:59]\n"
"v_mfma_f32_16x16x32_bf16 v[56:59], a[184:187], v[148:151], v[56:59]\n"
"v_mfma_f32_16x16x32_bf16 v[60:63], a[172:175], v[140:143], v[60:63]\n"
"s_add_u32 s55, s45, 98304\n"
"v_mfma_f32_16x16x32_bf16 v[60:63], a[188:191], v[148:151], v[60:63]\n"
"v_mfma_f32_16x16x32_bf16 v[0:3], a[160:163], a[16:19], v[0:3]\n"
"v_mfma_f32_16x16x32_bf16 v[0:3], a[176:179], a[20:23], v[0:3]\n"
"buffer_load_dwordx4 v248, s[72:75], s55 offen offset:2048 sc1 lds\n"
"v_mfma_f32_16x16x32_bf16 v[4:7], a[164:167], a[16:19], v[4:7]\n"
"v_mfma_f32_16x16x32_bf16 v[4:7], a[180:183], a[20:23], v[4:7]\n"
"v_mfma_f32_16x16x32_bf16 v[8:11], a[168:171], a[16:19], v[8:11]\n"
"buffer_load_dwordx4 v248, s[72:75], s55 offen offset:3072 sc1 lds\n"
"v_mfma_f32_16x16x32_bf16 v[8:11], a[184:187], a[20:23], v[8:11]\n"
"v_mfma_f32_16x16x32_bf16 v[12:15], a[172:175], a[16:19], v[12:15]\n"
"v_mfma_f32_16x16x32_bf16 v[12:15], a[188:191], a[20:23], v[12:15]\n"
"s_waitcnt vmcnt(31) lgkmcnt(0)\n"
"v_mfma_f32_16x16x32_bf16 v[16:19], v[160:163], a[24:27], v[16:19]\n"
"s_barrier\n"
"v_mfma_f32_16x16x32_bf16 v[16:19], v[176:179], a[28:31], v[16:19]\n"
"ds_read_b128 a[120:123], v233 offset:49152\n"
"s_add_u32 m0, s90, 98304\n"
"v_mfma_f32_16x16x32_bf16 v[20:23], v[164:167], a[24:27], v[20:23]\n"
"v_mfma_f32_16x16x32_bf16 v[20:23], v[180:183], a[28:31], v[20:23]\n"
"ds_read_b128 a[124:127], v233 offset:50176\n"
"s_add_u32 s51, s64, 24576\n"
"v_mfma_f32_16x16x32_bf16 v[24:27], v[168:171], a[24:27], v[24:27]\n"
"v_mfma_f32_16x16x32_bf16 v[24:27], v[184:187], a[28:31], v[24:27]\n"
"v_mfma_f32_16x16x32_bf16 v[28:31], v[172:175], a[24:27], v[28:31]\n"
"buffer_load_dwordx4 v248, s[24:27], s51 offen offset:0 sc1 lds\n"
"v_mfma_f32_16x16x32_bf16 v[28:31], v[188:191], a[28:31], v[28:31]\n"
"ds_read_b128 a[32:35], v234 offset:49152\n"
"v_mfma_f32_16x16x32_bf16 v[32:35], v[160:163], v[216:219], v[32:35]\n"
"ds_read_b128 v[224:227], v234 offset:50176\n"
"v_mfma_f32_16x16x32_bf16 v[32:35], v[176:179], v[192:195], v[32:35]\n"
"buffer_load_dwordx4 v248, s[24:27], s51 offen offset:1024 sc1 lds\n"
"v_mfma_f32_16x16x32_bf16 v[36:39], v[164:167], v[216:219], v[36:39]\n"
"v_mfma_f32_16x16x32_bf16 v[36:39], v[180:183], v[192:195], v[36:39]\n"
"ds_read_b128 a[216:219], v235 offset:49152\n"
"v_mfma_f32_16x16x32_bf16 v[40:43], v[168:171], v[216:219], v[40:43]\n"
"s_add_u32 s51, s64, 169984\n"
"v_mfma_f32_16x16x32_bf16 v[40:43], v[184:187], v[192:195], v[40:43]\n"
"ds_read_b128 a[200:203], v235 offset:50176\n"
"v_mfma_f32_16x16x32_bf16 v[44:47], v[172:175], v[216:219], v[44:47]\n"
"v_mfma_f32_16x16x32_bf16 v[44:47], v[188:191], v[192:195], v[44:47]\n"
"buffer_load_dwordx4 v248, s[24:27], s51 offen offset:2048 sc1 lds\n"
"v_mfma_f32_16x16x32_bf16 v[48:51], v[160:163], v[200:203], v[48:51]\n"
"ds_read_b128 a[36:39], v232 offset:49152\n"
"v_mfma_f32_16x16x32_bf16 v[48:51], v[176:179], v[208:211], v[48:51]\n"
"buffer_load_dwordx4 v248, s[24:27], s51 offen offset:3072 sc1 lds\n"
"v_mfma_f32_16x16x32_bf16 v[52:55], v[164:167], v[200:203], v[52:55]\n"
"ds_read_b128 v[228:231], v232 offset:50176\n"
"v_mfma_f32_16x16x32_bf16 v[52:55], v[180:183], v[208:211], v[52:55]\n"
"v_mfma_f32_16x16x32_bf16 v[56:59], v[168:171], v[200:203], v[56:59]\n"
"s_add_u32 m0, s90, 49152\n"
"v_mfma_f32_16x16x32_bf16 v[56:59], v[184:187], v[208:211], v[56:59]\n"
"v_mfma_f32_16x16x32_bf16 v[60:63], v[172:175], v[200:203], v[60:63]\n"
"v_mfma_f32_16x16x32_bf16 v[60:63], v[188:191], v[208:211], v[60:63]\n"
"s_add_u32 s55, s45, 106496\n"
"v_mfma_f32_16x16x32_bf16 v[0:3], v[160:163], a[96:99], v[0:3]\n"
"v_mfma_f32_16x16x32_bf16 v[0:3], v[176:179], a[100:103], v[0:3]\n"
"v_mfma_f32_16x16x32_bf16 v[4:7], v[164:167], a[96:99], v[4:7]\n"
"buffer_load_dwordx4 v248, s[72:75], s55 offen offset:0 sc1 lds\n"
"v_mfma_f32_16x16x32_bf16 v[4:7], v[180:183], a[100:103], v[4:7]\n"
"v_mfma_f32_16x16x32_bf16 v[8:11], v[168:171], a[96:99], v[8:11]\n"
"v_mfma_f32_16x16x32_bf16 v[8:11], v[184:187], a[100:103], v[8:11]\n"
"buffer_load_dwordx4 v248, s[72:75], s55 offen offset:1024 sc1 lds\n"
"v_mfma_f32_16x16x32_bf16 v[12:15], v[172:175], a[96:99], v[12:15]\n"
"v_mfma_f32_16x16x32_bf16 v[12:15], v[188:191], a[100:103], v[12:15]\n"
"s_waitcnt vmcnt(30) lgkmcnt(0)\n"
"v_mfma_f32_16x16x32_bf16 v[16:19], a[40:43], a[120:123], v[16:19]\n"
"s_barrier\n"
"v_mfma_f32_16x16x32_bf16 v[16:19], a[204:207], a[124:127], v[16:19]\n"
"v_add_u32 v232, 32768, v232\n"
"s_add_u32 m0, s90, 114688\n"
"v_mfma_f32_16x16x32_bf16 v[20:23], a[44:47], a[120:123], v[20:23]\n"
"v_add_u32 v233, 32768, v233\n"
"v_mfma_f32_16x16x32_bf16 v[20:23], a[212:215], a[124:127], v[20:23]\n"
"v_add_u32 v234, 32768, v234\n"
"s_add_u32 s51, s64, 26624\n"
"v_mfma_f32_16x16x32_bf16 v[24:27], a[220:223], a[120:123], v[24:27]\n"
"v_add_u32 v235, 32768, v235\n"
"v_mfma_f32_16x16x32_bf16 v[24:27], a[48:51], a[124:127], v[24:27]\n"
"ds_read_b128 a[192:195], v233 offset:32768\n"
"v_mfma_f32_16x16x32_bf16 v[28:31], a[196:199], a[120:123], v[28:31]\n"
"buffer_load_dwordx4 v248, s[24:27], s51 offen offset:0 sc1 lds\n"
"v_mfma_f32_16x16x32_bf16 v[28:31], a[52:55], a[124:127], v[28:31]\n"
"ds_read_b128 a[208:211], v233 offset:33792\n"
"ds_read_b128 a[128:131], v234 offset:32768\n"
"v_mfma_f32_16x16x32_bf16 v[32:35], a[40:43], a[32:35], v[32:35]\n"
"ds_read_b128 a[56:59], v234 offset:33792\n"
"v_mfma_f32_16x16x32_bf16 v[32:35], a[204:207], v[224:227], v[32:35]\n"
"buffer_load_dwordx4 v248, s[24:27], s51 offen offset:1024 sc1 lds\n"
"v_mfma_f32_16x16x32_bf16 v[36:39], a[44:47], a[32:35], v[36:39]\n"
"ds_read_b128 a[248:251], v235 offset:32768\n"
"ds_read_b128 a[232:235], v235 offset:33792\n"
"v_mfma_f32_16x16x32_bf16 v[36:39], a[212:215], v[224:227], v[36:39]\n"
"ds_read_b128 a[132:135], v232 offset:32768\n"
"v_mfma_f32_16x16x32_bf16 v[40:43], a[220:223], a[32:35], v[40:43]\n"
"ds_read_b128 a[60:63], v232 offset:33792\n"
"s_add_u32 s51, s64, 172032\n"
"v_mfma_f32_16x16x32_bf16 v[40:43], a[48:51], v[224:227], v[40:43]\n"
"ds_read_b128 a[224:227], v246 offset:0\n"
"v_mfma_f32_16x16x32_bf16 v[44:47], a[196:199], a[32:35], v[44:47]\n"
"ds_read_b128 a[240:243], v246 offset:1024\n"
"v_mfma_f32_16x16x32_bf16 v[44:47], a[52:55], v[224:227], v[44:47]\n"
"buffer_load_dwordx4 v248, s[24:27], s51 offen offset:2048 sc1 lds\n"
"v_mfma_f32_16x16x32_bf16 v[48:51], a[40:43], a[216:219], v[48:51]\n"
"ds_read_b128 a[144:147], v246 offset:2048\n"
"ds_read_b128 a[136:139], v246 offset:3072\n"
"v_mfma_f32_16x16x32_bf16 v[48:51], a[204:207], a[200:203], v[48:51]\n"
"buffer_load_dwordx4 v248, s[24:27], s51 offen offset:3072 sc1 lds\n"
"v_mfma_f32_16x16x32_bf16 v[52:55], a[44:47], a[216:219], v[52:55]\n"
"ds_read_b128 a[252:255], v246 offset:8192\n"
"ds_read_b128 a[236:239], v246 offset:9216\n"
"v_mfma_f32_16x16x32_bf16 v[52:55], a[212:215], a[200:203], v[52:55]\n"
"ds_read_b128 a[64:67], v246 offset:10240\n"
"v_mfma_f32_16x16x32_bf16 v[56:59], a[220:223], a[216:219], v[56:59]\n"
"ds_read_b128 a[68:71], v246 offset:11264\n"
"s_add_u32 m0, s90, 49152\n"
"v_mfma_f32_16x16x32_bf16 v[56:59], a[48:51], a[200:203], v[56:59]\n"
"v_mfma_f32_16x16x32_bf16 v[60:63], a[196:199], a[216:219], v[60:63]\n"
"v_mfma_f32_16x16x32_bf16 v[60:63], a[52:55], a[200:203], v[60:63]\n"
"s_add_u32 s55, s45, 106496\n"
"v_mfma_f32_16x16x32_bf16 v[0:3], a[40:43], a[36:39], v[0:3]\n"
"v_mfma_f32_16x16x32_bf16 v[0:3], a[204:207], v[228:231], v[0:3]\n"
"v_mfma_f32_16x16x32_bf16 v[4:7], a[44:47], a[36:39], v[4:7]\n"
"buffer_load_dwordx4 v248, s[72:75], s55 offen offset:2048 sc1 lds\n"
"v_mfma_f32_16x16x32_bf16 v[4:7], a[212:215], v[228:231], v[4:7]\n"
"v_mfma_f32_16x16x32_bf16 v[8:11], a[220:223], a[36:39], v[8:11]\n"
"v_mfma_f32_16x16x32_bf16 v[8:11], a[48:51], v[228:231], v[8:11]\n"
"buffer_load_dwordx4 v248, s[72:75], s55 offen offset:3072 sc1 lds\n"
"v_mfma_f32_16x16x32_bf16 v[12:15], a[196:199], a[36:39], v[12:15]\n"
"v_mfma_f32_16x16x32_bf16 v[12:15], a[52:55], v[228:231], v[12:15]\n"
"s_waitcnt vmcnt(28) lgkmcnt(0)\n"
"v_mfma_f32_16x16x32_bf16 v[16:19], a[152:155], a[192:195], v[16:19]\n"
"s_barrier\n"
"v_mfma_f32_16x16x32_bf16 v[16:19], v[136:139], a[208:211], v[16:19]\n"
"ds_read_b128 a[72:75], v233 offset:49152\n"
"s_add_u32 m0, s90, 131072\n"
"v_mfma_f32_16x16x32_bf16 v[20:23], a[156:159], a[192:195], v[20:23]\n"
"ds_read_b128 a[76:79], v233 offset:50176\n"
"v_mfma_f32_16x16x32_bf16 v[20:23], v[144:147], a[208:211], v[20:23]\n"
"ds_read_b128 a[148:151], v234 offset:49152\n"
"s_add_u32 s51, s64, 28672\n"
"v_mfma_f32_16x16x32_bf16 v[24:27], v[152:155], a[192:195], v[24:27]\n"
"ds_read_b128 a[140:143], v234 offset:50176\n"
"v_mfma_f32_16x16x32_bf16 v[24:27], a[0:3], a[208:211], v[24:27]\n"
"v_mfma_f32_16x16x32_bf16 v[28:31], v[128:131], a[192:195], v[28:31]\n"
"buffer_load_dwordx4 v248, s[24:27], s51 offen offset:0 sc1 lds\n"
"v_mfma_f32_16x16x32_bf16 v[28:31], a[4:7], a[208:211], v[28:31]\n"
"ds_read_b128 a[228:231], v235 offset:49152\n"
"ds_read_b128 a[244:247], v235 offset:50176\n"
"v_mfma_f32_16x16x32_bf16 v[32:35], a[152:155], a[128:131], v[32:35]\n"
"ds_read_b128 a[80:83], v232 offset:49152\n"
"v_mfma_f32_16x16x32_bf16 v[32:35], v[136:139], a[56:59], v[32:35]\n"
"buffer_load_dwordx4 v248, s[24:27], s51 offen offset:1024 sc1 lds\n"
"v_mfma_f32_16x16x32_bf16 v[36:39], a[156:159], a[128:131], v[36:39]\n"
"ds_read_b128 a[84:87], v232 offset:50176\n"
"v_mfma_f32_16x16x32_bf16 v[36:39], v[144:147], a[56:59], v[36:39]\n"
"ds_read_b128 a[88:91], v246 offset:16384\n"
"v_mfma_f32_16x16x32_bf16 v[40:43], v[152:155], a[128:131], v[40:43]\n"
"ds_read_b128 a[92:95], v246 offset:17408\n"
"s_add_u32 s51, s64, 174080\n"
"v_mfma_f32_16x16x32_bf16 v[40:43], a[0:3], a[56:59], v[40:43]\n"
"ds_read_b128 a[16:19], v246 offset:18432\n"
"v_mfma_f32_16x16x32_bf16 v[44:47], v[128:131], a[128:131], v[44:47]\n"
"ds_read_b128 a[8:11], v246 offset:19456\n"
"v_mfma_f32_16x16x32_bf16 v[44:47], a[4:7], a[56:59], v[44:47]\n"
"buffer_load_dwordx4 v248, s[24:27], s51 offen offset:2048 sc1 lds\n"
"v_mfma_f32_16x16x32_bf16 v[48:51], a[152:155], a[248:251], v[48:51]\n"
"ds_read_b128 v[156:159], v246 offset:24576\n"
"v_mfma_f32_16x16x32_bf16 v[48:51], v[136:139], a[232:235], v[48:51]\n"
"buffer_load_dwordx4 v248, s[24:27], s51 offen offset:3072 sc1 lds\n"
"v_mfma_f32_16x16x32_bf16 v[52:55], a[156:159], a[248:251], v[52:55]\n"
"ds_read_b128 v[140:143], v246 offset:25600\n"
"ds_read_b128 a[160:163], v246 offset:26624\n"
"v_mfma_f32_16x16x32_bf16 v[52:55], v[144:147], a[232:235], v[52:55]\n"
"ds_read_b128 a[164:167], v246 offset:27648\n"
"v_mfma_f32_16x16x32_bf16 v[56:59], v[152:155], a[248:251], v[56:59]\n"
"s_add_u32 m0, s90, 0\n"
"v_mfma_f32_16x16x32_bf16 v[56:59], a[0:3], a[232:235], v[56:59]\n"
"v_mfma_f32_16x16x32_bf16 v[60:63], v[128:131], a[248:251], v[60:63]\n"
"v_mfma_f32_16x16x32_bf16 v[60:63], a[4:7], a[232:235], v[60:63]\n"
"s_add_u32 s55, s45, 114688\n"
"v_mfma_f32_16x16x32_bf16 v[0:3], a[152:155], a[132:135], v[0:3]\n"
"v_mfma_f32_16x16x32_bf16 v[0:3], v[136:139], a[60:63], v[0:3]\n"
"v_mfma_f32_16x16x32_bf16 v[4:7], a[156:159], a[132:135], v[4:7]\n"
"buffer_load_dwordx4 v248, s[72:75], s55 offen offset:0 sc1 lds\n"
"v_mfma_f32_16x16x32_bf16 v[4:7], v[144:147], a[60:63], v[4:7]\n"
"v_mfma_f32_16x16x32_bf16 v[8:11], v[152:155], a[132:135], v[8:11]\n"
"v_mfma_f32_16x16x32_bf16 v[8:11], a[0:3], a[60:63], v[8:11]\n"
"buffer_load_dwordx4 v248, s[72:75], s55 offen offset:1024 sc1 lds\n"
"v_mfma_f32_16x16x32_bf16 v[12:15], v[128:131], a[132:135], v[12:15]\n"
"v_mfma_f32_16x16x32_bf16 v[12:15], a[4:7], a[60:63], v[12:15]\n"
"s_waitcnt vmcnt(26) lgkmcnt(0)\n"
"v_mfma_f32_16x16x32_bf16 v[16:19], a[104:107], a[72:75], v[16:19]\n"
"s_barrier\n"
"v_mfma_f32_16x16x32_bf16 v[16:19], v[204:207], a[76:79], v[16:19]\n"
"ds_read_b64 a[168:169], v241 offset:0\n"
"s_add_u32 m0, s90, 147456\n"
"v_mfma_f32_16x16x32_bf16 v[20:23], a[108:111], a[72:75], v[20:23]\n"
"ds_read_b64 a[170:171], v241 offset:1024\n"
"ds_read_b64 a[172:173], v241 offset:2048\n"
"v_mfma_f32_16x16x32_bf16 v[20:23], v[212:215], a[76:79], v[20:23]\n"
"ds_read_b64 a[174:175], v241 offset:3072\n"
"s_add_u32 s51, s64, 30720\n"
"v_mfma_f32_16x16x32_bf16 v[24:27], v[220:223], a[72:75], v[24:27]\n"
"ds_read_b64 a[20:21], v242 offset:0\n"
"v_mfma_f32_16x16x32_bf16 v[24:27], a[112:115], a[76:79], v[24:27]\n"
"buffer_load_dwordx4 v248, s[24:27], s51 offen offset:0 sc1 lds\n"
"v_mfma_f32_16x16x32_bf16 v[28:31], v[196:199], a[72:75], v[28:31]\n"
"ds_read_b64 a[22:23], v242 offset:1024\n"
"ds_read_b64 a[12:13], v242 offset:2048\n"
"v_mfma_f32_16x16x32_bf16 v[28:31], a[116:119], a[76:79], v[28:31]\n"
"buffer_load_dwordx4 v248, s[24:27], s51 offen offset:1024 sc1 lds\n"
"v_mfma_f32_16x16x32_bf16 v[32:35], a[104:107], a[148:151], v[32:35]\n"
"ds_read_b64 a[14:15], v242 offset:3072\n"
"ds_read_b64 v[132:133], v243 offset:0\n"
"v_mfma_f32_16x16x32_bf16 v[32:35], v[204:207], a[140:143], v[32:35]\n"
"ds_read_b64 v[134:135], v243 offset:1024\n"
"s_add_u32 s51, s64, 176128\n"
"v_mfma_f32_16x16x32_bf16 v[36:39], a[108:111], a[148:151], v[36:39]\n"
"ds_read_b64 v[148:149], v243 offset:2048\n"
"v_mfma_f32_16x16x32_bf16 v[36:39], v[212:215], a[140:143], v[36:39]\n"
"buffer_load_dwordx4 v248, s[24:27], s51 offen offset:2048 sc1 lds\n"
"v_mfma_f32_16x16x32_bf16 v[40:43], v[220:223], a[148:151], v[40:43]\n"
"ds_read_b64 v[150:151], v243 offset:3072\n"
"ds_read_b64 a[176:177], v240 offset:0\n"
"v_mfma_f32_16x16x32_bf16 v[40:43], a[112:115], a[140:143], v[40:43]\n"
"buffer_load_dwordx4 v248, s[24:27], s51 offen offset:3072 sc1 lds\n"
"v_mfma_f32_16x16x32_bf16 v[44:47], v[196:199], a[148:151], v[44:47]\n"
"ds_read_b64 a[178:179], v240 offset:1024\n"
"ds_read_b64 a[180:181], v240 offset:2048\n"
"v_mfma_f32_16x16x32_bf16 v[44:47], a[116:119], a[140:143], v[44:47]\n"
"ds_read_b64 a[182:183], v240 offset:3072\n"
"s_add_u32 m0, s90, 0\n"
"v_mfma_f32_16x16x32_bf16 v[48:51], a[104:107], a[228:231], v[48:51]\n"
"v_mfma_f32_16x16x32_bf16 v[48:51], v[204:207], a[244:247], v[48:51]\n"
"s_add_u32 s55, s45, 114688\n"
"v_mfma_f32_16x16x32_bf16 v[52:55], a[108:111], a[228:231], v[52:55]\n"
"v_mfma_f32_16x16x32_bf16 v[52:55], v[212:215], a[244:247], v[52:55]\n"
"buffer_load_dwordx4 v248, s[72:75], s55 offen offset:2048 sc1 lds\n"
"v_mfma_f32_16x16x32_bf16 v[56:59], v[220:223], a[228:231], v[56:59]\n"
"v_mfma_f32_16x16x32_bf16 v[56:59], a[112:115], a[244:247], v[56:59]\n"
"buffer_load_dwordx4 v248, s[72:75], s55 offen offset:3072 sc1 lds\n"
"v_mfma_f32_16x16x32_bf16 v[60:63], v[196:199], a[228:231], v[60:63]\n"
"v_mfma_f32_16x16x32_bf16 v[60:63], a[116:119], a[244:247], v[60:63]\n"
"s_waitcnt vmcnt(24) lgkmcnt(0)\n"
"v_mfma_f32_16x16x32_bf16 v[80:83], a[224:227], a[168:171], 0\n"
"s_barrier\n"
"v_mfma_f32_16x16x32_bf16 v[80:83], a[252:255], a[172:175], v[80:83]\n"
"ds_read_b64 a[184:185], v241 offset:16384\n"
"s_add_u32 m0, s90, 16384\n"
"v_mfma_f32_16x16x32_bf16 v[84:87], a[240:243], a[168:171], 0\n"
"ds_read_b64 a[186:187], v241 offset:17408\n"
"ds_read_b64 a[188:189], v241 offset:18432\n"
"v_mfma_f32_16x16x32_bf16 v[84:87], a[236:239], a[172:175], v[84:87]\n"
"ds_read_b64 a[190:191], v241 offset:19456\n"
"s_add_u32 s55, s45, 122880\n"
"v_mfma_f32_16x16x32_bf16 v[88:91], a[144:147], a[168:171], 0\n"
"ds_read_b64 a[96:97], v242 offset:16384\n"
"ds_read_b64 a[98:99], v242 offset:17408\n"
"v_mfma_f32_16x16x32_bf16 v[88:91], a[64:67], a[172:175], v[88:91]\n"
"ds_read_b64 a[24:25], v242 offset:18432\n"
"ds_read_b64 a[26:27], v242 offset:19456\n"
"v_mfma_f32_16x16x32_bf16 v[92:95], a[136:139], a[168:171], 0\n"
"buffer_load_dwordx4 v248, s[72:75], s55 offen offset:0 sc1 lds\n"
"v_mfma_f32_16x16x32_bf16 v[92:95], a[68:71], a[172:175], v[92:95]\n"
"ds_read_b64 v[216:217], v243 offset:16384\n"
"ds_read_b64 v[218:219], v243 offset:17408\n"
"v_mfma_f32_16x16x32_bf16 v[96:99], a[224:227], a[20:23], 0\n"
"ds_read_b64 v[200:201], v243 offset:18432\n"
"ds_read_b64 v[202:203], v243 offset:19456\n"
"v_mfma_f32_16x16x32_bf16 v[96:99], a[252:255], a[12:15], v[96:99]\n"
"buffer_load_dwordx4 v248, s[72:75], s55 offen offset:1024 sc1 lds\n"
"v_mfma_f32_16x16x32_bf16 v[100:103], a[240:243], a[20:23], 0\n"
"ds_read_b64 v[160:161], v240 offset:16384\n"
"ds_read_b64 v[162:163], v240 offset:17408\n"
"v_mfma_f32_16x16x32_bf16 v[100:103], a[236:239], a[12:15], v[100:103]\n"
"ds_read_b64 v[164:165], v240 offset:18432\n"
"ds_read_b64 v[166:167], v240 offset:19456\n"
"v_mfma_f32_16x16x32_bf16 v[104:107], a[144:147], a[20:23], 0\n"
"ds_read_b128 v[168:171], v246 offset:32768\n"
"s_add_u32 m0, s90, 16384\n"
"v_mfma_f32_16x16x32_bf16 v[104:107], a[64:67], a[12:15], v[104:107]\n"
"ds_read_b128 v[172:175], v246 offset:33792\n"
"ds_read_b128 a[100:103], v246 offset:34816\n"
"v_mfma_f32_16x16x32_bf16 v[108:111], a[136:139], a[20:23], 0\n"
"ds_read_b128 a[28:31], v246 offset:35840\n"
"ds_read_b128 v[192:195], v246 offset:40960\n"
"v_mfma_f32_16x16x32_bf16 v[108:111], a[68:71], a[12:15], v[108:111]\n"
"ds_read_b128 v[208:211], v246 offset:41984\n"
"s_add_u32 s55, s45, 122880\n"
"v_mfma_f32_16x16x32_bf16 v[112:115], a[224:227], v[132:135], 0\n"
"ds_read_b128 v[176:179], v246 offset:43008\n"
"ds_read_b128 v[180:183], v246 offset:44032\n"
"v_mfma_f32_16x16x32_bf16 v[112:115], a[252:255], v[148:151], v[112:115]\n"
"v_mfma_f32_16x16x32_bf16 v[116:119], a[240:243], v[132:135], 0\n"
"buffer_load_dwordx4 v248, s[72:75], s55 offen offset:2048 sc1 lds\n"
"v_mfma_f32_16x16x32_bf16 v[116:119], a[236:239], v[148:151], v[116:119]\n"
"v_mfma_f32_16x16x32_bf16 v[120:123], a[144:147], v[132:135], 0\n"
"v_mfma_f32_16x16x32_bf16 v[120:123], a[64:67], v[148:151], v[120:123]\n"
"buffer_load_dwordx4 v248, s[72:75], s55 offen offset:3072 sc1 lds\n"
"v_mfma_f32_16x16x32_bf16 v[124:127], a[136:139], v[132:135], 0\n"
"v_mfma_f32_16x16x32_bf16 v[124:127], a[68:71], v[148:151], v[124:127]\n"
"s_waitcnt vmcnt(16) lgkmcnt(0)\n"
"v_mfma_f32_16x16x32_bf16 v[80:83], a[88:91], a[184:187], v[80:83]\n"
"s_barrier\n"
"v_mfma_f32_16x16x32_bf16 v[80:83], v[156:159], a[188:191], v[80:83]\n"
"ds_read_b128 v[184:187], v233 offset:0\n"
"ds_read_b128 v[188:191], v233 offset:1024\n"
"v_mfma_f32_16x16x32_bf16 v[84:87], a[92:95], a[184:187], v[84:87]\n"
"ds_read_b128 a[36:39], v234 offset:0\n"
"v_mfma_f32_16x16x32_bf16 v[84:87], v[140:143], a[188:191], v[84:87]\n"
"ds_read_b128 a[120:123], v234 offset:1024\n"
"v_mfma_f32_16x16x32_bf16 v[88:91], a[16:19], a[184:187], v[88:91]\n"
"ds_read_b128 a[32:35], v235 offset:0\n"
"v_mfma_f32_16x16x32_bf16 v[88:91], a[160:163], a[188:191], v[88:91]\n"
"ds_read_b128 a[216:219], v235 offset:1024\n"
"v_mfma_f32_16x16x32_bf16 v[92:95], a[8:11], a[184:187], v[92:95]\n"
"ds_read_b128 a[40:43], v232 offset:0\n"
"v_mfma_f32_16x16x32_bf16 v[92:95], a[164:167], a[188:191], v[92:95]\n"
"ds_read_b128 a[44:47], v232 offset:1024\n"
"v_mfma_f32_16x16x32_bf16 v[96:99], a[88:91], a[96:99], v[96:99]\n"
"ds_read_b128 a[220:223], v246 offset:49152\n"
"v_mfma_f32_16x16x32_bf16 v[96:99], v[156:159], a[24:27], v[96:99]\n"
"ds_read_b128 a[196:199], v246 offset:50176\n"
"v_mfma_f32_16x16x32_bf16 v[100:103], a[92:95], a[96:99], v[100:103]\n"
"ds_read_b128 v[228:231], v246 offset:51200\n"
"v_mfma_f32_16x16x32_bf16 v[100:103], v[140:143], a[24:27], v[100:103]\n"
"ds_read_b128 a[124:127], v246 offset:52224\n"
"v_mfma_f32_16x16x32_bf16 v[104:107], a[16:19], a[96:99], v[104:107]\n"
"ds_read_b128 v[224:227], v246 offset:57344\n"
"v_mfma_f32_16x16x32_bf16 v[104:107], a[160:163], a[24:27], v[104:107]\n"
"Lfused_hop15:\n"
"s_cbranch_execz Lfused_hopend\n"
"ds_read_b128 a[200:203], v246 offset:58368\n"
"v_mfma_f32_16x16x32_bf16 v[108:111], a[8:11], a[96:99], v[108:111]\n"
"ds_read_b128 a[204:207], v246 offset:59392\n"
"v_mfma_f32_16x16x32_bf16 v[108:111], a[164:167], a[24:27], v[108:111]\n"
"ds_read_b128 a[212:215], v246 offset:60416\n"
"v_mfma_f32_16x16x32_bf16 v[112:115], a[88:91], v[216:219], v[112:115]\n"
"v_mfma_f32_16x16x32_bf16 v[112:115], v[156:159], v[200:203], v[112:115]\n"
"v_mfma_f32_16x16x32_bf16 v[116:119], a[92:95], v[216:219], v[116:119]\n"
"v_mfma_f32_16x16x32_bf16 v[116:119], v[140:143], v[200:203], v[116:119]\n"
"v_mfma_f32_16x16x32_bf16 v[120:123], a[16:19], v[216:219], v[120:123]\n"
"v_mfma_f32_16x16x32_bf16 v[120:123], a[160:163], v[200:203], v[120:123]\n"
"v_mfma_f32_16x16x32_bf16 v[124:127], a[8:11], v[216:219], v[124:127]\n"
"v_mfma_f32_16x16x32_bf16 v[124:127], a[164:167], v[200:203], v[124:127]\n"
"s_waitcnt vmcnt(18) lgkmcnt(0)\n"
"v_mfma_f32_16x16x32_bf16 v[16:19], v[168:171], v[184:187], v[16:19]\n"
"s_barrier\n"
"v_mfma_f32_16x16x32_bf16 v[16:19], v[192:195], v[188:191], v[16:19]\n"
"ds_read_b128 a[48:51], v233 offset:16384\n"
"v_mfma_f32_16x16x32_bf16 v[20:23], v[172:175], v[184:187], v[20:23]\n"
"ds_read_b128 a[52:55], v233 offset:17408\n"
"v_mfma_f32_16x16x32_bf16 v[20:23], v[208:211], v[188:191], v[20:23]\n"
"v_mul_f32 v80, v80, v237\n"
"v_mul_f32 v81, v81, v237\n"
"v_mfma_f32_16x16x32_bf16 v[24:27], a[100:103], v[184:187], v[24:27]\n"
"ds_read_b128 a[132:135], v234 offset:16384\n"
"v_mul_f32 v82, v82, v237\n"
"v_mfma_f32_16x16x32_bf16 v[24:27], v[176:179], v[188:191], v[24:27]\n"
"v_mul_f32 v83, v83, v237\n"
"v_mul_f32 v84, v84, v237\n"
"v_mfma_f32_16x16x32_bf16 v[28:31], a[28:31], v[184:187], v[28:31]\n"
"ds_read_b128 a[192:195], v234 offset:17408\n"
"v_mul_f32 v85, v85, v237\n"
"v_mfma_f32_16x16x32_bf16 v[28:31], v[180:183], v[188:191], v[28:31]\n"
"v_mul_f32 v86, v86, v237\n"
"v_mul_f32 v87, v87, v237\n"
"v_mfma_f32_16x16x32_bf16 v[32:35], v[168:171], a[36:39], v[32:35]\n"
"ds_read_b128 a[128:131], v235 offset:16384\n"
"v_mul_f32 v88, v88, v237\n"
"v_mfma_f32_16x16x32_bf16 v[32:35], v[192:195], a[120:123], v[32:35]\n"
"v_mul_f32 v89, v89, v237\n"
"v_mul_f32 v90, v90, v237\n"
"v_mfma_f32_16x16x32_bf16 v[36:39], v[172:175], a[36:39], v[36:39]\n"
"ds_read_b128 a[248:251], v235 offset:17408\n"
"v_mul_f32 v91, v91, v237\n"
"v_mfma_f32_16x16x32_bf16 v[36:39], v[208:211], a[120:123], v[36:39]\n"
"v_mul_f32 v92, v92, v237\n"
"v_mul_f32 v93, v93, v237\n"
"v_mfma_f32_16x16x32_bf16 v[40:43], a[100:103], a[36:39], v[40:43]\n"
"ds_read_b128 a[152:155], v232 offset:16384\n"
"v_mul_f32 v94, v94, v237\n"
"v_mfma_f32_16x16x32_bf16 v[40:43], v[176:179], a[120:123], v[40:43]\n"
"v_mul_f32 v95, v95, v237\n"
"v_mul_f32 v96, v96, v238\n"
"v_mfma_f32_16x16x32_bf16 v[44:47], a[28:31], a[36:39], v[44:47]\n"
"ds_read_b128 a[156:159], v232 offset:17408\n"
"v_mul_f32 v97, v97, v238\n"
"v_mfma_f32_16x16x32_bf16 v[44:47], v[180:183], a[120:123], v[44:47]\n"
"v_mul_f32 v98, v98, v238\n"
"v_mul_f32 v99, v99, v238\n"
"v_mfma_f32_16x16x32_bf16 v[48:51], v[168:171], a[32:35], v[48:51]\n"
"v_mul_f32 v100, v100, v238\n"
"v_mul_f32 v101, v101, v238\n"
"v_mfma_f32_16x16x32_bf16 v[48:51], v[192:195], a[216:219], v[48:51]\n"
"v_mul_f32 v102, v102, v238\n"
"v_mul_f32 v103, v103, v238\n"
"v_mfma_f32_16x16x32_bf16 v[52:55], v[172:175], a[32:35], v[52:55]\n"
"v_mul_f32 v104, v104, v238\n"
"v_mul_f32 v105, v105, v238\n"
"v_mfma_f32_16x16x32_bf16 v[52:55], v[208:211], a[216:219], v[52:55]\n"
"v_mul_f32 v106, v106, v238\n"
"v_mul_f32 v107, v107, v238\n"
"v_mfma_f32_16x16x32_bf16 v[56:59], a[100:103], a[32:35], v[56:59]\n"
"v_mul_f32 v108, v108, v238\n"
"v_mul_f32 v109, v109, v238\n"
"v_mfma_f32_16x16x32_bf16 v[56:59], v[176:179], a[216:219], v[56:59]\n"
"v_mul_f32 v110, v110, v238\n"
"v_mul_f32 v111, v111, v238\n"
"v_mfma_f32_16x16x32_bf16 v[60:63], a[28:31], a[32:35], v[60:63]\n"
"v_mul_f32 v112, v112, v239\n"
"v_mul_f32 v113, v113, v239\n"
"v_mfma_f32_16x16x32_bf16 v[60:63], v[180:183], a[216:219], v[60:63]\n"
"v_mul_f32 v114, v114, v239\n"
"v_mul_f32 v115, v115, v239\n"
"s_waitcnt vmcnt(4) lgkmcnt(0)\n"
"v_mfma_f32_16x16x32_bf16 v[16:19], a[220:223], a[48:51], v[16:19]\n"
"s_barrier\n"
"v_mfma_f32_16x16x32_bf16 v[16:19], v[224:227], a[52:55], v[16:19]\n"
"ds_read_b128 v[152:155], v233 offset:32768\n"
"ds_read_b128 v[128:131], v233 offset:33792\n"
"v_mfma_f32_16x16x32_bf16 v[20:23], a[196:199], a[48:51], v[20:23]\n"
"ds_read_b128 a[60:63], v234 offset:32768\n"
"v_mfma_f32_16x16x32_bf16 v[20:23], a[200:203], a[52:55], v[20:23]\n"
"ds_read_b128 a[208:211], v234 offset:33792\n"
"v_mul_f32 v116, v116, v239\n"
"v_mfma_f32_16x16x32_bf16 v[24:27], v[228:231], a[48:51], v[24:27]\n"
"ds_read_b128 a[56:59], v235 offset:32768\n"
"v_mul_f32 v117, v117, v239\n"
"v_mfma_f32_16x16x32_bf16 v[24:27], a[204:207], a[52:55], v[24:27]\n"
"ds_read_b128 a[232:235], v235 offset:33792\n"
"v_mul_f32 v118, v118, v239\n"
"v_mfma_f32_16x16x32_bf16 v[28:31], a[124:127], a[48:51], v[28:31]\n"
"ds_read_b128 v[136:139], v232 offset:32768\n"
"v_mul_f32 v119, v119, v239\n"
"v_mfma_f32_16x16x32_bf16 v[28:31], a[212:215], a[52:55], v[28:31]\n"
"ds_read_b128 v[144:147], v232 offset:33792\n"
"v_mul_f32 v120, v120, v239\n"
"v_mfma_f32_16x16x32_bf16 v[32:35], a[220:223], a[132:135], v[32:35]\n"
"ds_read_b128 a[0:3], v246 offset:0\n"
"v_mul_f32 v121, v121, v239\n"
"v_mfma_f32_16x16x32_bf16 v[32:35], v[224:227], a[192:195], v[32:35]\n"
"ds_read_b128 a[4:7], v246 offset:1024\n"
"v_mul_f32 v122, v122, v239\n"
"v_mfma_f32_16x16x32_bf16 v[36:39], a[196:199], a[132:135], v[36:39]\n"
"ds_read_b128 a[72:75], v246 offset:2048\n"
"v_mul_f32 v123, v123, v239\n"
"v_mfma_f32_16x16x32_bf16 v[36:39], a[200:203], a[192:195], v[36:39]\n"
"ds_read_b128 a[148:151], v246 offset:3072\n"
"v_mul_f32 v124, v124, v239\n"
"v_mfma_f32_16x16x32_bf16 v[40:43], v[228:231], a[132:135], v[40:43]\n"
"ds_read_b128 a[228:231], v246 offset:8192\n"
"v_mul_f32 v125, v125, v239\n"
"v_mfma_f32_16x16x32_bf16 v[40:43], a[204:207], a[192:195], v[40:43]\n"
"ds_read_b128 a[76:79], v246 offset:9216\n"
"v_mul_f32 v126, v126, v239\n"
"v_mfma_f32_16x16x32_bf16 v[44:47], a[124:127], a[132:135], v[44:47]\n"
"ds_read_b128 a[140:143], v246 offset:10240\n"
"v_mul_f32 v127, v127, v239\n"
"v_mfma_f32_16x16x32_bf16 v[44:47], a[212:215], a[192:195], v[44:47]\n"
"ds_read_b128 a[244:247], v246 offset:11264\n"
"v_sub_f32 v237, 1.0, v237\n"
"v_mfma_f32_16x16x32_bf16 v[48:51], a[220:223], a[128:131], v[48:51]\n"
"v_sub_f32 v238, 1.0, v238\n"
"v_sub_f32 v239, 1.0, v239\n"
"v_mfma_f32_16x16x32_bf16 v[48:51], v[224:227], a[248:251], v[48:51]\n"
"v_mfma_f32_16x16x32_bf16 v[52:55], a[196:199], a[128:131], v[52:55]\n"
"v_mfma_f32_16x16x32_bf16 v[52:55], a[200:203], a[248:251], v[52:55]\n"
"v_mfma_f32_16x16x32_bf16 v[56:59], v[228:231], a[128:131], v[56:59]\n"
"v_mfma_f32_16x16x32_bf16 v[56:59], a[204:207], a[248:251], v[56:59]\n"
"v_mfma_f32_16x16x32_bf16 v[60:63], a[124:127], a[128:131], v[60:63]\n"
"v_mfma_f32_16x16x32_bf16 v[60:63], a[212:215], a[248:251], v[60:63]\n"
"s_waitcnt vmcnt(0) lgkmcnt(0)\n"
"v_mfma_f32_16x16x32_bf16 v[16:19], a[0:3], v[152:155], v[16:19]\n"
"s_barrier\n"
"v_mfma_f32_16x16x32_bf16 v[16:19], a[228:231], v[128:131], v[16:19]\n"
"ds_read_b128 a[168:171], v233 offset:49152\n"
"ds_read_b128 a[20:23], v233 offset:50176\n"
"v_mfma_f32_16x16x32_bf16 v[20:23], a[4:7], v[152:155], v[20:23]\n"
"ds_read_b128 v[132:135], v234 offset:49152\n"
"v_mfma_f32_16x16x32_bf16 v[20:23], a[76:79], v[128:131], v[20:23]\n"
"ds_read_b128 a[172:175], v234 offset:50176\n"
"v_mfma_f32_16x16x32_bf16 v[24:27], a[72:75], v[152:155], v[24:27]\n"
"ds_read_b128 a[12:15], v235 offset:49152\n"
"v_mfma_f32_16x16x32_bf16 v[24:27], a[140:143], v[128:131], v[24:27]\n"
"ds_read_b128 v[148:151], v235 offset:50176\n"
"v_mfma_f32_16x16x32_bf16 v[28:31], a[148:151], v[152:155], v[28:31]\n"
"ds_read_b128 a[184:187], v232 offset:49152\n"
"v_mfma_f32_16x16x32_bf16 v[28:31], a[244:247], v[128:131], v[28:31]\n"
"ds_read_b128 a[96:99], v232 offset:50176\n"
"v_mfma_f32_16x16x32_bf16 v[32:35], a[0:3], a[60:63], v[32:35]\n"
"ds_read_b128 v[216:219], v246 offset:16384\n"
"v_mfma_f32_16x16x32_bf16 v[32:35], a[228:231], a[208:211], v[32:35]\n"
"ds_read_b128 a[188:191], v246 offset:17408\n"
"v_mfma_f32_16x16x32_bf16 v[36:39], a[4:7], a[60:63], v[36:39]\n"
"ds_read_b128 a[24:27], v246 offset:18432\n"
"v_mfma_f32_16x16x32_bf16 v[36:39], a[76:79], a[208:211], v[36:39]\n"
"ds_read_b128 v[200:203], v246 offset:19456\n"
"v_mfma_f32_16x16x32_bf16 v[40:43], a[72:75], a[60:63], v[40:43]\n"
"ds_read_b128 v[184:187], v246 offset:24576\n"
"v_mfma_f32_16x16x32_bf16 v[40:43], a[140:143], a[208:211], v[40:43]\n"
"ds_read_b128 a[36:39], v246 offset:25600\n"
"v_mfma_f32_16x16x32_bf16 v[44:47], a[148:151], a[60:63], v[44:47]\n"
"ds_read_b128 a[32:35], v246 offset:26624\n"
"v_mfma_f32_16x16x32_bf16 v[44:47], a[244:247], a[208:211], v[44:47]\n"
"ds_read_b128 v[188:191], v246 offset:27648\n"
"v_mfma_f32_16x16x32_bf16 v[48:51], a[0:3], a[56:59], v[48:51]\n"
"v_mfma_f32_16x16x32_bf16 v[48:51], a[228:231], a[232:235], v[48:51]\n"
"v_mfma_f32_16x16x32_bf16 v[52:55], a[4:7], a[56:59], v[52:55]\n"
"v_mfma_f32_16x16x32_bf16 v[52:55], a[76:79], a[232:235], v[52:55]\n"
"v_mfma_f32_16x16x32_bf16 v[56:59], a[72:75], a[56:59], v[56:59]\n"
"v_mfma_f32_16x16x32_bf16 v[56:59], a[140:143], a[232:235], v[56:59]\n"
"v_mfma_f32_16x16x32_bf16 v[60:63], a[148:151], a[56:59], v[60:63]\n"
"v_mfma_f32_16x16x32_bf16 v[60:63], a[244:247], a[232:235], v[60:63]\n"
"s_waitcnt vmcnt(6) lgkmcnt(0)\n"
"v_mfma_f32_16x16x32_bf16 v[16:19], v[216:219], a[168:171], v[16:19]\n"
"s_barrier\n"
"v_mfma_f32_16x16x32_bf16 v[16:19], v[184:187], a[20:23], v[16:19]\n"
"v_mfma_f32_16x16x32_bf16 v[20:23], a[188:191], a[168:171], v[20:23]\n"
"v_mfma_f32_16x16x32_bf16 v[20:23], a[36:39], a[20:23], v[20:23]\n"
"v_mfma_f32_16x16x32_bf16 v[24:27], a[24:27], a[168:171], v[24:27]\n"
"v_mfma_f32_16x16x32_bf16 v[24:27], a[32:35], a[20:23], v[24:27]\n"
"v_mfma_f32_16x16x32_bf16 v[28:31], v[200:203], a[168:171], v[28:31]\n"
"v_mfma_f32_16x16x32_bf16 v[28:31], v[188:191], a[20:23], v[28:31]\n"
"v_mfma_f32_16x16x32_bf16 v[32:35], v[216:219], v[132:135], v[32:35]\n"
"s_nop 0\n"
"v_fma_f32 v16, v16, v237, v80\n"
"v_fma_f32 v17, v17, v237, v81\n"
"v_mfma_f32_16x16x32_bf16 v[32:35], v[184:187], a[172:175], v[32:35]\n"
"v_fma_f32 v18, v18, v237, v82\n"
"v_fma_f32 v19, v19, v237, v83\n"
"v_mfma_f32_16x16x32_bf16 v[36:39], a[188:191], v[132:135], v[36:39]\n"
"v_fma_f32 v20, v20, v237, v84\n"
"v_fma_f32 v21, v21, v237, v85\n"
"v_mfma_f32_16x16x32_bf16 v[36:39], a[36:39], a[172:175], v[36:39]\n"
"v_fma_f32 v22, v22, v237, v86\n"
"v_fma_f32 v23, v23, v237, v87\n"
"v_mfma_f32_16x16x32_bf16 v[40:43], a[24:27], v[132:135], v[40:43]\n"
"v_cvt_pk_bf16_f32 v16, v16, v17\n"
"v_cvt_pk_bf16_f32 v17, v18, v19\n"
"v_mfma_f32_16x16x32_bf16 v[40:43], a[32:35], a[172:175], v[40:43]\n"
"v_cvt_pk_bf16_f32 v18, v20, v21\n"
"v_cvt_pk_bf16_f32 v19, v22, v23\n"
"v_mfma_f32_16x16x32_bf16 v[44:47], v[200:203], v[132:135], v[44:47]\n"
"buffer_store_dwordx4 v[16:19], v248, s[68:71], s17 offen offset:0 sc1\n"
"v_mfma_f32_16x16x32_bf16 v[44:47], v[188:191], a[172:175], v[44:47]\n"
"v_fma_f32 v24, v24, v237, v88\n"
"v_fma_f32 v25, v25, v237, v89\n"
"v_mfma_f32_16x16x32_bf16 v[48:51], v[216:219], a[12:15], v[48:51]\n"
"v_fma_f32 v26, v26, v237, v90\n"
"v_fma_f32 v27, v27, v237, v91\n"
"v_mfma_f32_16x16x32_bf16 v[48:51], v[184:187], v[148:151], v[48:51]\n"
"v_fma_f32 v28, v28, v237, v92\n"
"v_fma_f32 v29, v29, v237, v93\n"
"v_mfma_f32_16x16x32_bf16 v[52:55], a[188:191], a[12:15], v[52:55]\n"
"v_fma_f32 v30, v30, v237, v94\n"
"v_fma_f32 v31, v31, v237, v95\n"
"v_mfma_f32_16x16x32_bf16 v[52:55], a[36:39], v[148:151], v[52:55]\n"
"v_cvt_pk_bf16_f32 v24, v24, v25\n"
"v_cvt_pk_bf16_f32 v25, v26, v27\n"
"v_mfma_f32_16x16x32_bf16 v[56:59], a[24:27], a[12:15], v[56:59]\n"
"v_cvt_pk_bf16_f32 v26, v28, v29\n"
"v_cvt_pk_bf16_f32 v27, v30, v31\n"
"v_mfma_f32_16x16x32_bf16 v[56:59], a[32:35], v[148:151], v[56:59]\n"
"buffer_store_dwordx4 v[24:27], v248, s[68:71], s17 offen offset:1024 sc1\n"
"v_mfma_f32_16x16x32_bf16 v[60:63], v[200:203], a[12:15], v[60:63]\n"
"v_fma_f32 v32, v32, v238, v96\n"
"v_fma_f32 v33, v33, v238, v97\n"
"v_mfma_f32_16x16x32_bf16 v[60:63], v[188:191], v[148:151], v[60:63]\n"
"v_fma_f32 v34, v34, v238, v98\n"
"v_fma_f32 v35, v35, v238, v99\n"
"v_fma_f32 v36, v36, v238, v100\n"
"v_fma_f32 v37, v37, v238, v101\n"
"v_fma_f32 v38, v38, v238, v102\n"
"v_fma_f32 v39, v39, v238, v103\n"
"v_cvt_pk_bf16_f32 v32, v32, v33\n"
"v_cvt_pk_bf16_f32 v33, v34, v35\n"
"v_cvt_pk_bf16_f32 v34, v36, v37\n"
"v_cvt_pk_bf16_f32 v35, v38, v39\n"
"buffer_store_dwordx4 v[32:35], v248, s[68:71], s18 offen offset:0 sc1\n"
"v_fma_f32 v40, v40, v238, v104\n"
"v_fma_f32 v41, v41, v238, v105\n"
"v_fma_f32 v42, v42, v238, v106\n"
"v_fma_f32 v43, v43, v238, v107\n"
"v_fma_f32 v44, v44, v238, v108\n"
"v_fma_f32 v45, v45, v238, v109\n"
"v_fma_f32 v46, v46, v238, v110\n"
"v_fma_f32 v47, v47, v238, v111\n"
"v_cvt_pk_bf16_f32 v40, v40, v41\n"
"v_cvt_pk_bf16_f32 v41, v42, v43\n"
"v_cvt_pk_bf16_f32 v42, v44, v45\n"
"v_cvt_pk_bf16_f32 v43, v46, v47\n"
"buffer_store_dwordx4 v[40:43], v248, s[68:71], s18 offen offset:1024 sc1\n"
"v_fma_f32 v48, v48, v239, v112\n"
"v_fma_f32 v49, v49, v239, v113\n"
"v_fma_f32 v50, v50, v239, v114\n"
"v_fma_f32 v51, v51, v239, v115\n"
"v_fma_f32 v52, v52, v239, v116\n"
"v_fma_f32 v53, v53, v239, v117\n"
"v_fma_f32 v54, v54, v239, v118\n"
"v_fma_f32 v55, v55, v239, v119\n"
"v_cvt_pk_bf16_f32 v48, v48, v49\n"
"v_cvt_pk_bf16_f32 v49, v50, v51\n"
"v_cvt_pk_bf16_f32 v50, v52, v53\n"
"v_cvt_pk_bf16_f32 v51, v54, v55\n"
"buffer_store_dwordx4 v[48:51], v248, s[68:71], s19 offen offset:0 sc1\n"
"v_fma_f32 v56, v56, v239, v120\n"
"v_fma_f32 v57, v57, v239, v121\n"
"v_fma_f32 v58, v58, v239, v122\n"
"v_fma_f32 v59, v59, v239, v123\n"
"v_fma_f32 v60, v60, v239, v124\n"
"v_fma_f32 v61, v61, v239, v125\n"
"v_fma_f32 v62, v62, v239, v126\n"
"v_fma_f32 v63, v63, v239, v127\n"
"v_cvt_pk_bf16_f32 v56, v56, v57\n"
"v_cvt_pk_bf16_f32 v57, v58, v59\n"
"v_cvt_pk_bf16_f32 v58, v60, v61\n"
"v_cvt_pk_bf16_f32 v59, v62, v63\n"
"buffer_store_dwordx4 v[56:59], v248, s[68:71], s19 offen offset:1024 sc1\n"
"v_mfma_f32_16x16x32_bf16 v[0:3], a[104:107], a[80:83], v[0:3]\n"
"global_load_dwordx2 v[252:253], v245, s[62:63] sc1\n"
"v_mfma_f32_16x16x32_bf16 v[0:3], v[204:207], a[84:87], v[0:3]\n"
"v_mfma_f32_16x16x32_bf16 v[64:67], a[224:227], a[176:179], 0\n"
"v_mfma_f32_16x16x32_bf16 v[64:67], a[252:255], a[180:183], v[64:67]\n"
"v_mfma_f32_16x16x32_bf16 v[64:67], a[88:91], v[160:163], v[64:67]\n"
"v_mfma_f32_16x16x32_bf16 v[64:67], v[156:159], v[164:167], v[64:67]\n"
"v_mfma_f32_16x16x32_bf16 v[0:3], v[168:171], a[40:43], v[0:3]\n"
"v_mfma_f32_16x16x32_bf16 v[0:3], v[192:195], a[44:47], v[0:3]\n"
"v_mfma_f32_16x16x32_bf16 v[0:3], a[220:223], a[152:155], v[0:3]\n"
"v_mfma_f32_16x16x32_bf16 v[0:3], v[224:227], a[156:159], v[0:3]\n"
"v_mfma_f32_16x16x32_bf16 v[0:3], a[0:3], v[136:139], v[0:3]\n"
"v_mfma_f32_16x16x32_bf16 v[0:3], a[228:231], v[144:147], v[0:3]\n"
"v_mfma_f32_16x16x32_bf16 v[0:3], v[216:219], a[184:187], v[0:3]\n"
"v_mfma_f32_16x16x32_bf16 v[0:3], v[184:187], a[96:99], v[0:3]\n"
"v_mfma_f32_16x16x32_bf16 v[4:7], a[108:111], a[80:83], v[4:7]\n"
"v_mfma_f32_16x16x32_bf16 v[4:7], v[212:215], a[84:87], v[4:7]\n"
"v_mfma_f32_16x16x32_bf16 v[68:71], a[240:243], a[176:179], 0\n"
"v_mfma_f32_16x16x32_bf16 v[68:71], a[236:239], a[180:183], v[68:71]\n"
"v_mfma_f32_16x16x32_bf16 v[68:71], a[92:95], v[160:163], v[68:71]\n"
"v_mfma_f32_16x16x32_bf16 v[68:71], v[140:143], v[164:167], v[68:71]\n"
"v_mfma_f32_16x16x32_bf16 v[4:7], v[172:175], a[40:43], v[4:7]\n"
"v_mfma_f32_16x16x32_bf16 v[4:7], v[208:211], a[44:47], v[4:7]\n"
"v_mfma_f32_16x16x32_bf16 v[4:7], a[196:199], a[152:155], v[4:7]\n"
"v_mfma_f32_16x16x32_bf16 v[4:7], a[200:203], a[156:159], v[4:7]\n"
"v_mfma_f32_16x16x32_bf16 v[4:7], a[4:7], v[136:139], v[4:7]\n"
"v_mfma_f32_16x16x32_bf16 v[4:7], a[76:79], v[144:147], v[4:7]\n"
"v_mfma_f32_16x16x32_bf16 v[4:7], a[188:191], a[184:187], v[4:7]\n"
"v_mfma_f32_16x16x32_bf16 v[4:7], a[36:39], a[96:99], v[4:7]\n"
"v_mfma_f32_16x16x32_bf16 v[8:11], v[220:223], a[80:83], v[8:11]\n"
"v_mfma_f32_16x16x32_bf16 v[8:11], a[112:115], a[84:87], v[8:11]\n"
"v_mfma_f32_16x16x32_bf16 v[72:75], a[144:147], a[176:179], 0\n"
"v_mfma_f32_16x16x32_bf16 v[72:75], a[64:67], a[180:183], v[72:75]\n"
"v_mfma_f32_16x16x32_bf16 v[72:75], a[16:19], v[160:163], v[72:75]\n"
"v_mfma_f32_16x16x32_bf16 v[72:75], a[160:163], v[164:167], v[72:75]\n"
"v_mfma_f32_16x16x32_bf16 v[8:11], a[100:103], a[40:43], v[8:11]\n"
"v_mfma_f32_16x16x32_bf16 v[8:11], v[176:179], a[44:47], v[8:11]\n"
"v_mfma_f32_16x16x32_bf16 v[8:11], v[228:231], a[152:155], v[8:11]\n"
"v_mfma_f32_16x16x32_bf16 v[8:11], a[204:207], a[156:159], v[8:11]\n"
"v_mfma_f32_16x16x32_bf16 v[8:11], a[72:75], v[136:139], v[8:11]\n"
"v_mfma_f32_16x16x32_bf16 v[8:11], a[140:143], v[144:147], v[8:11]\n"
"v_mfma_f32_16x16x32_bf16 v[8:11], a[24:27], a[184:187], v[8:11]\n"
"v_mfma_f32_16x16x32_bf16 v[8:11], a[32:35], a[96:99], v[8:11]\n"
"v_mfma_f32_16x16x32_bf16 v[12:15], v[196:199], a[80:83], v[12:15]\n"
"v_mfma_f32_16x16x32_bf16 v[12:15], a[116:119], a[84:87], v[12:15]\n"
"v_mfma_f32_16x16x32_bf16 v[76:79], a[136:139], a[176:179], 0\n"
"v_mfma_f32_16x16x32_bf16 v[76:79], a[68:71], a[180:183], v[76:79]\n"
"v_mfma_f32_16x16x32_bf16 v[76:79], a[8:11], v[160:163], v[76:79]\n"
"v_mfma_f32_16x16x32_bf16 v[76:79], a[164:167], v[164:167], v[76:79]\n"
"v_mfma_f32_16x16x32_bf16 v[12:15], a[28:31], a[40:43], v[12:15]\n"
"v_mfma_f32_16x16x32_bf16 v[12:15], v[180:183], a[44:47], v[12:15]\n"
"v_mfma_f32_16x16x32_bf16 v[12:15], a[124:127], a[152:155], v[12:15]\n"
"v_mfma_f32_16x16x32_bf16 v[12:15], a[212:215], a[156:159], v[12:15]\n"
"v_mfma_f32_16x16x32_bf16 v[12:15], a[148:151], v[136:139], v[12:15]\n"
"v_mfma_f32_16x16x32_bf16 v[12:15], a[244:247], v[144:147], v[12:15]\n"
"v_mfma_f32_16x16x32_bf16 v[12:15], v[200:203], a[184:187], v[12:15]\n"
"v_mfma_f32_16x16x32_bf16 v[12:15], v[188:191], a[96:99], v[12:15]\n"
"v_mov_b32 v250, s98\n"
"v_mov_b32 v251, s99\n"
"s_lshl_b32 s50, s56, 14\n"
"s_lshl_b32 s51, s77, 7\n"
"s_add_u32 s50, s50, s51\n"
"s_waitcnt vmcnt(0)\n"
"s_mov_b64 exec, 14\n"
"global_store_dwordx2 v247, v[250:251], s[62:63] sc1\n"
"s_mov_b64 exec, -1\n"
"v_mov_b32 v232, 0x3f80\n"
"v_mov_b32 v233, 0x3f800000\n"
"v_sub_f32 v64, v64, v0\n"
"v_sub_f32 v65, v65, v1\n"
"v_sub_f32 v66, v66, v2\n"
"v_sub_f32 v67, v67, v3\n"
"v_sub_f32 v68, v68, v4\n"
"v_sub_f32 v69, v69, v5\n"
"v_sub_f32 v70, v70, v6\n"
"v_sub_f32 v71, v71, v7\n"
"v_sub_f32 v72, v72, v8\n"
"v_sub_f32 v73, v73, v9\n"
"v_sub_f32 v74, v74, v10\n"
"v_sub_f32 v75, v75, v11\n"
"v_sub_f32 v76, v76, v12\n"
"v_sub_f32 v77, v77, v13\n"
"v_sub_f32 v78, v78, v14\n"
"v_sub_f32 v79, v79, v15\n"
"v_fmac_f32 v0, v64, v236\n"
"v_fmac_f32 v1, v65, v236\n"
"v_fmac_f32 v2, v66, v236\n"
"v_fmac_f32 v3, v67, v236\n"
"v_fmac_f32 v4, v68, v236\n"
"v_fmac_f32 v5, v69, v236\n"
"v_fmac_f32 v6, v70, v236\n"
"v_fmac_f32 v7, v71, v236\n"
"v_fmac_f32 v8, v72, v236\n"
"v_fmac_f32 v9, v73, v236\n"
"v_fmac_f32 v10, v74, v236\n"
"v_fmac_f32 v11, v75, v236\n"
"v_fmac_f32 v12, v76, v236\n"
"v_fmac_f32 v13, v77, v236\n"
"v_fmac_f32 v14, v78, v236\n"
"v_fmac_f32 v15, v79, v236\n"
"v_cmp_ne_u64 vcc, s[98:99], v[252:253]\n"
"s_cbranch_vccz Lfused_lk\n"
"s_mov_b32 s58, 32768\n"
"Lfused_pl5:\n"
"global_load_dwordx2 v[252:253], v245, s[62:63] sc1\n"
"s_waitcnt vmcnt(0)\n"
"v_cmp_ne_u64 vcc, s[98:99], v[252:253]\n"
"s_cbranch_vccz Lfused_pd5\n"
"s_sleep 1\n"
"s_sub_u32 s58, s58, 1\n"
"s_cbranch_scc0 Lfused_pl5\n"
"Lfused_pd5:\n"
"Lfused_lk:\n"
"buffer_load_dwordx4 v[132:135], v248, s[68:71], s20 offen offset:0 sc1\n"
"buffer_load_dwordx4 v[136:139], v248, s[68:71], s21 offen offset:0 sc1\n"
"buffer_load_dwordx4 v[140:143], v248, s[68:71], s22 offen offset:0 sc1\n"
"buffer_load_dwordx4 v[148:151], v248, s[68:71], s20 offen offset:1024 sc1\n"
"buffer_load_dwordx4 v[152:155], v248, s[68:71], s21 offen offset:1024 sc1\n"
"buffer_load_dwordx4 v[156:159], v248, s[68:71], s22 offen offset:1024 sc1\n"
"s_waitcnt vmcnt(5)\n"
"v_dot2c_f32_bf16 v0, v132, v232\n"
"v_dot2c_f32_bf16 v1, v132, v233\n"
"v_dot2c_f32_bf16 v2, v133, v232\n"
"v_dot2c_f32_bf16 v3, v133, v233\n"
"v_dot2c_f32_bf16 v4, v134, v232\n"
"v_dot2c_f32_bf16 v5, v134, v233\n"
"v_dot2c_f32_bf16 v6, v135, v232\n"
"v_dot2c_f32_bf16 v7, v135, v233\n"
"s_waitcnt vmcnt(4)\n"
"v_dot2c_f32_bf16 v0, v136, v232\n"
"v_dot2c_f32_bf16 v1, v136, v233\n"
"v_dot2c_f32_bf16 v2, v137, v232\n"
"v_dot2c_f32_bf16 v3, v137, v233\n"
"v_dot2c_f32_bf16 v4, v138, v232\n"
"v_dot2c_f32_bf16 v5, v138, v233\n"
"v_dot2c_f32_bf16 v6, v139, v232\n"
"v_dot2c_f32_bf16 v7, v139, v233\n"
"s_waitcnt vmcnt(3)\n"
"v_dot2c_f32_bf16 v0, v140, v232\n"
"v_dot2c_f32_bf16 v1, v140, v233\n"
"v_dot2c_f32_bf16 v2, v141, v232\n"
"v_dot2c_f32_bf16 v3, v141, v233\n"
"v_dot2c_f32_bf16 v4, v142, v232\n"
"v_dot2c_f32_bf16 v5, v142, v233\n"
"v_dot2c_f32_bf16 v6, v143, v232\n"
"v_dot2c_f32_bf16 v7, v143, v233\n"
"s_nop 2\n"
"v_cvt_pk_bf16_f32 v0, v0, v1\n"
"v_cvt_pk_bf16_f32 v1, v2, v3\n"
"v_cvt_pk_bf16_f32 v2, v4, v5\n"
"v_cvt_pk_bf16_f32 v3, v6, v7\n"
"buffer_store_dwordx4 v[0:3], v254, s[80:83], s50 offen offset:0 sc1\n"
"s_waitcnt vmcnt(3)\n"
"v_dot2c_f32_bf16 v8, v148, v232\n"
"v_dot2c_f32_bf16 v9, v148, v233\n"
"v_dot2c_f32_bf16 v10, v149, v232\n"
"v_dot2c_f32_bf16 v11, v149, v233\n"
"v_dot2c_f32_bf16 v12, v150, v232\n"
"v_dot2c_f32_bf16 v13, v150, v233\n"
"v_dot2c_f32_bf16 v14, v151, v232\n"
"v_dot2c_f32_bf16 v15, v151, v233\n"
"s_waitcnt vmcnt(2)\n"
"v_dot2c_f32_bf16 v8, v152, v232\n"
"v_dot2c_f32_bf16 v9, v152, v233\n"
"v_dot2c_f32_bf16 v10, v153, v232\n"
"v_dot2c_f32_bf16 v11, v153, v233\n"
"v_dot2c_f32_bf16 v12, v154, v232\n"
"v_dot2c_f32_bf16 v13, v154, v233\n"
"v_dot2c_f32_bf16 v14, v155, v232\n"
"v_dot2c_f32_bf16 v15, v155, v233\n"
"s_waitcnt vmcnt(1)\n"
"v_dot2c_f32_bf16 v8, v156, v232\n"
"v_dot2c_f32_bf16 v9, v156, v233\n"
"v_dot2c_f32_bf16 v10, v157, v232\n"
"v_dot2c_f32_bf16 v11, v157, v233\n"
"v_dot2c_f32_bf16 v12, v158, v232\n"
"v_dot2c_f32_bf16 v13, v158, v233\n"
"v_dot2c_f32_bf16 v14, v159, v232\n"
"v_dot2c_f32_bf16 v15, v159, v233\n"
"s_nop 2\n"
"v_cvt_pk_bf16_f32 v8, v8, v9\n"
"v_cvt_pk_bf16_f32 v9, v10, v11\n"
"v_cvt_pk_bf16_f32 v10, v12, v13\n"
"v_cvt_pk_bf16_f32 v11, v14, v15\n"
"buffer_store_dwordx4 v[8:11], v254, s[80:83], s50 offen offset:64 sc1\n"
"s_or_b32 s50, s13, s12\n"
"s_cbranch_scc1 Lfused_end\n"
"v_mov_b32 v250, s98\n"
"v_mov_b32 v251, s99\n"
"v_mov_b32 v252, 0\n"
"s_mov_b64 exec, 1\n"
"global_store_dwordx2 v252, v[250:251], s[62:63] sc1\n"
"s_mov_b64 exec, -1\n"
"Lfused_end:\n"
"Lfused_fin:\n"
"s_endpgm\n"
"Lfused_xG0:\n"
"s_sub_u32 s15, s14, 1\n"
"s_getpc_b64 s[36:37]\n"
"Lfused_yG0:\n"
"s_sub_u32 s36, s36, Lfused_yG0-Lfused_rG0\n"
"s_subb_u32 s37, s37, 0\n"
"s_branch Lfused_fb1o\n"
"Lfused_xG1:\n"
"s_sub_u32 s15, s14, 1\n"
"s_getpc_b64 s[36:37]\n"
"Lfused_yG1:\n"
"s_sub_u32 s36, s36, Lfused_yG1-Lfused_rG1\n"
"s_subb_u32 s37, s37, 0\n"
"s_branch Lfused_fb0q\n"
"Lfused_xG2:\n"
"s_sub_u32 s15, s14, 1\n"
"s_getpc_b64 s[36:37]\n"
"Lfused_yG2:\n"
"s_sub_u32 s36, s36, Lfused_yG2-Lfused_rG2\n"
"s_subb_u32 s37, s37, 0\n"
"s_branch Lfused_fb0n\n"
"Lfused_xG3:\n"
"s_sub_u32 s15, s14, 1\n"
"s_getpc_b64 s[36:37]\n"
"Lfused_yG3:\n"
"s_sub_u32 s36, s36, Lfused_yG3-Lfused_rG3\n"
"s_subb_u32 s37, s37, 0\n"
"s_branch Lfused_fb1q\n"
"Lfused_xA0:\n"
"s_mov_b32 s15, 0\n"
"s_getpc_b64 s[36:37]\n"
"Lfused_yA0:\n"
"s_sub_u32 s36, s36, Lfused_yA0-Lfused_rA0\n"
"s_subb_u32 s37, s37, 0\n"
"s_branch Lfused_fb1o\n"
"Lfused_xA1:\n"
"s_mov_b32 s15, 0\n"
"s_getpc_b64 s[36:37]\n"
"Lfused_yA1:\n"
"s_sub_u32 s36, s36, Lfused_yA1-Lfused_rA1\n"
"s_subb_u32 s37, s37, 0\n"
"s_branch Lfused_fb0q\n"
"Lfused_xA2:\n"
"s_mov_b32 s15, 0\n"
"s_getpc_b64 s[36:37]\n"
"Lfused_yA2:\n"
"s_sub_u32 s36, s36, Lfused_yA2-Lfused_rA2\n"
"s_subb_u32 s37, s37, 0\n"
"s_branch Lfused_fb0o\n"
"Lfused_xA3:\n"
"s_mov_b32 s15, 0\n"
"s_getpc_b64 s[36:37]\n"
"Lfused_yA3:\n"
"s_sub_u32 s36, s36, Lfused_yA3-Lfused_rA3\n"
"s_subb_u32 s37, s37, 0\n"
"s_branch Lfused_fb1q\n"
"Lfused_xB0:\n"
"s_sub_u32 s15, s14, 1\n"
"s_getpc_b64 s[36:37]\n"
"Lfused_yB0:\n"
"s_sub_u32 s36, s36, Lfused_yB0-Lfused_rB0\n"
"s_subb_u32 s37, s37, 0\n"
"s_branch Lfused_fb1o\n"
"Lfused_xB1:\n"
"s_mov_b32 s15, 0\n"
"s_getpc_b64 s[36:37]\n"
"Lfused_yB1:\n"
"s_sub_u32 s36, s36, Lfused_yB1-Lfused_rB1\n"
"s_subb_u32 s37, s37, 0\n"
"s_branch Lfused_fb0q\n"
"Lfused_xB2:\n"
"s_sub_u32 s15, s14, 1\n"
"s_getpc_b64 s[36:37]\n"
"Lfused_yB2:\n"
"s_sub_u32 s36, s36, Lfused_yB2-Lfused_rB2\n"
"s_subb_u32 s37, s37, 0\n"
"s_branch Lfused_fb0o\n"
"Lfused_xB3:\n"
"s_mov_b32 s15, 0\n"
"s_getpc_b64 s[36:37]\n"
"Lfused_yB3:\n"
"s_sub_u32 s36, s36, Lfused_yB3-Lfused_rB3\n"
"s_subb_u32 s37, s37, 0\n"
"s_branch Lfused_fb1q\n"
"Lfused_xC0:\n"
"s_sub_u32 s15, s14, 1\n"
"s_getpc_b64 s[36:37]\n"
"Lfused_yC0:\n"
"s_sub_u32 s36, s36, Lfused_yC0-Lfused_rC0\n"
"s_subb_u32 s37, s37, 0\n"
"s_branch Lfused_fb1o\n"
"Lfused_xC1:\n"
"s_mov_b32 s15, 0\n"
"s_getpc_b64 s[36:37]\n"
"Lfused_yC1:\n"
"s_sub_u32 s36, s36, Lfused_yC1-Lfused_rC1\n"
"s_subb_u32 s37, s37, 0\n"
"s_branch Lfused_fb0q\n"
"Lfused_xC2:\n"
"s_mov_b32 s15, 0\n"
"s_getpc_b64 s[36:37]\n"
"Lfused_yC2:\n"
"s_sub_u32 s36, s36, Lfused_yC2-Lfused_rC2\n"
"s_subb_u32 s37, s37, 0\n"
"s_branch Lfused_fb0n\n"
"Lfused_xC3:\n"
"s_mov_b32 s15, 0\n"
"s_getpc_b64 s[36:37]\n"
"Lfused_yC3:\n"
"s_sub_u32 s36, s36, Lfused_yC3-Lfused_rC3\n"
"s_subb_u32 s37, s37, 0\n"
"s_branch Lfused_fb1q\n"
"Lfused_xD0:\n"
"s_mov_b32 s15, 0\n"
"s_getpc_b64 s[36:37]\n"
"Lfused_yD0:\n"
"s_sub_u32 s36, s36, Lfused_yD0-Lfused_rD0\n"
"s_subb_u32 s37, s37, 0\n"
"s_branch Lfused_fb1n\n"
"Lfused_xD1:\n"
"s_mov_b32 s15, 0\n"
"s_getpc_b64 s[36:37]\n"
"Lfused_yD1:\n"
"s_sub_u32 s36, s36, Lfused_yD1-Lfused_rD1\n"
"s_subb_u32 s37, s37, 0\n"
"s_branch Lfused_fb0q\n"
"Lfused_xD2:\n"
"s_mov_b32 s15, 0\n"
"s_getpc_b64 s[36:37]\n"
"Lfused_yD2:\n"
"s_sub_u32 s36, s36, Lfused_yD2-Lfused_rD2\n"
"s_subb_u32 s37, s37, 0\n"
"s_branch Lfused_fb0n\n"
"Lfused_xD3:\n"
"s_mov_b32 s15, 0\n"
"s_getpc_b64 s[36:37]\n"
"Lfused_yD3:\n"
"s_sub_u32 s36, s36, Lfused_yD3-Lfused_rD3\n"
"s_subb_u32 s37, s37, 0\n"
"s_branch Lfused_fb1q\n"
"Lfused_xE0:\n"
"s_sub_u32 s15, s14, 1\n"
"s_getpc_b64 s[36:37]\n"
"Lfused_yE0:\n"
"s_sub_u32 s36, s36, Lfused_yE0-Lfused_rE0\n"
"s_subb_u32 s37, s37, 0\n"
"s_branch Lfused_fb1n\n"
"Lfused_xE1:\n"
"s_sub_u32 s15, s14, 1\n"
"s_getpc_b64 s[36:37]\n"
"Lfused_yE1:\n"
"s_sub_u32 s36, s36, Lfused_yE1-Lfused_rE1\n"
"s_subb_u32 s37, s37, 0\n"
"s_branch Lfused_fb0q\n"
"Lfused_xE2:\n"
"s_sub_u32 s15, s14, 1\n"
"s_getpc_b64 s[36:37]\n"
"Lfused_yE2:\n"
"s_sub_u32 s36, s36, Lfused_yE2-Lfused_rE2\n"
"s_subb_u32 s37, s37, 0\n"
"s_branch Lfused_fb0n\n"
"Lfused_xE3:\n"
"s_sub_u32 s15, s14, 1\n"
"s_getpc_b64 s[36:37]\n"
"Lfused_yE3:\n"
"s_sub_u32 s36, s36, Lfused_yE3-Lfused_rE3\n"
"s_subb_u32 s37, s37, 0\n"
"s_branch Lfused_fb1q\n"
"Lfused_xF0:\n"
"s_sub_u32 s15, s14, 1\n"
"s_getpc_b64 s[36:37]\n"
"Lfused_yF0:\n"
"s_sub_u32 s36, s36, Lfused_yF0-Lfused_rF0\n"
"s_subb_u32 s37, s37, 0\n"
"s_branch Lfused_fb1n\n"
"Lfused_xF1:\n"
"s_sub_u32 s15, s14, 1\n"
"s_getpc_b64 s[36:37]\n"
"Lfused_yF1:\n"
"s_sub_u32 s36, s36, Lfused_yF1-Lfused_rF1\n"
"s_subb_u32 s37, s37, 0\n"
"s_branch Lfused_fb0q\n"
"Lfused_xF2:\n"
"s_sub_u32 s15, s14, 1\n"
"s_getpc_b64 s[36:37]\n"
"Lfused_yF2:\n"
"s_sub_u32 s36, s36, Lfused_yF2-Lfused_rF2\n"
"s_subb_u32 s37, s37, 0\n"
"s_branch Lfused_fb0n\n"
"Lfused_xF3:\n"
"s_sub_u32 s15, s14, 1\n"
"s_getpc_b64 s[36:37]\n"
"Lfused_yF3:\n"
"s_sub_u32 s36, s36, Lfused_yF3-Lfused_rF3\n"
"s_subb_u32 s37, s37, 0\n"
"s_branch Lfused_fb1q\n"
"Lfused_xZ:\n"
"s_sub_u32 s15, s14, 1\n"
"s_getpc_b64 s[36:37]\n"
"Lfused_yZ:\n"
"s_sub_u32 s36, s36, Lfused_yZ-Lfused_rZ\n"
"s_subb_u32 s37, s37, 0\n"
"s_branch Lfused_fb1n\n"
"Lfused_fb0q:\n"
"s_cmp_ge_u32 s15, s38\n"
"s_cselect_b32 s86, 0xff800000, 0\n"
"s_cmp_gt_u32 s15, s38\n"
"s_cselect_b32 s87, 0xff800000, 0\n"
"v_add_f32 v224, s87, v192\n"
"v_add_f32 v225, s86, v196\n"
"v_add_f32 v132, s86, v192\n"
"v_add_f32 v133, s86, v192\n"
"v_add_f32 v134, s86, v192\n"
"v_add_f32 v135, s86, v192\n"
"v_cndmask_b32 v128, v224, v132, s[64:65]\n"
"v_cndmask_b32 v129, v224, v132, s[66:67]\n"
"v_cndmask_b32 v130, v224, v132, s[68:69]\n"
"v_cndmask_b32 v131, v224, v132, s[70:71]\n"
"v_add_f32 v136, s87, v196\n"
"v_add_f32 v137, s87, v196\n"
"v_add_f32 v138, s87, v196\n"
"v_add_f32 v139, s87, v196\n"
"v_cndmask_b32 v140, v136, v225, s[64:65]\n"
"v_cndmask_b32 v141, v136, v225, s[66:67]\n"
"v_cndmask_b32 v142, v136, v225, s[68:69]\n"
"v_cndmask_b32 v143, v136, v225, s[70:71]\n"
"v_mfma_f32_16x16x32_bf16 v[128:131], a[128:131], a[0:3], v[128:131]\n"
"v_mfma_f32_16x16x32_bf16 v[128:131], a[136:139], a[4:7], v[128:131]\n"
"v_mfma_f32_16x16x32_bf16 v[128:131], a[144:147], a[8:11], v[128:131]\n"
"v_mfma_f32_16x16x32_bf16 v[128:131], a[152:155], a[12:15], v[128:131]\n"
"v_mfma_f32_16x16x32_bf16 v[132:135], a[132:135], a[0:3], v[132:135]\n"
"v_mfma_f32_16x16x32_bf16 v[132:135], a[140:143], a[4:7], v[132:135]\n"
"v_mfma_f32_16x16x32_bf16 v[132:135], a[148:151], a[8:11], v[132:135]\n"
"v_mfma_f32_16x16x32_bf16 v[132:135], a[156:159], a[12:15], v[132:135]\n"
"v_mfma_f32_16x16x32_bf16 v[136:139], a[128:131], a[16:19], v[136:139]\n"
"v_mfma_f32_16x16x32_bf16 v[136:139], a[136:139], a[20:23], v[136:139]\n"
"v_mfma_f32_16x16x32_bf16 v[136:139], a[144:147], a[24:27], v[136:139]\n"
"v_mfma_f32_16x16x32_bf16 v[136:139], a[152:155], a[28:31], v[136:139]\n"
"v_mfma_f32_16x16x32_bf16 v[140:143], a[132:135], a[16:19], v[140:143]\n"
"v_mfma_f32_16x16x32_bf16 v[140:143], a[140:143], a[20:23], v[140:143]\n"
"v_mfma_f32_16x16x32_bf16 v[140:143], a[148:151], a[24:27], v[140:143]\n"
"v_mfma_f32_16x16x32_bf16 v[140:143], a[156:159], a[28:31], v[140:143]\n"
"v_log_f32 v230, v212\n"
"v_log_f32 v231, v213\n"
"v_max3_f32 v226, v128, v129, v130\n"
"v_max3_f32 v228, v131, v132, v133\n"
"v_max3_f32 v227, v136, v137, v138\n"
"s_nop 2\n"
"v_max3_f32 v229, v139, v140, v141\n"
"v_max3_f32 v226, v226, v134, v135\n"
"v_max3_f32 v227, v227, v142, v143\n"
"v_max_f32 v226, v226, v228\n"
"v_max_f32 v227, v227, v229\n"
"v_mov_b32 v228, v226\n"
"v_mov_b32 v229, v227\n"
"s_nop 0\n"
"v_permlane16_swap_b32 v226, v228\n"
"v_permlane16_swap_b32 v227, v229\n"
"v_max_f32 v226, v226, v228\n"
"v_max_f32 v227, v227, v229\n"
"v_mov_b32 v228, v226\n"
"v_mov_b32 v229, v227\n"
"s_nop 0\n"
"v_permlane32_swap_b32 v226, v228\n"
"v_permlane32_swap_b32 v227, v229\n"
"v_max_f32 v226, v226, v228\n"
"v_max_f32 v227, v227, v229\n"
"v_add_f32 v230, 0xc1300000, v230\n"
"v_max3_f32 v226, v226, v230, 0\n"
"v_add_f32 v231, 0xc1300000, v231\n"
"v_max3_f32 v227, v227, v231, 0\n"
"v_exp_f32 v230, -v226\n"
"v_exp_f32 v231, -v227\n"
"v_sub_f32 v128, v128, v226\n"
"v_sub_f32 v129, v129, v226\n"
"v_sub_f32 v130, v130, v226\n"
"v_sub_f32 v131, v131, v226\n"
"v_sub_f32 v132, v132, v226\n"
"v_sub_f32 v133, v133, v226\n"
"v_sub_f32 v134, v134, v226\n"
"v_sub_f32 v135, v135, v226\n"
"v_sub_f32 v136, v136, v227\n"
"v_sub_f32 v137, v137, v227\n"
"v_sub_f32 v138, v138, v227\n"
"v_sub_f32 v139, v139, v227\n"
"v_sub_f32 v140, v140, v227\n"
"v_sub_f32 v141, v141, v227\n"
"v_sub_f32 v142, v142, v227\n"
"v_sub_f32 v143, v143, v227\n"
"v_sub_f32 v192, v192, v226\n"
"v_sub_f32 v193, v193, v226\n"
"v_sub_f32 v194, v194, v226\n"
"v_sub_f32 v195, v195, v226\n"
"v_sub_f32 v196, v196, v227\n"
"v_sub_f32 v197, v197, v227\n"
"v_sub_f32 v198, v198, v227\n"
"v_sub_f32 v199, v199, v227\n"
"v_mul_f32 v212, v212, v230\n"
"v_mul_f32 v213, v213, v231\n"
"v_add_f32 v214, v212, v213\n"
"v_mul_f32 v0, v0, v230\n"
"v_mul_f32 v1, v1, v230\n"
"v_mul_f32 v2, v2, v230\n"
"v_mul_f32 v3, v3, v230\n"
"v_mul_f32 v4, v4, v230\n"
"v_mul_f32 v5, v5, v230\n"
"v_mul_f32 v6, v6, v230\n"
"v_mul_f32 v7, v7, v230\n"
"v_mul_f32 v8, v8, v230\n"
"v_mul_f32 v9, v9, v230\n"
"v_mul_f32 v10, v10, v230\n"
"v_mul_f32 v11, v11, v230\n"
"v_mul_f32 v12, v12, v230\n"
"v_mul_f32 v13, v13, v230\n"
"v_mul_f32 v14, v14, v230\n"
"v_mul_f32 v15, v15, v230\n"
"v_mul_f32 v16, v16, v230\n"
"v_mul_f32 v17, v17, v230\n"
"v_mul_f32 v18, v18, v230\n"
"v_mul_f32 v19, v19, v230\n"
"v_mul_f32 v20, v20, v230\n"
"v_mul_f32 v21, v21, v230\n"
"v_mul_f32 v22, v22, v230\n"
"v_mul_f32 v23, v23, v230\n"
"v_mul_f32 v24, v24, v230\n"
"v_mul_f32 v25, v25, v230\n"
"v_mul_f32 v26, v26, v230\n"
"v_mul_f32 v27, v27, v230\n"
"v_mul_f32 v28, v28, v230\n"
"v_mul_f32 v29, v29, v230\n"
"v_mul_f32 v30, v30, v230\n"
"v_mul_f32 v31, v31, v230\n"
"v_mul_f32 v32, v32, v231\n"
"v_mul_f32 v33, v33, v231\n"
"v_mul_f32 v34, v34, v231\n"
"v_mul_f32 v35, v35, v231\n"
"v_mul_f32 v36, v36, v231\n"
"v_mul_f32 v37, v37, v231\n"
"v_mul_f32 v38, v38, v231\n"
"v_mul_f32 v39, v39, v231\n"
"v_mul_f32 v40, v40, v231\n"
"v_mul_f32 v41, v41, v231\n"
"v_mul_f32 v42, v42, v231\n"
"v_mul_f32 v43, v43, v231\n"
"v_mul_f32 v44, v44, v231\n"
"v_mul_f32 v45, v45, v231\n"
"v_mul_f32 v46, v46, v231\n"
"v_mul_f32 v47, v47, v231\n"
"v_mul_f32 v48, v48, v231\n"
"v_mul_f32 v49, v49, v231\n"
"v_mul_f32 v50, v50, v231\n"
"v_mul_f32 v51, v51, v231\n"
"v_mul_f32 v52, v52, v231\n"
"v_mul_f32 v53, v53, v231\n"
"v_mul_f32 v54, v54, v231\n"
"v_mul_f32 v55, v55, v231\n"
"v_mul_f32 v56, v56, v231\n"
"v_mul_f32 v57, v57, v231\n"
"v_mul_f32 v58, v58, v231\n"
"v_mul_f32 v59, v59, v231\n"
"v_mul_f32 v60, v60, v231\n"
"v_mul_f32 v61, v61, v231\n"
"v_mul_f32 v62, v62, v231\n"
"v_mul_f32 v63, v63, v231\n"
"v_exp_f32 v128, v128\n"
"v_exp_f32 v129, v129\n"
"v_exp_f32 v130, v130\n"
"v_exp_f32 v131, v131\n"
"v_exp_f32 v132, v132\n"
"v_exp_f32 v133, v133\n"
"v_exp_f32 v134, v134\n"
"v_exp_f32 v135, v135\n"
"v_exp_f32 v136, v136\n"
"v_exp_f32 v137, v137\n"
"v_exp_f32 v138, v138\n"
"v_exp_f32 v139, v139\n"
"v_exp_f32 v140, v140\n"
"v_exp_f32 v141, v141\n"
"v_exp_f32 v142, v142\n"
"v_exp_f32 v143, v143\n"
"v_cvt_pk_bf16_f32 v128, v128, v129\n"
"v_cvt_pk_bf16_f32 v129, v130, v131\n"
"v_cvt_pk_bf16_f32 v130, v132, v133\n"
"v_cvt_pk_bf16_f32 v131, v134, v135\n"
"v_cvt_pk_bf16_f32 v132, v136, v137\n"
"v_cvt_pk_bf16_f32 v133, v138, v139\n"
"v_cvt_pk_bf16_f32 v134, v140, v141\n"
"v_cvt_pk_bf16_f32 v135, v142, v143\n"
"v_mfma_f32_16x16x32_bf16 v[208:211], a[232:235], v[128:131], v[212:215]\n"
"s_nop 0\n"
"v_mfma_f32_16x16x32_bf16 v[208:211], a[236:239], v[132:135], v[208:211]\n"
"s_nop 7\n"
"s_setpc_b64 s[36:37]\n"
"Lfused_fb0o:\n"
"s_cmp_ge_u32 s15, s39\n"
"s_cselect_b32 s86, 0xff800000, 0\n"
"s_cmp_gt_u32 s15, s39\n"
"s_cselect_b32 s87, 0xff800000, 0\n"
"v_add_f32 v224, s87, v200\n"
"v_add_f32 v225, s86, v204\n"
"v_add_f32 v148, s86, v200\n"
"v_add_f32 v149, s86, v200\n"
"v_add_f32 v150, s86, v200\n"
"v_add_f32 v151, s86, v200\n"
"v_cndmask_b32 v144, v224, v148, s[64:65]\n"
"v_cndmask_b32 v145, v224, v148, s[66:67]\n"
"v_cndmask_b32 v146, v224, v148, s[68:69]\n"
"v_cndmask_b32 v147, v224, v148, s[70:71]\n"
"v_add_f32 v152, s87, v204\n"
"v_add_f32 v153, s87, v204\n"
"v_add_f32 v154, s87, v204\n"
"v_add_f32 v155, s87, v204\n"
"v_cndmask_b32 v156, v152, v225, s[64:65]\n"
"v_cndmask_b32 v157, v152, v225, s[66:67]\n"
"v_cndmask_b32 v158, v152, v225, s[68:69]\n"
"v_cndmask_b32 v159, v152, v225, s[70:71]\n"
"v_mfma_f32_16x16x32_bf16 v[144:147], a[128:131], a[32:35], v[144:147]\n"
"v_mfma_f32_16x16x32_bf16 v[144:147], a[136:139], a[36:39], v[144:147]\n"
"v_mfma_f32_16x16x32_bf16 v[144:147], a[144:147], a[40:43], v[144:147]\n"
"v_mfma_f32_16x16x32_bf16 v[144:147], a[152:155], a[44:47], v[144:147]\n"
"v_mfma_f32_16x16x32_bf16 v[148:151], a[132:135], a[32:35], v[148:151]\n"
"v_mfma_f32_16x16x32_bf16 v[148:151], a[140:143], a[36:39], v[148:151]\n"
"v_mfma_f32_16x16x32_bf16 v[148:151], a[148:151], a[40:43], v[148:151]\n"
"v_mfma_f32_16x16x32_bf16 v[148:151], a[156:159], a[44:47], v[148:151]\n"
"v_mfma_f32_16x16x32_bf16 v[152:155], a[128:131], a[48:51], v[152:155]\n"
"v_mfma_f32_16x16x32_bf16 v[152:155], a[136:139], a[52:55], v[152:155]\n"
"v_mfma_f32_16x16x32_bf16 v[152:155], a[144:147], a[56:59], v[152:155]\n"
"v_mfma_f32_16x16x32_bf16 v[152:155], a[152:155], a[60:63], v[152:155]\n"
"v_mfma_f32_16x16x32_bf16 v[156:159], a[132:135], a[48:51], v[156:159]\n"
"v_mfma_f32_16x16x32_bf16 v[156:159], a[140:143], a[52:55], v[156:159]\n"
"v_mfma_f32_16x16x32_bf16 v[156:159], a[148:151], a[56:59], v[156:159]\n"
"v_mfma_f32_16x16x32_bf16 v[156:159], a[156:159], a[60:63], v[156:159]\n"
"v_log_f32 v230, v220\n"
"v_log_f32 v231, v221\n"
"v_max3_f32 v226, v144, v145, v146\n"
"v_max3_f32 v228, v147, v148, v149\n"
"v_max3_f32 v227, v152, v153, v154\n"
"s_nop 2\n"
"v_max3_f32 v229, v155, v156, v157\n"
"v_max3_f32 v226, v226, v150, v151\n"
"v_max3_f32 v227, v227, v158, v159\n"
"v_max_f32 v226, v226, v228\n"
"v_max_f32 v227, v227, v229\n"
"v_mov_b32 v228, v226\n"
"v_mov_b32 v229, v227\n"
"s_nop 0\n"
"v_permlane16_swap_b32 v226, v228\n"
"v_permlane16_swap_b32 v227, v229\n"
"v_max_f32 v226, v226, v228\n"
"v_max_f32 v227, v227, v229\n"
"v_mov_b32 v228, v226\n"
"v_mov_b32 v229, v227\n"
"s_nop 0\n"
"v_permlane32_swap_b32 v226, v228\n"
"v_permlane32_swap_b32 v227, v229\n"
"v_max_f32 v226, v226, v228\n"
"v_max_f32 v227, v227, v229\n"
"v_add_f32 v230, 0xc1300000, v230\n"
"v_max3_f32 v226, v226, v230, 0\n"
"v_add_f32 v231, 0xc1300000, v231\n"
"v_max3_f32 v227, v227, v231, 0\n"
"v_exp_f32 v230, -v226\n"
"v_exp_f32 v231, -v227\n"
"v_sub_f32 v144, v144, v226\n"
"v_sub_f32 v145, v145, v226\n"
"v_sub_f32 v146, v146, v226\n"
"v_sub_f32 v147, v147, v226\n"
"v_sub_f32 v148, v148, v226\n"
"v_sub_f32 v149, v149, v226\n"
"v_sub_f32 v150, v150, v226\n"
"v_sub_f32 v151, v151, v226\n"
"v_sub_f32 v152, v152, v227\n"
"v_sub_f32 v153, v153, v227\n"
"v_sub_f32 v154, v154, v227\n"
"v_sub_f32 v155, v155, v227\n"
"v_sub_f32 v156, v156, v227\n"
"v_sub_f32 v157, v157, v227\n"
"v_sub_f32 v158, v158, v227\n"
"v_sub_f32 v159, v159, v227\n"
"v_sub_f32 v200, v200, v226\n"
"v_sub_f32 v201, v201, v226\n"
"v_sub_f32 v202, v202, v226\n"
"v_sub_f32 v203, v203, v226\n"
"v_sub_f32 v204, v204, v227\n"
"v_sub_f32 v205, v205, v227\n"
"v_sub_f32 v206, v206, v227\n"
"v_sub_f32 v207, v207, v227\n"
"v_mul_f32 v220, v220, v230\n"
"v_mul_f32 v221, v221, v231\n"
"v_add_f32 v222, v220, v221\n"
"v_mul_f32 v64, v64, v230\n"
"v_mul_f32 v65, v65, v230\n"
"v_mul_f32 v66, v66, v230\n"
"v_mul_f32 v67, v67, v230\n"
"v_mul_f32 v68, v68, v230\n"
"v_mul_f32 v69, v69, v230\n"
"v_mul_f32 v70, v70, v230\n"
"v_mul_f32 v71, v71, v230\n"
"v_mul_f32 v72, v72, v230\n"
"v_mul_f32 v73, v73, v230\n"
"v_mul_f32 v74, v74, v230\n"
"v_mul_f32 v75, v75, v230\n"
"v_mul_f32 v76, v76, v230\n"
"v_mul_f32 v77, v77, v230\n"
"v_mul_f32 v78, v78, v230\n"
"v_mul_f32 v79, v79, v230\n"
"v_mul_f32 v80, v80, v230\n"
"v_mul_f32 v81, v81, v230\n"
"v_mul_f32 v82, v82, v230\n"
"v_mul_f32 v83, v83, v230\n"
"v_mul_f32 v84, v84, v230\n"
"v_mul_f32 v85, v85, v230\n"
"v_mul_f32 v86, v86, v230\n"
"v_mul_f32 v87, v87, v230\n"
"v_mul_f32 v88, v88, v230\n"
"v_mul_f32 v89, v89, v230\n"
"v_mul_f32 v90, v90, v230\n"
"v_mul_f32 v91, v91, v230\n"
"v_mul_f32 v92, v92, v230\n"
"v_mul_f32 v93, v93, v230\n"
"v_mul_f32 v94, v94, v230\n"
"v_mul_f32 v95, v95, v230\n"
"v_mul_f32 v96, v96, v231\n"
"v_mul_f32 v97, v97, v231\n"
"v_mul_f32 v98, v98, v231\n"
"v_mul_f32 v99, v99, v231\n"
"v_mul_f32 v100, v100, v231\n"
"v_mul_f32 v101, v101, v231\n"
"v_mul_f32 v102, v102, v231\n"
"v_mul_f32 v103, v103, v231\n"
"v_mul_f32 v104, v104, v231\n"
"v_mul_f32 v105, v105, v231\n"
"v_mul_f32 v106, v106, v231\n"
"v_mul_f32 v107, v107, v231\n"
"v_mul_f32 v108, v108, v231\n"
"v_mul_f32 v109, v109, v231\n"
"v_mul_f32 v110, v110, v231\n"
"v_mul_f32 v111, v111, v231\n"
"v_mul_f32 v112, v112, v231\n"
"v_mul_f32 v113, v113, v231\n"
"v_mul_f32 v114, v114, v231\n"
"v_mul_f32 v115, v115, v231\n"
"v_mul_f32 v116, v116, v231\n"
"v_mul_f32 v117, v117, v231\n"
"v_mul_f32 v118, v118, v231\n"
"v_mul_f32 v119, v119, v231\n"
"v_mul_f32 v120, v120, v231\n"
"v_mul_f32 v121, v121, v231\n"
"v_mul_f32 v122, v122, v231\n"
"v_mul_f32 v123, v123, v231\n"
"v_mul_f32 v124, v124, v231\n"
"v_mul_f32 v125, v125, v231\n"
"v_mul_f32 v126, v126, v231\n"
"v_mul_f32 v127, v127, v231\n"
"v_exp_f32 v144, v144\n"
"v_exp_f32 v145, v145\n"
"v_exp_f32 v146, v146\n"
"v_exp_f32 v147, v147\n"
"v_exp_f32 v148, v148\n"
"v_exp_f32 v149, v149\n"
"v_exp_f32 v150, v150\n"
"v_exp_f32 v151, v151\n"
"v_exp_f32 v152, v152\n"
"v_exp_f32 v153, v153\n"
"v_exp_f32 v154, v154\n"
"v_exp_f32 v155, v155\n"
"v_exp_f32 v156, v156\n"
"v_exp_f32 v157, v157\n"
"v_exp_f32 v158, v158\n"
"v_exp_f32 v159, v159\n"
"v_cvt_pk_bf16_f32 v144, v144, v145\n"
"v_cvt_pk_bf16_f32 v145, v146, v147\n"
"v_cvt_pk_bf16_f32 v146, v148, v149\n"
"v_cvt_pk_bf16_f32 v147, v150, v151\n"
"v_cvt_pk_bf16_f32 v148, v152, v153\n"
"v_cvt_pk_bf16_f32 v149, v154, v155\n"
"v_cvt_pk_bf16_f32 v150, v156, v157\n"
"v_cvt_pk_bf16_f32 v151, v158, v159\n"
"v_mfma_f32_16x16x32_bf16 v[216:219], a[232:235], v[144:147], v[220:223]\n"
"s_nop 0\n"
"v_mfma_f32_16x16x32_bf16 v[216:219], a[236:239], v[148:151], v[216:219]\n"
"s_nop 7\n"
"s_setpc_b64 s[36:37]\n"
"Lfused_fb0n:\n"
"s_cmp_ge_u32 s15, s40\n"
"s_cselect_b32 s86, 0xff800000, 0\n"
"s_cmp_gt_u32 s15, s40\n"
"s_cselect_b32 s87, 0xff800000, 0\n"
"v_add_f32 v224, s87, v200\n"
"v_add_f32 v225, s86, v204\n"
"v_add_f32 v148, s86, v200\n"
"v_add_f32 v149, s86, v200\n"
"v_add_f32 v150, s86, v200\n"
"v_add_f32 v151, s86, v200\n"
"v_cndmask_b32 v144, v224, v148, s[64:65]\n"
"v_cndmask_b32 v145, v224, v148, s[66:67]\n"
"v_cndmask_b32 v146, v224, v148, s[68:69]\n"
"v_cndmask_b32 v147, v224, v148, s[70:71]\n"
"v_add_f32 v152, s87, v204\n"
"v_add_f32 v153, s87, v204\n"
"v_add_f32 v154, s87, v204\n"
"v_add_f32 v155, s87, v204\n"
"v_cndmask_b32 v156, v152, v225, s[64:65]\n"
"v_cndmask_b32 v157, v152, v225, s[66:67]\n"
"v_cndmask_b32 v158, v152, v225, s[68:69]\n"
"v_cndmask_b32 v159, v152, v225, s[70:71]\n"
"v_mfma_f32_16x16x32_bf16 v[144:147], a[128:131], a[64:67], v[144:147]\n"
"v_mfma_f32_16x16x32_bf16 v[144:147], a[136:139], a[68:71], v[144:147]\n"
"v_mfma_f32_16x16x32_bf16 v[144:147], a[144:147], a[72:75], v[144:147]\n"
"v_mfma_f32_16x16x32_bf16 v[144:147], a[152:155], a[76:79], v[144:147]\n"
"v_mfma_f32_16x16x32_bf16 v[148:151], a[132:135], a[64:67], v[148:151]\n"
"v_mfma_f32_16x16x32_bf16 v[148:151], a[140:143], a[68:71], v[148:151]\n"
"v_mfma_f32_16x16x32_bf16 v[148:151], a[148:151], a[72:75], v[148:151]\n"
"v_mfma_f32_16x16x32_bf16 v[148:151], a[156:159], a[76:79], v[148:151]\n"
"v_mfma_f32_16x16x32_bf16 v[152:155], a[128:131], a[80:83], v[152:155]\n"
"v_mfma_f32_16x16x32_bf16 v[152:155], a[136:139], a[84:87], v[152:155]\n"
"v_mfma_f32_16x16x32_bf16 v[152:155], a[144:147], a[88:91], v[152:155]\n"
"v_mfma_f32_16x16x32_bf16 v[152:155], a[152:155], a[92:95], v[152:155]\n"
"v_mfma_f32_16x16x32_bf16 v[156:159], a[132:135], a[80:83], v[156:159]\n"
"v_mfma_f32_16x16x32_bf16 v[156:159], a[140:143], a[84:87], v[156:159]\n"
"v_mfma_f32_16x16x32_bf16 v[156:159], a[148:151], a[88:91], v[156:159]\n"
"v_mfma_f32_16x16x32_bf16 v[156:159], a[156:159], a[92:95], v[156:159]\n"
"v_log_f32 v230, v220\n"
"v_log_f32 v231, v221\n"
"v_max3_f32 v226, v144, v145, v146\n"
"v_max3_f32 v228, v147, v148, v149\n"
"v_max3_f32 v227, v152, v153, v154\n"
"s_nop 2\n"
"v_max3_f32 v229, v155, v156, v157\n"
"v_max3_f32 v226, v226, v150, v151\n"
"v_max3_f32 v227, v227, v158, v159\n"
"v_max_f32 v226, v226, v228\n"
"v_max_f32 v227, v227, v229\n"
"v_mov_b32 v228, v226\n"
"v_mov_b32 v229, v227\n"
"s_nop 0\n"
"v_permlane16_swap_b32 v226, v228\n"
"v_permlane16_swap_b32 v227, v229\n"
"v_max_f32 v226, v226, v228\n"
"v_max_f32 v227, v227, v229\n"
"v_mov_b32 v228, v226\n"
"v_mov_b32 v229, v227\n"
"s_nop 0\n"
"v_permlane32_swap_b32 v226, v228\n"
"v_permlane32_swap_b32 v227, v229\n"
"v_max_f32 v226, v226, v228\n"
"v_max_f32 v227, v227, v229\n"
"v_add_f32 v230, 0xc1300000, v230\n"
"v_max3_f32 v226, v226, v230, 0\n"
"v_add_f32 v231, 0xc1300000, v231\n"
"v_max3_f32 v227, v227, v231, 0\n"
"v_exp_f32 v230, -v226\n"
"v_exp_f32 v231, -v227\n"
"v_sub_f32 v144, v144, v226\n"
"v_sub_f32 v145, v145, v226\n"
"v_sub_f32 v146, v146, v226\n"
"v_sub_f32 v147, v147, v226\n"
"v_sub_f32 v148, v148, v226\n"
"v_sub_f32 v149, v149, v226\n"
"v_sub_f32 v150, v150, v226\n"
"v_sub_f32 v151, v151, v226\n"
"v_sub_f32 v152, v152, v227\n"
"v_sub_f32 v153, v153, v227\n"
"v_sub_f32 v154, v154, v227\n"
"v_sub_f32 v155, v155, v227\n"
"v_sub_f32 v156, v156, v227\n"
"v_sub_f32 v157, v157, v227\n"
"v_sub_f32 v158, v158, v227\n"
"v_sub_f32 v159, v159, v227\n"
"v_sub_f32 v200, v200, v226\n"
"v_sub_f32 v201, v201, v226\n"
"v_sub_f32 v202, v202, v226\n"
"v_sub_f32 v203, v203, v226\n"
"v_sub_f32 v204, v204, v227\n"
"v_sub_f32 v205, v205, v227\n"
"v_sub_f32 v206, v206, v227\n"
"v_sub_f32 v207, v207, v227\n"
"v_mul_f32 v220, v220, v230\n"
"v_mul_f32 v221, v221, v231\n"
"v_add_f32 v222, v220, v221\n"
"v_mul_f32 v64, v64, v230\n"
"v_mul_f32 v65, v65, v230\n"
"v_mul_f32 v66, v66, v230\n"
"v_mul_f32 v67, v67, v230\n"
"v_mul_f32 v68, v68, v230\n"
"v_mul_f32 v69, v69, v230\n"
"v_mul_f32 v70, v70, v230\n"
"v_mul_f32 v71, v71, v230\n"
"v_mul_f32 v72, v72, v230\n"
"v_mul_f32 v73, v73, v230\n"
"v_mul_f32 v74, v74, v230\n"
"v_mul_f32 v75, v75, v230\n"
"v_mul_f32 v76, v76, v230\n"
"v_mul_f32 v77, v77, v230\n"
"v_mul_f32 v78, v78, v230\n"
"v_mul_f32 v79, v79, v230\n"
"v_mul_f32 v80, v80, v230\n"
"v_mul_f32 v81, v81, v230\n"
"v_mul_f32 v82, v82, v230\n"
"v_mul_f32 v83, v83, v230\n"
"v_mul_f32 v84, v84, v230\n"
"v_mul_f32 v85, v85, v230\n"
"v_mul_f32 v86, v86, v230\n"
"v_mul_f32 v87, v87, v230\n"
"v_mul_f32 v88, v88, v230\n"
"v_mul_f32 v89, v89, v230\n"
"v_mul_f32 v90, v90, v230\n"
"v_mul_f32 v91, v91, v230\n"
"v_mul_f32 v92, v92, v230\n"
"v_mul_f32 v93, v93, v230\n"
"v_mul_f32 v94, v94, v230\n"
"v_mul_f32 v95, v95, v230\n"
"v_mul_f32 v96, v96, v231\n"
"v_mul_f32 v97, v97, v231\n"
"v_mul_f32 v98, v98, v231\n"
"v_mul_f32 v99, v99, v231\n"
"v_mul_f32 v100, v100, v231\n"
"v_mul_f32 v101, v101, v231\n"
"v_mul_f32 v102, v102, v231\n"
"v_mul_f32 v103, v103, v231\n"
"v_mul_f32 v104, v104, v231\n"
"v_mul_f32 v105, v105, v231\n"
"v_mul_f32 v106, v106, v231\n"
"v_mul_f32 v107, v107, v231\n"
"v_mul_f32 v108, v108, v231\n"
"v_mul_f32 v109, v109, v231\n"
"v_mul_f32 v110, v110, v231\n"
"v_mul_f32 v111, v111, v231\n"
"v_mul_f32 v112, v112, v231\n"
"v_mul_f32 v113, v113, v231\n"
"v_mul_f32 v114, v114, v231\n"
"v_mul_f32 v115, v115, v231\n"
"v_mul_f32 v116, v116, v231\n"
"v_mul_f32 v117, v117, v231\n"
"v_mul_f32 v118, v118, v231\n"
"v_mul_f32 v119, v119, v231\n"
"v_mul_f32 v120, v120, v231\n"
"v_mul_f32 v121, v121, v231\n"
"v_mul_f32 v122, v122, v231\n"
"v_mul_f32 v123, v123, v231\n"
"v_mul_f32 v124, v124, v231\n"
"v_mul_f32 v125, v125, v231\n"
"v_mul_f32 v126, v126, v231\n"
"v_mul_f32 v127, v127, v231\n"
"v_exp_f32 v144, v144\n"
"v_exp_f32 v145, v145\n"
"v_exp_f32 v146, v146\n"
"v_exp_f32 v147, v147\n"
"v_exp_f32 v148, v148\n"
"v_exp_f32 v149, v149\n"
"v_exp_f32 v150, v150\n"
"v_exp_f32 v151, v151\n"
"v_exp_f32 v152, v152\n"
"v_exp_f32 v153, v153\n"
"v_exp_f32 v154, v154\n"
"v_exp_f32 v155, v155\n"
"v_exp_f32 v156, v156\n"
"v_exp_f32 v157, v157\n"
"v_exp_f32 v158, v158\n"
"v_exp_f32 v159, v159\n"
"v_cvt_pk_bf16_f32 v144, v144, v145\n"
"v_cvt_pk_bf16_f32 v145, v146, v147\n"
"v_cvt_pk_bf16_f32 v146, v148, v149\n"
"v_cvt_pk_bf16_f32 v147, v150, v151\n"
"v_cvt_pk_bf16_f32 v148, v152, v153\n"
"v_cvt_pk_bf16_f32 v149, v154, v155\n"
"v_cvt_pk_bf16_f32 v150, v156, v157\n"
"v_cvt_pk_bf16_f32 v151, v158, v159\n"
"v_mfma_f32_16x16x32_bf16 v[216:219], a[232:235], v[144:147], v[220:223]\n"
"s_nop 0\n"
"v_mfma_f32_16x16x32_bf16 v[216:219], a[236:239], v[148:151], v[216:219]\n"
"s_nop 7\n"
"s_setpc_b64 s[36:37]\n"
"Lfused_fb1q:\n"
"s_cmp_ge_u32 s15, s38\n"
"s_cselect_b32 s86, 0xff800000, 0\n"
"s_cmp_gt_u32 s15, s38\n"
"s_cselect_b32 s87, 0xff800000, 0\n"
"v_add_f32 v224, s87, v192\n"
"v_add_f32 v225, s86, v196\n"
"v_add_f32 v164, s86, v192\n"
"v_add_f32 v165, s86, v192\n"
"v_add_f32 v166, s86, v192\n"
"v_add_f32 v167, s86, v192\n"
"v_cndmask_b32 v160, v224, v164, s[64:65]\n"
"v_cndmask_b32 v161, v224, v164, s[66:67]\n"
"v_cndmask_b32 v162, v224, v164, s[68:69]\n"
"v_cndmask_b32 v163, v224, v164, s[70:71]\n"
"v_add_f32 v168, s87, v196\n"
"v_add_f32 v169, s87, v196\n"
"v_add_f32 v170, s87, v196\n"
"v_add_f32 v171, s87, v196\n"
"v_cndmask_b32 v172, v168, v225, s[64:65]\n"
"v_cndmask_b32 v173, v168, v225, s[66:67]\n"
"v_cndmask_b32 v174, v168, v225, s[68:69]\n"
"v_cndmask_b32 v175, v168, v225, s[70:71]\n"
"v_mfma_f32_16x16x32_bf16 v[160:163], a[160:163], a[0:3], v[160:163]\n"
"v_mfma_f32_16x16x32_bf16 v[160:163], a[168:171], a[4:7], v[160:163]\n"
"v_mfma_f32_16x16x32_bf16 v[160:163], a[176:179], a[8:11], v[160:163]\n"
"v_mfma_f32_16x16x32_bf16 v[160:163], a[184:187], a[12:15], v[160:163]\n"
"v_mfma_f32_16x16x32_bf16 v[164:167], a[164:167], a[0:3], v[164:167]\n"
"v_mfma_f32_16x16x32_bf16 v[164:167], a[172:175], a[4:7], v[164:167]\n"
"v_mfma_f32_16x16x32_bf16 v[164:167], a[180:183], a[8:11], v[164:167]\n"
"v_mfma_f32_16x16x32_bf16 v[164:167], a[188:191], a[12:15], v[164:167]\n"
"v_mfma_f32_16x16x32_bf16 v[168:171], a[160:163], a[16:19], v[168:171]\n"
"v_mfma_f32_16x16x32_bf16 v[168:171], a[168:171], a[20:23], v[168:171]\n"
"v_mfma_f32_16x16x32_bf16 v[168:171], a[176:179], a[24:27], v[168:171]\n"
"v_mfma_f32_16x16x32_bf16 v[168:171], a[184:187], a[28:31], v[168:171]\n"
"v_mfma_f32_16x16x32_bf16 v[172:175], a[164:167], a[16:19], v[172:175]\n"
"v_mfma_f32_16x16x32_bf16 v[172:175], a[172:175], a[20:23], v[172:175]\n"
"v_mfma_f32_16x16x32_bf16 v[172:175], a[180:183], a[24:27], v[172:175]\n"
"v_mfma_f32_16x16x32_bf16 v[172:175], a[188:191], a[28:31], v[172:175]\n"
"v_log_f32 v230, v208\n"
"v_log_f32 v231, v209\n"
"v_max3_f32 v226, v160, v161, v162\n"
"v_max3_f32 v228, v163, v164, v165\n"
"v_max3_f32 v227, v168, v169, v170\n"
"s_nop 2\n"
"v_max3_f32 v229, v171, v172, v173\n"
"v_max3_f32 v226, v226, v166, v167\n"
"v_max3_f32 v227, v227, v174, v175\n"
"v_max_f32 v226, v226, v228\n"
"v_max_f32 v227, v227, v229\n"
"v_mov_b32 v228, v226\n"
"v_mov_b32 v229, v227\n"
"s_nop 0\n"
"v_permlane16_swap_b32 v226, v228\n"
"v_permlane16_swap_b32 v227, v229\n"
"v_max_f32 v226, v226, v228\n"
"v_max_f32 v227, v227, v229\n"
"v_mov_b32 v228, v226\n"
"v_mov_b32 v229, v227\n"
"s_nop 0\n"
"v_permlane32_swap_b32 v226, v228\n"
"v_permlane32_swap_b32 v227, v229\n"
"v_max_f32 v226, v226, v228\n"
"v_max_f32 v227, v227, v229\n"
"v_add_f32 v230, 0xc1300000, v230\n"
"v_max3_f32 v226, v226, v230, 0\n"
"v_add_f32 v231, 0xc1300000, v231\n"
"v_max3_f32 v227, v227, v231, 0\n"
"v_exp_f32 v230, -v226\n"
"v_exp_f32 v231, -v227\n"
"v_sub_f32 v160, v160, v226\n"
"v_sub_f32 v161, v161, v226\n"
"v_sub_f32 v162, v162, v226\n"
"v_sub_f32 v163, v163, v226\n"
"v_sub_f32 v164, v164, v226\n"
"v_sub_f32 v165, v165, v226\n"
"v_sub_f32 v166, v166, v226\n"
"v_sub_f32 v167, v167, v226\n"
"v_sub_f32 v168, v168, v227\n"
"v_sub_f32 v169, v169, v227\n"
"v_sub_f32 v170, v170, v227\n"
"v_sub_f32 v171, v171, v227\n"
"v_sub_f32 v172, v172, v227\n"
"v_sub_f32 v173, v173, v227\n"
"v_sub_f32 v174, v174, v227\n"
"v_sub_f32 v175, v175, v227\n"
"v_sub_f32 v192, v192, v226\n"
"v_sub_f32 v193, v193, v226\n"
"v_sub_f32 v194, v194, v226\n"
"v_sub_f32 v195, v195, v226\n"
"v_sub_f32 v196, v196, v227\n"
"v_sub_f32 v197, v197, v227\n"
"v_sub_f32 v198, v198, v227\n"
"v_sub_f32 v199, v199, v227\n"
"v_mul_f32 v208, v208, v230\n"
"v_mul_f32 v209, v209, v231\n"
"v_add_f32 v210, v208, v209\n"
"v_mul_f32 v0, v0, v230\n"
"v_mul_f32 v1, v1, v230\n"
"v_mul_f32 v2, v2, v230\n"
"v_mul_f32 v3, v3, v230\n"
"v_mul_f32 v4, v4, v230\n"
"v_mul_f32 v5, v5, v230\n"
"v_mul_f32 v6, v6, v230\n"
"v_mul_f32 v7, v7, v230\n"
"v_mul_f32 v8, v8, v230\n"
"v_mul_f32 v9, v9, v230\n"
"v_mul_f32 v10, v10, v230\n"
"v_mul_f32 v11, v11, v230\n"
"v_mul_f32 v12, v12, v230\n"
"v_mul_f32 v13, v13, v230\n"
"v_mul_f32 v14, v14, v230\n"
"v_mul_f32 v15, v15, v230\n"
"v_mul_f32 v16, v16, v230\n"
"v_mul_f32 v17, v17, v230\n"
"v_mul_f32 v18, v18, v230\n"
"v_mul_f32 v19, v19, v230\n"
"v_mul_f32 v20, v20, v230\n"
"v_mul_f32 v21, v21, v230\n"
"v_mul_f32 v22, v22, v230\n"
"v_mul_f32 v23, v23, v230\n"
"v_mul_f32 v24, v24, v230\n"
"v_mul_f32 v25, v25, v230\n"
"v_mul_f32 v26, v26, v230\n"
"v_mul_f32 v27, v27, v230\n"
"v_mul_f32 v28, v28, v230\n"
"v_mul_f32 v29, v29, v230\n"
"v_mul_f32 v30, v30, v230\n"
"v_mul_f32 v31, v31, v230\n"
"v_mul_f32 v32, v32, v231\n"
"v_mul_f32 v33, v33, v231\n"
"v_mul_f32 v34, v34, v231\n"
"v_mul_f32 v35, v35, v231\n"
"v_mul_f32 v36, v36, v231\n"
"v_mul_f32 v37, v37, v231\n"
"v_mul_f32 v38, v38, v231\n"
"v_mul_f32 v39, v39, v231\n"
"v_mul_f32 v40, v40, v231\n"
"v_mul_f32 v41, v41, v231\n"
"v_mul_f32 v42, v42, v231\n"
"v_mul_f32 v43, v43, v231\n"
"v_mul_f32 v44, v44, v231\n"
"v_mul_f32 v45, v45, v231\n"
"v_mul_f32 v46, v46, v231\n"
"v_mul_f32 v47, v47, v231\n"
"v_mul_f32 v48, v48, v231\n"
"v_mul_f32 v49, v49, v231\n"
"v_mul_f32 v50, v50, v231\n"
"v_mul_f32 v51, v51, v231\n"
"v_mul_f32 v52, v52, v231\n"
"v_mul_f32 v53, v53, v231\n"
"v_mul_f32 v54, v54, v231\n"
"v_mul_f32 v55, v55, v231\n"
"v_mul_f32 v56, v56, v231\n"
"v_mul_f32 v57, v57, v231\n"
"v_mul_f32 v58, v58, v231\n"
"v_mul_f32 v59, v59, v231\n"
"v_mul_f32 v60, v60, v231\n"
"v_mul_f32 v61, v61, v231\n"
"v_mul_f32 v62, v62, v231\n"
"v_mul_f32 v63, v63, v231\n"
"v_exp_f32 v160, v160\n"
"v_exp_f32 v161, v161\n"
"v_exp_f32 v162, v162\n"
"v_exp_f32 v163, v163\n"
"v_exp_f32 v164, v164\n"
"v_exp_f32 v165, v165\n"
"v_exp_f32 v166, v166\n"
"v_exp_f32 v167, v167\n"
"v_exp_f32 v168, v168\n"
"v_exp_f32 v169, v169\n"
"v_exp_f32 v170, v170\n"
"v_exp_f32 v171, v171\n"
"v_exp_f32 v172, v172\n"
"v_exp_f32 v173, v173\n"
"v_exp_f32 v174, v174\n"
"v_exp_f32 v175, v175\n"
"v_cvt_pk_bf16_f32 v160, v160, v161\n"
"v_cvt_pk_bf16_f32 v161, v162, v163\n"
"v_cvt_pk_bf16_f32 v162, v164, v165\n"
"v_cvt_pk_bf16_f32 v163, v166, v167\n"
"v_cvt_pk_bf16_f32 v164, v168, v169\n"
"v_cvt_pk_bf16_f32 v165, v170, v171\n"
"v_cvt_pk_bf16_f32 v166, v172, v173\n"
"v_cvt_pk_bf16_f32 v167, v174, v175\n"
"v_mfma_f32_16x16x32_bf16 v[212:215], a[232:235], v[160:163], v[208:211]\n"
"s_nop 0\n"
"v_mfma_f32_16x16x32_bf16 v[212:215], a[236:239], v[164:167], v[212:215]\n"
"s_nop 7\n"
"s_setpc_b64 s[36:37]\n"
"Lfused_fb1o:\n"
"s_cmp_ge_u32 s15, s39\n"
"s_cselect_b32 s86, 0xff800000, 0\n"
"s_cmp_gt_u32 s15, s39\n"
"s_cselect_b32 s87, 0xff800000, 0\n"
"v_add_f32 v224, s87, v200\n"
"v_add_f32 v225, s86, v204\n"
"v_add_f32 v180, s86, v200\n"
"v_add_f32 v181, s86, v200\n"
"v_add_f32 v182, s86, v200\n"
"v_add_f32 v183, s86, v200\n"
"v_cndmask_b32 v176, v224, v180, s[64:65]\n"
"v_cndmask_b32 v177, v224, v180, s[66:67]\n"
"v_cndmask_b32 v178, v224, v180, s[68:69]\n"
"v_cndmask_b32 v179, v224, v180, s[70:71]\n"
"v_add_f32 v184, s87, v204\n"
"v_add_f32 v185, s87, v204\n"
"v_add_f32 v186, s87, v204\n"
"v_add_f32 v187, s87, v204\n"
"v_cndmask_b32 v188, v184, v225, s[64:65]\n"
"v_cndmask_b32 v189, v184, v225, s[66:67]\n"
"v_cndmask_b32 v190, v184, v225, s[68:69]\n"
"v_cndmask_b32 v191, v184, v225, s[70:71]\n"
"v_mfma_f32_16x16x32_bf16 v[176:179], a[160:163], a[32:35], v[176:179]\n"
"v_mfma_f32_16x16x32_bf16 v[176:179], a[168:171], a[36:39], v[176:179]\n"
"v_mfma_f32_16x16x32_bf16 v[176:179], a[176:179], a[40:43], v[176:179]\n"
"v_mfma_f32_16x16x32_bf16 v[176:179], a[184:187], a[44:47], v[176:179]\n"
"v_mfma_f32_16x16x32_bf16 v[180:183], a[164:167], a[32:35], v[180:183]\n"
"v_mfma_f32_16x16x32_bf16 v[180:183], a[172:175], a[36:39], v[180:183]\n"
"v_mfma_f32_16x16x32_bf16 v[180:183], a[180:183], a[40:43], v[180:183]\n"
"v_mfma_f32_16x16x32_bf16 v[180:183], a[188:191], a[44:47], v[180:183]\n"
"v_mfma_f32_16x16x32_bf16 v[184:187], a[160:163], a[48:51], v[184:187]\n"
"v_mfma_f32_16x16x32_bf16 v[184:187], a[168:171], a[52:55], v[184:187]\n"
"v_mfma_f32_16x16x32_bf16 v[184:187], a[176:179], a[56:59], v[184:187]\n"
"v_mfma_f32_16x16x32_bf16 v[184:187], a[184:187], a[60:63], v[184:187]\n"
"v_mfma_f32_16x16x32_bf16 v[188:191], a[164:167], a[48:51], v[188:191]\n"
"v_mfma_f32_16x16x32_bf16 v[188:191], a[172:175], a[52:55], v[188:191]\n"
"v_mfma_f32_16x16x32_bf16 v[188:191], a[180:183], a[56:59], v[188:191]\n"
"v_mfma_f32_16x16x32_bf16 v[188:191], a[188:191], a[60:63], v[188:191]\n"
"v_log_f32 v230, v216\n"
"v_log_f32 v231, v217\n"
"v_max3_f32 v226, v176, v177, v178\n"
"v_max3_f32 v228, v179, v180, v181\n"
"v_max3_f32 v227, v184, v185, v186\n"
"s_nop 2\n"
"v_max3_f32 v229, v187, v188, v189\n"
"v_max3_f32 v226, v226, v182, v183\n"
"v_max3_f32 v227, v227, v190, v191\n"
"v_max_f32 v226, v226, v228\n"
"v_max_f32 v227, v227, v229\n"
"v_mov_b32 v228, v226\n"
"v_mov_b32 v229, v227\n"
"s_nop 0\n"
"v_permlane16_swap_b32 v226, v228\n"
"v_permlane16_swap_b32 v227, v229\n"
"v_max_f32 v226, v226, v228\n"
"v_max_f32 v227, v227, v229\n"
"v_mov_b32 v228, v226\n"
"v_mov_b32 v229, v227\n"
"s_nop 0\n"
"v_permlane32_swap_b32 v226, v228\n"
"v_permlane32_swap_b32 v227, v229\n"
"v_max_f32 v226, v226, v228\n"
"v_max_f32 v227, v227, v229\n"
"v_add_f32 v230, 0xc1300000, v230\n"
"v_max3_f32 v226, v226, v230, 0\n"
"v_add_f32 v231, 0xc1300000, v231\n"
"v_max3_f32 v227, v227, v231, 0\n"
"v_exp_f32 v230, -v226\n"
"v_exp_f32 v231, -v227\n"
"v_sub_f32 v176, v176, v226\n"
"v_sub_f32 v177, v177, v226\n"
"v_sub_f32 v178, v178, v226\n"
"v_sub_f32 v179, v179, v226\n"
"v_sub_f32 v180, v180, v226\n"
"v_sub_f32 v181, v181, v226\n"
"v_sub_f32 v182, v182, v226\n"
"v_sub_f32 v183, v183, v226\n"
"v_sub_f32 v184, v184, v227\n"
"v_sub_f32 v185, v185, v227\n"
"v_sub_f32 v186, v186, v227\n"
"v_sub_f32 v187, v187, v227\n"
"v_sub_f32 v188, v188, v227\n"
"v_sub_f32 v189, v189, v227\n"
"v_sub_f32 v190, v190, v227\n"
"v_sub_f32 v191, v191, v227\n"
"v_sub_f32 v200, v200, v226\n"
"v_sub_f32 v201, v201, v226\n"
"v_sub_f32 v202, v202, v226\n"
"v_sub_f32 v203, v203, v226\n"
"v_sub_f32 v204, v204, v227\n"
"v_sub_f32 v205, v205, v227\n"
"v_sub_f32 v206, v206, v227\n"
"v_sub_f32 v207, v207, v227\n"
"v_mul_f32 v216, v216, v230\n"
"v_mul_f32 v217, v217, v231\n"
"v_add_f32 v218, v216, v217\n"
"v_mul_f32 v64, v64, v230\n"
"v_mul_f32 v65, v65, v230\n"
"v_mul_f32 v66, v66, v230\n"
"v_mul_f32 v67, v67, v230\n"
"v_mul_f32 v68, v68, v230\n"
"v_mul_f32 v69, v69, v230\n"
"v_mul_f32 v70, v70, v230\n"
"v_mul_f32 v71, v71, v230\n"
"v_mul_f32 v72, v72, v230\n"
"v_mul_f32 v73, v73, v230\n"
"v_mul_f32 v74, v74, v230\n"
"v_mul_f32 v75, v75, v230\n"
"v_mul_f32 v76, v76, v230\n"
"v_mul_f32 v77, v77, v230\n"
"v_mul_f32 v78, v78, v230\n"
"v_mul_f32 v79, v79, v230\n"
"v_mul_f32 v80, v80, v230\n"
"v_mul_f32 v81, v81, v230\n"
"v_mul_f32 v82, v82, v230\n"
"v_mul_f32 v83, v83, v230\n"
"v_mul_f32 v84, v84, v230\n"
"v_mul_f32 v85, v85, v230\n"
"v_mul_f32 v86, v86, v230\n"
"v_mul_f32 v87, v87, v230\n"
"v_mul_f32 v88, v88, v230\n"
"v_mul_f32 v89, v89, v230\n"
"v_mul_f32 v90, v90, v230\n"
"v_mul_f32 v91, v91, v230\n"
"v_mul_f32 v92, v92, v230\n"
"v_mul_f32 v93, v93, v230\n"
"v_mul_f32 v94, v94, v230\n"
"v_mul_f32 v95, v95, v230\n"
"v_mul_f32 v96, v96, v231\n"
"v_mul_f32 v97, v97, v231\n"
"v_mul_f32 v98, v98, v231\n"
"v_mul_f32 v99, v99, v231\n"
"v_mul_f32 v100, v100, v231\n"
"v_mul_f32 v101, v101, v231\n"
"v_mul_f32 v102, v102, v231\n"
"v_mul_f32 v103, v103, v231\n"
"v_mul_f32 v104, v104, v231\n"
"v_mul_f32 v105, v105, v231\n"
"v_mul_f32 v106, v106, v231\n"
"v_mul_f32 v107, v107, v231\n"
"v_mul_f32 v108, v108, v231\n"
"v_mul_f32 v109, v109, v231\n"
"v_mul_f32 v110, v110, v231\n"
"v_mul_f32 v111, v111, v231\n"
"v_mul_f32 v112, v112, v231\n"
"v_mul_f32 v113, v113, v231\n"
"v_mul_f32 v114, v114, v231\n"
"v_mul_f32 v115, v115, v231\n"
"v_mul_f32 v116, v116, v231\n"
"v_mul_f32 v117, v117, v231\n"
"v_mul_f32 v118, v118, v231\n"
"v_mul_f32 v119, v119, v231\n"
"v_mul_f32 v120, v120, v231\n"
"v_mul_f32 v121, v121, v231\n"
"v_mul_f32 v122, v122, v231\n"
"v_mul_f32 v123, v123, v231\n"
"v_mul_f32 v124, v124, v231\n"
"v_mul_f32 v125, v125, v231\n"
"v_mul_f32 v126, v126, v231\n"
"v_mul_f32 v127, v127, v231\n"
"v_exp_f32 v176, v176\n"
"v_exp_f32 v177, v177\n"
"v_exp_f32 v178, v178\n"
"v_exp_f32 v179, v179\n"
"v_exp_f32 v180, v180\n"
"v_exp_f32 v181, v181\n"
"v_exp_f32 v182, v182\n"
"v_exp_f32 v183, v183\n"
"v_exp_f32 v184, v184\n"
"v_exp_f32 v185, v185\n"
"v_exp_f32 v186, v186\n"
"v_exp_f32 v187, v187\n"
"v_exp_f32 v188, v188\n"
"v_exp_f32 v189, v189\n"
"v_exp_f32 v190, v190\n"
"v_exp_f32 v191, v191\n"
"v_cvt_pk_bf16_f32 v176, v176, v177\n"
"v_cvt_pk_bf16_f32 v177, v178, v179\n"
"v_cvt_pk_bf16_f32 v178, v180, v181\n"
"v_cvt_pk_bf16_f32 v179, v182, v183\n"
"v_cvt_pk_bf16_f32 v180, v184, v185\n"
"v_cvt_pk_bf16_f32 v181, v186, v187\n"
"v_cvt_pk_bf16_f32 v182, v188, v189\n"
"v_cvt_pk_bf16_f32 v183, v190, v191\n"
"v_mfma_f32_16x16x32_bf16 v[220:223], a[232:235], v[176:179], v[216:219]\n"
"s_nop 0\n"
"v_mfma_f32_16x16x32_bf16 v[220:223], a[236:239], v[180:183], v[220:223]\n"
"s_nop 7\n"
"s_setpc_b64 s[36:37]\n"
"Lfused_fb1n:\n"
"s_cmp_ge_u32 s15, s40\n"
"s_cselect_b32 s86, 0xff800000, 0\n"
"s_cmp_gt_u32 s15, s40\n"
"s_cselect_b32 s87, 0xff800000, 0\n"
"v_add_f32 v224, s87, v200\n"
"v_add_f32 v225, s86, v204\n"
"v_add_f32 v180, s86, v200\n"
"v_add_f32 v181, s86, v200\n"
"v_add_f32 v182, s86, v200\n"
"v_add_f32 v183, s86, v200\n"
"v_cndmask_b32 v176, v224, v180, s[64:65]\n"
"v_cndmask_b32 v177, v224, v180, s[66:67]\n"
"v_cndmask_b32 v178, v224, v180, s[68:69]\n"
"v_cndmask_b32 v179, v224, v180, s[70:71]\n"
"v_add_f32 v184, s87, v204\n"
"v_add_f32 v185, s87, v204\n"
"v_add_f32 v186, s87, v204\n"
"v_add_f32 v187, s87, v204\n"
"v_cndmask_b32 v188, v184, v225, s[64:65]\n"
"v_cndmask_b32 v189, v184, v225, s[66:67]\n"
"v_cndmask_b32 v190, v184, v225, s[68:69]\n"
"v_cndmask_b32 v191, v184, v225, s[70:71]\n"
"v_mfma_f32_16x16x32_bf16 v[176:179], a[160:163], a[64:67], v[176:179]\n"
"v_mfma_f32_16x16x32_bf16 v[176:179], a[168:171], a[68:71], v[176:179]\n"
"v_mfma_f32_16x16x32_bf16 v[176:179], a[176:179], a[72:75], v[176:179]\n"
"v_mfma_f32_16x16x32_bf16 v[176:179], a[184:187], a[76:79], v[176:179]\n"
"v_mfma_f32_16x16x32_bf16 v[180:183], a[164:167], a[64:67], v[180:183]\n"
"v_mfma_f32_16x16x32_bf16 v[180:183], a[172:175], a[68:71], v[180:183]\n"
"v_mfma_f32_16x16x32_bf16 v[180:183], a[180:183], a[72:75], v[180:183]\n"
"v_mfma_f32_16x16x32_bf16 v[180:183], a[188:191], a[76:79], v[180:183]\n"
"v_mfma_f32_16x16x32_bf16 v[184:187], a[160:163], a[80:83], v[184:187]\n"
"v_mfma_f32_16x16x32_bf16 v[184:187], a[168:171], a[84:87], v[184:187]\n"
"v_mfma_f32_16x16x32_bf16 v[184:187], a[176:179], a[88:91], v[184:187]\n"
"v_mfma_f32_16x16x32_bf16 v[184:187], a[184:187], a[92:95], v[184:187]\n"
"v_mfma_f32_16x16x32_bf16 v[188:191], a[164:167], a[80:83], v[188:191]\n"
"v_mfma_f32_16x16x32_bf16 v[188:191], a[172:175], a[84:87], v[188:191]\n"
"v_mfma_f32_16x16x32_bf16 v[188:191], a[180:183], a[88:91], v[188:191]\n"
"v_mfma_f32_16x16x32_bf16 v[188:191], a[188:191], a[92:95], v[188:191]\n"
"v_log_f32 v230, v216\n"
"v_log_f32 v231, v217\n"
"v_max3_f32 v226, v176, v177, v178\n"
"v_max3_f32 v228, v179, v180, v181\n"
"v_max3_f32 v227, v184, v185, v186\n"
"s_nop 2\n"
"v_max3_f32 v229, v187, v188, v189\n"
"v_max3_f32 v226, v226, v182, v183\n"
"v_max3_f32 v227, v227, v190, v191\n"
"v_max_f32 v226, v226, v228\n"
"v_max_f32 v227, v227, v229\n"
"v_mov_b32 v228, v226\n"
"v_mov_b32 v229, v227\n"
"s_nop 0\n"
"v_permlane16_swap_b32 v226, v228\n"
"v_permlane16_swap_b32 v227, v229\n"
"v_max_f32 v226, v226, v228\n"
"v_max_f32 v227, v227, v229\n"
"v_mov_b32 v228, v226\n"
"v_mov_b32 v229, v227\n"
"s_nop 0\n"
"v_permlane32_swap_b32 v226, v228\n"
"v_permlane32_swap_b32 v227, v229\n"
"v_max_f32 v226, v226, v228\n"
"v_max_f32 v227, v227, v229\n"
"v_add_f32 v230, 0xc1300000, v230\n"
"v_max3_f32 v226, v226, v230, 0\n"
"v_add_f32 v231, 0xc1300000, v231\n"
"v_max3_f32 v227, v227, v231, 0\n"
"v_exp_f32 v230, -v226\n"
"v_exp_f32 v231, -v227\n"
"v_sub_f32 v176, v176, v226\n"
"v_sub_f32 v177, v177, v226\n"
"v_sub_f32 v178, v178, v226\n"
"v_sub_f32 v179, v179, v226\n"
"v_sub_f32 v180, v180, v226\n"
"v_sub_f32 v181, v181, v226\n"
"v_sub_f32 v182, v182, v226\n"
"v_sub_f32 v183, v183, v226\n"
"v_sub_f32 v184, v184, v227\n"
"v_sub_f32 v185, v185, v227\n"
"v_sub_f32 v186, v186, v227\n"
"v_sub_f32 v187, v187, v227\n"
"v_sub_f32 v188, v188, v227\n"
"v_sub_f32 v189, v189, v227\n"
"v_sub_f32 v190, v190, v227\n"
"v_sub_f32 v191, v191, v227\n"
"v_sub_f32 v200, v200, v226\n"
"v_sub_f32 v201, v201, v226\n"
"v_sub_f32 v202, v202, v226\n"
"v_sub_f32 v203, v203, v226\n"
"v_sub_f32 v204, v204, v227\n"
"v_sub_f32 v205, v205, v227\n"
"v_sub_f32 v206, v206, v227\n"
"v_sub_f32 v207, v207, v227\n"
"v_mul_f32 v216, v216, v230\n"
"v_mul_f32 v217, v217, v231\n"
"v_add_f32 v218, v216, v217\n"
"v_mul_f32 v64, v64, v230\n"
"v_mul_f32 v65, v65, v230\n"
"v_mul_f32 v66, v66, v230\n"
"v_mul_f32 v67, v67, v230\n"
"v_mul_f32 v68, v68, v230\n"
"v_mul_f32 v69, v69, v230\n"
"v_mul_f32 v70, v70, v230\n"
"v_mul_f32 v71, v71, v230\n"
"v_mul_f32 v72, v72, v230\n"
"v_mul_f32 v73, v73, v230\n"
"v_mul_f32 v74, v74, v230\n"
"v_mul_f32 v75, v75, v230\n"
"v_mul_f32 v76, v76, v230\n"
"v_mul_f32 v77, v77, v230\n"
"v_mul_f32 v78, v78, v230\n"
"v_mul_f32 v79, v79, v230\n"
"v_mul_f32 v80, v80, v230\n"
"v_mul_f32 v81, v81, v230\n"
"v_mul_f32 v82, v82, v230\n"
"v_mul_f32 v83, v83, v230\n"
"v_mul_f32 v84, v84, v230\n"
"v_mul_f32 v85, v85, v230\n"
"v_mul_f32 v86, v86, v230\n"
"v_mul_f32 v87, v87, v230\n"
"v_mul_f32 v88, v88, v230\n"
"v_mul_f32 v89, v89, v230\n"
"v_mul_f32 v90, v90, v230\n"
"v_mul_f32 v91, v91, v230\n"
"v_mul_f32 v92, v92, v230\n"
"v_mul_f32 v93, v93, v230\n"
"v_mul_f32 v94, v94, v230\n"
"v_mul_f32 v95, v95, v230\n"
"v_mul_f32 v96, v96, v231\n"
"v_mul_f32 v97, v97, v231\n"
"v_mul_f32 v98, v98, v231\n"
"v_mul_f32 v99, v99, v231\n"
"v_mul_f32 v100, v100, v231\n"
"v_mul_f32 v101, v101, v231\n"
"v_mul_f32 v102, v102, v231\n"
"v_mul_f32 v103, v103, v231\n"
"v_mul_f32 v104, v104, v231\n"
"v_mul_f32 v105, v105, v231\n"
"v_mul_f32 v106, v106, v231\n"
"v_mul_f32 v107, v107, v231\n"
"v_mul_f32 v108, v108, v231\n"
"v_mul_f32 v109, v109, v231\n"
"v_mul_f32 v110, v110, v231\n"
"v_mul_f32 v111, v111, v231\n"
"v_mul_f32 v112, v112, v231\n"
"v_mul_f32 v113, v113, v231\n"
"v_mul_f32 v114, v114, v231\n"
"v_mul_f32 v115, v115, v231\n"
"v_mul_f32 v116, v116, v231\n"
"v_mul_f32 v117, v117, v231\n"
"v_mul_f32 v118, v118, v231\n"
"v_mul_f32 v119, v119, v231\n"
"v_mul_f32 v120, v120, v231\n"
"v_mul_f32 v121, v121, v231\n"
"v_mul_f32 v122, v122, v231\n"
"v_mul_f32 v123, v123, v231\n"
"v_mul_f32 v124, v124, v231\n"
"v_mul_f32 v125, v125, v231\n"
"v_mul_f32 v126, v126, v231\n"
"v_mul_f32 v127, v127, v231\n"
"v_exp_f32 v176, v176\n"
"v_exp_f32 v177, v177\n"
"v_exp_f32 v178, v178\n"
"v_exp_f32 v179, v179\n"
"v_exp_f32 v180, v180\n"
"v_exp_f32 v181, v181\n"
"v_exp_f32 v182, v182\n"
"v_exp_f32 v183, v183\n"
"v_exp_f32 v184, v184\n"
"v_exp_f32 v185, v185\n"
"v_exp_f32 v186, v186\n"
"v_exp_f32 v187, v187\n"
"v_exp_f32 v188, v188\n"
"v_exp_f32 v189, v189\n"
"v_exp_f32 v190, v190\n"
"v_exp_f32 v191, v191\n"
"v_cvt_pk_bf16_f32 v176, v176, v177\n"
"v_cvt_pk_bf16_f32 v177, v178, v179\n"
"v_cvt_pk_bf16_f32 v178, v180, v181\n"
"v_cvt_pk_bf16_f32 v179, v182, v183\n"
"v_cvt_pk_bf16_f32 v180, v184, v185\n"
"v_cvt_pk_bf16_f32 v181, v186, v187\n"
"v_cvt_pk_bf16_f32 v182, v188, v189\n"
"v_cvt_pk_bf16_f32 v183, v190, v191\n"
"v_mfma_f32_16x16x32_bf16 v[220:223], a[232:235], v[176:179], v[216:219]\n"
"s_nop 0\n"
"v_mfma_f32_16x16x32_bf16 v[220:223], a[236:239], v[180:183], v[220:223]\n"
"s_nop 7\n"
"s_setpc_b64 s[36:37]\n"
"Lfused_ps0:\n"
"s_mov_b32 s58, 32768\n"
"Lfused_pl0:\n"
"global_load_dwordx2 v[252:253], v254, s[62:63] sc1\n"
"s_waitcnt vmcnt(0)\n"
"v_cmp_ne_u64 vcc, s[98:99], v[252:253]\n"
"s_cbranch_vccz Lfused_pd0\n"
"s_sleep 1\n"
"s_sub_u32 s58, s58, 1\n"
"s_cbranch_scc0 Lfused_pl0\n"
"s_branch Lfused_pd0\n"
"Lfused_ps1:\n"
"s_mov_b32 s58, 32768\n"
"Lfused_pl1:\n"
"global_load_dwordx2 v[242:243], v251, s[62:63] sc1\n"
"s_waitcnt vmcnt(0)\n"
"v_cmp_ne_u64 vcc, s[98:99], v[242:243]\n"
"s_cbranch_vccz Lfused_pd1\n"
"s_sleep 1\n"
"s_sub_u32 s58, s58, 1\n"
"s_cbranch_scc0 Lfused_pl1\n"
"s_branch Lfused_pd1\n"
"Lfused_ps2:\n"
"s_mov_b32 s58, 32768\n"
"Lfused_pl2:\n"
"global_load_dwordx2 v[242:243], v251, s[62:63] sc1\n"
"s_waitcnt vmcnt(0)\n"
"v_cmp_ne_u64 vcc, s[98:99], v[242:243]\n"
"s_cbranch_vccz Lfused_pd2\n"
"s_sleep 1\n"
"s_sub_u32 s58, s58, 1\n"
"s_cbranch_scc0 Lfused_pl2\n"
"s_branch Lfused_pd2\n"
"Lfused_ps4:\n"
"s_mov_b32 s58, 32768\n"
"Lfused_pl4:\n"
"global_load_dwordx2 v[250:251], v244, s[62:63] sc1\n"
"s_waitcnt vmcnt(0)\n"
"v_cmp_ne_u64 vcc, s[98:99], v[250:251]\n"
"s_cbranch_vccz Lfused_pd4\n"
"s_sleep 1\n"
"s_sub_u32 s58, s58, 1\n"
"s_cbranch_scc0 Lfused_pl4\n"
"s_branch Lfused_pd4\n"
  :: "s"(__builtin_amdgcn_kernarg_segment_ptr()), "s"(blockIdx.x), "s"(wv), "s"(lb) :
    "v0", "v1", "v2", "v3", "v4", "v5", "v6", "v7", "v8", "v9", "v10", "v11", "v12", "v13", "v14", "v15", "v16", "v17", "v18", "v19", "v20", "v21", "v22", "v23",
    "v24", "v25", "v26", "v27", "v28", "v29", "v30", "v31", "v32", "v33", "v34", "v35", "v36", "v37", "v38", "v39", "v40", "v41", "v42", "v43", "v44", "v45", "v46", "v47",
    "v48", "v49", "v50", "v51", "v52", "v53", "v54", "v55", "v56", "v57", "v58", "v59", "v60", "v61", "v62", "v63", "v64", "v65", "v66", "v67", "v68", "v69", "v70", "v71",
    "v72", "v73", "v74", "v75", "v76", "v77", "v78", "v79", "v80", "v81", "v82", "v83", "v84", "v85", "v86", "v87", "v88", "v89", "v90", "v91", "v92", "v93", "v94", "v95",
    "v96", "v97", "v98", "v99", "v100", "v101", "v102", "v103", "v104", "v105", "v106", "v107", "v108", "v109", "v110", "v111", "v112", "v113", "v114", "v115", "v116", "v117", "v118", "v119",
    "v120", "v121", "v122", "v123", "v124", "v125", "v126", "v127", "v128", "v129", "v130", "v131", "v132", "v133", "v134", "v135", "v136", "v137", "v138", "v139", "v140", "v141", "v142", "v143",
    "v144", "v145", "v146", "v147", "v148", "v149", "v150", "v151", "v152", "v153", "v154", "v155", "v156", "v157", "v158", "v159", "v160", "v161", "v162", "v163", "v164", "v165", "v166", "v167",
    "v168", "v169", "v170", "v171", "v172", "v173", "v174", "v175", "v176", "v177", "v178", "v179", "v180", "v181", "v182", "v183", "v184", "v185", "v186", "v187", "v188", "v189", "v190", "v191",
    "v192", "v193", "v194", "v195", "v196", "v197", "v198", "v199", "v200", "v201", "v202", "v203", "v204", "v205", "v206", "v207", "v208", "v209", "v210", "v211", "v212", "v213", "v214", "v215",
    "v216", "v217", "v218", "v219", "v220", "v221", "v222", "v223", "v224", "v225", "v226", "v227", "v228", "v229", "v230", "v231", "v232", "v233", "v234", "v235", "v236", "v237", "v238", "v239",
    "v240", "v241", "v242", "v243", "v244", "v245", "v246", "v247", "v248", "v249", "v250", "v251", "v252", "v253", "v254", "v255", "a0", "a1", "a2", "a3", "a4", "a5", "a6", "a7",
    "a8", "a9", "a10", "a11", "a12", "a13", "a14", "a15", "a16", "a17", "a18", "a19", "a20", "a21", "a22", "a23", "a24", "a25", "a26", "a27", "a28", "a29", "a30", "a31",
    "a32", "a33", "a34", "a35", "a36", "a37", "a38", "a39", "a40", "a41", "a42", "a43", "a44", "a45", "a46", "a47", "a48", "a49", "a50", "a51", "a52", "a53", "a54", "a55",
    "a56", "a57", "a58", "a59", "a60", "a61", "a62", "a63", "a64", "a65", "a66", "a67", "a68", "a69", "a70", "a71", "a72", "a73", "a74", "a75", "a76", "a77", "a78", "a79",
    "a80", "a81", "a82", "a83", "a84", "a85", "a86", "a87", "a88", "a89", "a90", "a91", "a92", "a93", "a94", "a95", "a96", "a97", "a98", "a99", "a100", "a101", "a102", "a103",
    "a104", "a105", "a106", "a107", "a108", "a109", "a110", "a111", "a112", "a113", "a114", "a115", "a116", "a117", "a118", "a119", "a120", "a121", "a122", "a123", "a124", "a125", "a126", "a127",
    "a128", "a129", "a130", "a131", "a132", "a133", "a134", "a135", "a136", "a137", "a138", "a139", "a140", "a141", "a142", "a143", "a144", "a145", "a146", "a147", "a148", "a149", "a150", "a151",
    "a152", "a153", "a154", "a155", "a156", "a157", "a158", "a159", "a160", "a161", "a162", "a163", "a164", "a165", "a166", "a167", "a168", "a169", "a170", "a171", "a172", "a173", "a174", "a175",
    "a176", "a177", "a178", "a179", "a180", "a181", "a182", "a183", "a184", "a185", "a186", "a187", "a188", "a189", "a190", "a191", "a192", "a193", "a194", "a195", "a196", "a197", "a198", "a199",
    "a200", "a201", "a202", "a203", "a204", "a205", "a206", "a207", "a208", "a209", "a210", "a211", "a212", "a213", "a214", "a215", "a216", "a217", "a218", "a219", "a220", "a221", "a222", "a223",
    "a224", "a225", "a226", "a227", "a228", "a229", "a230", "a231", "a232", "a233", "a234", "a235", "a236", "a237", "a238", "a239", "a240", "a241", "a242", "a243", "a244", "a245", "a246", "a247",
    "a248", "a249", "a250", "a251", "a252", "a253", "a254", "a255", "s8", "s9", "s10", "s11", "s12", "s13", "s14", "s15", "s16", "s17", "s18", "s19", "s20", "s21", "s22", "s23",
    "s24", "s25", "s26", "s27", "s28", "s29", "s30", "s31", "s34", "s35", "s36", "s37", "s38", "s39", "s40", "s41", "s42", "s43", "s44", "s45", "s46", "s47", "s48", "s49",
    "s50", "s51", "s52", "s53", "s54", "s55", "s56", "s57", "s58", "s59", "s60", "s61", "s62", "s63", "s64", "s65", "s66", "s67", "s68", "s69", "s70", "s71", "s72", "s73",
    "s74", "s75", "s76", "s77", "s78", "s79", "s80", "s81", "s82", "s83", "s84", "s85", "s86", "s87", "s88", "s89", "s90", "s91", "s92", "s93", "s94", "s95", "s96", "s97",
    "s98", "s99", "vcc", "scc", "memory");
}

extern "C" void kernel_launch(void* const* d_in, const int* in_sizes, int n_in, void* d_out, int out_size, void* d_ws, size_t ws_size, hipStream_t stream) {
  fused<<<256, 256, 0, stream>>>(d_in[0], d_in[1], d_in[2], d_in[4], d_in[5], d_in[6], d_in[7], d_in[8], d_in[9], d_out, d_ws);
}
```
